```python
import torch

def _softmax(x, axis=-1):
    return torch.softmax(x, dim=axis)
def _gelu(x):
    return torch.nn.functional.gelu(x, approximate="tanh")
def _silu(x):
    return torch.nn.functional.silu(x)
def _sigmoid(x):
    return torch.sigmoid(x)
def _layernorm(x, axis=-1, eps=1e-5):
    m = x.mean(dim=axis, keepdim=True)
    v = x.var(dim=axis, keepdim=True, unbiased=False)
    return (x - m) / torch.sqrt(v + eps)
def _scatter_add(tgt, idx, src, axis=0):
    return tgt.clone().index_add_(axis, idx, src)
def _segment_sum(src, seg, n):
    out = torch.zeros((n,) + src.shape[1:], dtype=src.dtype)
    return out.index_add_(0, seg, src)
def _pad(x, pw):
    flat = tuple(v for pair in reversed(pw) for v in pair)
    return torch.nn.functional.pad(x, flat)

kernel_name = 'concat_heads__mha_block_cached__gqa_block_cached__near_ridge'

def setup_inputs():

    _w = (lambda a: (lambda t: t.to(torch.bfloat16) if t.is_floating_point() else t)(torch.as_tensor(a)))
    def _A_setup():
        B, Sq, D = 16, 112, 448
        H, Sk, Dh = 16, 512, 256
        return {
            "x": _w((lambda *s: torch.randn(*s, dtype=torch.bfloat16) * 0.8)(B, Sq, D)),
            "k_cache": _w((lambda *s: torch.randn(*s, dtype=torch.bfloat16) * 0.8)(B, H, Sk, Dh)),
            "v_cache": _w((lambda *s: torch.randn(*s, dtype=torch.bfloat16) * 0.05)(B, H, Sk, Dh)),
            "wq": _w((lambda *s: torch.randn(*s, dtype=torch.bfloat16) * 0.8)(D, H * Dh) * (D ** -0.5)),
            "wo": _w((lambda *s: torch.randn(*s, dtype=torch.bfloat16) * 0.05)(H * Dh, D) * ((H * Dh) ** -0.5)),
        }
    _A_in = _A_setup()

    def _B_setup():
        B, Sq, D = 16, 112, 448
        Hq, Hkv, Sk, Dh = 16, 16, 512, 256
        assert Hq % Hkv == 0, (Hq, Hkv)
        return {
            "x": _w((lambda *s: torch.randn(*s, dtype=torch.bfloat16) * 0.8)(B, Sq, D)),
            "k_cache": _w((lambda *s: torch.randn(*s, dtype=torch.bfloat16) * 0.8)(B, Hkv, Sk, Dh)),
            "v_cache": _w((lambda *s: torch.randn(*s, dtype=torch.bfloat16) * 0.05)(B, Hkv, Sk, Dh)),
            "wq": _w((lambda *s: torch.randn(*s, dtype=torch.bfloat16) * 0.8)(D, Hq * Dh) * (D ** -0.5)),
            "wo": _w((lambda *s: torch.randn(*s, dtype=torch.bfloat16) * 0.05)(Hq * Dh, D) * ((Hq * Dh) ** -0.5)),
        }
    _B_in = _B_setup()

    return {
        "A_x": _A_in["x"],
        "A_k_cache": _A_in["k_cache"],
        "A_v_cache": _A_in["v_cache"],
        "A_wq": _A_in["wq"],
        "A_wo": _A_in["wo"],
        "B_x": _B_in["x"],
        "B_k_cache": _B_in["k_cache"],
        "B_v_cache": _B_in["v_cache"],
        "B_wq": _B_in["wq"],
        "B_wo": _B_in["wo"],
    }

def reference(A_x, A_k_cache, A_v_cache, A_wq, A_wo, B_x, B_k_cache, B_v_cache, B_wq, B_wo):
    def _A_ref(x, k_cache, v_cache, wq, wo):
        B, Sq, D = x.shape
        H, Dh = 16, 256
        q = (lambda x, *d: x.permute(*d))((x @ wq).reshape(B, Sq, H, Dh), 0, 2, 1, 3)
        scores = torch.einsum("bhqd,bhkd->bhqk", q, k_cache) * (Dh ** -0.5)
        p = _softmax(scores, axis=-1)
        o = torch.einsum("bhqk,bhkd->bhqd", p, v_cache)
        return (lambda x, *d: x.permute(*d))(o, 0, 2, 1, 3).reshape(B, Sq, H * Dh) @ wo

    def _B_ref(x, k_cache, v_cache, wq, wo):
        B, Sq, D = x.shape
        Hq, Dh = 16, 256
        Hkv = k_cache.shape[1]
        rep = Hq // Hkv
        q = (lambda x, *d: x.permute(*d))((x @ wq).reshape(B, Sq, Hq, Dh), 0, 2, 1, 3)
        k = (lambda x, n, axis: torch.repeat_interleave(x, n, dim=axis))(k_cache, rep, axis=1)
        v = (lambda x, n, axis: torch.repeat_interleave(x, n, dim=axis))(v_cache, rep, axis=1)
        scores = torch.einsum("bhqd,bhkd->bhqk", q, k) * (Dh ** -0.5)
        p = _softmax(scores, axis=-1)
        o = torch.einsum("bhqk,bhkd->bhqd", p, v)
        return (lambda x, *d: x.permute(*d))(o, 0, 2, 1, 3).reshape(B, Sq, Hq * Dh) @ wo

    a = _A_ref(A_x, A_k_cache, A_v_cache, A_wq, A_wo)
    b = _B_ref(B_x, B_k_cache, B_v_cache, B_wq, B_wo)
    return torch.concatenate([a, b], axis=-1)
```

```cpp
#include <hip/hip_runtime.h>
#include <hip/hip_bf16.h>
#include <stdint.h>
#define STAMP 0
#define PART_BYTES 114688

typedef __attribute__((ext_vector_type(4))) unsigned u32x4;
__device__ __forceinline__ unsigned pack_bf16(float a, float b) {
  unsigned r;
  asm("v_cvt_pk_bf16_f32 %0, %1, %2" : "=v"(r) : "v"(a), "v"(b));
  return r;
}
typedef __attribute__((ext_vector_type(2))) unsigned u32x2;

__global__ __launch_bounds__(256, 1) void fused_attn_kernel(
    const char* x0, const char* k0, const char* v0, const char* wq0, const char* wo0,
    const char* x1, const char* k1, const char* v1, const char* wq1, const char* wo1, char* ws, char* out) {
  __shared__ char lds[163840];
  const unsigned g = blockIdx.x;
  const unsigned combo = (g & 7) * 2 + (g >> 7), b = (g >> 3) & 15;
  const unsigned br = combo >> 3, hp = combo & 7;
  const unsigned w = __builtin_amdgcn_readfirstlane(threadIdx.x >> 6);
  const char* pk = (br ? k1 : k0) + (size_t)(b * 16 + hp * 2) * 262144;
  const char* pv = (br ? v1 : v0) + (size_t)(b * 16 + hp * 2) * 262144;
  const char* px = (br ? x1 : x0) + (size_t)b * 100352;
  const char* pq = (br ? wq1 : wq0) + (size_t)hp * 1024;
  const char* po = (br ? wo1 : wo0) + (size_t)hp * 458752;
  char* pp = ws + (size_t)((br * 16 + b) * 8 + hp) * PART_BYTES;
  char* ps = ws + (size_t)256 * PART_BYTES + (size_t)(g * 4 + w) * 64;
  asm volatile("" ::"s"((unsigned)(size_t)lds));
  asm volatile(
R"x(.macro M df,d,af,a,bf,b
v_mfma_f32_16x16x32_bf16 \df[\d:\d+3], \af[\a:\a+3], \bf[\b:\b+3], \df[\d:\d+3]
.endm
.macro T f,d,b,o
ds_read_b64_tr_b16 \f[\d:\d+1], v\b offset:\o
.endm
.macro R f,d,b,o
ds_read_b64 \f[\d:\d+1], v\b offset:\o
.endm
.macro A d,a
v_accvgpr_read_b32 v\d, a\a
.endm
.macro W n
s_waitcnt lgkmcnt(\n)
.endm
s_mov_b64 s[36:37], %0
s_mov_b32 s38, 524288
s_mov_b32 s39, 0x20000
s_mov_b64 s[40:41], %1
s_mov_b32 s42, 524288
s_mov_b32 s43, 0x20000
s_mov_b64 s[44:45], %2
s_mov_b32 s46, 100352
s_mov_b32 s47, 0x20000
s_mov_b64 s[48:49], %3
s_mov_b32 s50, 3662848
s_mov_b32 s51, 0x20000
s_mov_b64 s[52:53], %4
s_mov_b32 s54, 458752
s_mov_b32 s55, 0x20000
s_mov_b64 s[56:57], %5
s_mov_b32 s58, 114688
s_mov_b32 s59, 0x20000
s_lshl_b32 s60, %6, 12
s_mov_b32 s62, 0x3db8aa3b
s_mov_b32 s63, 0x3db8aa3b
s_lshl_b32 s33, %10, 4
s_add_u32 s33, s33, %9
s_getpc_b64 s[92:93]
.Lpf_start%=:
s_mov_b32 s94, .Lkern_end-.Lpf_start%=
s_mov_b32 s95, 0x20000
v_mbcnt_lo_u32_b32 v0, -1, 0
v_mbcnt_hi_u32_b32 v0, -1, v0
s_cmp_eq_u32 %6, 3
s_cbranch_scc0 .Lw3%=
v_bfe_u32 v1, v0, 2, 3
v_lshrrev_b32 v2, 5, v0
v_lshlrev_b32 v2, 6, v2
v_and_b32 v3, 3, v0
v_bfe_u32 v5, v0, 4, 1
v_lshlrev_b32 v4, 1, v5
v_xor_b32 v4, v4, v3
v_lshl_add_u32 v4, v4, 4, v2
v_xor_b32 v5, v5, v3
v_lshl_add_u32 v5, v5, 4, v2
v_mul_u32_u24 v7, 896, v1
v_add_u32 v20, v7, v5
v_xor_b32 v21, 32, v20
v_lshl_add_u32 v22, v1, 13, v4
v_lshl_add_u32 v23, v1, 9, v4
v_add_u32 v24, v7, v4
v_lshl_add_u32 v19, v1, 9, v5
s_mul_i32 s61, s33, 8
v_add_u32 v25, s61, v0
v_cmp_gt_u32 vcc, 8, v0
v_lshlrev_b32 v25, 7, v25
s_mov_b32 s61, 0
v_cndmask_b32 v25, 2.0, v25, vcc
buffer_load_dword v29, v25, s[92:95], s61 offen
s_mul_i32 s61, s33, 8
v_add_u32 v25, s61, v0
v_cmp_gt_u32 vcc, 8, v0
v_lshlrev_b32 v25, 7, v25
s_mov_b32 s61, .Lw3%=-.Lpf_start%=
v_cndmask_b32 v25, 2.0, v25, vcc
buffer_load_dword v29, v25, s[92:95], s61 offen
s_mov_b32 m0, 0
s_mov_b32 s61, 0
buffer_load_dwordx4 v20, s[44:47], s61 offen offset:0 lds
s_mov_b32 m0, 1024
s_mov_b32 s61, 7168
buffer_load_dwordx4 v21, s[44:47], s61 offen offset:0 lds
s_mov_b32 m0, 2048
s_mov_b32 s61, 14336
buffer_load_dwordx4 v20, s[44:47], s61 offen offset:0 lds
s_mov_b32 m0, 3072
s_mov_b32 s61, 21504
buffer_load_dwordx4 v21, s[44:47], s61 offen offset:0 lds
s_mov_b32 m0, 4096
s_mov_b32 s61, 28672
buffer_load_dwordx4 v20, s[44:47], s61 offen offset:0 lds
s_mov_b32 m0, 5120
s_mov_b32 s61, 35840
buffer_load_dwordx4 v21, s[44:47], s61 offen offset:0 lds
s_mov_b32 m0, 6144
s_mov_b32 s61, 43008
buffer_load_dwordx4 v20, s[44:47], s61 offen offset:0 lds
s_mov_b32 m0, 7168
s_mov_b32 s61, 50176
buffer_load_dwordx4 v21, s[44:47], s61 offen offset:0 lds
s_mov_b32 m0, 8192
s_mov_b32 s61, 57344
buffer_load_dwordx4 v20, s[44:47], s61 offen offset:0 lds
s_mov_b32 m0, 9216
s_mov_b32 s61, 64512
buffer_load_dwordx4 v21, s[44:47], s61 offen offset:0 lds
s_mov_b32 m0, 10240
s_mov_b32 s61, 71680
buffer_load_dwordx4 v20, s[44:47], s61 offen offset:0 lds
s_mov_b32 m0, 11264
s_mov_b32 s61, 78848
buffer_load_dwordx4 v21, s[44:47], s61 offen offset:0 lds
s_mov_b32 m0, 12288
s_mov_b32 s61, 86016
buffer_load_dwordx4 v20, s[44:47], s61 offen offset:0 lds
s_mov_b32 m0, 13312
s_mov_b32 s61, 93184
buffer_load_dwordx4 v21, s[44:47], s61 offen offset:0 lds
s_mov_b32 m0, 16384
s_mov_b32 s61, 0
buffer_load_dwordx4 v22, s[48:51], s61 offen offset:0 lds
s_mov_b32 m0, 17280
s_nop 0
buffer_load_dwordx4 v22, s[48:51], s61 offen offset:128 lds
s_mov_b32 m0, 18176
s_nop 0
buffer_load_dwordx4 v22, s[48:51], s61 offen offset:256 lds
s_mov_b32 m0, 19072
s_nop 0
buffer_load_dwordx4 v22, s[48:51], s61 offen offset:384 lds
s_mov_b32 m0, 20480
s_mov_b32 s61, 65536
buffer_load_dwordx4 v22, s[48:51], s61 offen offset:0 lds
s_mov_b32 m0, 21376
s_nop 0
buffer_load_dwordx4 v22, s[48:51], s61 offen offset:128 lds
s_mov_b32 m0, 22272
s_nop 0
buffer_load_dwordx4 v22, s[48:51], s61 offen offset:256 lds
s_mov_b32 m0, 23168
s_nop 0
buffer_load_dwordx4 v22, s[48:51], s61 offen offset:384 lds
s_mov_b32 m0, 24576
s_mov_b32 s61, 131072
buffer_load_dwordx4 v22, s[48:51], s61 offen offset:0 lds
s_mov_b32 m0, 25472
s_nop 0
buffer_load_dwordx4 v22, s[48:51], s61 offen offset:128 lds
s_mov_b32 m0, 26368
s_nop 0
buffer_load_dwordx4 v22, s[48:51], s61 offen offset:256 lds
s_mov_b32 m0, 27264
s_nop 0
buffer_load_dwordx4 v22, s[48:51], s61 offen offset:384 lds
s_mov_b32 m0, 28672
s_mov_b32 s61, 196608
buffer_load_dwordx4 v22, s[48:51], s61 offen offset:0 lds
s_mov_b32 m0, 29568
s_nop 0
buffer_load_dwordx4 v22, s[48:51], s61 offen offset:128 lds
s_mov_b32 m0, 30464
s_nop 0
buffer_load_dwordx4 v22, s[48:51], s61 offen offset:256 lds
s_mov_b32 m0, 31360
s_nop 0
buffer_load_dwordx4 v22, s[48:51], s61 offen offset:384 lds
v_lshrrev_b32 v30, 5, v0
v_bfe_u32 v31, v0, 2, 2
v_xor_b32 v31, v31, v30
v_and_b32 v26, 7, v0
v_lshlrev_b32 v26, 6, v26
v_lshl_add_u32 v26, v31, 4, v26
v_bfe_u32 v27, v0, 4, 1
v_lshl_add_u32 v26, v27, 3, v26
v_bfe_u32 v31, v0, 3, 1
v_lshl_add_u32 v1, v31, 12, v26
v_xor_b32 v4, 32, v1
v_lshl_add_u32 v26, v31, 10, v26
v_add_u32 v13, s60, v26
v_xor_b32 v16, 32, v13
v_bfe_u32 v31, v0, 2, 3
v_lshlrev_b32 v31, 6, v31
v_lshl_add_u32 v31, v30, 12, v31
v_lshl_add_u32 v31, v27, 5, v31
v_and_b32 v26, 3, v0
v_lshl_add_u32 v7, v26, 3, v31
v_xor_b32 v10, 32, v7
v_add_u32 v2, 0x10000, v1
v_add_u32 v3, 0x20000, v1
v_add_u32 v5, 0x10000, v4
v_add_u32 v6, 0x20000, v4
v_add_u32 v14, 0x10000, v13
v_add_u32 v15, 0x20000, v13
v_add_u32 v17, 0x10000, v16
v_add_u32 v18, 0x20000, v16
v_add_u32 v8, 0x10000, v7
v_add_u32 v9, 0x20000, v7
v_add_u32 v11, 0x10000, v10
v_add_u32 v12, 0x20000, v10
v_xor_b32 v27, 32, v19
s_mov_b32 m0, 32768
s_mov_b32 s61, 262144
buffer_load_dwordx4 v22, s[48:51], s61 offen offset:0 lds
s_mov_b32 m0, 33664
s_nop 0
buffer_load_dwordx4 v22, s[48:51], s61 offen offset:128 lds
s_mov_b32 m0, 34560
s_nop 0
buffer_load_dwordx4 v22, s[48:51], s61 offen offset:256 lds
s_mov_b32 m0, 35456
s_nop 0
buffer_load_dwordx4 v22, s[48:51], s61 offen offset:384 lds
s_mov_b32 m0, 36864
s_mov_b32 s61, 327680
buffer_load_dwordx4 v22, s[48:51], s61 offen offset:0 lds
s_mov_b32 m0, 37760
s_nop 0
buffer_load_dwordx4 v22, s[48:51], s61 offen offset:128 lds
s_mov_b32 m0, 38656
s_nop 0
buffer_load_dwordx4 v22, s[48:51], s61 offen offset:256 lds
s_mov_b32 m0, 39552
s_nop 0
buffer_load_dwordx4 v22, s[48:51], s61 offen offset:384 lds
s_mov_b32 m0, 40960
s_mov_b32 s61, 393216
buffer_load_dwordx4 v22, s[48:51], s61 offen offset:0 lds
s_mov_b32 m0, 41856
s_nop 0
buffer_load_dwordx4 v22, s[48:51], s61 offen offset:128 lds
s_mov_b32 m0, 42752
s_nop 0
buffer_load_dwordx4 v22, s[48:51], s61 offen offset:256 lds
s_mov_b32 m0, 43648
s_nop 0
buffer_load_dwordx4 v22, s[48:51], s61 offen offset:384 lds
s_mov_b32 m0, 45056
s_mov_b32 s61, 458752
buffer_load_dwordx4 v22, s[48:51], s61 offen offset:0 lds
s_mov_b32 m0, 45952
s_nop 0
buffer_load_dwordx4 v22, s[48:51], s61 offen offset:128 lds
s_mov_b32 m0, 46848
s_nop 0
buffer_load_dwordx4 v22, s[48:51], s61 offen offset:256 lds
s_mov_b32 m0, 47744
s_nop 0
buffer_load_dwordx4 v22, s[48:51], s61 offen offset:384 lds
s_waitcnt vmcnt(16)
s_barrier
R a,144,13,0
R a,146,16,0
T v,224,7,16384
T v,226,7,24576
T v,228,10,16384
T v,230,10,24576
T v,232,7,16896
T v,234,7,25088
T v,236,10,16896
T v,238,10,25088
T v,240,7,17408
T v,242,7,25600
T v,244,10,17408
W 7
s_mov_b32 m0, 49152
s_mov_b32 s61, 128
buffer_load_dwordx4 v20, s[44:47], s61 offen offset:0 lds
s_mov_b32 m0, 50176
v_mfma_f32_16x16x32_bf16 v[96:99], v[224:227], a[144:147], 0
T v,246,10,25600
s_mov_b32 s61, 7296
buffer_load_dwordx4 v21, s[44:47], s61 offen offset:0 lds
s_mov_b32 m0, 51200
s_mov_b32 s61, 14464
T v,248,7,17920
buffer_load_dwordx4 v20, s[44:47], s61 offen offset:0 lds
s_mov_b32 m0, 52224
s_mov_b32 s61, 21632
buffer_load_dwordx4 v21, s[44:47], s61 offen offset:0 lds
v_mfma_f32_16x16x32_bf16 v[100:103], v[228:231], a[144:147], 0
T v,250,7,26112
s_mov_b32 m0, 53248
s_mov_b32 s61, 28800
buffer_load_dwordx4 v20, s[44:47], s61 offen offset:0 lds
s_mov_b32 m0, 54272
T v,252,10,17920
W 7
s_mov_b32 s61, 35968
buffer_load_dwordx4 v21, s[44:47], s61 offen offset:0 lds
s_mov_b32 m0, 55296
s_mov_b32 s61, 43136
v_mfma_f32_16x16x32_bf16 v[112:115], v[232:235], a[144:147], 0
T v,254,10,26112
buffer_load_dwordx4 v20, s[44:47], s61 offen offset:0 lds
s_mov_b32 m0, 56320
s_mov_b32 s61, 50304
buffer_load_dwordx4 v21, s[44:47], s61 offen offset:0 lds
T v,224,7,18432
s_mov_b32 m0, 57344
s_mov_b32 s61, 57472
buffer_load_dwordx4 v20, s[44:47], s61 offen offset:0 lds
s_mov_b32 m0, 58368
v_mfma_f32_16x16x32_bf16 v[116:119], v[236:239], a[144:147], 0
T v,226,7,26624
s_mov_b32 s61, 64640
buffer_load_dwordx4 v21, s[44:47], s61 offen offset:0 lds
s_mov_b32 m0, 59392
s_mov_b32 s61, 71808
T v,228,10,18432
W 7
buffer_load_dwordx4 v20, s[44:47], s61 offen offset:0 lds
s_mov_b32 m0, 60416
s_mov_b32 s61, 78976
buffer_load_dwordx4 v21, s[44:47], s61 offen offset:0 lds
v_mfma_f32_16x16x32_bf16 v[128:131], v[240:243], a[144:147], 0
T v,230,10,26624
s_mov_b32 m0, 61440
s_mov_b32 s61, 86144
buffer_load_dwordx4 v20, s[44:47], s61 offen offset:0 lds
s_mov_b32 m0, 62464
T v,232,7,18944
s_mov_b32 s61, 93312
buffer_load_dwordx4 v21, s[44:47], s61 offen offset:0 lds
s_mov_b32 m0, 65536
s_mov_b32 s61, 524288
v_mfma_f32_16x16x32_bf16 v[132:135], v[244:247], a[144:147], 0
T v,234,7,27136
buffer_load_dwordx4 v22, s[48:51], s61 offen offset:0 lds
s_mov_b32 m0, 66432
T v,236,10,18944
W 7
buffer_load_dwordx4 v22, s[48:51], s61 offen offset:128 lds
s_mov_b32 m0, 67328
v_mfma_f32_16x16x32_bf16 v[144:147], v[248:251], a[144:147], 0
T v,238,10,27136
buffer_load_dwordx4 v22, s[48:51], s61 offen offset:256 lds
s_mov_b32 m0, 68224
T v,240,7,19456
buffer_load_dwordx4 v22, s[48:51], s61 offen offset:384 lds
s_mov_b32 m0, 69632
s_mov_b32 s61, 589824
buffer_load_dwordx4 v22, s[48:51], s61 offen offset:0 lds
v_mfma_f32_16x16x32_bf16 v[148:151], v[252:255], a[144:147], 0
T v,242,7,27648
s_mov_b32 m0, 70528
T v,244,10,19456
W 7
buffer_load_dwordx4 v22, s[48:51], s61 offen offset:128 lds
s_mov_b32 m0, 71424
v_mfma_f32_16x16x32_bf16 v[160:163], v[224:227], a[144:147], 0
T v,246,10,27648
buffer_load_dwordx4 v22, s[48:51], s61 offen offset:256 lds
s_mov_b32 m0, 72320
T v,248,7,19968
buffer_load_dwordx4 v22, s[48:51], s61 offen offset:384 lds
s_mov_b32 m0, 73728
s_mov_b32 s61, 655360
buffer_load_dwordx4 v22, s[48:51], s61 offen offset:0 lds
v_mfma_f32_16x16x32_bf16 v[164:167], v[228:231], a[144:147], 0
T v,250,7,28160
s_mov_b32 m0, 74624
T v,252,10,19968
W 7
buffer_load_dwordx4 v22, s[48:51], s61 offen offset:128 lds
s_mov_b32 m0, 75520
v_mfma_f32_16x16x32_bf16 v[176:179], v[232:235], a[144:147], 0
T v,254,10,28160
buffer_load_dwordx4 v22, s[48:51], s61 offen offset:256 lds
s_mov_b32 m0, 76416
s_waitcnt vmcnt(25)
s_barrier
R a,152,13,512
R a,154,16,512
T v,224,7,32768
buffer_load_dwordx4 v22, s[48:51], s61 offen offset:384 lds
s_mov_b32 m0, 77824
s_mov_b32 s61, 720896
buffer_load_dwordx4 v22, s[48:51], s61 offen offset:0 lds
v_mfma_f32_16x16x32_bf16 v[180:183], v[236:239], a[144:147], 0
T v,226,7,40960
s_mov_b32 m0, 78720
T v,228,10,32768
W 9
buffer_load_dwordx4 v22, s[48:51], s61 offen offset:128 lds
s_mov_b32 m0, 79616
v_mfma_f32_16x16x32_bf16 v[192:195], v[240:243], a[144:147], 0
T v,230,10,40960
buffer_load_dwordx4 v22, s[48:51], s61 offen offset:256 lds
s_mov_b32 m0, 80512
T v,232,7,33280
buffer_load_dwordx4 v22, s[48:51], s61 offen offset:384 lds
s_mov_b32 m0, 81920
s_mov_b32 s61, 786432
buffer_load_dwordx4 v22, s[48:51], s61 offen offset:0 lds
v_mfma_f32_16x16x32_bf16 v[196:199], v[244:247], a[144:147], 0
T v,234,7,41472
s_mov_b32 m0, 82816
T v,236,10,33280
W 9
)x"
R"x(buffer_load_dwordx4 v22, s[48:51], s61 offen offset:128 lds
s_mov_b32 m0, 83712
v_mfma_f32_16x16x32_bf16 v[208:211], v[248:251], a[144:147], 0
T v,238,10,41472
buffer_load_dwordx4 v22, s[48:51], s61 offen offset:256 lds
s_mov_b32 m0, 84608
T v,240,7,33792
buffer_load_dwordx4 v22, s[48:51], s61 offen offset:384 lds
s_mov_b32 m0, 86016
s_mov_b32 s61, 851968
buffer_load_dwordx4 v22, s[48:51], s61 offen offset:0 lds
v_mfma_f32_16x16x32_bf16 v[212:215], v[252:255], a[144:147], 0
T v,242,7,41984
s_mov_b32 m0, 86912
T v,244,10,33792
W 7
buffer_load_dwordx4 v22, s[48:51], s61 offen offset:128 lds
s_mov_b32 m0, 87808
M v,96,v,224,a,152
T v,246,10,41984
buffer_load_dwordx4 v22, s[48:51], s61 offen offset:256 lds
s_mov_b32 m0, 88704
T v,248,7,34304
buffer_load_dwordx4 v22, s[48:51], s61 offen offset:384 lds
s_mov_b32 m0, 90112
s_mov_b32 s61, 917504
buffer_load_dwordx4 v22, s[48:51], s61 offen offset:0 lds
M v,100,v,228,a,152
T v,250,7,42496
s_mov_b32 m0, 91008
T v,252,10,34304
W 7
buffer_load_dwordx4 v22, s[48:51], s61 offen offset:128 lds
s_mov_b32 m0, 91904
M v,112,v,232,a,152
T v,254,10,42496
buffer_load_dwordx4 v22, s[48:51], s61 offen offset:256 lds
s_mov_b32 m0, 92800
T v,224,7,34816
buffer_load_dwordx4 v22, s[48:51], s61 offen offset:384 lds
s_mov_b32 m0, 94208
s_mov_b32 s61, 983040
buffer_load_dwordx4 v22, s[48:51], s61 offen offset:0 lds
M v,116,v,236,a,152
T v,226,7,43008
s_mov_b32 m0, 95104
T v,228,10,34816
W 7
buffer_load_dwordx4 v22, s[48:51], s61 offen offset:128 lds
s_mov_b32 m0, 96000
M v,128,v,240,a,152
T v,230,10,43008
buffer_load_dwordx4 v22, s[48:51], s61 offen offset:256 lds
s_mov_b32 m0, 96896
T v,232,7,35328
buffer_load_dwordx4 v22, s[48:51], s61 offen offset:384 lds
s_mov_b32 m0, 98304
s_mov_b32 s61, 256
buffer_load_dwordx4 v20, s[44:47], s61 offen offset:0 lds
M v,132,v,244,a,152
T v,234,7,43520
s_mov_b32 m0, 99328
s_mov_b32 s61, 7424
buffer_load_dwordx4 v21, s[44:47], s61 offen offset:0 lds
s_mov_b32 m0, 100352
T v,236,10,35328
W 7
s_mov_b32 s61, 14592
buffer_load_dwordx4 v20, s[44:47], s61 offen offset:0 lds
s_mov_b32 m0, 101376
s_mov_b32 s61, 21760
M v,144,v,248,a,152
T v,238,10,43520
buffer_load_dwordx4 v21, s[44:47], s61 offen offset:0 lds
s_mov_b32 m0, 102400
s_mov_b32 s61, 28928
buffer_load_dwordx4 v20, s[44:47], s61 offen offset:0 lds
T v,240,7,35840
s_mov_b32 m0, 103424
s_mov_b32 s61, 36096
buffer_load_dwordx4 v21, s[44:47], s61 offen offset:0 lds
s_mov_b32 m0, 104448
M v,148,v,252,a,152
T v,242,7,44032
s_mov_b32 s61, 43264
buffer_load_dwordx4 v20, s[44:47], s61 offen offset:0 lds
s_mov_b32 m0, 105472
s_mov_b32 s61, 50432
T v,244,10,35840
W 7
buffer_load_dwordx4 v21, s[44:47], s61 offen offset:0 lds
s_mov_b32 m0, 106496
s_mov_b32 s61, 57600
buffer_load_dwordx4 v20, s[44:47], s61 offen offset:0 lds
M v,160,v,224,a,152
T v,246,10,44032
s_mov_b32 m0, 107520
s_mov_b32 s61, 64768
buffer_load_dwordx4 v21, s[44:47], s61 offen offset:0 lds
s_mov_b32 m0, 108544
T v,248,7,36352
s_mov_b32 s61, 71936
buffer_load_dwordx4 v20, s[44:47], s61 offen offset:0 lds
s_mov_b32 m0, 109568
s_mov_b32 s61, 79104
M v,164,v,228,a,152
T v,250,7,44544
buffer_load_dwordx4 v21, s[44:47], s61 offen offset:0 lds
s_mov_b32 m0, 110592
s_mov_b32 s61, 86272
buffer_load_dwordx4 v20, s[44:47], s61 offen offset:0 lds
T v,252,10,36352
W 7
s_mov_b32 m0, 111616
s_mov_b32 s61, 93440
buffer_load_dwordx4 v21, s[44:47], s61 offen offset:0 lds
s_mov_b32 m0, 114688
M v,176,v,232,a,152
T v,254,10,44544
s_mov_b32 s61, 1048576
buffer_load_dwordx4 v22, s[48:51], s61 offen offset:0 lds
s_mov_b32 m0, 115584
s_waitcnt vmcnt(31)
s_barrier
R a,160,13,49152
R a,162,16,49152
T v,224,8,0
buffer_load_dwordx4 v22, s[48:51], s61 offen offset:128 lds
s_mov_b32 m0, 116480
M v,180,v,236,a,152
T v,226,8,8192
buffer_load_dwordx4 v22, s[48:51], s61 offen offset:256 lds
s_mov_b32 m0, 117376
T v,228,11,0
W 9
buffer_load_dwordx4 v22, s[48:51], s61 offen offset:384 lds
s_mov_b32 m0, 118784
s_mov_b32 s61, 1114112
buffer_load_dwordx4 v22, s[48:51], s61 offen offset:0 lds
M v,192,v,240,a,152
T v,230,11,8192
s_mov_b32 m0, 119680
T v,232,8,512
buffer_load_dwordx4 v22, s[48:51], s61 offen offset:128 lds
s_mov_b32 m0, 120576
M v,196,v,244,a,152
T v,234,8,8704
buffer_load_dwordx4 v22, s[48:51], s61 offen offset:256 lds
s_mov_b32 m0, 121472
T v,236,11,512
W 9
buffer_load_dwordx4 v22, s[48:51], s61 offen offset:384 lds
s_mov_b32 m0, 122880
s_mov_b32 s61, 1179648
buffer_load_dwordx4 v22, s[48:51], s61 offen offset:0 lds
M v,208,v,248,a,152
T v,238,11,8704
s_mov_b32 m0, 123776
T v,240,8,1024
buffer_load_dwordx4 v22, s[48:51], s61 offen offset:128 lds
s_mov_b32 m0, 124672
M v,212,v,252,a,152
T v,242,8,9216
buffer_load_dwordx4 v22, s[48:51], s61 offen offset:256 lds
s_mov_b32 m0, 125568
T v,244,11,1024
W 7
buffer_load_dwordx4 v22, s[48:51], s61 offen offset:384 lds
s_mov_b32 m0, 126976
s_mov_b32 s61, 1245184
buffer_load_dwordx4 v22, s[48:51], s61 offen offset:0 lds
M v,96,v,224,a,160
T v,246,11,9216
s_mov_b32 m0, 127872
T v,248,8,1536
buffer_load_dwordx4 v22, s[48:51], s61 offen offset:128 lds
s_mov_b32 m0, 128768
M v,100,v,228,a,160
T v,250,8,9728
buffer_load_dwordx4 v22, s[48:51], s61 offen offset:256 lds
s_mov_b32 m0, 129664
T v,252,11,1536
W 7
buffer_load_dwordx4 v22, s[48:51], s61 offen offset:384 lds
v_lshlrev_b32 v25, 16, v0
buffer_load_dword v29, v25, s[36:39], 0 offen nt
buffer_load_dword v29, v25, s[40:43], 0 offen nt
M v,112,v,232,a,160
T v,254,11,9728
buffer_load_dword v29, v25, s[52:55], 0 offen nt
s_mov_b32 m0, 131072
s_mov_b32 s61, 1310720
buffer_load_dwordx4 v22, s[48:51], s61 offen offset:0 lds
T v,224,8,2048
s_mov_b32 m0, 131968
M v,116,v,236,a,160
T v,226,8,10240
buffer_load_dwordx4 v22, s[48:51], s61 offen offset:128 lds
s_mov_b32 m0, 132864
T v,228,11,2048
W 7
buffer_load_dwordx4 v22, s[48:51], s61 offen offset:256 lds
s_mov_b32 m0, 133760
M v,128,v,240,a,160
T v,230,11,10240
buffer_load_dwordx4 v22, s[48:51], s61 offen offset:384 lds
s_mov_b32 m0, 135168
s_mov_b32 s61, 1376256
buffer_load_dwordx4 v22, s[48:51], s61 offen offset:0 lds
T v,232,8,2560
s_mov_b32 m0, 136064
M v,132,v,244,a,160
T v,234,8,10752
buffer_load_dwordx4 v22, s[48:51], s61 offen offset:128 lds
s_mov_b32 m0, 136960
T v,236,11,2560
W 7
buffer_load_dwordx4 v22, s[48:51], s61 offen offset:256 lds
s_mov_b32 m0, 137856
M v,144,v,248,a,160
T v,238,11,10752
buffer_load_dwordx4 v22, s[48:51], s61 offen offset:384 lds
s_mov_b32 m0, 139264
s_mov_b32 s61, 1441792
buffer_load_dwordx4 v22, s[48:51], s61 offen offset:0 lds
T v,240,8,3072
s_mov_b32 m0, 140160
M v,148,v,252,a,160
T v,242,8,11264
buffer_load_dwordx4 v22, s[48:51], s61 offen offset:128 lds
s_mov_b32 m0, 141056
T v,244,11,3072
W 7
buffer_load_dwordx4 v22, s[48:51], s61 offen offset:256 lds
s_mov_b32 m0, 141952
M v,160,v,224,a,160
T v,246,11,11264
buffer_load_dwordx4 v22, s[48:51], s61 offen offset:384 lds
s_mov_b32 m0, 143360
s_mov_b32 s61, 1507328
buffer_load_dwordx4 v22, s[48:51], s61 offen offset:0 lds
T v,248,8,3584
s_mov_b32 m0, 144256
M v,164,v,228,a,160
T v,250,8,11776
buffer_load_dwordx4 v22, s[48:51], s61 offen offset:128 lds
s_mov_b32 m0, 145152
T v,252,11,3584
W 7
buffer_load_dwordx4 v22, s[48:51], s61 offen offset:256 lds
s_mov_b32 m0, 146048
M v,176,v,232,a,160
T v,254,11,11776
buffer_load_dwordx4 v22, s[48:51], s61 offen offset:384 lds
s_mov_b32 m0, 147456
s_mov_b32 s61, 384
buffer_load_dwordx4 v20, s[44:47], s61 offen offset:0 lds
s_waitcnt vmcnt(50)
s_barrier
R a,168,13,49664
R a,170,16,49664
T v,224,8,16384
s_mov_b32 m0, 148480
s_mov_b32 s61, 7552
buffer_load_dwordx4 v21, s[44:47], s61 offen offset:0 lds
s_mov_b32 m0, 149504
M v,180,v,236,a,160
T v,226,8,24576
s_mov_b32 s61, 14720
buffer_load_dwordx4 v20, s[44:47], s61 offen offset:0 lds
s_mov_b32 m0, 150528
s_mov_b32 s61, 21888
T v,228,11,16384
W 9
buffer_load_dwordx4 v21, s[44:47], s61 offen offset:0 lds
s_mov_b32 m0, 151552
s_mov_b32 s61, 29056
buffer_load_dwordx4 v20, s[44:47], s61 offen offset:0 lds
M v,192,v,240,a,160
T v,230,11,24576
s_mov_b32 m0, 152576
s_mov_b32 s61, 36224
buffer_load_dwordx4 v21, s[44:47], s61 offen offset:0 lds
s_mov_b32 m0, 153600
T v,232,8,16896
s_mov_b32 s61, 43392
buffer_load_dwordx4 v20, s[44:47], s61 offen offset:0 lds
s_mov_b32 m0, 154624
s_mov_b32 s61, 50560
M v,196,v,244,a,160
T v,234,8,25088
buffer_load_dwordx4 v21, s[44:47], s61 offen offset:0 lds
s_mov_b32 m0, 155648
s_mov_b32 s61, 57728
buffer_load_dwordx4 v20, s[44:47], s61 offen offset:0 lds
T v,236,11,16896
W 9
s_mov_b32 m0, 156672
s_mov_b32 s61, 64896
buffer_load_dwordx4 v21, s[44:47], s61 offen offset:0 lds
s_mov_b32 m0, 157696
M v,208,v,248,a,160
T v,238,11,25088
s_mov_b32 s61, 72064
buffer_load_dwordx4 v20, s[44:47], s61 offen offset:0 lds
s_mov_b32 m0, 158720
s_mov_b32 s61, 79232
T v,240,8,17408
buffer_load_dwordx4 v21, s[44:47], s61 offen offset:0 lds
s_mov_b32 m0, 159744
s_mov_b32 s61, 86400
buffer_load_dwordx4 v20, s[44:47], s61 offen offset:0 lds
M v,212,v,252,a,160
T v,242,8,25600
s_mov_b32 m0, 160768
s_mov_b32 s61, 93568
buffer_load_dwordx4 v21, s[44:47], s61 offen offset:0 lds
s_mov_b32 m0, 0
T v,244,11,17408
W 7
s_mov_b32 s61, 1572864
buffer_load_dwordx4 v22, s[48:51], s61 offen offset:0 lds
s_mov_b32 m0, 896
M v,96,v,224,a,168
T v,246,11,25600
buffer_load_dwordx4 v22, s[48:51], s61 offen offset:128 lds
s_mov_b32 m0, 1792
T v,248,8,17920
buffer_load_dwordx4 v22, s[48:51], s61 offen offset:256 lds
s_mov_b32 m0, 2688
M v,100,v,228,a,168
T v,250,8,26112
buffer_load_dwordx4 v22, s[48:51], s61 offen offset:384 lds
s_mov_b32 m0, 4096
s_mov_b32 s61, 1638400
buffer_load_dwordx4 v22, s[48:51], s61 offen offset:0 lds
T v,252,11,17920
W 7
s_mov_b32 m0, 4992
M v,112,v,232,a,168
T v,254,11,26112
buffer_load_dwordx4 v22, s[48:51], s61 offen offset:128 lds
s_mov_b32 m0, 5888
T v,224,8,18432
buffer_load_dwordx4 v22, s[48:51], s61 offen offset:256 lds
s_mov_b32 m0, 6784
M v,116,v,236,a,168
T v,226,8,26624
buffer_load_dwordx4 v22, s[48:51], s61 offen offset:384 lds
s_mov_b32 m0, 8192
s_mov_b32 s61, 1703936
buffer_load_dwordx4 v22, s[48:51], s61 offen offset:0 lds
T v,228,11,18432
W 7
s_mov_b32 m0, 9088
M v,128,v,240,a,168
T v,230,11,26624
buffer_load_dwordx4 v22, s[48:51], s61 offen offset:128 lds
s_mov_b32 m0, 9984
T v,232,8,18944
buffer_load_dwordx4 v22, s[48:51], s61 offen offset:256 lds
s_mov_b32 m0, 10880
M v,132,v,244,a,168
T v,234,8,27136
buffer_load_dwordx4 v22, s[48:51], s61 offen offset:384 lds
T v,236,11,18944
W 7
s_mov_b32 m0, 12288
s_mov_b32 s61, 1769472
buffer_load_dwordx4 v22, s[48:51], s61 offen offset:0 lds
s_mov_b32 m0, 13184
M v,144,v,248,a,168
T v,238,11,27136
buffer_load_dwordx4 v22, s[48:51], s61 offen offset:128 lds
T v,240,8,19456
s_mov_b32 m0, 14080
M v,148,v,252,a,168
T v,242,8,27648
buffer_load_dwordx4 v22, s[48:51], s61 offen offset:256 lds
T v,244,11,19456
W 7
s_mov_b32 m0, 14976
M v,160,v,224,a,168
T v,246,11,27648
buffer_load_dwordx4 v22, s[48:51], s61 offen offset:384 lds
s_mov_b32 m0, 16384
s_mov_b32 s61, 1835008
buffer_load_dwordx4 v22, s[48:51], s61 offen offset:0 lds
T v,248,8,19968
s_mov_b32 m0, 17280
M v,164,v,228,a,168
T v,250,8,28160
buffer_load_dwordx4 v22, s[48:51], s61 offen offset:128 lds
T v,252,11,19968
W 7
s_mov_b32 m0, 18176
M v,176,v,232,a,168
T v,254,11,28160
buffer_load_dwordx4 v22, s[48:51], s61 offen offset:256 lds
s_mov_b32 m0, 19072
)x"
R"x(s_waitcnt vmcnt(52)
s_barrier
R a,176,14,32768
R a,178,17,32768
T v,224,8,49152
buffer_load_dwordx4 v22, s[48:51], s61 offen offset:384 lds
s_mov_b32 m0, 20480
s_mov_b32 s61, 1900544
buffer_load_dwordx4 v22, s[48:51], s61 offen offset:0 lds
M v,180,v,236,a,168
T v,226,8,57344
T v,228,11,49152
W 9
s_mov_b32 m0, 21376
M v,192,v,240,a,168
T v,230,11,57344
buffer_load_dwordx4 v22, s[48:51], s61 offen offset:128 lds
s_mov_b32 m0, 22272
T v,232,8,49664
buffer_load_dwordx4 v22, s[48:51], s61 offen offset:256 lds
s_mov_b32 m0, 23168
M v,196,v,244,a,168
T v,234,8,57856
buffer_load_dwordx4 v22, s[48:51], s61 offen offset:384 lds
T v,236,11,49664
W 9
s_mov_b32 m0, 24576
s_mov_b32 s61, 1966080
buffer_load_dwordx4 v22, s[48:51], s61 offen offset:0 lds
s_mov_b32 m0, 25472
M v,208,v,248,a,168
T v,238,11,57856
buffer_load_dwordx4 v22, s[48:51], s61 offen offset:128 lds
T v,240,8,50176
s_mov_b32 m0, 26368
M v,212,v,252,a,168
T v,242,8,58368
buffer_load_dwordx4 v22, s[48:51], s61 offen offset:256 lds
T v,244,11,50176
W 7
s_mov_b32 m0, 27264
M v,96,v,224,a,176
T v,246,11,58368
buffer_load_dwordx4 v22, s[48:51], s61 offen offset:384 lds
s_mov_b32 m0, 28672
s_mov_b32 s61, 2031616
buffer_load_dwordx4 v22, s[48:51], s61 offen offset:0 lds
T v,248,8,50688
s_mov_b32 m0, 29568
M v,100,v,228,a,176
T v,250,8,58880
buffer_load_dwordx4 v22, s[48:51], s61 offen offset:128 lds
T v,252,11,50688
W 7
s_mov_b32 m0, 30464
M v,112,v,232,a,176
T v,254,11,58880
buffer_load_dwordx4 v22, s[48:51], s61 offen offset:256 lds
s_mov_b32 m0, 31360
T v,224,8,51200
buffer_load_dwordx4 v22, s[48:51], s61 offen offset:384 lds
s_mov_b32 m0, 32768
s_mov_b32 s61, 512
buffer_load_dwordx4 v20, s[44:47], s61 offen offset:0 lds
M v,116,v,236,a,176
T v,226,8,59392
T v,228,11,51200
W 7
s_mov_b32 m0, 33792
s_mov_b32 s61, 7680
buffer_load_dwordx4 v21, s[44:47], s61 offen offset:0 lds
s_mov_b32 m0, 34816
M v,128,v,240,a,176
T v,230,11,59392
s_mov_b32 s61, 14848
buffer_load_dwordx4 v20, s[44:47], s61 offen offset:0 lds
T v,232,8,51712
s_mov_b32 m0, 35840
s_mov_b32 s61, 22016
buffer_load_dwordx4 v21, s[44:47], s61 offen offset:0 lds
M v,132,v,244,a,176
T v,234,8,59904
T v,236,11,51712
W 7
s_mov_b32 m0, 36864
s_mov_b32 s61, 29184
buffer_load_dwordx4 v20, s[44:47], s61 offen offset:0 lds
s_mov_b32 m0, 37888
M v,144,v,248,a,176
T v,238,11,59904
s_mov_b32 s61, 36352
buffer_load_dwordx4 v21, s[44:47], s61 offen offset:0 lds
T v,240,8,52224
s_mov_b32 m0, 38912
s_mov_b32 s61, 43520
buffer_load_dwordx4 v20, s[44:47], s61 offen offset:0 lds
M v,148,v,252,a,176
T v,242,8,60416
T v,244,11,52224
W 7
s_mov_b32 m0, 39936
s_mov_b32 s61, 50688
buffer_load_dwordx4 v21, s[44:47], s61 offen offset:0 lds
s_mov_b32 m0, 40960
M v,160,v,224,a,176
T v,246,11,60416
s_mov_b32 s61, 57856
buffer_load_dwordx4 v20, s[44:47], s61 offen offset:0 lds
T v,248,8,52736
s_mov_b32 m0, 41984
s_mov_b32 s61, 65024
buffer_load_dwordx4 v21, s[44:47], s61 offen offset:0 lds
M v,164,v,228,a,176
T v,250,8,60928
T v,252,11,52736
W 7
s_mov_b32 m0, 43008
s_mov_b32 s61, 72192
buffer_load_dwordx4 v20, s[44:47], s61 offen offset:0 lds
s_mov_b32 m0, 44032
M v,176,v,232,a,176
T v,254,11,60928
s_mov_b32 s61, 79360
buffer_load_dwordx4 v21, s[44:47], s61 offen offset:0 lds
s_waitcnt vmcnt(58)
s_barrier
R a,184,14,33280
R a,186,17,33280
T v,224,9,0
s_mov_b32 m0, 45056
s_mov_b32 s61, 86528
buffer_load_dwordx4 v20, s[44:47], s61 offen offset:0 lds
M v,180,v,236,a,176
T v,226,9,8192
T v,228,12,0
W 9
s_mov_b32 m0, 46080
s_mov_b32 s61, 93696
buffer_load_dwordx4 v21, s[44:47], s61 offen offset:0 lds
s_mov_b32 m0, 49152
M v,192,v,240,a,176
T v,230,12,8192
s_mov_b32 s61, 2097152
buffer_load_dwordx4 v22, s[48:51], s61 offen offset:0 lds
T v,232,9,512
s_mov_b32 m0, 50048
M v,196,v,244,a,176
T v,234,9,8704
buffer_load_dwordx4 v22, s[48:51], s61 offen offset:128 lds
T v,236,12,512
W 9
s_mov_b32 m0, 50944
M v,208,v,248,a,176
T v,238,12,8704
buffer_load_dwordx4 v22, s[48:51], s61 offen offset:256 lds
s_mov_b32 m0, 51840
T v,240,9,1024
buffer_load_dwordx4 v22, s[48:51], s61 offen offset:384 lds
s_mov_b32 m0, 53248
s_mov_b32 s61, 2162688
buffer_load_dwordx4 v22, s[48:51], s61 offen offset:0 lds
M v,212,v,252,a,176
T v,242,9,9216
T v,244,12,1024
W 7
s_mov_b32 m0, 54144
M v,96,v,224,a,184
T v,246,12,9216
buffer_load_dwordx4 v22, s[48:51], s61 offen offset:128 lds
s_mov_b32 m0, 55040
T v,248,9,1536
buffer_load_dwordx4 v22, s[48:51], s61 offen offset:256 lds
s_mov_b32 m0, 55936
M v,100,v,228,a,184
T v,250,9,9728
buffer_load_dwordx4 v22, s[48:51], s61 offen offset:384 lds
T v,252,12,1536
W 7
s_mov_b32 m0, 57344
s_mov_b32 s61, 2228224
buffer_load_dwordx4 v22, s[48:51], s61 offen offset:0 lds
s_mov_b32 m0, 58240
M v,112,v,232,a,184
T v,254,12,9728
buffer_load_dwordx4 v22, s[48:51], s61 offen offset:128 lds
T v,224,9,2048
s_mov_b32 m0, 59136
M v,116,v,236,a,184
T v,226,9,10240
buffer_load_dwordx4 v22, s[48:51], s61 offen offset:256 lds
T v,228,12,2048
W 7
s_mov_b32 m0, 60032
M v,128,v,240,a,184
T v,230,12,10240
buffer_load_dwordx4 v22, s[48:51], s61 offen offset:384 lds
s_mov_b32 m0, 61440
s_mov_b32 s61, 2293760
buffer_load_dwordx4 v22, s[48:51], s61 offen offset:0 lds
T v,232,9,2560
s_mov_b32 m0, 62336
M v,132,v,244,a,184
T v,234,9,10752
buffer_load_dwordx4 v22, s[48:51], s61 offen offset:128 lds
T v,236,12,2560
W 7
s_mov_b32 m0, 63232
M v,144,v,248,a,184
T v,238,12,10752
buffer_load_dwordx4 v22, s[48:51], s61 offen offset:256 lds
s_mov_b32 m0, 64128
T v,240,9,3072
buffer_load_dwordx4 v22, s[48:51], s61 offen offset:384 lds
s_mov_b32 m0, 65536
s_mov_b32 s61, 2359296
buffer_load_dwordx4 v22, s[48:51], s61 offen offset:0 lds
M v,148,v,252,a,184
T v,242,9,11264
T v,244,12,3072
W 7
s_mov_b32 m0, 66432
M v,160,v,224,a,184
T v,246,12,11264
buffer_load_dwordx4 v22, s[48:51], s61 offen offset:128 lds
s_mov_b32 m0, 67328
T v,248,9,3584
buffer_load_dwordx4 v22, s[48:51], s61 offen offset:256 lds
s_mov_b32 m0, 68224
M v,164,v,228,a,184
T v,250,9,11776
buffer_load_dwordx4 v22, s[48:51], s61 offen offset:384 lds
T v,252,12,3584
W 7
s_mov_b32 m0, 69632
s_mov_b32 s61, 2424832
buffer_load_dwordx4 v22, s[48:51], s61 offen offset:0 lds
s_mov_b32 m0, 70528
M v,176,v,232,a,184
T v,254,12,11776
buffer_load_dwordx4 v22, s[48:51], s61 offen offset:128 lds
s_waitcnt vmcnt(52)
s_barrier
R a,192,15,16384
R a,194,18,16384
T v,224,7,0
s_mov_b32 m0, 71424
M v,180,v,236,a,184
T v,226,7,8192
buffer_load_dwordx4 v22, s[48:51], s61 offen offset:256 lds
s_mov_b32 m0, 72320
T v,228,10,0
W 9
buffer_load_dwordx4 v22, s[48:51], s61 offen offset:384 lds
s_mov_b32 m0, 73728
s_mov_b32 s61, 2490368
buffer_load_dwordx4 v22, s[48:51], s61 offen offset:0 lds
M v,192,v,240,a,184
T v,230,10,8192
s_mov_b32 m0, 74624
T v,232,7,512
buffer_load_dwordx4 v22, s[48:51], s61 offen offset:128 lds
s_mov_b32 m0, 75520
M v,196,v,244,a,184
T v,234,7,8704
buffer_load_dwordx4 v22, s[48:51], s61 offen offset:256 lds
s_mov_b32 m0, 76416
T v,236,10,512
W 9
buffer_load_dwordx4 v22, s[48:51], s61 offen offset:384 lds
s_mov_b32 m0, 77824
s_mov_b32 s61, 2555904
buffer_load_dwordx4 v22, s[48:51], s61 offen offset:0 lds
M v,208,v,248,a,184
T v,238,10,8704
s_mov_b32 m0, 78720
T v,240,7,1024
buffer_load_dwordx4 v22, s[48:51], s61 offen offset:128 lds
s_mov_b32 m0, 79616
M v,212,v,252,a,184
T v,242,7,9216
buffer_load_dwordx4 v22, s[48:51], s61 offen offset:256 lds
s_mov_b32 m0, 80512
T v,244,10,1024
W 7
buffer_load_dwordx4 v22, s[48:51], s61 offen offset:384 lds
s_mov_b32 m0, 81920
s_mov_b32 s61, 640
buffer_load_dwordx4 v20, s[44:47], s61 offen offset:0 lds
M v,96,v,224,a,192
T v,246,10,9216
s_mov_b32 m0, 82944
s_mov_b32 s61, 7808
buffer_load_dwordx4 v21, s[44:47], s61 offen offset:0 lds
s_mov_b32 m0, 83968
T v,248,7,1536
s_mov_b32 s61, 14976
buffer_load_dwordx4 v20, s[44:47], s61 offen offset:0 lds
s_mov_b32 m0, 84992
s_mov_b32 s61, 22144
M v,100,v,228,a,192
T v,250,7,9728
buffer_load_dwordx4 v21, s[44:47], s61 offen offset:0 lds
s_mov_b32 m0, 86016
s_mov_b32 s61, 29312
buffer_load_dwordx4 v20, s[44:47], s61 offen offset:0 lds
T v,252,10,1536
W 7
s_mov_b32 m0, 87040
s_mov_b32 s61, 36480
buffer_load_dwordx4 v21, s[44:47], s61 offen offset:0 lds
M v,112,v,232,a,192
T v,254,10,9728
T v,224,7,2048
s_mov_b32 m0, 88064
s_mov_b32 s61, 43648
buffer_load_dwordx4 v20, s[44:47], s61 offen offset:0 lds
s_mov_b32 m0, 89088
M v,116,v,236,a,192
T v,226,7,10240
s_mov_b32 s61, 50816
buffer_load_dwordx4 v21, s[44:47], s61 offen offset:0 lds
T v,228,10,2048
W 7
s_mov_b32 m0, 90112
s_mov_b32 s61, 57984
buffer_load_dwordx4 v20, s[44:47], s61 offen offset:0 lds
M v,128,v,240,a,192
T v,230,10,10240
T v,232,7,2560
s_mov_b32 m0, 91136
s_mov_b32 s61, 65152
buffer_load_dwordx4 v21, s[44:47], s61 offen offset:0 lds
s_mov_b32 m0, 92160
M v,132,v,244,a,192
T v,234,7,10752
s_mov_b32 s61, 72320
buffer_load_dwordx4 v20, s[44:47], s61 offen offset:0 lds
T v,236,10,2560
W 7
s_mov_b32 m0, 93184
s_mov_b32 s61, 79488
buffer_load_dwordx4 v21, s[44:47], s61 offen offset:0 lds
M v,144,v,248,a,192
T v,238,10,10752
T v,240,7,3072
s_mov_b32 m0, 94208
s_mov_b32 s61, 86656
buffer_load_dwordx4 v20, s[44:47], s61 offen offset:0 lds
s_mov_b32 m0, 95232
M v,148,v,252,a,192
T v,242,7,11264
s_mov_b32 s61, 93824
buffer_load_dwordx4 v21, s[44:47], s61 offen offset:0 lds
T v,244,10,3072
W 7
s_mov_b32 m0, 98304
s_mov_b32 s61, 2621440
buffer_load_dwordx4 v22, s[48:51], s61 offen offset:0 lds
M v,160,v,224,a,192
T v,246,10,11264
T v,248,7,3584
s_mov_b32 m0, 99200
M v,164,v,228,a,192
T v,250,7,11776
buffer_load_dwordx4 v22, s[48:51], s61 offen offset:128 lds
s_mov_b32 m0, 100096
T v,252,10,3584
W 7
buffer_load_dwordx4 v22, s[48:51], s61 offen offset:256 lds
s_mov_b32 m0, 100992
M v,176,v,232,a,192
T v,254,10,11776
buffer_load_dwordx4 v22, s[48:51], s61 offen offset:384 lds
s_waitcnt vmcnt(63)
s_barrier
R a,200,15,16896
R a,202,18,16896
T v,224,7,16384
s_mov_b32 m0, 102400
s_mov_b32 s61, 2686976
buffer_load_dwordx4 v22, s[48:51], s61 offen offset:0 lds
s_mov_b32 m0, 103296
M v,180,v,236,a,192
T v,226,7,24576
buffer_load_dwordx4 v22, s[48:51], s61 offen offset:128 lds
T v,228,10,16384
W 9
s_mov_b32 m0, 104192
M v,192,v,240,a,192
T v,230,10,24576
buffer_load_dwordx4 v22, s[48:51], s61 offen offset:256 lds
T v,232,7,16896
s_mov_b32 m0, 105088
M v,196,v,244,a,192
T v,234,7,25088
buffer_load_dwordx4 v22, s[48:51], s61 offen offset:384 lds
s_mov_b32 m0, 106496
s_mov_b32 s61, 2752512
buffer_load_dwordx4 v22, s[48:51], s61 offen offset:0 lds
T v,236,10,16896
)x"
R"x(W 9
s_mov_b32 m0, 107392
M v,208,v,248,a,192
T v,238,10,25088
buffer_load_dwordx4 v22, s[48:51], s61 offen offset:128 lds
T v,240,7,17408
s_mov_b32 m0, 108288
M v,212,v,252,a,192
T v,242,7,25600
buffer_load_dwordx4 v22, s[48:51], s61 offen offset:256 lds
s_mov_b32 m0, 109184
T v,244,10,17408
W 7
buffer_load_dwordx4 v22, s[48:51], s61 offen offset:384 lds
s_mov_b32 m0, 110592
s_mov_b32 s61, 2818048
buffer_load_dwordx4 v22, s[48:51], s61 offen offset:0 lds
M v,96,v,224,a,200
T v,246,10,25600
T v,248,7,17920
s_mov_b32 m0, 111488
M v,100,v,228,a,200
T v,250,7,26112
buffer_load_dwordx4 v22, s[48:51], s61 offen offset:128 lds
s_mov_b32 m0, 112384
T v,252,10,17920
W 7
buffer_load_dwordx4 v22, s[48:51], s61 offen offset:256 lds
s_mov_b32 m0, 113280
M v,112,v,232,a,200
T v,254,10,26112
buffer_load_dwordx4 v22, s[48:51], s61 offen offset:384 lds
T v,224,7,18432
s_mov_b32 m0, 114688
s_mov_b32 s61, 2883584
buffer_load_dwordx4 v22, s[48:51], s61 offen offset:0 lds
s_mov_b32 m0, 115584
M v,116,v,236,a,200
T v,226,7,26624
buffer_load_dwordx4 v22, s[48:51], s61 offen offset:128 lds
T v,228,10,18432
W 7
s_mov_b32 m0, 116480
M v,128,v,240,a,200
T v,230,10,26624
buffer_load_dwordx4 v22, s[48:51], s61 offen offset:256 lds
T v,232,7,18944
s_mov_b32 m0, 117376
M v,132,v,244,a,200
T v,234,7,27136
buffer_load_dwordx4 v22, s[48:51], s61 offen offset:384 lds
s_mov_b32 m0, 118784
s_mov_b32 s61, 2949120
buffer_load_dwordx4 v22, s[48:51], s61 offen offset:0 lds
T v,236,10,18944
W 7
s_mov_b32 m0, 119680
M v,144,v,248,a,200
T v,238,10,27136
buffer_load_dwordx4 v22, s[48:51], s61 offen offset:128 lds
T v,240,7,19456
s_mov_b32 m0, 120576
M v,148,v,252,a,200
T v,242,7,27648
buffer_load_dwordx4 v22, s[48:51], s61 offen offset:256 lds
s_mov_b32 m0, 121472
T v,244,10,19456
W 7
buffer_load_dwordx4 v22, s[48:51], s61 offen offset:384 lds
s_mov_b32 m0, 122880
s_mov_b32 s61, 3014656
buffer_load_dwordx4 v22, s[48:51], s61 offen offset:0 lds
M v,160,v,224,a,200
T v,246,10,27648
T v,248,7,19968
s_mov_b32 m0, 123776
M v,164,v,228,a,200
T v,250,7,28160
buffer_load_dwordx4 v22, s[48:51], s61 offen offset:128 lds
s_mov_b32 m0, 124672
T v,252,10,19968
W 7
buffer_load_dwordx4 v22, s[48:51], s61 offen offset:256 lds
s_mov_b32 m0, 125568
M v,176,v,232,a,200
T v,254,10,28160
buffer_load_dwordx4 v22, s[48:51], s61 offen offset:384 lds
s_waitcnt vmcnt(58)
s_barrier
R a,208,13,32768
R a,210,16,32768
T v,224,7,49152
s_mov_b32 m0, 126976
s_mov_b32 s61, 3080192
buffer_load_dwordx4 v22, s[48:51], s61 offen offset:0 lds
s_mov_b32 m0, 127872
M v,180,v,236,a,200
T v,226,7,57344
buffer_load_dwordx4 v22, s[48:51], s61 offen offset:128 lds
T v,228,10,49152
W 9
s_mov_b32 m0, 128768
M v,192,v,240,a,200
T v,230,10,57344
buffer_load_dwordx4 v22, s[48:51], s61 offen offset:256 lds
T v,232,7,49664
s_mov_b32 m0, 129664
M v,196,v,244,a,200
T v,234,7,57856
buffer_load_dwordx4 v22, s[48:51], s61 offen offset:384 lds
s_mul_i32 s61, %9, 28
v_lshrrev_b32 v25, 2, v0
v_cmp_gt_u32 vcc, 56, v0
T v,236,10,49664
W 9
v_add_u32 v25, s61, v25
v_lshlrev_b32 v25, 6, v25
v_and_or_b32 v25, v0, 3, v25
v_lshlrev_b32 v25, 7, v25
M v,208,v,248,a,200
T v,238,10,57856
s_mov_b32 s61, 512
v_cndmask_b32 v25, 2.0, v25, vcc
buffer_load_dword v29, v25, s[48:51], s61 offen
T v,240,7,50176
s_mov_b32 m0, 131072
s_mov_b32 s61, 768
buffer_load_dwordx4 v20, s[44:47], s61 offen offset:0 lds
M v,212,v,252,a,200
T v,242,7,58368
T v,244,10,50176
W 7
s_mov_b32 m0, 132096
s_mov_b32 s61, 7936
buffer_load_dwordx4 v21, s[44:47], s61 offen offset:0 lds
M v,96,v,224,a,208
T v,246,10,58368
T v,248,7,50688
s_mov_b32 m0, 133120
s_mov_b32 s61, 15104
buffer_load_dwordx4 v20, s[44:47], s61 offen offset:0 lds
s_mov_b32 m0, 134144
M v,100,v,228,a,208
T v,250,7,58880
s_mov_b32 s61, 22272
buffer_load_dwordx4 v21, s[44:47], s61 offen offset:0 lds
T v,252,10,50688
W 7
s_mov_b32 m0, 135168
s_mov_b32 s61, 29440
buffer_load_dwordx4 v20, s[44:47], s61 offen offset:0 lds
M v,112,v,232,a,208
T v,254,10,58880
T v,224,7,51200
s_mov_b32 m0, 136192
s_mov_b32 s61, 36608
buffer_load_dwordx4 v21, s[44:47], s61 offen offset:0 lds
s_mov_b32 m0, 137216
M v,116,v,236,a,208
T v,226,7,59392
s_mov_b32 s61, 43776
buffer_load_dwordx4 v20, s[44:47], s61 offen offset:0 lds
T v,228,10,51200
W 7
s_mov_b32 m0, 138240
s_mov_b32 s61, 50944
buffer_load_dwordx4 v21, s[44:47], s61 offen offset:0 lds
M v,128,v,240,a,208
T v,230,10,59392
T v,232,7,51712
s_mov_b32 m0, 139264
s_mov_b32 s61, 58112
buffer_load_dwordx4 v20, s[44:47], s61 offen offset:0 lds
s_mov_b32 m0, 140288
M v,132,v,244,a,208
T v,234,7,59904
s_mov_b32 s61, 65280
buffer_load_dwordx4 v21, s[44:47], s61 offen offset:0 lds
T v,236,10,51712
W 7
s_mov_b32 m0, 141312
s_mov_b32 s61, 72448
buffer_load_dwordx4 v20, s[44:47], s61 offen offset:0 lds
M v,144,v,248,a,208
T v,238,10,59904
T v,240,7,52224
s_mov_b32 m0, 142336
s_mov_b32 s61, 79616
buffer_load_dwordx4 v21, s[44:47], s61 offen offset:0 lds
s_mov_b32 m0, 143360
M v,148,v,252,a,208
T v,242,7,60416
s_mov_b32 s61, 86784
buffer_load_dwordx4 v20, s[44:47], s61 offen offset:0 lds
T v,244,10,52224
W 7
s_mov_b32 m0, 144384
s_mov_b32 s61, 93952
buffer_load_dwordx4 v21, s[44:47], s61 offen offset:0 lds
M v,160,v,224,a,208
T v,246,10,60416
T v,248,7,52736
s_mul_i32 s61, %9, 28
v_lshrrev_b32 v25, 2, v0
v_cmp_gt_u32 vcc, 56, v0
v_add_u32 v25, s61, v25
M v,164,v,228,a,208
T v,250,7,60928
v_lshlrev_b32 v25, 6, v25
v_and_or_b32 v25, v0, 3, v25
v_lshlrev_b32 v25, 7, v25
s_mov_b32 s61, 115200
T v,252,10,52736
W 7
v_cndmask_b32 v25, 2.0, v25, vcc
buffer_load_dword v29, v25, s[48:51], s61 offen
M v,176,v,232,a,208
T v,254,10,60928
s_waitcnt vmcnt(62)
s_barrier
R a,216,13,33280
R a,218,16,33280
T v,224,8,0
s_mov_b32 m0, 147456
s_mov_b32 s61, 3145728
buffer_load_dwordx4 v22, s[48:51], s61 offen offset:0 lds
s_mov_b32 m0, 148352
M v,180,v,236,a,208
T v,226,8,8192
buffer_load_dwordx4 v22, s[48:51], s61 offen offset:128 lds
T v,228,11,0
W 9
s_mov_b32 m0, 149248
M v,192,v,240,a,208
T v,230,11,8192
buffer_load_dwordx4 v22, s[48:51], s61 offen offset:256 lds
T v,232,8,512
s_mov_b32 m0, 150144
M v,196,v,244,a,208
T v,234,8,8704
buffer_load_dwordx4 v22, s[48:51], s61 offen offset:384 lds
s_mov_b32 m0, 151552
s_mov_b32 s61, 3211264
buffer_load_dwordx4 v22, s[48:51], s61 offen offset:0 lds
T v,236,11,512
W 9
s_mov_b32 m0, 152448
M v,208,v,248,a,208
T v,238,11,8704
buffer_load_dwordx4 v22, s[48:51], s61 offen offset:128 lds
T v,240,8,1024
s_mov_b32 m0, 153344
M v,212,v,252,a,208
T v,242,8,9216
buffer_load_dwordx4 v22, s[48:51], s61 offen offset:256 lds
s_mov_b32 m0, 154240
T v,244,11,1024
W 7
buffer_load_dwordx4 v22, s[48:51], s61 offen offset:384 lds
s_mov_b32 m0, 155648
s_mov_b32 s61, 3276800
buffer_load_dwordx4 v22, s[48:51], s61 offen offset:0 lds
M v,96,v,224,a,216
T v,246,11,9216
T v,248,8,1536
s_mov_b32 m0, 156544
M v,100,v,228,a,216
T v,250,8,9728
buffer_load_dwordx4 v22, s[48:51], s61 offen offset:128 lds
s_mov_b32 m0, 157440
T v,252,11,1536
W 7
buffer_load_dwordx4 v22, s[48:51], s61 offen offset:256 lds
s_mov_b32 m0, 158336
M v,112,v,232,a,216
T v,254,11,9728
buffer_load_dwordx4 v22, s[48:51], s61 offen offset:384 lds
T v,224,8,2048
s_mov_b32 m0, 159744
s_mov_b32 s61, 3342336
buffer_load_dwordx4 v22, s[48:51], s61 offen offset:0 lds
s_mov_b32 m0, 160640
M v,116,v,236,a,216
T v,226,8,10240
buffer_load_dwordx4 v22, s[48:51], s61 offen offset:128 lds
T v,228,11,2048
W 7
s_mov_b32 m0, 161536
M v,128,v,240,a,216
T v,230,11,10240
buffer_load_dwordx4 v22, s[48:51], s61 offen offset:256 lds
T v,232,8,2560
s_mov_b32 m0, 162432
M v,132,v,244,a,216
T v,234,8,10752
buffer_load_dwordx4 v22, s[48:51], s61 offen offset:384 lds
s_mov_b32 m0, 0
s_mov_b32 s61, 3407872
buffer_load_dwordx4 v22, s[48:51], s61 offen offset:0 lds
T v,236,11,2560
W 7
s_mov_b32 m0, 896
M v,144,v,248,a,216
T v,238,11,10752
buffer_load_dwordx4 v22, s[48:51], s61 offen offset:128 lds
T v,240,8,3072
s_mov_b32 m0, 1792
M v,148,v,252,a,216
T v,242,8,11264
buffer_load_dwordx4 v22, s[48:51], s61 offen offset:256 lds
s_mov_b32 m0, 2688
T v,244,11,3072
W 7
buffer_load_dwordx4 v22, s[48:51], s61 offen offset:384 lds
s_mov_b32 m0, 4096
s_mov_b32 s61, 3473408
buffer_load_dwordx4 v22, s[48:51], s61 offen offset:0 lds
M v,160,v,224,a,216
T v,246,11,11264
T v,248,8,3584
s_mov_b32 m0, 4992
M v,164,v,228,a,216
T v,250,8,11776
buffer_load_dwordx4 v22, s[48:51], s61 offen offset:128 lds
s_mov_b32 m0, 5888
T v,252,11,3584
W 7
buffer_load_dwordx4 v22, s[48:51], s61 offen offset:256 lds
s_mov_b32 m0, 6784
M v,176,v,232,a,216
T v,254,11,11776
buffer_load_dwordx4 v22, s[48:51], s61 offen offset:384 lds
s_waitcnt vmcnt(56)
s_barrier
.Lc1_22_%=:
R a,224,14,16384
R a,226,17,16384
T v,224,8,32768
s_mov_b32 m0, 8192
s_mov_b32 s61, 3538944
buffer_load_dwordx4 v22, s[48:51], s61 offen offset:0 lds
s_mov_b32 m0, 9088
M v,180,v,236,a,216
T v,226,8,40960
buffer_load_dwordx4 v22, s[48:51], s61 offen offset:128 lds
T v,228,11,32768
W 9
s_mov_b32 m0, 9984
M v,192,v,240,a,216
T v,230,11,40960
buffer_load_dwordx4 v22, s[48:51], s61 offen offset:256 lds
T v,232,8,33280
s_mov_b32 m0, 10880
M v,196,v,244,a,216
T v,234,8,41472
buffer_load_dwordx4 v22, s[48:51], s61 offen offset:384 lds
s_mov_b32 m0, 12288
s_mov_b32 s61, 3604480
buffer_load_dwordx4 v22, s[48:51], s61 offen offset:0 lds
T v,236,11,33280
W 9
s_mov_b32 m0, 13184
M v,208,v,248,a,216
T v,238,11,41472
buffer_load_dwordx4 v22, s[48:51], s61 offen offset:128 lds
T v,240,8,33792
s_mov_b32 m0, 14080
M v,212,v,252,a,216
T v,242,8,41984
buffer_load_dwordx4 v22, s[48:51], s61 offen offset:256 lds
s_mov_b32 m0, 14976
T v,244,11,33792
W 7
buffer_load_dwordx4 v22, s[48:51], s61 offen offset:384 lds
s_mov_b32 m0, 16384
s_mov_b32 s61, 0
buffer_load_dwordx4 v19, s[36:39], s61 offen offset:0 nt lds
M v,96,v,224,a,224
T v,246,11,41984
T v,248,8,34304
M v,100,v,228,a,224
T v,250,8,42496
T v,252,11,34304
W 7
s_mov_b32 m0, 17280
M v,112,v,232,a,224
T v,254,11,42496
buffer_load_dwordx4 v19, s[36:39], s61 offen offset:128 nt lds
T v,224,8,34816
M v,116,v,236,a,224
T v,226,8,43008
T v,228,11,34816
W 7
s_mov_b32 m0, 18176
M v,128,v,240,a,224
T v,230,11,43008
buffer_load_dwordx4 v19, s[36:39], s61 offen offset:256 nt lds
T v,232,8,35328
M v,132,v,244,a,224
T v,234,8,43520
)x"
R"x(T v,236,11,35328
W 7
s_mov_b32 m0, 19072
M v,144,v,248,a,224
T v,238,11,43520
buffer_load_dwordx4 v19, s[36:39], s61 offen offset:384 nt lds
T v,240,8,35840
s_mov_b32 m0, 20480
s_mov_b32 s61, 4096
buffer_load_dwordx4 v27, s[36:39], s61 offen offset:0 nt lds
M v,148,v,252,a,224
T v,242,8,44032
T v,244,11,35840
W 7
M v,160,v,224,a,224
T v,246,11,44032
T v,248,8,36352
s_mov_b32 m0, 21376
M v,164,v,228,a,224
T v,250,8,44544
buffer_load_dwordx4 v27, s[36:39], s61 offen offset:128 nt lds
T v,252,11,36352
W 7
M v,176,v,232,a,224
T v,254,11,44544
s_waitcnt vmcnt(54)
s_barrier
R a,232,14,16896
R a,234,17,16896
T v,224,8,49152
s_mov_b32 m0, 22272
M v,180,v,236,a,224
T v,226,8,57344
buffer_load_dwordx4 v27, s[36:39], s61 offen offset:256 nt lds
T v,228,11,49152
W 9
M v,192,v,240,a,224
T v,230,11,57344
T v,232,8,49664
s_mov_b32 m0, 23168
M v,196,v,244,a,224
T v,234,8,57856
buffer_load_dwordx4 v27, s[36:39], s61 offen offset:384 nt lds
T v,236,11,49664
W 9
M v,208,v,248,a,224
T v,238,11,57856
T v,240,8,50176
s_mov_b32 m0, 24576
s_mov_b32 s61, 8192
buffer_load_dwordx4 v19, s[36:39], s61 offen offset:0 nt lds
M v,212,v,252,a,224
T v,242,8,58368
T v,244,11,50176
W 7
M v,96,v,224,a,232
T v,246,11,58368
T v,248,8,50688
s_mov_b32 m0, 25472
M v,100,v,228,a,232
T v,250,8,58880
buffer_load_dwordx4 v19, s[36:39], s61 offen offset:128 nt lds
T v,252,11,50688
W 7
M v,112,v,232,a,232
T v,254,11,58880
T v,224,8,51200
s_mov_b32 m0, 26368
M v,116,v,236,a,232
T v,226,8,59392
buffer_load_dwordx4 v19, s[36:39], s61 offen offset:256 nt lds
T v,228,11,51200
W 7
M v,128,v,240,a,232
T v,230,11,59392
T v,232,8,51712
s_mov_b32 m0, 27264
M v,132,v,244,a,232
T v,234,8,59904
buffer_load_dwordx4 v19, s[36:39], s61 offen offset:384 nt lds
T v,236,11,51712
W 7
M v,144,v,248,a,232
T v,238,11,59904
T v,240,8,52224
s_mov_b32 m0, 28672
s_mov_b32 s61, 12288
buffer_load_dwordx4 v27, s[36:39], s61 offen offset:0 nt lds
M v,148,v,252,a,232
T v,242,8,60416
T v,244,11,52224
W 7
M v,160,v,224,a,232
T v,246,11,60416
T v,248,8,52736
s_mov_b32 m0, 29568
M v,164,v,228,a,232
T v,250,8,60928
buffer_load_dwordx4 v27, s[36:39], s61 offen offset:128 nt lds
T v,252,11,52736
W 7
M v,176,v,232,a,232
T v,254,11,60928
s_waitcnt vmcnt(30)
s_barrier
R a,240,15,0
R a,242,18,0
T v,224,9,16384
s_mov_b32 m0, 30464
M v,180,v,236,a,232
T v,226,9,24576
buffer_load_dwordx4 v27, s[36:39], s61 offen offset:256 nt lds
T v,228,12,16384
W 9
s_mov_b32 m0, 31360
M v,192,v,240,a,232
T v,230,12,24576
buffer_load_dwordx4 v27, s[36:39], s61 offen offset:384 nt lds
T v,232,9,16896
M v,196,v,244,a,232
T v,234,9,25088
T v,236,12,16896
W 9
s_mul_i32 s61, s33, 8
v_add_u32 v20, s61, v0
v_cmp_gt_u32 vcc, 8, v0
v_lshlrev_b32 v20, 7, v20
M v,208,v,248,a,232
T v,238,12,25088
s_nop 0
v_cndmask_b32 v20, 2.0, v20, vcc
s_mov_b32 s61, .Lc0_22_%=-.Lpf_start%=+8192
buffer_load_dword v29, v20, s[92:95], s61 offen
T v,240,9,17408
M v,212,v,252,a,232
T v,242,9,25600
T v,244,12,17408
W 7
s_mov_b32 s61, .Lc1_22_%=-.Lpf_start%=+8192
buffer_load_dword v29, v20, s[92:95], s61 offen
M v,96,v,224,a,240
T v,246,12,25600
T v,248,9,17920
M v,100,v,228,a,240
T v,250,9,26112
T v,252,12,17920
W 7
s_mov_b32 m0, 32768
s_mov_b32 s61, 512
buffer_load_dwordx4 v22, s[48:51], s61 offen offset:0 lds
M v,112,v,232,a,240
T v,254,12,26112
T v,224,9,18432
s_mov_b32 m0, 33664
M v,116,v,236,a,240
T v,226,9,26624
buffer_load_dwordx4 v22, s[48:51], s61 offen offset:128 lds
s_mov_b32 m0, 34560
T v,228,12,18432
W 7
buffer_load_dwordx4 v22, s[48:51], s61 offen offset:256 lds
s_mov_b32 m0, 35456
M v,128,v,240,a,240
T v,230,12,26624
buffer_load_dwordx4 v22, s[48:51], s61 offen offset:384 lds
T v,232,9,18944
s_mov_b32 m0, 36864
s_mov_b32 s61, 66048
buffer_load_dwordx4 v22, s[48:51], s61 offen offset:0 lds
s_mov_b32 m0, 37760
M v,132,v,244,a,240
T v,234,9,27136
buffer_load_dwordx4 v22, s[48:51], s61 offen offset:128 lds
T v,236,12,18944
W 7
s_mov_b32 m0, 38656
M v,144,v,248,a,240
T v,238,12,27136
buffer_load_dwordx4 v22, s[48:51], s61 offen offset:256 lds
T v,240,9,19456
s_mov_b32 m0, 39552
M v,148,v,252,a,240
T v,242,9,27648
buffer_load_dwordx4 v22, s[48:51], s61 offen offset:384 lds
s_mov_b32 m0, 40960
s_mov_b32 s61, 131584
buffer_load_dwordx4 v22, s[48:51], s61 offen offset:0 lds
T v,244,12,19456
W 7
s_mov_b32 m0, 41856
M v,160,v,224,a,240
T v,246,12,27648
buffer_load_dwordx4 v22, s[48:51], s61 offen offset:128 lds
T v,248,9,19968
s_mov_b32 m0, 42752
M v,164,v,228,a,240
T v,250,9,28160
buffer_load_dwordx4 v22, s[48:51], s61 offen offset:256 lds
s_mov_b32 m0, 43648
T v,252,12,19968
W 7
buffer_load_dwordx4 v22, s[48:51], s61 offen offset:384 lds
s_mov_b32 m0, 45056
s_mov_b32 s61, 197120
buffer_load_dwordx4 v22, s[48:51], s61 offen offset:0 lds
M v,176,v,232,a,240
T v,254,12,28160
s_waitcnt vmcnt(31)
s_barrier
R a,248,15,512
R a,250,18,512
T v,224,7,0
s_mov_b32 m0, 45952
M v,180,v,236,a,240
T v,226,7,8192
buffer_load_dwordx4 v22, s[48:51], s61 offen offset:128 lds
s_mov_b32 m0, 46848
T v,228,10,0
W 9
buffer_load_dwordx4 v22, s[48:51], s61 offen offset:256 lds
s_mov_b32 m0, 47744
M v,192,v,240,a,240
T v,230,10,8192
buffer_load_dwordx4 v22, s[48:51], s61 offen offset:384 lds
T v,232,7,512
s_mov_b32 m0, 49152
s_mov_b32 s61, 16384
buffer_load_dwordx4 v19, s[36:39], s61 offen offset:0 nt lds
M v,196,v,244,a,240
T v,234,7,8704
T v,236,10,512
W 9
M v,208,v,248,a,240
T v,238,10,8704
T v,240,7,1024
s_mov_b32 m0, 50048
M v,212,v,252,a,240
T v,242,7,9216
buffer_load_dwordx4 v19, s[36:39], s61 offen offset:128 nt lds
T v,244,10,1024
W 7
M v,96,v,224,a,248
T v,246,10,9216
T v,248,7,1536
s_mov_b32 m0, 50944
M v,100,v,228,a,248
T v,250,7,9728
buffer_load_dwordx4 v19, s[36:39], s61 offen offset:256 nt lds
T v,252,10,1536
W 7
M v,112,v,232,a,248
T v,254,10,9728
T v,224,7,2048
s_mov_b32 m0, 51840
M v,116,v,236,a,248
T v,226,7,10240
buffer_load_dwordx4 v19, s[36:39], s61 offen offset:384 nt lds
T v,228,10,2048
W 7
s_mov_b32 m0, 53248
s_mov_b32 s61, 20480
buffer_load_dwordx4 v27, s[36:39], s61 offen offset:0 nt lds
M v,128,v,240,a,248
T v,230,10,10240
T v,232,7,2560
M v,132,v,244,a,248
T v,234,7,10752
T v,236,10,2560
W 7
s_mov_b32 m0, 54144
M v,144,v,248,a,248
T v,238,10,10752
buffer_load_dwordx4 v27, s[36:39], s61 offen offset:128 nt lds
T v,240,7,3072
M v,148,v,252,a,248
T v,242,7,11264
T v,244,10,3072
W 7
s_mov_b32 m0, 55040
M v,160,v,224,a,248
T v,246,10,11264
buffer_load_dwordx4 v27, s[36:39], s61 offen offset:256 nt lds
T v,248,7,3584
M v,164,v,228,a,248
T v,250,7,11776
T v,252,10,3584
W 7
s_mov_b32 m0, 55936
M v,176,v,232,a,248
T v,254,10,11776
buffer_load_dwordx4 v27, s[36:39], s61 offen offset:384 nt lds
s_waitcnt vmcnt(26)
s_barrier
.Lc1_28_%=:
R v,224,1,16384
s_mov_b32 m0, 57344
s_mov_b32 s61, 24576
buffer_load_dwordx4 v19, s[36:39], s61 offen offset:0 nt lds
s_mov_b32 m0, 58240
M v,180,v,236,a,248
R v,226,4,16384
buffer_load_dwordx4 v19, s[36:39], s61 offen offset:128 nt lds
s_mov_b32 m0, 59136
R v,228,1,24576
W 7
buffer_load_dwordx4 v19, s[36:39], s61 offen offset:256 nt lds
s_mov_b32 m0, 60032
M v,192,v,240,a,248
R v,230,4,24576
buffer_load_dwordx4 v19, s[36:39], s61 offen offset:384 nt lds
s_mov_b32 m0, 61440
s_mov_b32 s61, 28672
buffer_load_dwordx4 v27, s[36:39], s61 offen offset:0 nt lds
R v,232,1,16896
s_mov_b32 m0, 62336
M v,196,v,244,a,248
R v,234,4,16896
buffer_load_dwordx4 v27, s[36:39], s61 offen offset:128 nt lds
s_mov_b32 m0, 63232
R v,236,1,25088
W 7
buffer_load_dwordx4 v27, s[36:39], s61 offen offset:256 nt lds
s_mov_b32 m0, 64128
M v,208,v,248,a,248
R v,238,4,25088
buffer_load_dwordx4 v27, s[36:39], s61 offen offset:384 nt lds
s_mov_b32 m0, 65536
s_mov_b32 s61, 262656
buffer_load_dwordx4 v22, s[48:51], s61 offen offset:0 lds
R v,240,1,17408
s_mov_b32 m0, 66432
M v,212,v,252,a,248
R v,242,4,17408
buffer_load_dwordx4 v22, s[48:51], s61 offen offset:128 lds
s_mov_b32 m0, 67328
s_nop 0
buffer_load_dwordx4 v22, s[48:51], s61 offen offset:256 lds
s_nop 7
v_cvt_pk_bf16_f32 v32, v96, v97
v_cvt_pk_bf16_f32 v33, v98, v99
s_mov_b32 m0, 68224
s_nop 0
buffer_load_dwordx4 v22, s[48:51], s61 offen offset:384 lds
v_cvt_pk_bf16_f32 v34, v100, v101
v_cvt_pk_bf16_f32 v35, v102, v103
s_mov_b32 m0, 69632
s_mov_b32 s61, 328192
buffer_load_dwordx4 v22, s[48:51], s61 offen offset:0 lds
v_cvt_pk_bf16_f32 v40, v112, v113
v_cvt_pk_bf16_f32 v41, v114, v115
s_mov_b32 m0, 70528
s_nop 0
buffer_load_dwordx4 v22, s[48:51], s61 offen offset:128 lds
v_cvt_pk_bf16_f32 v42, v116, v117
v_cvt_pk_bf16_f32 v43, v118, v119
s_mov_b32 m0, 71424
s_nop 0
buffer_load_dwordx4 v22, s[48:51], s61 offen offset:256 lds
v_cvt_pk_bf16_f32 v48, v128, v129
v_cvt_pk_bf16_f32 v49, v130, v131
s_mov_b32 m0, 72320
s_nop 0
buffer_load_dwordx4 v22, s[48:51], s61 offen offset:384 lds
v_cvt_pk_bf16_f32 v50, v132, v133
v_cvt_pk_bf16_f32 v51, v134, v135
s_mov_b32 m0, 73728
s_mov_b32 s61, 393728
buffer_load_dwordx4 v22, s[48:51], s61 offen offset:0 lds
v_cvt_pk_bf16_f32 v56, v144, v145
v_cvt_pk_bf16_f32 v57, v146, v147
s_mov_b32 m0, 74624
s_nop 0
buffer_load_dwordx4 v22, s[48:51], s61 offen offset:128 lds
v_cvt_pk_bf16_f32 v58, v148, v149
v_cvt_pk_bf16_f32 v59, v150, v151
s_mov_b32 m0, 75520
s_nop 0
buffer_load_dwordx4 v22, s[48:51], s61 offen offset:256 lds
v_cvt_pk_bf16_f32 v64, v160, v161
v_cvt_pk_bf16_f32 v65, v162, v163
s_mov_b32 m0, 76416
s_nop 0
buffer_load_dwordx4 v22, s[48:51], s61 offen offset:384 lds
v_cvt_pk_bf16_f32 v66, v164, v165
v_cvt_pk_bf16_f32 v67, v166, v167
s_mov_b32 m0, 77824
s_mov_b32 s61, 459264
buffer_load_dwordx4 v22, s[48:51], s61 offen offset:0 lds
v_cvt_pk_bf16_f32 v72, v176, v177
v_cvt_pk_bf16_f32 v73, v178, v179
s_mov_b32 m0, 78720
s_nop 0
buffer_load_dwordx4 v22, s[48:51], s61 offen offset:128 lds
v_cvt_pk_bf16_f32 v74, v180, v181
v_cvt_pk_bf16_f32 v75, v182, v183
s_mov_b32 m0, 79616
s_nop 0
buffer_load_dwordx4 v22, s[48:51], s61 offen offset:256 lds
v_cvt_pk_bf16_f32 v80, v192, v193
v_cvt_pk_bf16_f32 v81, v194, v195
)x"
R"x(s_mov_b32 m0, 80512
s_nop 0
buffer_load_dwordx4 v22, s[48:51], s61 offen offset:384 lds
v_cvt_pk_bf16_f32 v82, v196, v197
v_cvt_pk_bf16_f32 v83, v198, v199
s_mov_b32 m0, 81920
s_mov_b32 s61, 32768
buffer_load_dwordx4 v19, s[36:39], s61 offen offset:0 nt lds
v_cvt_pk_bf16_f32 v88, v208, v209
v_cvt_pk_bf16_f32 v89, v210, v211
s_mov_b32 m0, 82816
s_nop 0
buffer_load_dwordx4 v19, s[36:39], s61 offen offset:128 nt lds
v_cvt_pk_bf16_f32 v90, v212, v213
v_cvt_pk_bf16_f32 v91, v214, v215
s_mov_b32 m0, 83712
s_nop 0
buffer_load_dwordx4 v19, s[36:39], s61 offen offset:256 nt lds
s_nop 1
R v,244,1,25600
W 7
s_mov_b32 m0, 84608
v_mfma_f32_16x16x32_bf16 a[0:3], v[224:227], v[32:35], 0
R v,246,4,25600
buffer_load_dwordx4 v19, s[36:39], s61 offen offset:384 nt lds
s_mov_b32 m0, 86016
R v,248,1,17920
s_mov_b32 s61, 36864
buffer_load_dwordx4 v27, s[36:39], s61 offen offset:0 nt lds
v_mfma_f32_16x16x32_bf16 a[4:7], v[228:231], v[32:35], 0
R v,250,4,17920
s_mov_b32 m0, 86912
R v,252,1,26112
W 7
buffer_load_dwordx4 v27, s[36:39], s61 offen offset:128 nt lds
s_mov_b32 m0, 87808
M a,0,v,232,v,40
R v,254,4,26112
buffer_load_dwordx4 v27, s[36:39], s61 offen offset:256 nt lds
s_mov_b32 m0, 88704
R v,224,1,18432
buffer_load_dwordx4 v27, s[36:39], s61 offen offset:384 nt lds
s_mov_b32 m0, 90112
M a,4,v,236,v,40
R v,226,4,18432
s_mov_b32 s61, 40960
buffer_load_dwordx4 v19, s[36:39], s61 offen offset:0 nt lds
R v,228,1,26624
W 7
s_mov_b32 m0, 91008
M a,0,v,240,v,48
R v,230,4,26624
buffer_load_dwordx4 v19, s[36:39], s61 offen offset:128 nt lds
s_mov_b32 m0, 91904
R v,232,1,18944
buffer_load_dwordx4 v19, s[36:39], s61 offen offset:256 nt lds
s_mov_b32 m0, 92800
M a,4,v,244,v,48
R v,234,4,18944
buffer_load_dwordx4 v19, s[36:39], s61 offen offset:384 nt lds
s_mov_b32 m0, 94208
R v,236,1,27136
W 7
s_mov_b32 s61, 45056
buffer_load_dwordx4 v27, s[36:39], s61 offen offset:0 nt lds
M a,0,v,248,v,56
R v,238,4,27136
s_mov_b32 m0, 95104
R v,240,1,19456
buffer_load_dwordx4 v27, s[36:39], s61 offen offset:128 nt lds
M a,4,v,252,v,56
R v,242,4,19456
R v,244,1,27648
W 7
s_mov_b32 m0, 96000
M a,0,v,224,v,64
R v,246,4,27648
buffer_load_dwordx4 v27, s[36:39], s61 offen offset:256 nt lds
R v,248,1,19968
M a,4,v,228,v,64
R v,250,4,19968
R v,252,1,28160
W 7
s_mov_b32 m0, 96896
M a,0,v,232,v,72
R v,254,4,28160
buffer_load_dwordx4 v27, s[36:39], s61 offen offset:384 nt lds
s_waitcnt vmcnt(48)
s_barrier
T v,224,7,32768
M a,4,v,236,v,72
T v,226,7,40960
T v,228,10,32768
W 7
s_mov_b32 m0, 98304
s_mov_b32 s61, 524800
M a,0,v,240,v,80
T v,230,10,40960
buffer_load_dwordx4 v22, s[48:51], s61 offen offset:0 lds
s_mov_b32 m0, 99200
T v,232,7,33280
buffer_load_dwordx4 v22, s[48:51], s61 offen offset:128 lds
s_mov_b32 m0, 100096
M a,4,v,244,v,80
T v,234,7,41472
buffer_load_dwordx4 v22, s[48:51], s61 offen offset:256 lds
s_mov_b32 m0, 100992
T v,236,10,33280
W 7
buffer_load_dwordx4 v22, s[48:51], s61 offen offset:384 lds
s_mov_b32 m0, 102400
M a,0,v,248,v,88
T v,238,10,41472
s_mov_b32 s61, 590336
buffer_load_dwordx4 v22, s[48:51], s61 offen offset:0 lds
T v,240,7,33792
s_mov_b32 m0, 103296
M a,4,v,252,v,88
T v,242,7,41984
buffer_load_dwordx4 v22, s[48:51], s61 offen offset:128 lds
s_mov_b32 m0, 104192
T v,244,10,33792
W 7
buffer_load_dwordx4 v22, s[48:51], s61 offen offset:256 lds
s_mov_b32 m0, 105088
v_mfma_f32_16x16x32_bf16 v[96:99], v[224:227], a[144:147], 0
T v,246,10,41984
buffer_load_dwordx4 v22, s[48:51], s61 offen offset:384 lds
s_mov_b32 m0, 106496
T v,248,7,34304
s_mov_b32 s61, 655872
buffer_load_dwordx4 v22, s[48:51], s61 offen offset:0 lds
v_mfma_f32_16x16x32_bf16 v[100:103], v[228:231], a[144:147], 0
T v,250,7,42496
s_mov_b32 m0, 107392
T v,252,10,34304
W 7
buffer_load_dwordx4 v22, s[48:51], s61 offen offset:128 lds
s_mov_b32 m0, 108288
v_mfma_f32_16x16x32_bf16 v[112:115], v[232:235], a[144:147], 0
T v,254,10,42496
buffer_load_dwordx4 v22, s[48:51], s61 offen offset:256 lds
s_mov_b32 m0, 109184
T v,224,7,34816
buffer_load_dwordx4 v22, s[48:51], s61 offen offset:384 lds
A 30,0
v_mfma_f32_16x16x32_bf16 v[116:119], v[236:239], a[144:147], 0
T v,226,7,43008
s_mov_b32 m0, 110592
A 31,1
T v,228,10,34816
W 7
s_mov_b32 s61, 721408
v_max_f32 v26, v30, v31
v_mfma_f32_16x16x32_bf16 v[128:131], v[240:243], a[144:147], 0
T v,230,10,43008
buffer_load_dwordx4 v22, s[48:51], s61 offen offset:0 lds
A 30,2
T v,232,7,35328
s_mov_b32 m0, 111488
A 31,3
v_mfma_f32_16x16x32_bf16 v[132:135], v[244:247], a[144:147], 0
T v,234,7,43520
buffer_load_dwordx4 v22, s[48:51], s61 offen offset:128 lds
v_max3_f32 v26, v30, v31, v26
T v,236,10,35328
W 7
s_mov_b32 m0, 112384
A 30,4
v_mfma_f32_16x16x32_bf16 v[144:147], v[248:251], a[144:147], 0
T v,238,10,43520
buffer_load_dwordx4 v22, s[48:51], s61 offen offset:256 lds
A 31,5
T v,240,7,35840
s_mov_b32 m0, 113280
v_max3_f32 v26, v30, v31, v26
v_mfma_f32_16x16x32_bf16 v[148:151], v[252:255], a[144:147], 0
T v,242,7,44032
buffer_load_dwordx4 v22, s[48:51], s61 offen offset:384 lds
A 30,6
T v,244,10,35840
W 7
s_mov_b32 m0, 114688
A 31,7
v_mfma_f32_16x16x32_bf16 v[160:163], v[224:227], a[144:147], 0
T v,246,10,44032
s_mov_b32 s61, 49152
v_max3_f32 v26, v30, v31, v26
T v,248,7,36352
buffer_load_dwordx4 v19, s[36:39], s61 offen offset:0 nt lds
s_mov_b32 m0, 115584
v_mfma_f32_16x16x32_bf16 v[164:167], v[228:231], a[144:147], 0
T v,250,7,44544
buffer_load_dwordx4 v19, s[36:39], s61 offen offset:128 nt lds
s_mov_b32 m0, 116480
T v,252,10,36352
W 7
buffer_load_dwordx4 v19, s[36:39], s61 offen offset:256 nt lds
s_mov_b32 m0, 117376
v_mfma_f32_16x16x32_bf16 v[176:179], v[232:235], a[144:147], 0
T v,254,10,44544
buffer_load_dwordx4 v19, s[36:39], s61 offen offset:384 nt lds
s_mov_b32 m0, 118784
s_waitcnt vmcnt(52)
s_barrier
R v,224,1,49152
s_mov_b32 s61, 53248
buffer_load_dwordx4 v27, s[36:39], s61 offen offset:0 nt lds
v_mfma_f32_16x16x32_bf16 v[180:183], v[236:239], a[144:147], 0
R v,226,4,49152
s_mov_b32 m0, 119680
R v,228,1,57344
W 7
buffer_load_dwordx4 v27, s[36:39], s61 offen offset:128 nt lds
s_mov_b32 m0, 120576
v_mfma_f32_16x16x32_bf16 v[192:195], v[240:243], a[144:147], 0
R v,230,4,57344
buffer_load_dwordx4 v27, s[36:39], s61 offen offset:256 nt lds
s_mov_b32 m0, 121472
R v,232,1,49664
buffer_load_dwordx4 v27, s[36:39], s61 offen offset:384 nt lds
s_mov_b32 m0, 122880
v_mfma_f32_16x16x32_bf16 v[196:199], v[244:247], a[144:147], 0
R v,234,4,49664
s_mov_b32 s61, 57344
buffer_load_dwordx4 v19, s[36:39], s61 offen offset:0 nt lds
R v,236,1,57856
W 7
s_mov_b32 m0, 123776
v_mfma_f32_16x16x32_bf16 v[208:211], v[248:251], a[144:147], 0
R v,238,4,57856
buffer_load_dwordx4 v19, s[36:39], s61 offen offset:128 nt lds
s_mov_b32 m0, 124672
R v,240,1,50176
buffer_load_dwordx4 v19, s[36:39], s61 offen offset:256 nt lds
s_mov_b32 m0, 125568
v_mfma_f32_16x16x32_bf16 v[212:215], v[252:255], a[144:147], 0
R v,242,4,50176
buffer_load_dwordx4 v19, s[36:39], s61 offen offset:384 nt lds
R v,244,1,58368
W 7
s_mov_b32 m0, 126976
s_mov_b32 s61, 61440
v_mfma_f32_16x16x32_bf16 a[16:19], v[224:227], v[32:35], 0
R v,246,4,58368
buffer_load_dwordx4 v27, s[36:39], s61 offen offset:0 nt lds
R v,248,1,50688
v_mfma_f32_16x16x32_bf16 a[20:23], v[228:231], v[32:35], 0
R v,250,4,50688
R v,252,1,58880
W 7
s_mov_b32 m0, 127872
M a,16,v,232,v,40
R v,254,4,58880
buffer_load_dwordx4 v27, s[36:39], s61 offen offset:128 nt lds
R v,224,1,51200
M a,20,v,236,v,40
R v,226,4,51200
R v,228,1,59392
W 7
s_mov_b32 m0, 128768
M a,16,v,240,v,48
R v,230,4,59392
buffer_load_dwordx4 v27, s[36:39], s61 offen offset:256 nt lds
R v,232,1,51712
M a,20,v,244,v,48
R v,234,4,51712
R v,236,1,59904
W 7
s_mov_b32 m0, 129664
M a,16,v,248,v,56
R v,238,4,59904
buffer_load_dwordx4 v27, s[36:39], s61 offen offset:384 nt lds
R v,240,1,52224
s_mov_b32 s61, .Lc0_28_%=-.Lpf_start%=+8192
buffer_load_dword v29, v20, s[92:95], s61 offen
M a,20,v,252,v,56
R v,242,4,52224
R v,244,1,60416
W 7
M a,16,v,224,v,64
R v,246,4,60416
R v,248,1,52736
s_mov_b32 s61, .Lc1_28_%=-.Lpf_start%=+8192
buffer_load_dword v29, v20, s[92:95], s61 offen
M a,20,v,228,v,64
R v,250,4,52736
R v,252,1,60928
W 7
M a,16,v,232,v,72
R v,254,4,60928
s_waitcnt vmcnt(50)
s_barrier
T v,224,8,0
s_mov_b32 m0, 131072
s_mov_b32 s61, 786944
M a,20,v,236,v,72
T v,226,8,8192
buffer_load_dwordx4 v22, s[48:51], s61 offen offset:0 lds
s_mov_b32 m0, 131968
T v,228,11,0
W 7
buffer_load_dwordx4 v22, s[48:51], s61 offen offset:128 lds
s_mov_b32 m0, 132864
M a,16,v,240,v,80
T v,230,11,8192
buffer_load_dwordx4 v22, s[48:51], s61 offen offset:256 lds
s_mov_b32 m0, 133760
T v,232,8,512
buffer_load_dwordx4 v22, s[48:51], s61 offen offset:384 lds
s_mov_b32 m0, 135168
M a,20,v,244,v,80
T v,234,8,8704
s_mov_b32 s61, 852480
buffer_load_dwordx4 v22, s[48:51], s61 offen offset:0 lds
T v,236,11,512
W 7
s_mov_b32 m0, 136064
M a,16,v,248,v,88
T v,238,11,8704
buffer_load_dwordx4 v22, s[48:51], s61 offen offset:128 lds
s_mov_b32 m0, 136960
T v,240,8,1024
buffer_load_dwordx4 v22, s[48:51], s61 offen offset:256 lds
s_mov_b32 m0, 137856
M a,20,v,252,v,88
T v,242,8,9216
buffer_load_dwordx4 v22, s[48:51], s61 offen offset:384 lds
s_mov_b32 m0, 139264
T v,244,11,1024
W 7
s_mov_b32 s61, 918016
buffer_load_dwordx4 v22, s[48:51], s61 offen offset:0 lds
M v,96,v,224,a,152
T v,246,11,9216
s_mov_b32 m0, 140160
T v,248,8,1536
buffer_load_dwordx4 v22, s[48:51], s61 offen offset:128 lds
s_mov_b32 m0, 141056
M v,100,v,228,a,152
T v,250,8,9728
buffer_load_dwordx4 v22, s[48:51], s61 offen offset:256 lds
s_mov_b32 m0, 141952
T v,252,11,1536
W 7
buffer_load_dwordx4 v22, s[48:51], s61 offen offset:384 lds
s_mov_b32 m0, 143360
M v,112,v,232,a,152
T v,254,11,9728
s_mov_b32 s61, 983552
buffer_load_dwordx4 v22, s[48:51], s61 offen offset:0 lds
T v,224,8,2048
s_mov_b32 m0, 144256
A 30,16
M v,116,v,236,a,152
T v,226,8,10240
buffer_load_dwordx4 v22, s[48:51], s61 offen offset:128 lds
A 31,17
T v,228,11,2048
W 7
s_mov_b32 m0, 145152
v_max3_f32 v26, v30, v31, v26
M v,128,v,240,a,152
T v,230,11,10240
buffer_load_dwordx4 v22, s[48:51], s61 offen offset:256 lds
A 30,18
T v,232,8,2560
s_mov_b32 m0, 146048
A 31,19
M v,132,v,244,a,152
T v,234,8,10752
buffer_load_dwordx4 v22, s[48:51], s61 offen offset:384 lds
v_max3_f32 v26, v30, v31, v26
T v,236,11,2560
W 7
s_mov_b32 m0, 147456
A 30,20
M v,144,v,248,a,152
T v,238,11,10752
s_mov_b32 s61, 65536
A 31,21
T v,240,8,3072
buffer_load_dwordx4 v19, s[36:39], s61 offen offset:0 nt lds
v_max3_f32 v26, v30, v31, v26
M v,148,v,252,a,152
T v,242,8,11264
s_mov_b32 m0, 148352
A 30,22
T v,244,11,3072
W 7
buffer_load_dwordx4 v19, s[36:39], s61 offen offset:128 nt lds
A 31,23
M v,160,v,224,a,152
T v,246,11,11264
s_mov_b32 m0, 149248
v_max3_f32 v26, v30, v31, v26
T v,248,8,3584
buffer_load_dwordx4 v19, s[36:39], s61 offen offset:256 nt lds
s_mov_b32 m0, 150144
M v,164,v,228,a,152
)x"
R"x(T v,250,8,11776
buffer_load_dwordx4 v19, s[36:39], s61 offen offset:384 nt lds
s_mov_b32 m0, 151552
T v,252,11,3584
W 7
s_mov_b32 s61, 69632
buffer_load_dwordx4 v27, s[36:39], s61 offen offset:0 nt lds
M v,176,v,232,a,152
T v,254,11,11776
s_mov_b32 m0, 152448
s_waitcnt vmcnt(55)
s_barrier
R v,224,2,16384
buffer_load_dwordx4 v27, s[36:39], s61 offen offset:128 nt lds
s_mov_b32 m0, 153344
M v,180,v,236,a,152
R v,226,5,16384
buffer_load_dwordx4 v27, s[36:39], s61 offen offset:256 nt lds
s_mov_b32 m0, 154240
R v,228,2,24576
W 7
buffer_load_dwordx4 v27, s[36:39], s61 offen offset:384 nt lds
s_mov_b32 m0, 155648
M v,192,v,240,a,152
R v,230,5,24576
s_mov_b32 s61, 73728
buffer_load_dwordx4 v19, s[36:39], s61 offen offset:0 nt lds
R v,232,2,16896
s_mov_b32 m0, 156544
M v,196,v,244,a,152
R v,234,5,16896
buffer_load_dwordx4 v19, s[36:39], s61 offen offset:128 nt lds
s_mov_b32 m0, 157440
R v,236,2,25088
W 7
buffer_load_dwordx4 v19, s[36:39], s61 offen offset:256 nt lds
s_mov_b32 m0, 158336
M v,208,v,248,a,152
R v,238,5,25088
buffer_load_dwordx4 v19, s[36:39], s61 offen offset:384 nt lds
s_mov_b32 m0, 159744
R v,240,2,17408
s_mov_b32 s61, 77824
buffer_load_dwordx4 v27, s[36:39], s61 offen offset:0 nt lds
M v,212,v,252,a,152
R v,242,5,17408
s_mov_b32 m0, 160640
R v,244,2,25600
W 7
buffer_load_dwordx4 v27, s[36:39], s61 offen offset:128 nt lds
s_mov_b32 m0, 161536
v_mfma_f32_16x16x32_bf16 a[32:35], v[224:227], v[32:35], 0
R v,246,5,25600
buffer_load_dwordx4 v27, s[36:39], s61 offen offset:256 nt lds
R v,248,2,17920
s_mov_b32 m0, 162432
v_mfma_f32_16x16x32_bf16 a[36:39], v[228:231], v[32:35], 0
R v,250,5,17920
buffer_load_dwordx4 v27, s[36:39], s61 offen offset:384 nt lds
R v,252,2,26112
W 7
M a,32,v,232,v,40
R v,254,5,26112
R v,224,2,18432
s_mov_b32 m0, 0
s_mov_b32 s61, 1049088
M a,36,v,236,v,40
R v,226,5,18432
buffer_load_dwordx4 v22, s[48:51], s61 offen offset:0 lds
s_mov_b32 m0, 896
R v,228,2,26624
W 7
buffer_load_dwordx4 v22, s[48:51], s61 offen offset:128 lds
s_mov_b32 m0, 1792
M a,32,v,240,v,48
R v,230,5,26624
buffer_load_dwordx4 v22, s[48:51], s61 offen offset:256 lds
s_mov_b32 m0, 2688
R v,232,2,18944
buffer_load_dwordx4 v22, s[48:51], s61 offen offset:384 lds
s_mov_b32 m0, 4096
M a,36,v,244,v,48
R v,234,5,18944
s_mov_b32 s61, 1114624
buffer_load_dwordx4 v22, s[48:51], s61 offen offset:0 lds
R v,236,2,27136
W 7
s_mov_b32 m0, 4992
M a,32,v,248,v,56
R v,238,5,27136
buffer_load_dwordx4 v22, s[48:51], s61 offen offset:128 lds
s_mov_b32 m0, 5888
R v,240,2,19456
buffer_load_dwordx4 v22, s[48:51], s61 offen offset:256 lds
s_mov_b32 m0, 6784
M a,36,v,252,v,56
R v,242,5,19456
buffer_load_dwordx4 v22, s[48:51], s61 offen offset:384 lds
s_mov_b32 m0, 8192
R v,244,2,27648
W 7
s_mov_b32 s61, 1180160
buffer_load_dwordx4 v22, s[48:51], s61 offen offset:0 lds
M a,32,v,224,v,64
R v,246,5,27648
s_mov_b32 m0, 9088
R v,248,2,19968
buffer_load_dwordx4 v22, s[48:51], s61 offen offset:128 lds
s_mov_b32 m0, 9984
M a,36,v,228,v,64
R v,250,5,19968
buffer_load_dwordx4 v22, s[48:51], s61 offen offset:256 lds
s_mov_b32 m0, 10880
R v,252,2,28160
W 7
buffer_load_dwordx4 v22, s[48:51], s61 offen offset:384 lds
s_mov_b32 m0, 12288
M a,32,v,232,v,72
R v,254,5,28160
s_mov_b32 s61, 1245696
buffer_load_dwordx4 v22, s[48:51], s61 offen offset:0 lds
s_waitcnt vmcnt(63)
s_barrier
.Lc1_34_%=:
T v,224,8,32768
s_mov_b32 m0, 13184
M a,36,v,236,v,72
T v,226,8,40960
buffer_load_dwordx4 v22, s[48:51], s61 offen offset:128 lds
s_mov_b32 m0, 14080
T v,228,11,32768
W 7
buffer_load_dwordx4 v22, s[48:51], s61 offen offset:256 lds
s_mov_b32 m0, 14976
M a,32,v,240,v,80
T v,230,11,40960
buffer_load_dwordx4 v22, s[48:51], s61 offen offset:384 lds
s_mov_b32 m0, 16384
T v,232,8,33280
s_mov_b32 s61, 81920
buffer_load_dwordx4 v19, s[36:39], s61 offen offset:0 nt lds
M a,36,v,244,v,80
T v,234,8,41472
s_mov_b32 m0, 17280
T v,236,11,33280
W 7
buffer_load_dwordx4 v19, s[36:39], s61 offen offset:128 nt lds
s_mov_b32 m0, 18176
M a,32,v,248,v,88
T v,238,11,41472
buffer_load_dwordx4 v19, s[36:39], s61 offen offset:256 nt lds
s_mov_b32 m0, 19072
T v,240,8,33792
buffer_load_dwordx4 v19, s[36:39], s61 offen offset:384 nt lds
s_mov_b32 m0, 20480
M a,36,v,252,v,88
T v,242,8,41984
s_mov_b32 s61, 86016
buffer_load_dwordx4 v27, s[36:39], s61 offen offset:0 nt lds
T v,244,11,33792
W 7
s_mov_b32 m0, 21376
M v,96,v,224,a,160
T v,246,11,41984
buffer_load_dwordx4 v27, s[36:39], s61 offen offset:128 nt lds
T v,248,8,34304
s_mov_b32 m0, 22272
M v,100,v,228,a,160
T v,250,8,42496
buffer_load_dwordx4 v27, s[36:39], s61 offen offset:256 nt lds
T v,252,11,34304
W 7
M v,112,v,232,a,160
T v,254,11,42496
T v,224,8,34816
s_mov_b32 m0, 23168
A 30,32
M v,116,v,236,a,160
T v,226,8,43008
buffer_load_dwordx4 v27, s[36:39], s61 offen offset:384 nt lds
A 31,33
T v,228,11,34816
W 7
s_mov_b32 m0, 24576
v_max3_f32 v26, v30, v31, v26
M v,128,v,240,a,160
T v,230,11,43008
s_mov_b32 s61, 90112
A 30,34
T v,232,8,35328
buffer_load_dwordx4 v19, s[36:39], s61 offen offset:0 nt lds
A 31,35
M v,132,v,244,a,160
T v,234,8,43520
T v,236,11,35328
W 7
s_mov_b32 m0, 25472
v_max3_f32 v26, v30, v31, v26
M v,144,v,248,a,160
T v,238,11,43520
buffer_load_dwordx4 v19, s[36:39], s61 offen offset:128 nt lds
A 30,36
T v,240,8,35840
M v,148,v,252,a,160
T v,242,8,44032
T v,244,11,35840
W 7
s_mov_b32 m0, 26368
A 31,37
M v,160,v,224,a,160
T v,246,11,44032
buffer_load_dwordx4 v19, s[36:39], s61 offen offset:256 nt lds
v_max3_f32 v26, v30, v31, v26
T v,248,8,36352
M v,164,v,228,a,160
T v,250,8,44544
T v,252,11,36352
W 7
s_mov_b32 m0, 27264
A 30,38
M v,176,v,232,a,160
T v,254,11,44544
buffer_load_dwordx4 v19, s[36:39], s61 offen offset:384 nt lds
A 31,39
s_waitcnt vmcnt(62)
s_barrier
R v,224,2,49152
s_mov_b32 m0, 28672
v_max3_f32 v26, v30, v31, v26
M v,180,v,236,a,160
R v,226,5,49152
s_mov_b32 s61, 94208
buffer_load_dwordx4 v27, s[36:39], s61 offen offset:0 nt lds
R v,228,2,57344
W 7
M v,192,v,240,a,160
R v,230,5,57344
R v,232,2,49664
s_mov_b32 m0, 29568
M v,196,v,244,a,160
R v,234,5,49664
buffer_load_dwordx4 v27, s[36:39], s61 offen offset:128 nt lds
R v,236,2,57856
W 7
M v,208,v,248,a,160
R v,238,5,57856
R v,240,2,50176
s_mov_b32 m0, 30464
M v,212,v,252,a,160
R v,242,5,50176
buffer_load_dwordx4 v27, s[36:39], s61 offen offset:256 nt lds
R v,244,2,58368
W 7
v_mfma_f32_16x16x32_bf16 a[48:51], v[224:227], v[32:35], 0
R v,246,5,58368
R v,248,2,50688
s_mov_b32 m0, 31360
v_mfma_f32_16x16x32_bf16 a[52:55], v[228:231], v[32:35], 0
R v,250,5,50688
buffer_load_dwordx4 v27, s[36:39], s61 offen offset:384 nt lds
R v,252,2,58880
W 7
M a,48,v,232,v,40
R v,254,5,58880
R v,224,2,51200
s_mov_b32 m0, 32768
s_mov_b32 s61, 1311232
M a,52,v,236,v,40
R v,226,5,51200
buffer_load_dwordx4 v22, s[48:51], s61 offen offset:0 lds
s_mov_b32 m0, 33664
R v,228,2,59392
W 7
buffer_load_dwordx4 v22, s[48:51], s61 offen offset:128 lds
s_mov_b32 m0, 34560
M a,48,v,240,v,48
R v,230,5,59392
buffer_load_dwordx4 v22, s[48:51], s61 offen offset:256 lds
s_mov_b32 m0, 35456
R v,232,2,51712
buffer_load_dwordx4 v22, s[48:51], s61 offen offset:384 lds
s_mov_b32 m0, 36864
M a,52,v,244,v,48
R v,234,5,51712
s_mov_b32 s61, 1376768
buffer_load_dwordx4 v22, s[48:51], s61 offen offset:0 lds
R v,236,2,59904
W 7
s_mov_b32 m0, 37760
M a,48,v,248,v,56
R v,238,5,59904
buffer_load_dwordx4 v22, s[48:51], s61 offen offset:128 lds
s_mov_b32 m0, 38656
R v,240,2,52224
buffer_load_dwordx4 v22, s[48:51], s61 offen offset:256 lds
s_mov_b32 m0, 39552
M a,52,v,252,v,56
R v,242,5,52224
buffer_load_dwordx4 v22, s[48:51], s61 offen offset:384 lds
s_mov_b32 m0, 40960
R v,244,2,60416
W 7
s_mov_b32 s61, 1442304
buffer_load_dwordx4 v22, s[48:51], s61 offen offset:0 lds
M a,48,v,224,v,64
R v,246,5,60416
s_mov_b32 m0, 41856
R v,248,2,52736
buffer_load_dwordx4 v22, s[48:51], s61 offen offset:128 lds
s_mov_b32 m0, 42752
M a,52,v,228,v,64
R v,250,5,52736
buffer_load_dwordx4 v22, s[48:51], s61 offen offset:256 lds
s_mov_b32 m0, 43648
R v,252,2,60928
W 7
buffer_load_dwordx4 v22, s[48:51], s61 offen offset:384 lds
s_mov_b32 m0, 45056
M a,48,v,232,v,72
R v,254,5,60928
s_mov_b32 s61, 1507840
buffer_load_dwordx4 v22, s[48:51], s61 offen offset:0 lds
s_waitcnt vmcnt(61)
s_barrier
T v,224,9,0
s_mov_b32 m0, 45952
M a,52,v,236,v,72
T v,226,9,8192
buffer_load_dwordx4 v22, s[48:51], s61 offen offset:128 lds
s_mov_b32 m0, 46848
T v,228,12,0
W 7
buffer_load_dwordx4 v22, s[48:51], s61 offen offset:256 lds
s_mov_b32 m0, 47744
M a,48,v,240,v,80
T v,230,12,8192
buffer_load_dwordx4 v22, s[48:51], s61 offen offset:384 lds
s_mov_b32 m0, 49152
T v,232,9,512
s_mov_b32 s61, 98304
buffer_load_dwordx4 v19, s[36:39], s61 offen offset:0 nt lds
M a,52,v,244,v,80
T v,234,9,8704
s_mov_b32 m0, 50048
T v,236,12,512
W 7
buffer_load_dwordx4 v19, s[36:39], s61 offen offset:128 nt lds
s_mov_b32 m0, 50944
M a,48,v,248,v,88
T v,238,12,8704
buffer_load_dwordx4 v19, s[36:39], s61 offen offset:256 nt lds
s_mov_b32 m0, 51840
T v,240,9,1024
buffer_load_dwordx4 v19, s[36:39], s61 offen offset:384 nt lds
s_mov_b32 m0, 53248
M a,52,v,252,v,88
T v,242,9,9216
s_mov_b32 s61, 102400
buffer_load_dwordx4 v27, s[36:39], s61 offen offset:0 nt lds
T v,244,12,1024
W 7
s_mov_b32 m0, 54144
M v,96,v,224,a,168
T v,246,12,9216
buffer_load_dwordx4 v27, s[36:39], s61 offen offset:128 nt lds
T v,248,9,1536
s_mov_b32 m0, 55040
M v,100,v,228,a,168
T v,250,9,9728
buffer_load_dwordx4 v27, s[36:39], s61 offen offset:256 nt lds
T v,252,12,1536
W 7
M v,112,v,232,a,168
T v,254,12,9728
T v,224,9,2048
s_mov_b32 m0, 55936
A 30,48
M v,116,v,236,a,168
T v,226,9,10240
buffer_load_dwordx4 v27, s[36:39], s61 offen offset:384 nt lds
A 31,49
T v,228,12,2048
W 7
s_mov_b32 m0, 57344
v_max3_f32 v26, v30, v31, v26
M v,128,v,240,a,168
T v,230,12,10240
s_mov_b32 s61, 106496
A 30,50
T v,232,9,2560
buffer_load_dwordx4 v19, s[36:39], s61 offen offset:0 nt lds
A 31,51
M v,132,v,244,a,168
T v,234,9,10752
T v,236,12,2560
W 7
s_mov_b32 m0, 58240
v_max3_f32 v26, v30, v31, v26
M v,144,v,248,a,168
T v,238,12,10752
)x"
R"x(buffer_load_dwordx4 v19, s[36:39], s61 offen offset:128 nt lds
A 30,52
T v,240,9,3072
M v,148,v,252,a,168
T v,242,9,11264
T v,244,12,3072
W 7
s_mov_b32 m0, 59136
A 31,53
M v,160,v,224,a,168
T v,246,12,11264
buffer_load_dwordx4 v19, s[36:39], s61 offen offset:256 nt lds
v_max3_f32 v26, v30, v31, v26
T v,248,9,3584
M v,164,v,228,a,168
T v,250,9,11776
T v,252,12,3584
W 7
s_mov_b32 m0, 60032
A 30,54
M v,176,v,232,a,168
T v,254,12,11776
buffer_load_dwordx4 v19, s[36:39], s61 offen offset:384 nt lds
A 31,55
s_waitcnt vmcnt(60)
s_barrier
R v,224,3,16384
s_mov_b32 m0, 61440
v_max3_f32 v26, v30, v31, v26
M v,180,v,236,a,168
R v,226,6,16384
s_mov_b32 s61, 110592
buffer_load_dwordx4 v27, s[36:39], s61 offen offset:0 nt lds
R v,228,3,24576
W 7
s_mov_b32 m0, 62336
M v,192,v,240,a,168
R v,230,6,24576
buffer_load_dwordx4 v27, s[36:39], s61 offen offset:128 nt lds
s_mov_b32 m0, 63232
R v,232,3,16896
buffer_load_dwordx4 v27, s[36:39], s61 offen offset:256 nt lds
s_mov_b32 m0, 64128
M v,196,v,244,a,168
R v,234,6,16896
buffer_load_dwordx4 v27, s[36:39], s61 offen offset:384 nt lds
R v,236,3,25088
W 7
M v,208,v,248,a,168
R v,238,6,25088
R v,240,3,17408
s_mov_b32 s61, .Lc0_34_%=-.Lpf_start%=+8192
buffer_load_dword v29, v20, s[92:95], s61 offen
M v,212,v,252,a,168
R v,242,6,17408
R v,244,3,25600
W 7
v_mfma_f32_16x16x32_bf16 a[64:67], v[224:227], v[32:35], 0
R v,246,6,25600
R v,248,3,17920
s_mov_b32 s61, .Lc1_34_%=-.Lpf_start%=+8192
buffer_load_dword v29, v20, s[92:95], s61 offen
v_mfma_f32_16x16x32_bf16 a[68:71], v[228:231], v[32:35], 0
R v,250,6,17920
R v,252,3,26112
W 7
M a,64,v,232,v,40
R v,254,6,26112
R v,224,3,18432
s_mov_b32 m0, 65536
s_mov_b32 s61, 1573376
M a,68,v,236,v,40
R v,226,6,18432
buffer_load_dwordx4 v22, s[48:51], s61 offen offset:0 lds
s_mov_b32 m0, 66432
R v,228,3,26624
W 7
buffer_load_dwordx4 v22, s[48:51], s61 offen offset:128 lds
s_mov_b32 m0, 67328
M a,64,v,240,v,48
R v,230,6,26624
buffer_load_dwordx4 v22, s[48:51], s61 offen offset:256 lds
s_mov_b32 m0, 68224
R v,232,3,18944
buffer_load_dwordx4 v22, s[48:51], s61 offen offset:384 lds
s_mov_b32 m0, 69632
M a,68,v,244,v,48
R v,234,6,18944
s_mov_b32 s61, 1638912
buffer_load_dwordx4 v22, s[48:51], s61 offen offset:0 lds
R v,236,3,27136
W 7
s_mov_b32 m0, 70528
M a,64,v,248,v,56
R v,238,6,27136
buffer_load_dwordx4 v22, s[48:51], s61 offen offset:128 lds
s_mov_b32 m0, 71424
R v,240,3,19456
buffer_load_dwordx4 v22, s[48:51], s61 offen offset:256 lds
s_mov_b32 m0, 72320
M a,68,v,252,v,56
R v,242,6,19456
buffer_load_dwordx4 v22, s[48:51], s61 offen offset:384 lds
s_mov_b32 m0, 73728
R v,244,3,27648
W 7
s_mov_b32 s61, 1704448
buffer_load_dwordx4 v22, s[48:51], s61 offen offset:0 lds
M a,64,v,224,v,64
R v,246,6,27648
s_mov_b32 m0, 74624
R v,248,3,19968
buffer_load_dwordx4 v22, s[48:51], s61 offen offset:128 lds
s_mov_b32 m0, 75520
M a,68,v,228,v,64
R v,250,6,19968
buffer_load_dwordx4 v22, s[48:51], s61 offen offset:256 lds
s_mov_b32 m0, 76416
R v,252,3,28160
W 7
buffer_load_dwordx4 v22, s[48:51], s61 offen offset:384 lds
s_mov_b32 m0, 77824
M a,64,v,232,v,72
R v,254,6,28160
s_mov_b32 s61, 1769984
buffer_load_dwordx4 v22, s[48:51], s61 offen offset:0 lds
s_waitcnt vmcnt(63)
s_barrier
T v,224,7,0
s_mov_b32 m0, 78720
M a,68,v,236,v,72
T v,226,7,8192
buffer_load_dwordx4 v22, s[48:51], s61 offen offset:128 lds
s_mov_b32 m0, 79616
T v,228,10,0
W 7
buffer_load_dwordx4 v22, s[48:51], s61 offen offset:256 lds
s_mov_b32 m0, 80512
M a,64,v,240,v,80
T v,230,10,8192
buffer_load_dwordx4 v22, s[48:51], s61 offen offset:384 lds
s_mov_b32 m0, 81920
T v,232,7,512
s_mov_b32 s61, 114688
buffer_load_dwordx4 v19, s[36:39], s61 offen offset:0 nt lds
M a,68,v,244,v,80
T v,234,7,8704
s_mov_b32 m0, 82816
T v,236,10,512
W 7
buffer_load_dwordx4 v19, s[36:39], s61 offen offset:128 nt lds
s_mov_b32 m0, 83712
M a,64,v,248,v,88
T v,238,10,8704
buffer_load_dwordx4 v19, s[36:39], s61 offen offset:256 nt lds
s_mov_b32 m0, 84608
T v,240,7,1024
buffer_load_dwordx4 v19, s[36:39], s61 offen offset:384 nt lds
s_mov_b32 m0, 86016
M a,68,v,252,v,88
T v,242,7,9216
s_mov_b32 s61, 118784
buffer_load_dwordx4 v27, s[36:39], s61 offen offset:0 nt lds
T v,244,10,1024
W 7
s_mov_b32 m0, 86912
M v,96,v,224,a,176
T v,246,10,9216
buffer_load_dwordx4 v27, s[36:39], s61 offen offset:128 nt lds
T v,248,7,1536
s_mov_b32 m0, 87808
M v,100,v,228,a,176
T v,250,7,9728
buffer_load_dwordx4 v27, s[36:39], s61 offen offset:256 nt lds
T v,252,10,1536
W 7
M v,112,v,232,a,176
T v,254,10,9728
T v,224,7,2048
s_mov_b32 m0, 88704
A 30,64
M v,116,v,236,a,176
T v,226,7,10240
buffer_load_dwordx4 v27, s[36:39], s61 offen offset:384 nt lds
A 31,65
T v,228,10,2048
W 7
s_mov_b32 m0, 90112
v_max3_f32 v26, v30, v31, v26
M v,128,v,240,a,176
T v,230,10,10240
s_mov_b32 s61, 122880
A 30,66
T v,232,7,2560
buffer_load_dwordx4 v19, s[36:39], s61 offen offset:0 nt lds
A 31,67
M v,132,v,244,a,176
T v,234,7,10752
T v,236,10,2560
W 7
s_mov_b32 m0, 91008
v_max3_f32 v26, v30, v31, v26
M v,144,v,248,a,176
T v,238,10,10752
buffer_load_dwordx4 v19, s[36:39], s61 offen offset:128 nt lds
A 30,68
T v,240,7,3072
M v,148,v,252,a,176
T v,242,7,11264
T v,244,10,3072
W 7
s_mov_b32 m0, 91904
A 31,69
M v,160,v,224,a,176
T v,246,10,11264
buffer_load_dwordx4 v19, s[36:39], s61 offen offset:256 nt lds
v_max3_f32 v26, v30, v31, v26
T v,248,7,3584
M v,164,v,228,a,176
T v,250,7,11776
T v,252,10,3584
W 7
s_mov_b32 m0, 92800
A 30,70
M v,176,v,232,a,176
T v,254,10,11776
buffer_load_dwordx4 v19, s[36:39], s61 offen offset:384 nt lds
A 31,71
s_waitcnt vmcnt(62)
s_barrier
R v,224,1,16384
s_mov_b32 m0, 94208
v_max3_f32 v26, v30, v31, v26
M v,180,v,236,a,176
R v,226,4,16384
s_mov_b32 s61, 126976
buffer_load_dwordx4 v27, s[36:39], s61 offen offset:0 nt lds
R v,228,1,24576
W 7
M v,192,v,240,a,176
R v,230,4,24576
R v,232,1,16896
s_mov_b32 m0, 95104
M v,196,v,244,a,176
R v,234,4,16896
buffer_load_dwordx4 v27, s[36:39], s61 offen offset:128 nt lds
R v,236,1,25088
W 7
M v,208,v,248,a,176
R v,238,4,25088
R v,240,1,17408
s_mov_b32 m0, 96000
M v,212,v,252,a,176
R v,242,4,17408
buffer_load_dwordx4 v27, s[36:39], s61 offen offset:256 nt lds
R v,244,1,25600
W 7
v_mfma_f32_16x16x32_bf16 a[80:83], v[224:227], v[32:35], 0
R v,246,4,25600
R v,248,1,17920
s_mov_b32 m0, 96896
v_mfma_f32_16x16x32_bf16 a[84:87], v[228:231], v[32:35], 0
R v,250,4,17920
buffer_load_dwordx4 v27, s[36:39], s61 offen offset:384 nt lds
R v,252,1,26112
W 7
M a,80,v,232,v,40
R v,254,4,26112
R v,224,1,18432
s_mov_b32 m0, 98304
s_mov_b32 s61, 1835520
M a,84,v,236,v,40
R v,226,4,18432
buffer_load_dwordx4 v22, s[48:51], s61 offen offset:0 lds
s_mov_b32 m0, 99200
R v,228,1,26624
W 7
buffer_load_dwordx4 v22, s[48:51], s61 offen offset:128 lds
s_mov_b32 m0, 100096
M a,80,v,240,v,48
R v,230,4,26624
buffer_load_dwordx4 v22, s[48:51], s61 offen offset:256 lds
s_mov_b32 m0, 100992
R v,232,1,18944
buffer_load_dwordx4 v22, s[48:51], s61 offen offset:384 lds
s_mov_b32 m0, 102400
M a,84,v,244,v,48
R v,234,4,18944
s_mov_b32 s61, 1901056
buffer_load_dwordx4 v22, s[48:51], s61 offen offset:0 lds
R v,236,1,27136
W 7
s_mov_b32 m0, 103296
M a,80,v,248,v,56
R v,238,4,27136
buffer_load_dwordx4 v22, s[48:51], s61 offen offset:128 lds
s_mov_b32 m0, 104192
R v,240,1,19456
buffer_load_dwordx4 v22, s[48:51], s61 offen offset:256 lds
s_mov_b32 m0, 105088
M a,84,v,252,v,56
R v,242,4,19456
buffer_load_dwordx4 v22, s[48:51], s61 offen offset:384 lds
s_mov_b32 m0, 106496
R v,244,1,27648
W 7
s_mov_b32 s61, 1966592
buffer_load_dwordx4 v22, s[48:51], s61 offen offset:0 lds
M a,80,v,224,v,64
R v,246,4,27648
s_mov_b32 m0, 107392
R v,248,1,19968
buffer_load_dwordx4 v22, s[48:51], s61 offen offset:128 lds
s_mov_b32 m0, 108288
M a,84,v,228,v,64
R v,250,4,19968
buffer_load_dwordx4 v22, s[48:51], s61 offen offset:256 lds
s_mov_b32 m0, 109184
R v,252,1,28160
W 7
buffer_load_dwordx4 v22, s[48:51], s61 offen offset:384 lds
s_mov_b32 m0, 110592
M a,80,v,232,v,72
R v,254,4,28160
s_mov_b32 s61, 2032128
buffer_load_dwordx4 v22, s[48:51], s61 offen offset:0 lds
s_waitcnt vmcnt(63)
s_barrier
.Lc1_40_%=:
T v,224,7,32768
s_mov_b32 m0, 111488
M a,84,v,236,v,72
T v,226,7,40960
buffer_load_dwordx4 v22, s[48:51], s61 offen offset:128 lds
s_mov_b32 m0, 112384
T v,228,10,32768
W 7
buffer_load_dwordx4 v22, s[48:51], s61 offen offset:256 lds
s_mov_b32 m0, 113280
M a,80,v,240,v,80
T v,230,10,40960
buffer_load_dwordx4 v22, s[48:51], s61 offen offset:384 lds
s_mul_i32 s61, %9, 56
T v,232,7,33280
v_add_u32 v25, s61, v0
v_cmp_gt_u32 vcc, 56, v0
M a,84,v,244,v,80
T v,234,7,41472
v_lshlrev_b32 v25, 7, v25
s_mov_b32 s61, 0
T v,236,10,33280
W 7
v_cndmask_b32 v25, 2.0, v25, vcc
buffer_load_dword v29, v25, s[52:55], s61 offen
M a,80,v,248,v,88
T v,238,10,41472
s_mov_b32 m0, 114688
s_mov_b32 s61, 131072
T v,240,7,33792
buffer_load_dwordx4 v19, s[36:39], s61 offen offset:0 nt lds
s_mov_b32 m0, 115584
M a,84,v,252,v,88
T v,242,7,41984
buffer_load_dwordx4 v19, s[36:39], s61 offen offset:128 nt lds
s_mov_b32 m0, 116480
T v,244,10,33792
W 7
buffer_load_dwordx4 v19, s[36:39], s61 offen offset:256 nt lds
s_mov_b32 m0, 117376
M v,96,v,224,a,184
T v,246,10,41984
buffer_load_dwordx4 v19, s[36:39], s61 offen offset:384 nt lds
s_mov_b32 m0, 118784
T v,248,7,34304
s_mov_b32 s61, 135168
buffer_load_dwordx4 v27, s[36:39], s61 offen offset:0 nt lds
M v,100,v,228,a,184
T v,250,7,42496
s_mov_b32 m0, 119680
T v,252,10,34304
W 7
buffer_load_dwordx4 v27, s[36:39], s61 offen offset:128 nt lds
M v,112,v,232,a,184
T v,254,10,42496
T v,224,7,34816
s_mov_b32 m0, 120576
A 30,80
M v,116,v,236,a,184
T v,226,7,43008
buffer_load_dwordx4 v27, s[36:39], s61 offen offset:256 nt lds
A 31,81
T v,228,10,34816
W 7
s_mov_b32 m0, 121472
v_max3_f32 v26, v30, v31, v26
M v,128,v,240,a,184
T v,230,10,43008
buffer_load_dwordx4 v27, s[36:39], s61 offen offset:384 nt lds
A 30,82
T v,232,7,35328
)x"
R"x(M v,132,v,244,a,184
T v,234,7,43520
T v,236,10,35328
W 7
s_mov_b32 m0, 122880
A 31,83
M v,144,v,248,a,184
T v,238,10,43520
s_mov_b32 s61, 139264
v_max3_f32 v26, v30, v31, v26
T v,240,7,35840
buffer_load_dwordx4 v19, s[36:39], s61 offen offset:0 nt lds
A 30,84
M v,148,v,252,a,184
T v,242,7,44032
T v,244,10,35840
W 7
s_mov_b32 m0, 123776
A 31,85
M v,160,v,224,a,184
T v,246,10,44032
buffer_load_dwordx4 v19, s[36:39], s61 offen offset:128 nt lds
v_max3_f32 v26, v30, v31, v26
T v,248,7,36352
M v,164,v,228,a,184
T v,250,7,44544
T v,252,10,36352
W 7
s_mov_b32 m0, 124672
A 30,86
M v,176,v,232,a,184
T v,254,10,44544
buffer_load_dwordx4 v19, s[36:39], s61 offen offset:256 nt lds
A 31,87
s_waitcnt vmcnt(62)
s_barrier
R v,224,1,49152
s_mov_b32 m0, 125568
v_max3_f32 v26, v30, v31, v26
M v,180,v,236,a,184
R v,226,4,49152
buffer_load_dwordx4 v19, s[36:39], s61 offen offset:384 nt lds
R v,228,1,57344
W 7
M v,192,v,240,a,184
R v,230,4,57344
R v,232,1,49664
s_mov_b32 m0, 126976
s_mov_b32 s61, 143360
M v,196,v,244,a,184
R v,234,4,49664
buffer_load_dwordx4 v27, s[36:39], s61 offen offset:0 nt lds
R v,236,1,57856
W 7
M v,208,v,248,a,184
R v,238,4,57856
R v,240,1,50176
s_mov_b32 m0, 127872
M v,212,v,252,a,184
R v,242,4,50176
buffer_load_dwordx4 v27, s[36:39], s61 offen offset:128 nt lds
R v,244,1,58368
W 7
v_mfma_f32_16x16x32_bf16 a[96:99], v[224:227], v[32:35], 0
R v,246,4,58368
R v,248,1,50688
s_mov_b32 m0, 128768
v_mfma_f32_16x16x32_bf16 a[100:103], v[228:231], v[32:35], 0
R v,250,4,50688
buffer_load_dwordx4 v27, s[36:39], s61 offen offset:256 nt lds
R v,252,1,58880
W 7
M a,96,v,232,v,40
R v,254,4,58880
R v,224,1,51200
s_mov_b32 m0, 129664
M a,100,v,236,v,40
R v,226,4,51200
buffer_load_dwordx4 v27, s[36:39], s61 offen offset:384 nt lds
R v,228,1,59392
W 7
M a,96,v,240,v,48
R v,230,4,59392
R v,232,1,51712
s_mov_b32 m0, 131072
s_mov_b32 s61, 2097664
M a,100,v,244,v,48
R v,234,4,51712
buffer_load_dwordx4 v22, s[48:51], s61 offen offset:0 lds
s_mov_b32 m0, 131968
R v,236,1,59904
W 7
buffer_load_dwordx4 v22, s[48:51], s61 offen offset:128 lds
s_mov_b32 m0, 132864
M a,96,v,248,v,56
R v,238,4,59904
buffer_load_dwordx4 v22, s[48:51], s61 offen offset:256 lds
s_mov_b32 m0, 133760
R v,240,1,52224
buffer_load_dwordx4 v22, s[48:51], s61 offen offset:384 lds
s_mov_b32 m0, 135168
M a,100,v,252,v,56
R v,242,4,52224
s_mov_b32 s61, 2163200
buffer_load_dwordx4 v22, s[48:51], s61 offen offset:0 lds
R v,244,1,60416
W 7
s_mov_b32 m0, 136064
M a,96,v,224,v,64
R v,246,4,60416
buffer_load_dwordx4 v22, s[48:51], s61 offen offset:128 lds
s_mov_b32 m0, 136960
R v,248,1,52736
buffer_load_dwordx4 v22, s[48:51], s61 offen offset:256 lds
s_mov_b32 m0, 137856
M a,100,v,228,v,64
R v,250,4,52736
buffer_load_dwordx4 v22, s[48:51], s61 offen offset:384 lds
s_mov_b32 m0, 139264
R v,252,1,60928
W 7
s_mov_b32 s61, 2228736
buffer_load_dwordx4 v22, s[48:51], s61 offen offset:0 lds
M a,96,v,232,v,72
R v,254,4,60928
s_mov_b32 m0, 140160
s_waitcnt vmcnt(58)
s_barrier
T v,224,8,0
buffer_load_dwordx4 v22, s[48:51], s61 offen offset:128 lds
s_mov_b32 m0, 141056
M a,100,v,236,v,72
T v,226,8,8192
buffer_load_dwordx4 v22, s[48:51], s61 offen offset:256 lds
s_mov_b32 m0, 141952
T v,228,11,0
W 7
buffer_load_dwordx4 v22, s[48:51], s61 offen offset:384 lds
s_mov_b32 m0, 143360
M a,96,v,240,v,80
T v,230,11,8192
s_mov_b32 s61, 2294272
buffer_load_dwordx4 v22, s[48:51], s61 offen offset:0 lds
T v,232,8,512
s_mov_b32 m0, 144256
M a,100,v,244,v,80
T v,234,8,8704
buffer_load_dwordx4 v22, s[48:51], s61 offen offset:128 lds
s_mov_b32 m0, 145152
T v,236,11,512
W 7
buffer_load_dwordx4 v22, s[48:51], s61 offen offset:256 lds
s_mov_b32 m0, 146048
M a,96,v,248,v,88
T v,238,11,8704
buffer_load_dwordx4 v22, s[48:51], s61 offen offset:384 lds
s_mov_b32 m0, 147456
T v,240,8,1024
s_mov_b32 s61, 147456
buffer_load_dwordx4 v19, s[36:39], s61 offen offset:0 nt lds
M a,100,v,252,v,88
T v,242,8,9216
s_mov_b32 m0, 148352
T v,244,11,1024
W 7
buffer_load_dwordx4 v19, s[36:39], s61 offen offset:128 nt lds
s_mov_b32 m0, 149248
M v,96,v,224,a,192
T v,246,11,9216
buffer_load_dwordx4 v19, s[36:39], s61 offen offset:256 nt lds
s_mov_b32 m0, 150144
T v,248,8,1536
buffer_load_dwordx4 v19, s[36:39], s61 offen offset:384 nt lds
s_mov_b32 m0, 151552
M v,100,v,228,a,192
T v,250,8,9728
s_mov_b32 s61, 151552
buffer_load_dwordx4 v27, s[36:39], s61 offen offset:0 nt lds
T v,252,11,1536
W 7
s_mov_b32 m0, 152448
M v,112,v,232,a,192
T v,254,11,9728
buffer_load_dwordx4 v27, s[36:39], s61 offen offset:128 nt lds
T v,224,8,2048
s_mov_b32 m0, 153344
A 30,96
M v,116,v,236,a,192
T v,226,8,10240
buffer_load_dwordx4 v27, s[36:39], s61 offen offset:256 nt lds
A 31,97
T v,228,11,2048
W 7
s_mov_b32 m0, 154240
v_max3_f32 v26, v30, v31, v26
M v,128,v,240,a,192
T v,230,11,10240
buffer_load_dwordx4 v27, s[36:39], s61 offen offset:384 nt lds
A 30,98
T v,232,8,2560
M v,132,v,244,a,192
T v,234,8,10752
T v,236,11,2560
W 7
s_mov_b32 m0, 155648
A 31,99
M v,144,v,248,a,192
T v,238,11,10752
s_mov_b32 s61, 155648
v_max3_f32 v26, v30, v31, v26
T v,240,8,3072
buffer_load_dwordx4 v19, s[36:39], s61 offen offset:0 nt lds
A 30,100
M v,148,v,252,a,192
T v,242,8,11264
T v,244,11,3072
W 7
s_mov_b32 m0, 156544
A 31,101
M v,160,v,224,a,192
T v,246,11,11264
buffer_load_dwordx4 v19, s[36:39], s61 offen offset:128 nt lds
v_max3_f32 v26, v30, v31, v26
T v,248,8,3584
M v,164,v,228,a,192
T v,250,8,11776
T v,252,11,3584
W 7
s_mov_b32 m0, 157440
A 30,102
M v,176,v,232,a,192
T v,254,11,11776
buffer_load_dwordx4 v19, s[36:39], s61 offen offset:256 nt lds
A 31,103
s_waitcnt vmcnt(60)
s_barrier
R v,224,2,16384
s_mov_b32 m0, 158336
v_max3_f32 v26, v30, v31, v26
M v,180,v,236,a,192
R v,226,5,16384
buffer_load_dwordx4 v19, s[36:39], s61 offen offset:384 nt lds
s_mov_b32 m0, 159744
R v,228,2,24576
W 7
s_mov_b32 s61, 159744
buffer_load_dwordx4 v27, s[36:39], s61 offen offset:0 nt lds
M v,192,v,240,a,192
R v,230,5,24576
s_mov_b32 m0, 160640
R v,232,2,16896
buffer_load_dwordx4 v27, s[36:39], s61 offen offset:128 nt lds
s_mov_b32 m0, 161536
M v,196,v,244,a,192
R v,234,5,16896
buffer_load_dwordx4 v27, s[36:39], s61 offen offset:256 nt lds
R v,236,2,25088
W 7
M v,208,v,248,a,192
R v,238,5,25088
R v,240,2,17408
s_mov_b32 m0, 162432
M v,212,v,252,a,192
R v,242,5,17408
buffer_load_dwordx4 v27, s[36:39], s61 offen offset:384 nt lds
R v,244,2,25600
W 7
v_mfma_f32_16x16x32_bf16 a[112:115], v[224:227], v[32:35], 0
R v,246,5,25600
R v,248,2,17920
v_and_b32 v25, 7, v0
v_mul_u32_u24 v25, 0x1c000, v25
v_mfma_f32_16x16x32_bf16 a[116:119], v[228:231], v[32:35], 0
R v,250,5,17920
global_load_dword v29, v25, %11 nt
v_mov_b32 v25, 0
R v,252,2,26112
W 7
global_load_dword v29, v25, %8 nt
M a,112,v,232,v,40
R v,254,5,26112
R v,224,2,18432
M a,116,v,236,v,40
R v,226,5,18432
R v,228,2,26624
W 7
M a,112,v,240,v,48
R v,230,5,26624
R v,232,2,18944
s_mov_b32 s61, .Lc0_40_%=-.Lpf_start%=+8192
buffer_load_dword v29, v20, s[92:95], s61 offen
M a,116,v,244,v,48
R v,234,5,18944
R v,236,2,27136
W 7
M a,112,v,248,v,56
R v,238,5,27136
R v,240,2,19456
s_mov_b32 s61, .Lc1_40_%=-.Lpf_start%=+8192
buffer_load_dword v29, v20, s[92:95], s61 offen
M a,116,v,252,v,56
R v,242,5,19456
R v,244,2,27648
W 7
M a,112,v,224,v,64
R v,246,5,27648
R v,248,2,19968
s_mov_b32 m0, 0
s_mov_b32 s61, 2359808
M a,116,v,228,v,64
R v,250,5,19968
buffer_load_dwordx4 v22, s[48:51], s61 offen offset:0 lds
s_mov_b32 m0, 896
R v,252,2,28160
W 7
buffer_load_dwordx4 v22, s[48:51], s61 offen offset:128 lds
s_mov_b32 m0, 1792
M a,112,v,232,v,72
R v,254,5,28160
buffer_load_dwordx4 v22, s[48:51], s61 offen offset:256 lds
s_mov_b32 m0, 2688
s_waitcnt vmcnt(56)
s_barrier
T v,224,8,32768
buffer_load_dwordx4 v22, s[48:51], s61 offen offset:384 lds
s_mov_b32 m0, 4096
M a,116,v,236,v,72
T v,226,8,40960
s_mov_b32 s61, 2425344
buffer_load_dwordx4 v22, s[48:51], s61 offen offset:0 lds
T v,228,11,32768
W 7
s_mov_b32 m0, 4992
M a,112,v,240,v,80
T v,230,11,40960
buffer_load_dwordx4 v22, s[48:51], s61 offen offset:128 lds
s_mov_b32 m0, 5888
T v,232,8,33280
buffer_load_dwordx4 v22, s[48:51], s61 offen offset:256 lds
s_mov_b32 m0, 6784
M a,116,v,244,v,80
T v,234,8,41472
buffer_load_dwordx4 v22, s[48:51], s61 offen offset:384 lds
s_mov_b32 m0, 8192
T v,236,11,33280
W 7
s_mov_b32 s61, 2490880
buffer_load_dwordx4 v22, s[48:51], s61 offen offset:0 lds
M a,112,v,248,v,88
T v,238,11,41472
s_mov_b32 m0, 9088
T v,240,8,33792
buffer_load_dwordx4 v22, s[48:51], s61 offen offset:128 lds
s_mov_b32 m0, 9984
M a,116,v,252,v,88
T v,242,8,41984
buffer_load_dwordx4 v22, s[48:51], s61 offen offset:256 lds
s_mov_b32 m0, 10880
T v,244,11,33792
W 7
buffer_load_dwordx4 v22, s[48:51], s61 offen offset:384 lds
s_mov_b32 m0, 12288
M v,96,v,224,a,200
T v,246,11,41984
s_mov_b32 s61, 2556416
buffer_load_dwordx4 v22, s[48:51], s61 offen offset:0 lds
T v,248,8,34304
s_mov_b32 m0, 13184
M v,100,v,228,a,200
T v,250,8,42496
buffer_load_dwordx4 v22, s[48:51], s61 offen offset:128 lds
s_mov_b32 m0, 14080
T v,252,11,34304
W 7
buffer_load_dwordx4 v22, s[48:51], s61 offen offset:256 lds
s_mov_b32 m0, 14976
M v,112,v,232,a,200
T v,254,11,42496
buffer_load_dwordx4 v22, s[48:51], s61 offen offset:384 lds
s_mul_i32 s61, %9, 56
T v,224,8,34816
v_add_u32 v25, s61, v0
A 30,112
M v,116,v,236,a,200
T v,226,8,43008
v_cmp_gt_u32 vcc, 56, v0
A 31,113
T v,228,11,34816
W 7
v_lshlrev_b32 v25, 7, v25
v_max3_f32 v26, v30, v31, v26
M v,128,v,240,a,200
T v,230,11,43008
s_mov_b32 s61, 114688
A 30,114
T v,232,8,35328
v_cndmask_b32 v25, 2.0, v25, vcc
A 31,115
M v,132,v,244,a,200
T v,234,8,43520
)x"
R"x(buffer_load_dword v29, v25, s[52:55], s61 offen
v_max3_f32 v26, v30, v31, v26
T v,236,11,35328
W 7
s_mov_b32 m0, 16384
A 30,116
M v,144,v,248,a,200
T v,238,11,43520
s_mov_b32 s61, 163840
A 31,117
T v,240,8,35840
buffer_load_dwordx4 v19, s[36:39], s61 offen offset:0 nt lds
v_max3_f32 v26, v30, v31, v26
M v,148,v,252,a,200
T v,242,8,44032
s_mov_b32 m0, 17280
A 30,118
T v,244,11,35840
W 7
buffer_load_dwordx4 v19, s[36:39], s61 offen offset:128 nt lds
A 31,119
M v,160,v,224,a,200
T v,246,11,44032
s_mov_b32 m0, 18176
v_max3_f32 v26, v30, v31, v26
T v,248,8,36352
buffer_load_dwordx4 v19, s[36:39], s61 offen offset:256 nt lds
s_mov_b32 m0, 19072
M v,164,v,228,a,200
T v,250,8,44544
buffer_load_dwordx4 v19, s[36:39], s61 offen offset:384 nt lds
s_mov_b32 m0, 20480
T v,252,11,36352
W 7
s_mov_b32 s61, 167936
buffer_load_dwordx4 v27, s[36:39], s61 offen offset:0 nt lds
M v,176,v,232,a,200
T v,254,11,44544
s_mov_b32 m0, 21376
s_waitcnt vmcnt(58)
s_barrier
R v,224,2,49152
buffer_load_dwordx4 v27, s[36:39], s61 offen offset:128 nt lds
s_mov_b32 m0, 22272
M v,180,v,236,a,200
R v,226,5,49152
buffer_load_dwordx4 v27, s[36:39], s61 offen offset:256 nt lds
s_mov_b32 m0, 23168
R v,228,2,57344
W 7
buffer_load_dwordx4 v27, s[36:39], s61 offen offset:384 nt lds
s_mov_b32 m0, 24576
M v,192,v,240,a,200
R v,230,5,57344
s_mov_b32 s61, 172032
buffer_load_dwordx4 v19, s[36:39], s61 offen offset:0 nt lds
R v,232,2,49664
s_mov_b32 m0, 25472
M v,196,v,244,a,200
R v,234,5,49664
buffer_load_dwordx4 v19, s[36:39], s61 offen offset:128 nt lds
s_mov_b32 m0, 26368
R v,236,2,57856
W 7
buffer_load_dwordx4 v19, s[36:39], s61 offen offset:256 nt lds
M v,208,v,248,a,200
R v,238,5,57856
R v,240,2,50176
s_mov_b32 m0, 27264
M v,212,v,252,a,200
R v,242,5,50176
buffer_load_dwordx4 v19, s[36:39], s61 offen offset:384 nt lds
R v,244,2,58368
W 7
v_mfma_f32_16x16x32_bf16 a[128:131], v[224:227], v[32:35], 0
R v,246,5,58368
R v,248,2,50688
s_mov_b32 m0, 28672
s_mov_b32 s61, 176128
v_mfma_f32_16x16x32_bf16 a[132:135], v[228:231], v[32:35], 0
R v,250,5,50688
buffer_load_dwordx4 v27, s[36:39], s61 offen offset:0 nt lds
R v,252,2,58880
W 7
M a,128,v,232,v,40
R v,254,5,58880
R v,224,2,51200
s_mov_b32 m0, 29568
M a,132,v,236,v,40
R v,226,5,51200
buffer_load_dwordx4 v27, s[36:39], s61 offen offset:128 nt lds
R v,228,2,59392
W 7
M a,128,v,240,v,48
R v,230,5,59392
R v,232,2,51712
s_mov_b32 m0, 30464
M a,132,v,244,v,48
R v,234,5,51712
buffer_load_dwordx4 v27, s[36:39], s61 offen offset:256 nt lds
R v,236,2,59904
W 7
M a,128,v,248,v,56
R v,238,5,59904
R v,240,2,52224
s_mov_b32 m0, 31360
M a,132,v,252,v,56
R v,242,5,52224
buffer_load_dwordx4 v27, s[36:39], s61 offen offset:384 nt lds
R v,244,2,60416
W 7
M a,128,v,224,v,64
R v,246,5,60416
R v,248,2,52736
s_mov_b32 m0, 32768
s_mov_b32 s61, 2621952
M a,132,v,228,v,64
R v,250,5,52736
buffer_load_dwordx4 v22, s[48:51], s61 offen offset:0 lds
s_mov_b32 m0, 33664
R v,252,2,60928
W 7
buffer_load_dwordx4 v22, s[48:51], s61 offen offset:128 lds
s_mov_b32 m0, 34560
M a,128,v,232,v,72
R v,254,5,60928
buffer_load_dwordx4 v22, s[48:51], s61 offen offset:256 lds
s_mov_b32 m0, 35456
s_waitcnt vmcnt(56)
s_barrier
.Lc1_46_%=:
T v,224,9,0
buffer_load_dwordx4 v22, s[48:51], s61 offen offset:384 lds
s_mov_b32 m0, 36864
M a,132,v,236,v,72
T v,226,9,8192
s_mov_b32 s61, 2687488
buffer_load_dwordx4 v22, s[48:51], s61 offen offset:0 lds
T v,228,12,0
W 7
s_mov_b32 m0, 37760
M a,128,v,240,v,80
T v,230,12,8192
buffer_load_dwordx4 v22, s[48:51], s61 offen offset:128 lds
s_mov_b32 m0, 38656
T v,232,9,512
buffer_load_dwordx4 v22, s[48:51], s61 offen offset:256 lds
s_mov_b32 m0, 39552
M a,132,v,244,v,80
T v,234,9,8704
buffer_load_dwordx4 v22, s[48:51], s61 offen offset:384 lds
s_mov_b32 m0, 40960
T v,236,12,512
W 7
s_mov_b32 s61, 2753024
buffer_load_dwordx4 v22, s[48:51], s61 offen offset:0 lds
M a,128,v,248,v,88
T v,238,12,8704
s_mov_b32 m0, 41856
T v,240,9,1024
buffer_load_dwordx4 v22, s[48:51], s61 offen offset:128 lds
s_mov_b32 m0, 42752
M a,132,v,252,v,88
T v,242,9,9216
buffer_load_dwordx4 v22, s[48:51], s61 offen offset:256 lds
s_mov_b32 m0, 43648
T v,244,12,1024
W 7
buffer_load_dwordx4 v22, s[48:51], s61 offen offset:384 lds
s_mov_b32 m0, 45056
M v,96,v,224,a,208
T v,246,12,9216
s_mov_b32 s61, 2818560
buffer_load_dwordx4 v22, s[48:51], s61 offen offset:0 lds
T v,248,9,1536
s_mov_b32 m0, 45952
M v,100,v,228,a,208
T v,250,9,9728
buffer_load_dwordx4 v22, s[48:51], s61 offen offset:128 lds
s_mov_b32 m0, 46848
T v,252,12,1536
W 7
buffer_load_dwordx4 v22, s[48:51], s61 offen offset:256 lds
s_mov_b32 m0, 47744
M v,112,v,232,a,208
T v,254,12,9728
buffer_load_dwordx4 v22, s[48:51], s61 offen offset:384 lds
s_mov_b32 m0, 49152
T v,224,9,2048
s_mov_b32 s61, 180224
A 30,128
M v,116,v,236,a,208
T v,226,9,10240
buffer_load_dwordx4 v19, s[36:39], s61 offen offset:0 nt lds
A 31,129
T v,228,12,2048
W 7
s_mov_b32 m0, 50048
v_max3_f32 v26, v30, v31, v26
M v,128,v,240,a,208
T v,230,12,10240
buffer_load_dwordx4 v19, s[36:39], s61 offen offset:128 nt lds
A 30,130
T v,232,9,2560
s_mov_b32 m0, 50944
A 31,131
M v,132,v,244,a,208
T v,234,9,10752
buffer_load_dwordx4 v19, s[36:39], s61 offen offset:256 nt lds
v_max3_f32 v26, v30, v31, v26
T v,236,12,2560
W 7
s_mov_b32 m0, 51840
A 30,132
M v,144,v,248,a,208
T v,238,12,10752
buffer_load_dwordx4 v19, s[36:39], s61 offen offset:384 nt lds
A 31,133
T v,240,9,3072
s_mov_b32 m0, 53248
v_max3_f32 v26, v30, v31, v26
M v,148,v,252,a,208
T v,242,9,11264
s_mov_b32 s61, 184320
A 30,134
T v,244,12,3072
W 7
buffer_load_dwordx4 v27, s[36:39], s61 offen offset:0 nt lds
A 31,135
M v,160,v,224,a,208
T v,246,12,11264
s_mov_b32 m0, 54144
v_max3_f32 v26, v30, v31, v26
T v,248,9,3584
buffer_load_dwordx4 v27, s[36:39], s61 offen offset:128 nt lds
s_mov_b32 m0, 55040
M v,164,v,228,a,208
T v,250,9,11776
buffer_load_dwordx4 v27, s[36:39], s61 offen offset:256 nt lds
s_mov_b32 m0, 55936
T v,252,12,3584
W 7
buffer_load_dwordx4 v27, s[36:39], s61 offen offset:384 nt lds
s_mov_b32 m0, 57344
M v,176,v,232,a,208
T v,254,12,11776
s_mov_b32 s61, 188416
buffer_load_dwordx4 v19, s[36:39], s61 offen offset:0 nt lds
s_waitcnt vmcnt(62)
s_barrier
R v,224,3,16384
s_mov_b32 m0, 58240
M v,180,v,236,a,208
R v,226,6,16384
buffer_load_dwordx4 v19, s[36:39], s61 offen offset:128 nt lds
R v,228,3,24576
W 7
M v,192,v,240,a,208
R v,230,6,24576
R v,232,3,16896
s_mov_b32 m0, 59136
M v,196,v,244,a,208
R v,234,6,16896
buffer_load_dwordx4 v19, s[36:39], s61 offen offset:256 nt lds
R v,236,3,25088
W 7
M v,208,v,248,a,208
R v,238,6,25088
R v,240,3,17408
s_mov_b32 m0, 60032
M v,212,v,252,a,208
R v,242,6,17408
buffer_load_dwordx4 v19, s[36:39], s61 offen offset:384 nt lds
R v,244,3,25600
W 7
v_mfma_f32_16x16x32_bf16 a[144:147], v[224:227], v[32:35], 0
R v,246,6,25600
R v,248,3,17920
s_mov_b32 m0, 61440
s_mov_b32 s61, 192512
v_mfma_f32_16x16x32_bf16 a[148:151], v[228:231], v[32:35], 0
R v,250,6,17920
buffer_load_dwordx4 v27, s[36:39], s61 offen offset:0 nt lds
R v,252,3,26112
W 7
M a,144,v,232,v,40
R v,254,6,26112
R v,224,3,18432
s_mov_b32 m0, 62336
M a,148,v,236,v,40
R v,226,6,18432
buffer_load_dwordx4 v27, s[36:39], s61 offen offset:128 nt lds
R v,228,3,26624
W 7
M a,144,v,240,v,48
R v,230,6,26624
R v,232,3,18944
s_mov_b32 m0, 63232
M a,148,v,244,v,48
R v,234,6,18944
buffer_load_dwordx4 v27, s[36:39], s61 offen offset:256 nt lds
R v,236,3,27136
W 7
M a,144,v,248,v,56
R v,238,6,27136
R v,240,3,19456
s_mov_b32 m0, 64128
M a,148,v,252,v,56
R v,242,6,19456
buffer_load_dwordx4 v27, s[36:39], s61 offen offset:384 nt lds
R v,244,3,27648
W 7
M a,144,v,224,v,64
R v,246,6,27648
R v,248,3,19968
s_mov_b32 m0, 65536
s_mov_b32 s61, 2884096
M a,148,v,228,v,64
R v,250,6,19968
buffer_load_dwordx4 v22, s[48:51], s61 offen offset:0 lds
s_mov_b32 m0, 66432
R v,252,3,28160
W 7
buffer_load_dwordx4 v22, s[48:51], s61 offen offset:128 lds
s_mov_b32 m0, 67328
M a,144,v,232,v,72
R v,254,6,28160
buffer_load_dwordx4 v22, s[48:51], s61 offen offset:256 lds
s_mov_b32 m0, 68224
s_waitcnt vmcnt(52)
s_barrier
T v,224,7,0
buffer_load_dwordx4 v22, s[48:51], s61 offen offset:384 lds
s_mov_b32 m0, 69632
M a,148,v,236,v,72
T v,226,7,8192
s_mov_b32 s61, 2949632
buffer_load_dwordx4 v22, s[48:51], s61 offen offset:0 lds
T v,228,10,0
W 7
s_mov_b32 m0, 70528
M a,144,v,240,v,80
T v,230,10,8192
buffer_load_dwordx4 v22, s[48:51], s61 offen offset:128 lds
s_mov_b32 m0, 71424
T v,232,7,512
buffer_load_dwordx4 v22, s[48:51], s61 offen offset:256 lds
s_mov_b32 m0, 72320
M a,148,v,244,v,80
T v,234,7,8704
buffer_load_dwordx4 v22, s[48:51], s61 offen offset:384 lds
s_mov_b32 m0, 73728
T v,236,10,512
W 7
s_mov_b32 s61, 3015168
buffer_load_dwordx4 v22, s[48:51], s61 offen offset:0 lds
M a,144,v,248,v,88
T v,238,10,8704
s_mov_b32 m0, 74624
T v,240,7,1024
buffer_load_dwordx4 v22, s[48:51], s61 offen offset:128 lds
s_mov_b32 m0, 75520
M a,148,v,252,v,88
T v,242,7,9216
buffer_load_dwordx4 v22, s[48:51], s61 offen offset:256 lds
s_mov_b32 m0, 76416
T v,244,10,1024
W 7
buffer_load_dwordx4 v22, s[48:51], s61 offen offset:384 lds
s_mov_b32 m0, 77824
M v,96,v,224,a,216
T v,246,10,9216
s_mov_b32 s61, 3080704
buffer_load_dwordx4 v22, s[48:51], s61 offen offset:0 lds
T v,248,7,1536
s_mov_b32 m0, 78720
M v,100,v,228,a,216
T v,250,7,9728
buffer_load_dwordx4 v22, s[48:51], s61 offen offset:128 lds
s_mov_b32 m0, 79616
T v,252,10,1536
W 7
buffer_load_dwordx4 v22, s[48:51], s61 offen offset:256 lds
s_mov_b32 m0, 80512
M v,112,v,232,a,216
T v,254,10,9728
buffer_load_dwordx4 v22, s[48:51], s61 offen offset:384 lds
s_mov_b32 m0, 81920
T v,224,7,2048
s_mov_b32 s61, 196608
A 30,144
M v,116,v,236,a,216
T v,226,7,10240
buffer_load_dwordx4 v19, s[36:39], s61 offen offset:0 nt lds
A 31,145
T v,228,10,2048
W 7
s_mov_b32 m0, 82816
v_max3_f32 v26, v30, v31, v26
M v,128,v,240,a,216
)x"
R"x(T v,230,10,10240
buffer_load_dwordx4 v19, s[36:39], s61 offen offset:128 nt lds
A 30,146
T v,232,7,2560
s_mov_b32 m0, 83712
A 31,147
M v,132,v,244,a,216
T v,234,7,10752
buffer_load_dwordx4 v19, s[36:39], s61 offen offset:256 nt lds
v_max3_f32 v26, v30, v31, v26
T v,236,10,2560
W 7
s_mov_b32 m0, 84608
A 30,148
M v,144,v,248,a,216
T v,238,10,10752
buffer_load_dwordx4 v19, s[36:39], s61 offen offset:384 nt lds
A 31,149
T v,240,7,3072
s_mov_b32 m0, 86016
v_max3_f32 v26, v30, v31, v26
M v,148,v,252,a,216
T v,242,7,11264
s_mov_b32 s61, 200704
A 30,150
T v,244,10,3072
W 7
buffer_load_dwordx4 v27, s[36:39], s61 offen offset:0 nt lds
A 31,151
M v,160,v,224,a,216
T v,246,10,11264
s_mov_b32 m0, 86912
v_max3_f32 v26, v30, v31, v26
T v,248,7,3584
buffer_load_dwordx4 v27, s[36:39], s61 offen offset:128 nt lds
s_mov_b32 m0, 87808
M v,164,v,228,a,216
T v,250,7,11776
buffer_load_dwordx4 v27, s[36:39], s61 offen offset:256 nt lds
s_mov_b32 m0, 88704
T v,252,10,3584
W 7
buffer_load_dwordx4 v27, s[36:39], s61 offen offset:384 nt lds
s_mov_b32 m0, 90112
M v,176,v,232,a,216
T v,254,10,11776
s_mov_b32 s61, 204800
buffer_load_dwordx4 v19, s[36:39], s61 offen offset:0 nt lds
s_waitcnt vmcnt(57)
s_barrier
R v,224,1,16384
s_mov_b32 m0, 91008
M v,180,v,236,a,216
R v,226,4,16384
buffer_load_dwordx4 v19, s[36:39], s61 offen offset:128 nt lds
s_mov_b32 m0, 91904
R v,228,1,24576
W 7
buffer_load_dwordx4 v19, s[36:39], s61 offen offset:256 nt lds
s_mov_b32 m0, 92800
M v,192,v,240,a,216
R v,230,4,24576
buffer_load_dwordx4 v19, s[36:39], s61 offen offset:384 nt lds
s_mov_b32 m0, 94208
R v,232,1,16896
s_mov_b32 s61, 208896
buffer_load_dwordx4 v27, s[36:39], s61 offen offset:0 nt lds
M v,196,v,244,a,216
R v,234,4,16896
s_mov_b32 m0, 95104
R v,236,1,25088
W 7
buffer_load_dwordx4 v27, s[36:39], s61 offen offset:128 nt lds
s_mov_b32 m0, 96000
M v,208,v,248,a,216
R v,238,4,25088
buffer_load_dwordx4 v27, s[36:39], s61 offen offset:256 nt lds
s_mov_b32 m0, 96896
R v,240,1,17408
buffer_load_dwordx4 v27, s[36:39], s61 offen offset:384 nt lds
s_mov_b32 s61, .Lc0_46_%=-.Lpf_start%=+8192
M v,212,v,252,a,216
R v,242,4,17408
buffer_load_dword v29, v20, s[92:95], s61 offen
R v,244,1,25600
W 7
v_mfma_f32_16x16x32_bf16 a[160:163], v[224:227], v[32:35], 0
R v,246,4,25600
R v,248,1,17920
s_mov_b32 s61, .Lc1_46_%=-.Lpf_start%=+8192
buffer_load_dword v29, v20, s[92:95], s61 offen
v_mfma_f32_16x16x32_bf16 a[164:167], v[228:231], v[32:35], 0
R v,250,4,17920
R v,252,1,26112
W 7
M a,160,v,232,v,40
R v,254,4,26112
R v,224,1,18432
s_mov_b32 m0, 98304
s_mov_b32 s61, 3146240
M a,164,v,236,v,40
R v,226,4,18432
buffer_load_dwordx4 v22, s[48:51], s61 offen offset:0 lds
s_mov_b32 m0, 99200
R v,228,1,26624
W 7
buffer_load_dwordx4 v22, s[48:51], s61 offen offset:128 lds
s_mov_b32 m0, 100096
M a,160,v,240,v,48
R v,230,4,26624
buffer_load_dwordx4 v22, s[48:51], s61 offen offset:256 lds
s_mov_b32 m0, 100992
R v,232,1,18944
buffer_load_dwordx4 v22, s[48:51], s61 offen offset:384 lds
s_mov_b32 m0, 102400
M a,164,v,244,v,48
R v,234,4,18944
s_mov_b32 s61, 3211776
buffer_load_dwordx4 v22, s[48:51], s61 offen offset:0 lds
R v,236,1,27136
W 7
s_mov_b32 m0, 103296
M a,160,v,248,v,56
R v,238,4,27136
buffer_load_dwordx4 v22, s[48:51], s61 offen offset:128 lds
s_mov_b32 m0, 104192
R v,240,1,19456
buffer_load_dwordx4 v22, s[48:51], s61 offen offset:256 lds
s_mov_b32 m0, 105088
M a,164,v,252,v,56
R v,242,4,19456
buffer_load_dwordx4 v22, s[48:51], s61 offen offset:384 lds
s_mov_b32 m0, 106496
R v,244,1,27648
W 7
s_mov_b32 s61, 3277312
buffer_load_dwordx4 v22, s[48:51], s61 offen offset:0 lds
M a,160,v,224,v,64
R v,246,4,27648
s_mov_b32 m0, 107392
R v,248,1,19968
buffer_load_dwordx4 v22, s[48:51], s61 offen offset:128 lds
s_mov_b32 m0, 108288
M a,164,v,228,v,64
R v,250,4,19968
buffer_load_dwordx4 v22, s[48:51], s61 offen offset:256 lds
s_mov_b32 m0, 109184
R v,252,1,28160
W 7
buffer_load_dwordx4 v22, s[48:51], s61 offen offset:384 lds
s_mov_b32 m0, 110592
M a,160,v,232,v,72
R v,254,4,28160
s_mov_b32 s61, 3342848
buffer_load_dwordx4 v22, s[48:51], s61 offen offset:0 lds
s_waitcnt vmcnt(63)
s_barrier
T v,224,7,32768
s_mov_b32 m0, 111488
M a,164,v,236,v,72
T v,226,7,40960
buffer_load_dwordx4 v22, s[48:51], s61 offen offset:128 lds
s_mov_b32 m0, 112384
T v,228,10,32768
W 7
buffer_load_dwordx4 v22, s[48:51], s61 offen offset:256 lds
s_mov_b32 m0, 113280
M a,160,v,240,v,80
T v,230,10,40960
buffer_load_dwordx4 v22, s[48:51], s61 offen offset:384 lds
s_mov_b32 m0, 114688
T v,232,7,33280
s_mov_b32 s61, 212992
buffer_load_dwordx4 v19, s[36:39], s61 offen offset:0 nt lds
M a,164,v,244,v,80
T v,234,7,41472
s_mov_b32 m0, 115584
T v,236,10,33280
W 7
buffer_load_dwordx4 v19, s[36:39], s61 offen offset:128 nt lds
s_mov_b32 m0, 116480
M a,160,v,248,v,88
T v,238,10,41472
buffer_load_dwordx4 v19, s[36:39], s61 offen offset:256 nt lds
s_mov_b32 m0, 117376
T v,240,7,33792
buffer_load_dwordx4 v19, s[36:39], s61 offen offset:384 nt lds
s_mov_b32 m0, 118784
M a,164,v,252,v,88
T v,242,7,41984
s_mov_b32 s61, 217088
buffer_load_dwordx4 v27, s[36:39], s61 offen offset:0 nt lds
T v,244,10,33792
W 7
s_mov_b32 m0, 119680
M v,96,v,224,a,224
T v,246,10,41984
buffer_load_dwordx4 v27, s[36:39], s61 offen offset:128 nt lds
T v,248,7,34304
s_mov_b32 m0, 120576
M v,100,v,228,a,224
T v,250,7,42496
buffer_load_dwordx4 v27, s[36:39], s61 offen offset:256 nt lds
T v,252,10,34304
W 7
M v,112,v,232,a,224
T v,254,10,42496
T v,224,7,34816
s_mov_b32 m0, 121472
A 30,160
M v,116,v,236,a,224
T v,226,7,43008
buffer_load_dwordx4 v27, s[36:39], s61 offen offset:384 nt lds
A 31,161
T v,228,10,34816
W 7
s_mov_b32 m0, 122880
v_max3_f32 v26, v30, v31, v26
M v,128,v,240,a,224
T v,230,10,43008
s_mov_b32 s61, 221184
A 30,162
T v,232,7,35328
buffer_load_dwordx4 v19, s[36:39], s61 offen offset:0 nt lds
A 31,163
M v,132,v,244,a,224
T v,234,7,43520
T v,236,10,35328
W 7
s_mov_b32 m0, 123776
v_max3_f32 v26, v30, v31, v26
M v,144,v,248,a,224
T v,238,10,43520
buffer_load_dwordx4 v19, s[36:39], s61 offen offset:128 nt lds
A 30,164
T v,240,7,35840
M v,148,v,252,a,224
T v,242,7,44032
T v,244,10,35840
W 7
s_mov_b32 m0, 124672
A 31,165
M v,160,v,224,a,224
T v,246,10,44032
buffer_load_dwordx4 v19, s[36:39], s61 offen offset:256 nt lds
v_max3_f32 v26, v30, v31, v26
T v,248,7,36352
M v,164,v,228,a,224
T v,250,7,44544
T v,252,10,36352
W 7
s_mov_b32 m0, 125568
A 30,166
M v,176,v,232,a,224
T v,254,10,44544
buffer_load_dwordx4 v19, s[36:39], s61 offen offset:384 nt lds
A 31,167
s_waitcnt vmcnt(62)
s_barrier
R v,224,1,49152
s_mov_b32 m0, 126976
v_max3_f32 v26, v30, v31, v26
M v,180,v,236,a,224
R v,226,4,49152
s_mov_b32 s61, 225280
buffer_load_dwordx4 v27, s[36:39], s61 offen offset:0 nt lds
R v,228,1,57344
W 7
M v,192,v,240,a,224
R v,230,4,57344
R v,232,1,49664
s_mov_b32 m0, 127872
M v,196,v,244,a,224
R v,234,4,49664
buffer_load_dwordx4 v27, s[36:39], s61 offen offset:128 nt lds
R v,236,1,57856
W 7
M v,208,v,248,a,224
R v,238,4,57856
R v,240,1,50176
s_mov_b32 m0, 128768
M v,212,v,252,a,224
R v,242,4,50176
buffer_load_dwordx4 v27, s[36:39], s61 offen offset:256 nt lds
R v,244,1,58368
W 7
v_mfma_f32_16x16x32_bf16 a[176:179], v[224:227], v[32:35], 0
R v,246,4,58368
R v,248,1,50688
s_mov_b32 m0, 129664
v_mfma_f32_16x16x32_bf16 a[180:183], v[228:231], v[32:35], 0
R v,250,4,50688
buffer_load_dwordx4 v27, s[36:39], s61 offen offset:384 nt lds
R v,252,1,58880
W 7
M a,176,v,232,v,40
R v,254,4,58880
R v,224,1,51200
s_mov_b32 m0, 131072
s_mov_b32 s61, 3408384
M a,180,v,236,v,40
R v,226,4,51200
buffer_load_dwordx4 v22, s[48:51], s61 offen offset:0 lds
s_mov_b32 m0, 131968
R v,228,1,59392
W 7
buffer_load_dwordx4 v22, s[48:51], s61 offen offset:128 lds
s_mov_b32 m0, 132864
M a,176,v,240,v,48
R v,230,4,59392
buffer_load_dwordx4 v22, s[48:51], s61 offen offset:256 lds
s_mov_b32 m0, 133760
R v,232,1,51712
buffer_load_dwordx4 v22, s[48:51], s61 offen offset:384 lds
s_mov_b32 m0, 135168
M a,180,v,244,v,48
R v,234,4,51712
s_mov_b32 s61, 3473920
buffer_load_dwordx4 v22, s[48:51], s61 offen offset:0 lds
R v,236,1,59904
W 7
s_mov_b32 m0, 136064
M a,176,v,248,v,56
R v,238,4,59904
buffer_load_dwordx4 v22, s[48:51], s61 offen offset:128 lds
s_mov_b32 m0, 136960
R v,240,1,52224
buffer_load_dwordx4 v22, s[48:51], s61 offen offset:256 lds
s_mov_b32 m0, 137856
M a,180,v,252,v,56
R v,242,4,52224
buffer_load_dwordx4 v22, s[48:51], s61 offen offset:384 lds
s_mov_b32 m0, 139264
R v,244,1,60416
W 7
s_mov_b32 s61, 3539456
buffer_load_dwordx4 v22, s[48:51], s61 offen offset:0 lds
M a,176,v,224,v,64
R v,246,4,60416
s_mov_b32 m0, 140160
R v,248,1,52736
buffer_load_dwordx4 v22, s[48:51], s61 offen offset:128 lds
s_mov_b32 m0, 141056
M a,180,v,228,v,64
R v,250,4,52736
buffer_load_dwordx4 v22, s[48:51], s61 offen offset:256 lds
s_mov_b32 m0, 141952
R v,252,1,60928
W 7
buffer_load_dwordx4 v22, s[48:51], s61 offen offset:384 lds
s_mov_b32 m0, 143360
M a,176,v,232,v,72
R v,254,4,60928
s_mov_b32 s61, 3604992
buffer_load_dwordx4 v22, s[48:51], s61 offen offset:0 lds
s_waitcnt vmcnt(63)
s_barrier
.Lc1_52_%=:
T v,224,8,0
s_mov_b32 m0, 144256
M a,180,v,236,v,72
T v,226,8,8192
buffer_load_dwordx4 v22, s[48:51], s61 offen offset:128 lds
s_mov_b32 m0, 145152
T v,228,11,0
W 7
buffer_load_dwordx4 v22, s[48:51], s61 offen offset:256 lds
s_mov_b32 m0, 146048
M a,176,v,240,v,80
T v,230,11,8192
buffer_load_dwordx4 v22, s[48:51], s61 offen offset:384 lds
s_mov_b32 m0, 147456
T v,232,8,512
s_mov_b32 s61, 229376
buffer_load_dwordx4 v19, s[36:39], s61 offen offset:0 nt lds
M a,180,v,244,v,80
T v,234,8,8704
s_mov_b32 m0, 148352
T v,236,11,512
W 7
buffer_load_dwordx4 v19, s[36:39], s61 offen offset:128 nt lds
s_mov_b32 m0, 149248
M a,176,v,248,v,88
T v,238,11,8704
buffer_load_dwordx4 v19, s[36:39], s61 offen offset:256 nt lds
s_mov_b32 m0, 150144
T v,240,8,1024
buffer_load_dwordx4 v19, s[36:39], s61 offen offset:384 nt lds
s_mov_b32 m0, 151552
M a,180,v,252,v,88
T v,242,8,9216
s_mov_b32 s61, 233472
buffer_load_dwordx4 v27, s[36:39], s61 offen offset:0 nt lds
T v,244,11,1024
W 7
s_mov_b32 m0, 152448
M v,96,v,224,a,232
T v,246,11,9216
buffer_load_dwordx4 v27, s[36:39], s61 offen offset:128 nt lds
T v,248,8,1536
)x"
R"x(s_mov_b32 m0, 153344
M v,100,v,228,a,232
T v,250,8,9728
buffer_load_dwordx4 v27, s[36:39], s61 offen offset:256 nt lds
T v,252,11,1536
W 7
M v,112,v,232,a,232
T v,254,11,9728
T v,224,8,2048
s_mov_b32 m0, 154240
A 30,176
M v,116,v,236,a,232
T v,226,8,10240
buffer_load_dwordx4 v27, s[36:39], s61 offen offset:384 nt lds
A 31,177
T v,228,11,2048
W 7
s_mov_b32 m0, 155648
v_max3_f32 v26, v30, v31, v26
M v,128,v,240,a,232
T v,230,11,10240
s_mov_b32 s61, 237568
A 30,178
T v,232,8,2560
buffer_load_dwordx4 v19, s[36:39], s61 offen offset:0 nt lds
A 31,179
M v,132,v,244,a,232
T v,234,8,10752
T v,236,11,2560
W 7
s_mov_b32 m0, 156544
v_max3_f32 v26, v30, v31, v26
M v,144,v,248,a,232
T v,238,11,10752
buffer_load_dwordx4 v19, s[36:39], s61 offen offset:128 nt lds
A 30,180
T v,240,8,3072
M v,148,v,252,a,232
T v,242,8,11264
T v,244,11,3072
W 7
s_mov_b32 m0, 157440
A 31,181
M v,160,v,224,a,232
T v,246,11,11264
buffer_load_dwordx4 v19, s[36:39], s61 offen offset:256 nt lds
v_max3_f32 v26, v30, v31, v26
T v,248,8,3584
M v,164,v,228,a,232
T v,250,8,11776
T v,252,11,3584
W 7
s_mov_b32 m0, 158336
A 30,182
M v,176,v,232,a,232
T v,254,11,11776
buffer_load_dwordx4 v19, s[36:39], s61 offen offset:384 nt lds
A 31,183
s_waitcnt vmcnt(62)
s_barrier
R v,224,2,16384
s_mov_b32 m0, 159744
v_max3_f32 v26, v30, v31, v26
M v,180,v,236,a,232
R v,226,5,16384
s_mov_b32 s61, 241664
buffer_load_dwordx4 v27, s[36:39], s61 offen offset:0 nt lds
R v,228,2,24576
W 7
M v,192,v,240,a,232
R v,230,5,24576
R v,232,2,16896
s_mov_b32 m0, 160640
M v,196,v,244,a,232
R v,234,5,16896
buffer_load_dwordx4 v27, s[36:39], s61 offen offset:128 nt lds
R v,236,2,25088
W 7
M v,208,v,248,a,232
R v,238,5,25088
R v,240,2,17408
s_mov_b32 m0, 161536
M v,212,v,252,a,232
R v,242,5,17408
buffer_load_dwordx4 v27, s[36:39], s61 offen offset:256 nt lds
R v,244,2,25600
W 7
v_mfma_f32_16x16x32_bf16 a[192:195], v[224:227], v[32:35], 0
R v,246,5,25600
R v,248,2,17920
s_mov_b32 m0, 162432
v_mfma_f32_16x16x32_bf16 a[196:199], v[228:231], v[32:35], 0
R v,250,5,17920
buffer_load_dwordx4 v27, s[36:39], s61 offen offset:384 nt lds
R v,252,2,26112
W 7
M a,192,v,232,v,40
R v,254,5,26112
R v,224,2,18432
s_mov_b32 m0, 0
s_mov_b32 s61, 245760
M a,196,v,236,v,40
R v,226,5,18432
buffer_load_dwordx4 v19, s[36:39], s61 offen offset:0 nt lds
R v,228,2,26624
W 7
M a,192,v,240,v,48
R v,230,5,26624
R v,232,2,18944
s_mov_b32 m0, 896
M a,196,v,244,v,48
R v,234,5,18944
buffer_load_dwordx4 v19, s[36:39], s61 offen offset:128 nt lds
R v,236,2,27136
W 7
M a,192,v,248,v,56
R v,238,5,27136
R v,240,2,19456
s_mov_b32 m0, 1792
M a,196,v,252,v,56
R v,242,5,19456
buffer_load_dwordx4 v19, s[36:39], s61 offen offset:256 nt lds
R v,244,2,27648
W 7
M a,192,v,224,v,64
R v,246,5,27648
R v,248,2,19968
s_mov_b32 m0, 2688
M a,196,v,228,v,64
R v,250,5,19968
buffer_load_dwordx4 v19, s[36:39], s61 offen offset:384 nt lds
R v,252,2,28160
W 7
M a,192,v,232,v,72
R v,254,5,28160
s_waitcnt vmcnt(52)
s_barrier
T v,224,8,32768
s_mov_b32 m0, 4096
s_mov_b32 s61, 249856
M a,196,v,236,v,72
T v,226,8,40960
buffer_load_dwordx4 v27, s[36:39], s61 offen offset:0 nt lds
T v,228,11,32768
W 7
s_mov_b32 m0, 4992
M a,192,v,240,v,80
T v,230,11,40960
buffer_load_dwordx4 v27, s[36:39], s61 offen offset:128 nt lds
T v,232,8,33280
M a,196,v,244,v,80
T v,234,8,41472
T v,236,11,33280
W 7
s_mov_b32 m0, 5888
M a,192,v,248,v,88
T v,238,11,41472
buffer_load_dwordx4 v27, s[36:39], s61 offen offset:256 nt lds
T v,240,8,33792
M a,196,v,252,v,88
T v,242,8,41984
T v,244,11,33792
W 7
s_mov_b32 m0, 6784
M v,96,v,224,a,240
T v,246,11,41984
buffer_load_dwordx4 v27, s[36:39], s61 offen offset:384 nt lds
T v,248,8,34304
M v,100,v,228,a,240
T v,250,8,42496
T v,252,11,34304
W 7
s_mov_b32 m0, 8192
s_mov_b32 s61, 253952
M v,112,v,232,a,240
T v,254,11,42496
buffer_load_dwordx4 v19, s[36:39], s61 offen offset:0 nt lds
T v,224,8,34816
M v,116,v,236,a,240
T v,226,8,43008
T v,228,11,34816
W 7
s_mov_b32 m0, 9088
A 30,192
M v,128,v,240,a,240
T v,230,11,43008
buffer_load_dwordx4 v19, s[36:39], s61 offen offset:128 nt lds
A 31,193
T v,232,8,35328
M v,132,v,244,a,240
T v,234,8,43520
T v,236,11,35328
W 7
s_mov_b32 m0, 9984
v_max3_f32 v26, v30, v31, v26
M v,144,v,248,a,240
T v,238,11,43520
buffer_load_dwordx4 v19, s[36:39], s61 offen offset:256 nt lds
A 30,194
T v,240,8,35840
M v,148,v,252,a,240
T v,242,8,44032
T v,244,11,35840
W 7
s_mov_b32 m0, 10880
A 31,195
M v,160,v,224,a,240
T v,246,11,44032
buffer_load_dwordx4 v19, s[36:39], s61 offen offset:384 nt lds
v_max3_f32 v26, v30, v31, v26
T v,248,8,36352
M v,164,v,228,a,240
T v,250,8,44544
T v,252,11,36352
W 7
s_mov_b32 m0, 12288
A 30,196
M v,176,v,232,a,240
T v,254,11,44544
s_mov_b32 s61, 258048
A 31,197
s_waitcnt vmcnt(44)
s_barrier
R v,224,2,49152
buffer_load_dwordx4 v27, s[36:39], s61 offen offset:0 nt lds
v_max3_f32 v26, v30, v31, v26
M v,180,v,236,a,240
R v,226,5,49152
s_mov_b32 m0, 13184
A 30,198
R v,228,2,57344
W 7
buffer_load_dwordx4 v27, s[36:39], s61 offen offset:128 nt lds
A 31,199
M v,192,v,240,a,240
R v,230,5,57344
s_mov_b32 m0, 14080
v_max3_f32 v26, v30, v31, v26
R v,232,2,49664
buffer_load_dwordx4 v27, s[36:39], s61 offen offset:256 nt lds
s_mov_b32 m0, 14976
M v,196,v,244,a,240
R v,234,5,49664
buffer_load_dwordx4 v27, s[36:39], s61 offen offset:384 nt lds
s_mov_b32 m0, 16384
R v,236,2,57856
W 7
s_mov_b32 s61, 0
buffer_load_dwordx4 v23, s[40:43], s61 offen offset:0 nt lds
M v,208,v,248,a,240
R v,238,5,57856
R v,240,2,50176
s_mov_b32 m0, 17408
s_mov_b32 s61, 4096
M v,212,v,252,a,240
R v,242,5,50176
buffer_load_dwordx4 v23, s[40:43], s61 offen offset:0 nt lds
R v,244,2,58368
W 7
v_mfma_f32_16x16x32_bf16 a[208:211], v[224:227], v[32:35], 0
R v,246,5,58368
R v,248,2,50688
s_mov_b32 m0, 18432
s_mov_b32 s61, 8192
v_mfma_f32_16x16x32_bf16 a[212:215], v[228:231], v[32:35], 0
R v,250,5,50688
buffer_load_dwordx4 v23, s[40:43], s61 offen offset:0 nt lds
R v,252,2,58880
W 7
M a,208,v,232,v,40
R v,254,5,58880
R v,224,2,51200
s_mov_b32 m0, 19456
s_mov_b32 s61, 12288
M a,212,v,236,v,40
R v,226,5,51200
buffer_load_dwordx4 v23, s[40:43], s61 offen offset:0 nt lds
R v,228,2,59392
W 7
s_mov_b32 m0, 20480
s_mov_b32 s61, 16384
M a,208,v,240,v,48
R v,230,5,59392
buffer_load_dwordx4 v23, s[40:43], s61 offen offset:0 nt lds
R v,232,2,51712
M a,212,v,244,v,48
R v,234,5,51712
R v,236,2,59904
W 7
s_mov_b32 m0, 21504
s_mov_b32 s61, 20480
M a,208,v,248,v,56
R v,238,5,59904
buffer_load_dwordx4 v23, s[40:43], s61 offen offset:0 nt lds
R v,240,2,52224
M a,212,v,252,v,56
R v,242,5,52224
R v,244,2,60416
W 7
s_mov_b32 m0, 22528
s_mov_b32 s61, 24576
M a,208,v,224,v,64
R v,246,5,60416
buffer_load_dwordx4 v23, s[40:43], s61 offen offset:0 nt lds
R v,248,2,52736
M a,212,v,228,v,64
R v,250,5,52736
R v,252,2,60928
W 7
s_mov_b32 m0, 23552
s_mov_b32 s61, 28672
M a,208,v,232,v,72
R v,254,5,60928
buffer_load_dwordx4 v23, s[40:43], s61 offen offset:0 nt lds
s_waitcnt vmcnt(40)
s_barrier
T v,224,9,0
M a,212,v,236,v,72
T v,226,9,8192
T v,228,12,0
W 7
s_mov_b32 m0, 24576
s_mov_b32 s61, 32768
M a,208,v,240,v,80
T v,230,12,8192
buffer_load_dwordx4 v23, s[40:43], s61 offen offset:0 nt lds
T v,232,9,512
M a,212,v,244,v,80
T v,234,9,8704
T v,236,12,512
W 7
s_mov_b32 m0, 25600
s_mov_b32 s61, 36864
M a,208,v,248,v,88
T v,238,12,8704
buffer_load_dwordx4 v23, s[40:43], s61 offen offset:0 nt lds
T v,240,9,1024
M a,212,v,252,v,88
T v,242,9,9216
T v,244,12,1024
W 7
s_mov_b32 m0, 26624
s_mov_b32 s61, 40960
M v,96,v,224,a,248
T v,246,12,9216
buffer_load_dwordx4 v23, s[40:43], s61 offen offset:0 nt lds
T v,248,9,1536
M v,100,v,228,a,248
T v,250,9,9728
T v,252,12,1536
W 7
s_mov_b32 m0, 27648
s_mov_b32 s61, 45056
M v,112,v,232,a,248
T v,254,12,9728
buffer_load_dwordx4 v23, s[40:43], s61 offen offset:0 nt lds
T v,224,9,2048
M v,116,v,236,a,248
T v,226,9,10240
T v,228,12,2048
W 7
s_mov_b32 m0, 28672
A 30,208
M v,128,v,240,a,248
T v,230,12,10240
s_mov_b32 s61, 49152
A 31,209
T v,232,9,2560
buffer_load_dwordx4 v23, s[40:43], s61 offen offset:0 nt lds
v_max3_f32 v26, v30, v31, v26
M v,132,v,244,a,248
T v,234,9,10752
T v,236,12,2560
W 7
s_mov_b32 m0, 29696
A 30,210
M v,144,v,248,a,248
T v,238,12,10752
s_mov_b32 s61, 53248
A 31,211
T v,240,9,3072
buffer_load_dwordx4 v23, s[40:43], s61 offen offset:0 nt lds
v_max3_f32 v26, v30, v31, v26
M v,148,v,252,a,248
T v,242,9,11264
s_mov_b32 m0, 30720
A 30,212
T v,244,12,3072
W 7
s_mov_b32 s61, 57344
A 31,213
M v,160,v,224,a,248
T v,246,12,11264
buffer_load_dwordx4 v23, s[40:43], s61 offen offset:0 nt lds
v_max3_f32 v26, v30, v31, v26
T v,248,9,3584
s_mov_b32 m0, 31744
A 30,214
M v,164,v,228,a,248
T v,250,9,11776
)x"
R"x(s_mov_b32 s61, 61440
A 31,215
T v,252,12,3584
W 7
buffer_load_dwordx4 v23, s[40:43], s61 offen offset:0 nt lds
v_max3_f32 v26, v30, v31, v26
M v,176,v,232,a,248
T v,254,12,11776
s_waitcnt vmcnt(32)
s_barrier
R v,224,3,16384
s_mov_b32 s61, .Lc0_52_%=-.Lpf_start%=+8192
buffer_load_dword v29, v20, s[92:95], s61 offen
M v,180,v,236,a,248
R v,226,6,16384
R v,228,3,24576
W 7
s_mov_b32 s61, .Lc1_52_%=-.Lpf_start%=+8192
buffer_load_dword v29, v20, s[92:95], s61 offen
M v,192,v,240,a,248
R v,230,6,24576
R v,232,3,16896
M v,196,v,244,a,248
R v,234,6,16896
R v,236,3,25088
W 7
s_mov_b32 m0, 32768
s_mov_b32 s61, 65536
M v,208,v,248,a,248
R v,238,6,25088
buffer_load_dwordx4 v23, s[40:43], s61 offen offset:0 nt lds
R v,240,3,17408
M v,212,v,252,a,248
R v,242,6,17408
R v,244,3,25600
W 7
s_mov_b32 m0, 33792
s_mov_b32 s61, 69632
v_mfma_f32_16x16x32_bf16 a[224:227], v[224:227], v[32:35], 0
R v,246,6,25600
buffer_load_dwordx4 v23, s[40:43], s61 offen offset:0 nt lds
R v,248,3,17920
v_mfma_f32_16x16x32_bf16 a[228:231], v[228:231], v[32:35], 0
R v,250,6,17920
R v,252,3,26112
W 7
s_mov_b32 m0, 34816
s_mov_b32 s61, 73728
M a,224,v,232,v,40
R v,254,6,26112
buffer_load_dwordx4 v23, s[40:43], s61 offen offset:0 nt lds
R v,224,3,18432
M a,228,v,236,v,40
R v,226,6,18432
R v,228,3,26624
W 7
s_mov_b32 m0, 35840
s_mov_b32 s61, 77824
M a,224,v,240,v,48
R v,230,6,26624
buffer_load_dwordx4 v23, s[40:43], s61 offen offset:0 nt lds
R v,232,3,18944
M a,228,v,244,v,48
R v,234,6,18944
R v,236,3,27136
W 7
s_mov_b32 m0, 36864
s_mov_b32 s61, 81920
M a,224,v,248,v,56
R v,238,6,27136
buffer_load_dwordx4 v23, s[40:43], s61 offen offset:0 nt lds
R v,240,3,19456
M a,228,v,252,v,56
R v,242,6,19456
R v,244,3,27648
W 7
s_mov_b32 m0, 37888
s_mov_b32 s61, 86016
M a,224,v,224,v,64
R v,246,6,27648
buffer_load_dwordx4 v23, s[40:43], s61 offen offset:0 nt lds
R v,248,3,19968
M a,228,v,228,v,64
R v,250,6,19968
R v,252,3,28160
W 7
s_mov_b32 m0, 38912
s_mov_b32 s61, 90112
M a,224,v,232,v,72
R v,254,6,28160
buffer_load_dwordx4 v23, s[40:43], s61 offen offset:0 nt lds
s_waitcnt vmcnt(25)
s_barrier
.Lc1_58_%=:
R v,224,1,0
s_mov_b32 m0, 39936
s_mov_b32 s61, 94208
M a,228,v,236,v,72
R v,226,4,0
buffer_load_dwordx4 v23, s[40:43], s61 offen offset:0 nt lds
s_mov_b32 m0, 40960
R v,228,1,8192
W 7
s_mov_b32 s61, 98304
buffer_load_dwordx4 v23, s[40:43], s61 offen offset:0 nt lds
M a,224,v,240,v,80
R v,230,4,8192
s_mov_b32 m0, 41984
s_mov_b32 s61, 102400
R v,232,1,512
buffer_load_dwordx4 v23, s[40:43], s61 offen offset:0 nt lds
s_mov_b32 m0, 43008
M a,228,v,244,v,80
R v,234,4,512
s_mov_b32 s61, 106496
buffer_load_dwordx4 v23, s[40:43], s61 offen offset:0 nt lds
R v,236,1,8704
W 7
s_mov_b32 m0, 44032
s_mov_b32 s61, 110592
M a,224,v,248,v,88
R v,238,4,8704
buffer_load_dwordx4 v23, s[40:43], s61 offen offset:0 nt lds
s_mov_b32 m0, 45056
R v,240,1,1024
s_mov_b32 s61, 114688
buffer_load_dwordx4 v23, s[40:43], s61 offen offset:0 nt lds
M a,228,v,252,v,88
R v,242,4,1024
s_mov_b32 m0, 46080
s_mov_b32 s61, 118784
R v,244,1,9216
W 7
buffer_load_dwordx4 v23, s[40:43], s61 offen offset:0 nt lds
s_mov_b32 m0, 47104
v_mfma_f32_16x16x32_bf16 a[240:243], v[224:227], v[32:35], 0
R v,246,4,9216
s_mov_b32 s61, 122880
buffer_load_dwordx4 v23, s[40:43], s61 offen offset:0 nt lds
R v,248,1,1536
s_mov_b32 m0, 48128
s_mov_b32 s61, 126976
v_mfma_f32_16x16x32_bf16 a[244:247], v[228:231], v[32:35], 0
R v,250,4,1536
buffer_load_dwordx4 v23, s[40:43], s61 offen offset:0 nt lds
s_mov_b32 m0, 49152
R v,252,1,9728
W 7
s_mov_b32 s61, 131072
buffer_load_dwordx4 v23, s[40:43], s61 offen offset:0 nt lds
M a,240,v,232,v,40
R v,254,4,9728
s_mov_b32 m0, 50176
s_mov_b32 s61, 135168
R v,224,1,2048
buffer_load_dwordx4 v23, s[40:43], s61 offen offset:0 nt lds
A 30,224
M a,244,v,236,v,40
R v,226,4,2048
s_mov_b32 m0, 51200
A 31,225
R v,228,1,10240
W 7
s_mov_b32 s61, 139264
v_max3_f32 v26, v30, v31, v26
M a,240,v,240,v,48
R v,230,4,10240
buffer_load_dwordx4 v23, s[40:43], s61 offen offset:0 nt lds
A 30,226
R v,232,1,2560
s_mov_b32 m0, 52224
A 31,227
M a,244,v,244,v,48
R v,234,4,2560
s_mov_b32 s61, 143360
v_max3_f32 v26, v30, v31, v26
R v,236,1,10752
W 7
buffer_load_dwordx4 v23, s[40:43], s61 offen offset:0 nt lds
A 30,228
M a,240,v,248,v,56
R v,238,4,10752
s_mov_b32 m0, 53248
A 31,229
R v,240,1,3072
s_mov_b32 s61, 147456
v_max3_f32 v26, v30, v31, v26
M a,244,v,252,v,56
R v,242,4,3072
buffer_load_dwordx4 v23, s[40:43], s61 offen offset:0 nt lds
A 30,230
R v,244,1,11264
W 7
s_mov_b32 m0, 54272
A 31,231
M a,240,v,224,v,64
R v,246,4,11264
s_mov_b32 s61, 151552
v_max3_f32 v26, v30, v31, v26
R v,248,1,3584
buffer_load_dwordx4 v23, s[40:43], s61 offen offset:0 nt lds
M a,244,v,228,v,64
R v,250,4,3584
R v,252,1,11776
W 7
s_mov_b32 m0, 55296
s_mov_b32 s61, 155648
M a,240,v,232,v,72
R v,254,4,11776
buffer_load_dwordx4 v23, s[40:43], s61 offen offset:0 nt lds
M a,244,v,236,v,72
W 6
s_mov_b32 m0, 56320
s_mov_b32 s61, 159744
M a,240,v,240,v,80
buffer_load_dwordx4 v23, s[40:43], s61 offen offset:0 nt lds
W 4
M a,244,v,244,v,80
W 2
s_mov_b32 m0, 57344
s_mov_b32 s61, 163840
M a,240,v,248,v,88
buffer_load_dwordx4 v23, s[40:43], s61 offen offset:0 nt lds
W 0
s_mov_b32 m0, 58368
s_mov_b32 s61, 167936
M a,244,v,252,v,88
buffer_load_dwordx4 v23, s[40:43], s61 offen offset:0 nt lds
s_barrier
s_nop 7
v_cvt_pk_bf16_f32 v32, v96, v97
v_cvt_pk_bf16_f32 v33, v98, v99
v_cvt_pk_bf16_f32 v34, v100, v101
v_cvt_pk_bf16_f32 v35, v102, v103
v_cvt_pk_bf16_f32 v40, v112, v113
v_cvt_pk_bf16_f32 v41, v114, v115
v_cvt_pk_bf16_f32 v42, v116, v117
v_cvt_pk_bf16_f32 v43, v118, v119
v_cvt_pk_bf16_f32 v48, v128, v129
v_cvt_pk_bf16_f32 v49, v130, v131
v_cvt_pk_bf16_f32 v50, v132, v133
v_cvt_pk_bf16_f32 v51, v134, v135
v_cvt_pk_bf16_f32 v56, v144, v145
v_cvt_pk_bf16_f32 v57, v146, v147
v_cvt_pk_bf16_f32 v58, v148, v149
v_cvt_pk_bf16_f32 v59, v150, v151
v_cvt_pk_bf16_f32 v64, v160, v161
v_cvt_pk_bf16_f32 v65, v162, v163
v_cvt_pk_bf16_f32 v66, v164, v165
v_cvt_pk_bf16_f32 v67, v166, v167
v_cvt_pk_bf16_f32 v72, v176, v177
v_cvt_pk_bf16_f32 v73, v178, v179
v_cvt_pk_bf16_f32 v74, v180, v181
v_cvt_pk_bf16_f32 v75, v182, v183
v_cvt_pk_bf16_f32 v80, v192, v193
v_cvt_pk_bf16_f32 v81, v194, v195
v_cvt_pk_bf16_f32 v82, v196, v197
v_cvt_pk_bf16_f32 v83, v198, v199
v_cvt_pk_bf16_f32 v88, v208, v209
v_cvt_pk_bf16_f32 v89, v210, v211
v_cvt_pk_bf16_f32 v90, v212, v213
v_cvt_pk_bf16_f32 v91, v214, v215
s_mov_b32 m0, 59392
s_mov_b32 s61, 172032
buffer_load_dwordx4 v23, s[40:43], s61 offen offset:0 nt lds
v_lshrrev_b32 v30, 5, v0
v_mul_u32_u24 v30, 0xc00, v30
s_mov_b32 m0, 60416
s_mov_b32 s61, 176128
buffer_load_dwordx4 v23, s[40:43], s61 offen offset:0 nt lds
v_sub_u32 v13, v7, v30
v_sub_u32 v16, v10, v30
s_mov_b32 m0, 61440
s_mov_b32 s61, 180224
buffer_load_dwordx4 v23, s[40:43], s61 offen offset:0 nt lds
v_add_u32 v14, 0x10000, v13
v_add_u32 v15, 0x20000, v13
s_mov_b32 m0, 62464
s_mov_b32 s61, 184320
buffer_load_dwordx4 v23, s[40:43], s61 offen offset:0 nt lds
v_add_u32 v17, 0x10000, v16
v_add_u32 v18, 0x20000, v16
s_mov_b32 m0, 63488
s_mov_b32 s61, 188416
buffer_load_dwordx4 v23, s[40:43], s61 offen offset:0 nt lds
A 30,240
A 31,241
v_max3_f32 v26, v30, v31, v26
A 30,242
s_mov_b32 m0, 64512
s_mov_b32 s61, 192512
buffer_load_dwordx4 v23, s[40:43], s61 offen offset:0 nt lds
A 31,243
v_max3_f32 v26, v30, v31, v26
A 30,244
A 31,245
v_max3_f32 v26, v30, v31, v26
A 30,246
A 31,247
v_max3_f32 v26, v30, v31, v26
s_mov_b32 m0, 65536
s_mov_b32 s61, 196608
buffer_load_dwordx4 v23, s[40:43], s61 offen offset:0 nt lds
v_mov_b32 v244, 0x3f803f80
v_mov_b32 v245, 0x3f803f80
v_mov_b32 v246, 0x3f803f80
v_mov_b32 v247, 0x3f803f80
v_mov_b32 v30, v26
s_nop 1
v_permlane32_swap_b32 v30, v26
s_nop 1
s_mov_b32 m0, 66560
s_mov_b32 s61, 200704
buffer_load_dwordx4 v23, s[40:43], s61 offen offset:0 nt lds
v_max_f32 v26, v30, v26
s_nop 0
v_mov_b32 v30, v26
s_nop 1
v_permlane16_swap_b32 v30, v26
s_nop 1
v_max_f32 v26, v30, v26
v_mul_f32 v30, s62, v26
s_mov_b32 m0, 67584
s_mov_b32 s61, 204800
buffer_load_dwordx4 v23, s[40:43], s61 offen offset:0 nt lds
v_sub_f32 v30, 0, v30
v_mov_b32 v31, v30
A 224,0
A 225,1
A 226,2
A 227,3
A 228,4
A 229,5
s_mov_b32 m0, 68608
s_mov_b32 s61, 208896
buffer_load_dwordx4 v23, s[40:43], s61 offen offset:0 nt lds
A 230,6
A 231,7
v_pk_fma_f32 v[224:225], v[224:225], s[62:63], v[30:31]
v_pk_fma_f32 v[226:227], v[226:227], s[62:63], v[30:31]
v_pk_fma_f32 v[228:229], v[228:229], s[62:63], v[30:31]
v_pk_fma_f32 v[230:231], v[230:231], s[62:63], v[30:31]
v_exp_f32 v224, v224
v_exp_f32 v225, v225
s_mov_b32 m0, 69632
s_mov_b32 s61, 212992
buffer_load_dwordx4 v23, s[40:43], s61 offen offset:0 nt lds
v_exp_f32 v226, v226
v_exp_f32 v227, v227
v_exp_f32 v228, v228
v_exp_f32 v229, v229
v_exp_f32 v230, v230
v_exp_f32 v231, v231
A 232,16
A 233,17
s_mov_b32 m0, 70656
s_mov_b32 s61, 217088
buffer_load_dwordx4 v23, s[40:43], s61 offen offset:0 nt lds
A 234,18
A 235,19
A 236,20
A 237,21
A 238,22
A 239,23
v_pk_fma_f32 v[232:233], v[232:233], s[62:63], v[30:31]
v_pk_fma_f32 v[234:235], v[234:235], s[62:63], v[30:31]
s_mov_b32 m0, 71680
s_mov_b32 s61, 221184
buffer_load_dwordx4 v23, s[40:43], s61 offen offset:0 nt lds
v_pk_fma_f32 v[236:237], v[236:237], s[62:63], v[30:31]
v_pk_fma_f32 v[238:239], v[238:239], s[62:63], v[30:31]
v_cvt_pk_bf16_f32 v96, v224, v225
v_cvt_pk_bf16_f32 v97, v226, v227
v_cvt_pk_bf16_f32 v98, v228, v229
v_cvt_pk_bf16_f32 v99, v230, v231
v_exp_f32 v232, v232
v_exp_f32 v233, v233
s_mov_b32 m0, 72704
s_mov_b32 s61, 225280
buffer_load_dwordx4 v23, s[40:43], s61 offen offset:0 nt lds
v_exp_f32 v234, v234
v_exp_f32 v235, v235
v_mfma_f32_16x16x32_bf16 v[240:243], v[244:247], v[96:99], 0
v_exp_f32 v236, v236
v_exp_f32 v237, v237
v_exp_f32 v238, v238
v_exp_f32 v239, v239
A 224,32
s_mov_b32 m0, 73728
s_mov_b32 s61, 229376
buffer_load_dwordx4 v23, s[40:43], s61 offen offset:0 nt lds
A 225,33
A 226,34
A 227,35
A 228,36
A 229,37
A 230,38
A 231,39
v_pk_fma_f32 v[224:225], v[224:225], s[62:63], v[30:31]
s_mov_b32 m0, 74752
)x"
R"x(s_mov_b32 s61, 233472
buffer_load_dwordx4 v23, s[40:43], s61 offen offset:0 nt lds
v_pk_fma_f32 v[226:227], v[226:227], s[62:63], v[30:31]
v_pk_fma_f32 v[228:229], v[228:229], s[62:63], v[30:31]
v_pk_fma_f32 v[230:231], v[230:231], s[62:63], v[30:31]
v_cvt_pk_bf16_f32 v104, v232, v233
v_cvt_pk_bf16_f32 v105, v234, v235
v_cvt_pk_bf16_f32 v106, v236, v237
v_cvt_pk_bf16_f32 v107, v238, v239
v_exp_f32 v224, v224
s_mov_b32 m0, 75776
s_mov_b32 s61, 237568
buffer_load_dwordx4 v23, s[40:43], s61 offen offset:0 nt lds
v_exp_f32 v225, v225
v_exp_f32 v226, v226
v_exp_f32 v227, v227
M v,240,v,244,v,104
v_exp_f32 v228, v228
v_exp_f32 v229, v229
v_exp_f32 v230, v230
v_exp_f32 v231, v231
s_mov_b32 m0, 76800
s_mov_b32 s61, 241664
buffer_load_dwordx4 v23, s[40:43], s61 offen offset:0 nt lds
A 232,48
A 233,49
A 234,50
A 235,51
A 236,52
A 237,53
A 238,54
A 239,55
s_mov_b32 m0, 77824
s_mov_b32 s61, 245760
buffer_load_dwordx4 v23, s[40:43], s61 offen offset:0 nt lds
v_pk_fma_f32 v[232:233], v[232:233], s[62:63], v[30:31]
v_pk_fma_f32 v[234:235], v[234:235], s[62:63], v[30:31]
v_pk_fma_f32 v[236:237], v[236:237], s[62:63], v[30:31]
v_pk_fma_f32 v[238:239], v[238:239], s[62:63], v[30:31]
v_cvt_pk_bf16_f32 v112, v224, v225
v_cvt_pk_bf16_f32 v113, v226, v227
v_cvt_pk_bf16_f32 v114, v228, v229
v_cvt_pk_bf16_f32 v115, v230, v231
s_mov_b32 m0, 78848
s_mov_b32 s61, 249856
buffer_load_dwordx4 v23, s[40:43], s61 offen offset:0 nt lds
v_exp_f32 v232, v232
v_exp_f32 v233, v233
v_exp_f32 v234, v234
v_exp_f32 v235, v235
M v,240,v,244,v,112
v_exp_f32 v236, v236
v_exp_f32 v237, v237
v_exp_f32 v238, v238
s_mov_b32 m0, 79872
s_mov_b32 s61, 253952
buffer_load_dwordx4 v23, s[40:43], s61 offen offset:0 nt lds
v_exp_f32 v239, v239
A 224,64
A 225,65
A 226,66
A 227,67
A 228,68
A 229,69
A 230,70
s_mov_b32 m0, 80896
s_mov_b32 s61, 258048
buffer_load_dwordx4 v23, s[40:43], s61 offen offset:0 nt lds
A 231,71
v_pk_fma_f32 v[224:225], v[224:225], s[62:63], v[30:31]
v_pk_fma_f32 v[226:227], v[226:227], s[62:63], v[30:31]
v_pk_fma_f32 v[228:229], v[228:229], s[62:63], v[30:31]
v_pk_fma_f32 v[230:231], v[230:231], s[62:63], v[30:31]
v_cvt_pk_bf16_f32 v120, v232, v233
v_cvt_pk_bf16_f32 v121, v234, v235
v_cvt_pk_bf16_f32 v122, v236, v237
s_mov_b32 m0, 81920
s_mov_b32 s61, 0
buffer_load_dwordx4 v24, s[52:55], s61 offen offset:0 lds
v_cvt_pk_bf16_f32 v123, v238, v239
v_exp_f32 v224, v224
s_mov_b32 m0, 82816
s_nop 0
buffer_load_dwordx4 v24, s[52:55], s61 offen offset:128 lds
v_exp_f32 v225, v225
v_exp_f32 v226, v226
s_mov_b32 m0, 83712
s_nop 0
buffer_load_dwordx4 v24, s[52:55], s61 offen offset:256 lds
v_exp_f32 v227, v227
M v,240,v,244,v,120
s_mov_b32 m0, 84608
s_nop 0
buffer_load_dwordx4 v24, s[52:55], s61 offen offset:384 lds
v_exp_f32 v228, v228
v_exp_f32 v229, v229
s_mov_b32 m0, 86016
s_mov_b32 s61, 7168
buffer_load_dwordx4 v24, s[52:55], s61 offen offset:0 lds
v_exp_f32 v230, v230
v_exp_f32 v231, v231
s_mov_b32 m0, 86912
s_nop 0
buffer_load_dwordx4 v24, s[52:55], s61 offen offset:128 lds
A 232,80
A 233,81
s_mov_b32 m0, 87808
s_nop 0
buffer_load_dwordx4 v24, s[52:55], s61 offen offset:256 lds
A 234,82
A 235,83
s_mov_b32 m0, 88704
s_nop 0
buffer_load_dwordx4 v24, s[52:55], s61 offen offset:384 lds
A 236,84
A 237,85
s_mov_b32 m0, 90112
s_mov_b32 s61, 14336
buffer_load_dwordx4 v24, s[52:55], s61 offen offset:0 lds
A 238,86
A 239,87
s_mov_b32 m0, 91008
s_nop 0
buffer_load_dwordx4 v24, s[52:55], s61 offen offset:128 lds
v_pk_fma_f32 v[232:233], v[232:233], s[62:63], v[30:31]
v_pk_fma_f32 v[234:235], v[234:235], s[62:63], v[30:31]
s_mov_b32 m0, 91904
s_nop 0
buffer_load_dwordx4 v24, s[52:55], s61 offen offset:256 lds
v_pk_fma_f32 v[236:237], v[236:237], s[62:63], v[30:31]
v_pk_fma_f32 v[238:239], v[238:239], s[62:63], v[30:31]
s_mov_b32 m0, 92800
s_nop 0
buffer_load_dwordx4 v24, s[52:55], s61 offen offset:384 lds
v_cvt_pk_bf16_f32 v128, v224, v225
v_cvt_pk_bf16_f32 v129, v226, v227
s_mov_b32 m0, 94208
s_mov_b32 s61, 21504
buffer_load_dwordx4 v24, s[52:55], s61 offen offset:0 lds
v_cvt_pk_bf16_f32 v130, v228, v229
v_cvt_pk_bf16_f32 v131, v230, v231
s_mov_b32 m0, 95104
s_nop 0
buffer_load_dwordx4 v24, s[52:55], s61 offen offset:128 lds
v_exp_f32 v232, v232
v_exp_f32 v233, v233
s_mov_b32 m0, 96000
s_nop 0
buffer_load_dwordx4 v24, s[52:55], s61 offen offset:256 lds
v_exp_f32 v234, v234
v_exp_f32 v235, v235
s_mov_b32 m0, 96896
s_nop 0
buffer_load_dwordx4 v24, s[52:55], s61 offen offset:384 lds
M v,240,v,244,v,128
v_exp_f32 v236, v236
s_mov_b32 m0, 97792
s_mov_b32 s61, 0
buffer_load_dwordx4 v24, s[52:55], s61 offen offset:512 lds
v_exp_f32 v237, v237
v_exp_f32 v238, v238
s_mov_b32 m0, 98688
s_nop 0
buffer_load_dwordx4 v24, s[52:55], s61 offen offset:640 lds
v_exp_f32 v239, v239
A 224,96
s_mov_b32 m0, 99584
s_nop 0
buffer_load_dwordx4 v24, s[52:55], s61 offen offset:768 lds
A 225,97
A 226,98
s_mov_b32 m0, 101888
s_mov_b32 s61, 7168
buffer_load_dwordx4 v24, s[52:55], s61 offen offset:512 lds
A 227,99
A 228,100
s_mov_b32 m0, 102784
s_nop 0
buffer_load_dwordx4 v24, s[52:55], s61 offen offset:640 lds
A 229,101
A 230,102
s_mov_b32 m0, 103680
s_nop 0
buffer_load_dwordx4 v24, s[52:55], s61 offen offset:768 lds
A 231,103
v_pk_fma_f32 v[224:225], v[224:225], s[62:63], v[30:31]
s_mov_b32 m0, 105984
s_mov_b32 s61, 14336
buffer_load_dwordx4 v24, s[52:55], s61 offen offset:512 lds
v_pk_fma_f32 v[226:227], v[226:227], s[62:63], v[30:31]
v_pk_fma_f32 v[228:229], v[228:229], s[62:63], v[30:31]
s_mov_b32 m0, 106880
s_nop 0
buffer_load_dwordx4 v24, s[52:55], s61 offen offset:640 lds
v_pk_fma_f32 v[230:231], v[230:231], s[62:63], v[30:31]
v_cvt_pk_bf16_f32 v136, v232, v233
s_mov_b32 m0, 107776
s_nop 0
buffer_load_dwordx4 v24, s[52:55], s61 offen offset:768 lds
v_cvt_pk_bf16_f32 v137, v234, v235
v_cvt_pk_bf16_f32 v138, v236, v237
s_mov_b32 m0, 110080
s_mov_b32 s61, 21504
buffer_load_dwordx4 v24, s[52:55], s61 offen offset:512 lds
v_cvt_pk_bf16_f32 v139, v238, v239
v_exp_f32 v224, v224
s_mov_b32 m0, 110976
s_nop 0
buffer_load_dwordx4 v24, s[52:55], s61 offen offset:640 lds
v_exp_f32 v225, v225
v_exp_f32 v226, v226
s_mov_b32 m0, 111872
s_nop 0
buffer_load_dwordx4 v24, s[52:55], s61 offen offset:768 lds
v_exp_f32 v227, v227
M v,240,v,244,v,136
s_mov_b32 m0, 114688
s_mov_b32 s61, 28672
buffer_load_dwordx4 v24, s[52:55], s61 offen offset:0 lds
v_exp_f32 v228, v228
v_exp_f32 v229, v229
s_mov_b32 m0, 115584
s_nop 0
buffer_load_dwordx4 v24, s[52:55], s61 offen offset:128 lds
v_exp_f32 v230, v230
v_exp_f32 v231, v231
s_mov_b32 m0, 116480
s_nop 0
buffer_load_dwordx4 v24, s[52:55], s61 offen offset:256 lds
A 232,112
A 233,113
s_mov_b32 m0, 117376
s_nop 0
buffer_load_dwordx4 v24, s[52:55], s61 offen offset:384 lds
A 234,114
A 235,115
s_mov_b32 m0, 118784
s_mov_b32 s61, 35840
buffer_load_dwordx4 v24, s[52:55], s61 offen offset:0 lds
A 236,116
A 237,117
s_mov_b32 m0, 119680
s_nop 0
buffer_load_dwordx4 v24, s[52:55], s61 offen offset:128 lds
A 238,118
A 239,119
s_mov_b32 m0, 120576
s_nop 0
buffer_load_dwordx4 v24, s[52:55], s61 offen offset:256 lds
v_pk_fma_f32 v[232:233], v[232:233], s[62:63], v[30:31]
v_pk_fma_f32 v[234:235], v[234:235], s[62:63], v[30:31]
s_mov_b32 m0, 121472
s_nop 0
buffer_load_dwordx4 v24, s[52:55], s61 offen offset:384 lds
v_pk_fma_f32 v[236:237], v[236:237], s[62:63], v[30:31]
v_pk_fma_f32 v[238:239], v[238:239], s[62:63], v[30:31]
s_mov_b32 m0, 122880
s_mov_b32 s61, 43008
buffer_load_dwordx4 v24, s[52:55], s61 offen offset:0 lds
v_cvt_pk_bf16_f32 v144, v224, v225
v_cvt_pk_bf16_f32 v145, v226, v227
s_mov_b32 m0, 123776
s_nop 0
buffer_load_dwordx4 v24, s[52:55], s61 offen offset:128 lds
v_cvt_pk_bf16_f32 v146, v228, v229
v_cvt_pk_bf16_f32 v147, v230, v231
s_mov_b32 m0, 124672
s_nop 0
buffer_load_dwordx4 v24, s[52:55], s61 offen offset:256 lds
v_exp_f32 v232, v232
v_exp_f32 v233, v233
s_mov_b32 m0, 125568
s_nop 0
buffer_load_dwordx4 v24, s[52:55], s61 offen offset:384 lds
v_exp_f32 v234, v234
v_exp_f32 v235, v235
s_mov_b32 m0, 126976
s_mov_b32 s61, 50176
buffer_load_dwordx4 v24, s[52:55], s61 offen offset:0 lds
M v,240,v,244,v,144
v_exp_f32 v236, v236
s_mov_b32 m0, 127872
s_nop 0
buffer_load_dwordx4 v24, s[52:55], s61 offen offset:128 lds
v_exp_f32 v237, v237
v_exp_f32 v238, v238
s_mov_b32 m0, 128768
s_nop 0
buffer_load_dwordx4 v24, s[52:55], s61 offen offset:256 lds
v_exp_f32 v239, v239
A 224,128
s_mov_b32 m0, 129664
s_nop 0
buffer_load_dwordx4 v24, s[52:55], s61 offen offset:384 lds
A 225,129
A 226,130
s_mov_b32 s61, .Lc0_58_%=-.Lpf_start%=+8192
buffer_load_dword v29, v20, s[92:95], s61 offen
A 227,131
A 228,132
s_mov_b32 s61, .Lc1_58_%=-.Lpf_start%=+8192
buffer_load_dword v29, v20, s[92:95], s61 offen
A 229,133
A 230,134
s_mov_b32 m0, 130560
s_mov_b32 s61, 28672
buffer_load_dwordx4 v24, s[52:55], s61 offen offset:512 lds
A 231,135
v_pk_fma_f32 v[224:225], v[224:225], s[62:63], v[30:31]
s_mov_b32 m0, 131456
s_nop 0
buffer_load_dwordx4 v24, s[52:55], s61 offen offset:640 lds
v_pk_fma_f32 v[226:227], v[226:227], s[62:63], v[30:31]
v_pk_fma_f32 v[228:229], v[228:229], s[62:63], v[30:31]
s_mov_b32 m0, 132352
s_nop 0
buffer_load_dwordx4 v24, s[52:55], s61 offen offset:768 lds
v_pk_fma_f32 v[230:231], v[230:231], s[62:63], v[30:31]
v_cvt_pk_bf16_f32 v152, v232, v233
s_mov_b32 m0, 134656
s_mov_b32 s61, 35840
buffer_load_dwordx4 v24, s[52:55], s61 offen offset:512 lds
v_cvt_pk_bf16_f32 v153, v234, v235
v_cvt_pk_bf16_f32 v154, v236, v237
s_mov_b32 m0, 135552
s_nop 0
buffer_load_dwordx4 v24, s[52:55], s61 offen offset:640 lds
v_cvt_pk_bf16_f32 v155, v238, v239
v_exp_f32 v224, v224
s_mov_b32 m0, 136448
s_nop 0
buffer_load_dwordx4 v24, s[52:55], s61 offen offset:768 lds
v_exp_f32 v225, v225
v_exp_f32 v226, v226
s_mov_b32 m0, 138752
s_mov_b32 s61, 43008
buffer_load_dwordx4 v24, s[52:55], s61 offen offset:512 lds
v_exp_f32 v227, v227
M v,240,v,244,v,152
s_mov_b32 m0, 139648
s_nop 0
buffer_load_dwordx4 v24, s[52:55], s61 offen offset:640 lds
v_exp_f32 v228, v228
v_exp_f32 v229, v229
s_mov_b32 m0, 140544
s_nop 0
buffer_load_dwordx4 v24, s[52:55], s61 offen offset:768 lds
v_exp_f32 v230, v230
v_exp_f32 v231, v231
s_mov_b32 m0, 142848
s_mov_b32 s61, 50176
buffer_load_dwordx4 v24, s[52:55], s61 offen offset:512 lds
A 232,144
A 233,145
s_mov_b32 m0, 143744
s_nop 0
buffer_load_dwordx4 v24, s[52:55], s61 offen offset:640 lds
A 234,146
A 235,147
s_mov_b32 m0, 144640
s_nop 0
buffer_load_dwordx4 v24, s[52:55], s61 offen offset:768 lds
A 236,148
A 237,149
s_mov_b32 m0, 147456
s_mov_b32 s61, 128
buffer_load_dwordx4 v23, s[40:43], s61 offen offset:0 nt lds
A 238,150
A 239,151
s_mov_b32 m0, 148480
s_mov_b32 s61, 4224
buffer_load_dwordx4 v23, s[40:43], s61 offen offset:0 nt lds
v_pk_fma_f32 v[232:233], v[232:233], s[62:63], v[30:31]
v_pk_fma_f32 v[234:235], v[234:235], s[62:63], v[30:31]
s_mov_b32 m0, 149504
s_mov_b32 s61, 8320
buffer_load_dwordx4 v23, s[40:43], s61 offen offset:0 nt lds
v_pk_fma_f32 v[236:237], v[236:237], s[62:63], v[30:31]
v_pk_fma_f32 v[238:239], v[238:239], s[62:63], v[30:31]
s_mov_b32 m0, 150528
s_mov_b32 s61, 12416
buffer_load_dwordx4 v23, s[40:43], s61 offen offset:0 nt lds
v_cvt_pk_bf16_f32 v160, v224, v225
v_cvt_pk_bf16_f32 v161, v226, v227
s_mov_b32 m0, 151552
s_mov_b32 s61, 16512
buffer_load_dwordx4 v23, s[40:43], s61 offen offset:0 nt lds
v_cvt_pk_bf16_f32 v162, v228, v229
v_cvt_pk_bf16_f32 v163, v230, v231
s_mov_b32 m0, 152576
s_mov_b32 s61, 20608
buffer_load_dwordx4 v23, s[40:43], s61 offen offset:0 nt lds
v_exp_f32 v232, v232
v_exp_f32 v233, v233
s_mov_b32 m0, 153600
s_mov_b32 s61, 24704
buffer_load_dwordx4 v23, s[40:43], s61 offen offset:0 nt lds
v_exp_f32 v234, v234
v_exp_f32 v235, v235
s_mov_b32 m0, 154624
)x"
R"x(s_mov_b32 s61, 28800
buffer_load_dwordx4 v23, s[40:43], s61 offen offset:0 nt lds
M v,240,v,244,v,160
v_exp_f32 v236, v236
s_mov_b32 m0, 155648
s_mov_b32 s61, 32896
buffer_load_dwordx4 v23, s[40:43], s61 offen offset:0 nt lds
v_exp_f32 v237, v237
v_exp_f32 v238, v238
s_mov_b32 m0, 156672
s_mov_b32 s61, 36992
buffer_load_dwordx4 v23, s[40:43], s61 offen offset:0 nt lds
v_exp_f32 v239, v239
A 224,160
s_mov_b32 m0, 157696
s_mov_b32 s61, 41088
buffer_load_dwordx4 v23, s[40:43], s61 offen offset:0 nt lds
A 225,161
A 226,162
s_mov_b32 m0, 158720
s_mov_b32 s61, 45184
buffer_load_dwordx4 v23, s[40:43], s61 offen offset:0 nt lds
A 227,163
A 228,164
A 229,165
A 230,166
A 231,167
v_pk_fma_f32 v[224:225], v[224:225], s[62:63], v[30:31]
v_pk_fma_f32 v[226:227], v[226:227], s[62:63], v[30:31]
v_pk_fma_f32 v[228:229], v[228:229], s[62:63], v[30:31]
s_mov_b32 m0, 159744
s_mov_b32 s61, 49280
buffer_load_dwordx4 v23, s[40:43], s61 offen offset:0 nt lds
v_pk_fma_f32 v[230:231], v[230:231], s[62:63], v[30:31]
v_cvt_pk_bf16_f32 v168, v232, v233
v_cvt_pk_bf16_f32 v169, v234, v235
v_cvt_pk_bf16_f32 v170, v236, v237
v_cvt_pk_bf16_f32 v171, v238, v239
v_exp_f32 v224, v224
v_exp_f32 v225, v225
v_exp_f32 v226, v226
s_mov_b32 m0, 160768
s_mov_b32 s61, 53376
buffer_load_dwordx4 v23, s[40:43], s61 offen offset:0 nt lds
v_exp_f32 v227, v227
M v,240,v,244,v,168
v_exp_f32 v228, v228
v_exp_f32 v229, v229
v_exp_f32 v230, v230
v_exp_f32 v231, v231
A 232,176
A 233,177
s_mov_b32 m0, 161792
s_mov_b32 s61, 57472
buffer_load_dwordx4 v23, s[40:43], s61 offen offset:0 nt lds
A 234,178
A 235,179
A 236,180
A 237,181
A 238,182
A 239,183
v_pk_fma_f32 v[232:233], v[232:233], s[62:63], v[30:31]
v_pk_fma_f32 v[234:235], v[234:235], s[62:63], v[30:31]
s_mov_b32 m0, 162816
s_mov_b32 s61, 61568
buffer_load_dwordx4 v23, s[40:43], s61 offen offset:0 nt lds
v_pk_fma_f32 v[236:237], v[236:237], s[62:63], v[30:31]
v_pk_fma_f32 v[238:239], v[238:239], s[62:63], v[30:31]
v_cvt_pk_bf16_f32 v176, v224, v225
v_cvt_pk_bf16_f32 v177, v226, v227
v_cvt_pk_bf16_f32 v178, v228, v229
v_cvt_pk_bf16_f32 v179, v230, v231
v_exp_f32 v232, v232
v_exp_f32 v233, v233
s_mov_b32 m0, 0
s_mov_b32 s61, 65664
buffer_load_dwordx4 v23, s[40:43], s61 offen offset:0 nt lds
v_exp_f32 v234, v234
v_exp_f32 v235, v235
M v,240,v,244,v,176
v_exp_f32 v236, v236
v_exp_f32 v237, v237
v_exp_f32 v238, v238
v_exp_f32 v239, v239
A 224,192
s_mov_b32 m0, 1024
s_mov_b32 s61, 69760
buffer_load_dwordx4 v23, s[40:43], s61 offen offset:0 nt lds
A 225,193
A 226,194
A 227,195
A 228,196
A 229,197
A 230,198
A 231,199
v_pk_fma_f32 v[224:225], v[224:225], s[62:63], v[30:31]
s_mov_b32 m0, 2048
s_mov_b32 s61, 73856
buffer_load_dwordx4 v23, s[40:43], s61 offen offset:0 nt lds
v_pk_fma_f32 v[226:227], v[226:227], s[62:63], v[30:31]
v_pk_fma_f32 v[228:229], v[228:229], s[62:63], v[30:31]
v_pk_fma_f32 v[230:231], v[230:231], s[62:63], v[30:31]
v_cvt_pk_bf16_f32 v184, v232, v233
v_cvt_pk_bf16_f32 v185, v234, v235
v_cvt_pk_bf16_f32 v186, v236, v237
v_cvt_pk_bf16_f32 v187, v238, v239
v_exp_f32 v224, v224
s_mov_b32 m0, 3072
s_mov_b32 s61, 77952
buffer_load_dwordx4 v23, s[40:43], s61 offen offset:0 nt lds
v_exp_f32 v225, v225
v_exp_f32 v226, v226
v_exp_f32 v227, v227
M v,240,v,244,v,184
v_exp_f32 v228, v228
v_exp_f32 v229, v229
v_exp_f32 v230, v230
v_exp_f32 v231, v231
s_mov_b32 m0, 4096
s_mov_b32 s61, 82048
buffer_load_dwordx4 v23, s[40:43], s61 offen offset:0 nt lds
A 232,208
A 233,209
A 234,210
A 235,211
A 236,212
A 237,213
A 238,214
A 239,215
s_mov_b32 m0, 5120
s_mov_b32 s61, 86144
buffer_load_dwordx4 v23, s[40:43], s61 offen offset:0 nt lds
v_pk_fma_f32 v[232:233], v[232:233], s[62:63], v[30:31]
v_pk_fma_f32 v[234:235], v[234:235], s[62:63], v[30:31]
v_pk_fma_f32 v[236:237], v[236:237], s[62:63], v[30:31]
v_pk_fma_f32 v[238:239], v[238:239], s[62:63], v[30:31]
v_cvt_pk_bf16_f32 v192, v224, v225
v_cvt_pk_bf16_f32 v193, v226, v227
v_cvt_pk_bf16_f32 v194, v228, v229
v_cvt_pk_bf16_f32 v195, v230, v231
s_mov_b32 m0, 6144
s_mov_b32 s61, 90240
buffer_load_dwordx4 v23, s[40:43], s61 offen offset:0 nt lds
v_exp_f32 v232, v232
v_exp_f32 v233, v233
v_exp_f32 v234, v234
v_exp_f32 v235, v235
M v,240,v,244,v,192
v_exp_f32 v236, v236
v_exp_f32 v237, v237
v_exp_f32 v238, v238
s_mov_b32 m0, 7168
s_mov_b32 s61, 94336
buffer_load_dwordx4 v23, s[40:43], s61 offen offset:0 nt lds
v_exp_f32 v239, v239
A 224,224
A 225,225
A 226,226
A 227,227
A 228,228
A 229,229
A 230,230
s_mov_b32 m0, 8192
s_mov_b32 s61, 98432
buffer_load_dwordx4 v23, s[40:43], s61 offen offset:0 nt lds
A 231,231
v_pk_fma_f32 v[224:225], v[224:225], s[62:63], v[30:31]
v_pk_fma_f32 v[226:227], v[226:227], s[62:63], v[30:31]
v_pk_fma_f32 v[228:229], v[228:229], s[62:63], v[30:31]
v_pk_fma_f32 v[230:231], v[230:231], s[62:63], v[30:31]
v_cvt_pk_bf16_f32 v200, v232, v233
v_cvt_pk_bf16_f32 v201, v234, v235
v_cvt_pk_bf16_f32 v202, v236, v237
s_mov_b32 m0, 9216
s_mov_b32 s61, 102528
buffer_load_dwordx4 v23, s[40:43], s61 offen offset:0 nt lds
v_cvt_pk_bf16_f32 v203, v238, v239
v_exp_f32 v224, v224
v_exp_f32 v225, v225
v_exp_f32 v226, v226
v_exp_f32 v227, v227
M v,240,v,244,v,200
v_exp_f32 v228, v228
v_exp_f32 v229, v229
s_mov_b32 m0, 10240
s_mov_b32 s61, 106624
buffer_load_dwordx4 v23, s[40:43], s61 offen offset:0 nt lds
v_exp_f32 v230, v230
v_exp_f32 v231, v231
A 232,240
A 233,241
A 234,242
A 235,243
A 236,244
A 237,245
s_mov_b32 m0, 11264
s_mov_b32 s61, 110720
buffer_load_dwordx4 v23, s[40:43], s61 offen offset:0 nt lds
A 238,246
A 239,247
v_pk_fma_f32 v[232:233], v[232:233], s[62:63], v[30:31]
v_pk_fma_f32 v[234:235], v[234:235], s[62:63], v[30:31]
v_pk_fma_f32 v[236:237], v[236:237], s[62:63], v[30:31]
v_pk_fma_f32 v[238:239], v[238:239], s[62:63], v[30:31]
v_cvt_pk_bf16_f32 v208, v224, v225
v_cvt_pk_bf16_f32 v209, v226, v227
s_mov_b32 m0, 12288
s_mov_b32 s61, 114816
buffer_load_dwordx4 v23, s[40:43], s61 offen offset:0 nt lds
v_cvt_pk_bf16_f32 v210, v228, v229
v_cvt_pk_bf16_f32 v211, v230, v231
v_exp_f32 v232, v232
v_exp_f32 v233, v233
v_exp_f32 v234, v234
v_exp_f32 v235, v235
M v,240,v,244,v,208
v_exp_f32 v236, v236
s_mov_b32 m0, 13312
s_mov_b32 s61, 118912
buffer_load_dwordx4 v23, s[40:43], s61 offen offset:0 nt lds
v_exp_f32 v237, v237
v_exp_f32 v238, v238
v_exp_f32 v239, v239
s_nop 0
v_cvt_pk_bf16_f32 v216, v232, v233
v_cvt_pk_bf16_f32 v217, v234, v235
v_cvt_pk_bf16_f32 v218, v236, v237
v_cvt_pk_bf16_f32 v219, v238, v239
s_mov_b32 m0, 14336
s_mov_b32 s61, 123008
buffer_load_dwordx4 v23, s[40:43], s61 offen offset:0 nt lds
s_nop 1
M v,240,v,244,v,216
s_nop 7
v_rcp_f32 v28, v240
s_nop 1
s_waitcnt vmcnt(63)
s_barrier
T a,224,13,16384
T a,226,13,18432
T a,228,16,16384
T a,230,16,18432
T a,232,13,16896
T a,234,13,18944
T a,236,16,16896
T a,238,16,18944
T a,240,13,20480
T a,242,13,22528
T a,244,16,20480
W 7
v_mfma_f32_16x16x32_bf16 v[224:227], a[224:227], v[96:99], 0
T a,246,16,22528
T a,248,13,20992
s_mov_b32 m0, 15360
s_mov_b32 s61, 127104
v_mfma_f32_16x16x32_bf16 v[228:231], a[228:231], v[96:99], 0
T a,250,13,23040
buffer_load_dwordx4 v23, s[40:43], s61 offen offset:0 nt lds
T a,252,16,20992
W 7
v_mfma_f32_16x16x32_bf16 v[240:243], a[232:235], v[96:99], 0
T a,254,16,23040
T a,224,13,24576
v_mfma_f32_16x16x32_bf16 v[244:247], a[236:239], v[96:99], 0
T a,226,13,26624
T a,228,16,24576
W 7
M v,224,a,240,v,104
T a,230,16,26624
T a,232,13,25088
M v,228,a,244,v,104
T a,234,13,27136
T a,236,16,25088
W 7
M v,240,a,248,v,104
T a,238,16,27136
T a,240,13,28672
M v,244,a,252,v,104
T a,242,13,30720
T a,244,16,28672
W 7
M v,224,a,224,v,112
T a,246,16,30720
T a,248,13,29184
M v,228,a,228,v,112
T a,250,13,31232
T a,252,16,29184
W 7
M v,240,a,232,v,112
T a,254,16,31232
s_waitcnt vmcnt(63)
s_barrier
T a,224,13,32768
M v,244,a,236,v,112
T a,226,13,34816
T a,228,16,32768
W 7
M v,224,a,240,v,120
T a,230,16,34816
T a,232,13,33280
M v,228,a,244,v,120
T a,234,13,35328
T a,236,16,33280
W 7
M v,240,a,248,v,120
T a,238,16,35328
T a,240,13,36864
M v,244,a,252,v,120
T a,242,13,38912
T a,244,16,36864
W 7
M v,224,a,224,v,128
T a,246,16,38912
T a,248,13,37376
M v,228,a,228,v,128
T a,250,13,39424
T a,252,16,37376
W 7
M v,240,a,232,v,128
T a,254,16,39424
T a,224,13,40960
M v,244,a,236,v,128
T a,226,13,43008
T a,228,16,40960
W 7
M v,224,a,240,v,136
T a,230,16,43008
T a,232,13,41472
M v,228,a,244,v,136
T a,234,13,43520
T a,236,16,41472
W 7
M v,240,a,248,v,136
T a,238,16,43520
T a,240,13,45056
M v,244,a,252,v,136
T a,242,13,47104
T a,244,16,45056
W 7
M v,224,a,224,v,144
T a,246,16,47104
T a,248,13,45568
M v,228,a,228,v,144
T a,250,13,47616
T a,252,16,45568
W 7
M v,240,a,232,v,144
T a,254,16,47616
s_waitcnt vmcnt(63)
s_barrier
T a,224,13,49152
s_mov_b32 m0, 16384
s_mov_b32 s61, 131200
M v,244,a,236,v,144
T a,226,13,51200
buffer_load_dwordx4 v23, s[40:43], s61 offen offset:0 nt lds
s_mov_b32 m0, 17408
T a,228,16,49152
W 7
s_mov_b32 s61, 135296
buffer_load_dwordx4 v23, s[40:43], s61 offen offset:0 nt lds
M v,224,a,240,v,152
T a,230,16,51200
s_mov_b32 m0, 18432
s_mov_b32 s61, 139392
T a,232,13,49664
buffer_load_dwordx4 v23, s[40:43], s61 offen offset:0 nt lds
s_mov_b32 m0, 19456
M v,228,a,244,v,152
T a,234,13,51712
s_mov_b32 s61, 143488
buffer_load_dwordx4 v23, s[40:43], s61 offen offset:0 nt lds
T a,236,16,49664
W 7
s_mov_b32 m0, 20480
s_mov_b32 s61, 147584
M v,240,a,248,v,152
T a,238,16,51712
buffer_load_dwordx4 v23, s[40:43], s61 offen offset:0 nt lds
s_mov_b32 m0, 21504
T a,240,13,53248
s_mov_b32 s61, 151680
buffer_load_dwordx4 v23, s[40:43], s61 offen offset:0 nt lds
M v,244,a,252,v,152
T a,242,13,55296
s_mov_b32 m0, 22528
s_mov_b32 s61, 155776
T a,244,16,53248
W 7
buffer_load_dwordx4 v23, s[40:43], s61 offen offset:0 nt lds
s_mov_b32 m0, 23552
M v,224,a,224,v,160
T a,246,16,55296
s_mov_b32 s61, 159872
buffer_load_dwordx4 v23, s[40:43], s61 offen offset:0 nt lds
T a,248,13,53760
s_mov_b32 m0, 24576
s_mov_b32 s61, 163968
M v,228,a,228,v,160
T a,250,13,55808
)x"
R"x(buffer_load_dwordx4 v23, s[40:43], s61 offen offset:0 nt lds
s_mov_b32 m0, 25600
T a,252,16,53760
W 7
s_mov_b32 s61, 168064
buffer_load_dwordx4 v23, s[40:43], s61 offen offset:0 nt lds
M v,240,a,232,v,160
T a,254,16,55808
s_mov_b32 m0, 26624
s_mov_b32 s61, 172160
T a,224,13,57344
buffer_load_dwordx4 v23, s[40:43], s61 offen offset:0 nt lds
s_mov_b32 m0, 27648
M v,244,a,236,v,160
T a,226,13,59392
s_mov_b32 s61, 176256
buffer_load_dwordx4 v23, s[40:43], s61 offen offset:0 nt lds
T a,228,16,57344
W 7
s_mov_b32 m0, 28672
s_mov_b32 s61, 180352
M v,224,a,240,v,168
T a,230,16,59392
buffer_load_dwordx4 v23, s[40:43], s61 offen offset:0 nt lds
s_mov_b32 m0, 29696
T a,232,13,57856
s_mov_b32 s61, 184448
buffer_load_dwordx4 v23, s[40:43], s61 offen offset:0 nt lds
M v,228,a,244,v,168
T a,234,13,59904
s_mov_b32 m0, 30720
s_mov_b32 s61, 188544
T a,236,16,57856
W 7
buffer_load_dwordx4 v23, s[40:43], s61 offen offset:0 nt lds
s_mov_b32 m0, 31744
M v,240,a,248,v,168
T a,238,16,59904
s_mov_b32 s61, 192640
buffer_load_dwordx4 v23, s[40:43], s61 offen offset:0 nt lds
T a,240,13,61440
M v,244,a,252,v,168
T a,242,13,63488
T a,244,16,61440
W 7
M v,224,a,224,v,176
T a,246,16,63488
T a,248,13,61952
M v,228,a,228,v,176
T a,250,13,64000
T a,252,16,61952
W 7
M v,240,a,232,v,176
T a,254,16,64000
s_waitcnt vmcnt(63)
s_barrier
T a,224,14,0
s_mov_b32 m0, 32768
s_mov_b32 s61, 196736
M v,244,a,236,v,176
T a,226,14,2048
buffer_load_dwordx4 v23, s[40:43], s61 offen offset:0 nt lds
s_mov_b32 m0, 33792
T a,228,17,0
W 7
s_mov_b32 s61, 200832
buffer_load_dwordx4 v23, s[40:43], s61 offen offset:0 nt lds
M v,224,a,240,v,184
T a,230,17,2048
s_mov_b32 m0, 34816
s_mov_b32 s61, 204928
T a,232,14,512
buffer_load_dwordx4 v23, s[40:43], s61 offen offset:0 nt lds
s_mov_b32 m0, 35840
M v,228,a,244,v,184
T a,234,14,2560
s_mov_b32 s61, 209024
buffer_load_dwordx4 v23, s[40:43], s61 offen offset:0 nt lds
T a,236,17,512
W 7
s_mov_b32 m0, 36864
s_mov_b32 s61, 213120
M v,240,a,248,v,184
T a,238,17,2560
buffer_load_dwordx4 v23, s[40:43], s61 offen offset:0 nt lds
s_mov_b32 m0, 37888
T a,240,14,4096
s_mov_b32 s61, 217216
buffer_load_dwordx4 v23, s[40:43], s61 offen offset:0 nt lds
M v,244,a,252,v,184
T a,242,14,6144
s_mov_b32 m0, 38912
s_mov_b32 s61, 221312
T a,244,17,4096
W 7
buffer_load_dwordx4 v23, s[40:43], s61 offen offset:0 nt lds
s_mov_b32 m0, 39936
M v,224,a,224,v,192
T a,246,17,6144
s_mov_b32 s61, 225408
buffer_load_dwordx4 v23, s[40:43], s61 offen offset:0 nt lds
T a,248,14,4608
s_mov_b32 m0, 40960
s_mov_b32 s61, 229504
M v,228,a,228,v,192
T a,250,14,6656
buffer_load_dwordx4 v23, s[40:43], s61 offen offset:0 nt lds
T a,252,17,4608
W 7
M v,240,a,232,v,192
T a,254,17,6656
T a,224,14,8192
s_mov_b32 m0, 41984
s_mov_b32 s61, 233600
M v,244,a,236,v,192
T a,226,14,10240
buffer_load_dwordx4 v23, s[40:43], s61 offen offset:0 nt lds
T a,228,17,8192
W 7
s_mov_b32 m0, 43008
s_mov_b32 s61, 237696
M v,224,a,240,v,200
T a,230,17,10240
buffer_load_dwordx4 v23, s[40:43], s61 offen offset:0 nt lds
T a,232,14,8704
M v,228,a,244,v,200
T a,234,14,10752
T a,236,17,8704
W 7
s_mov_b32 m0, 44032
s_mov_b32 s61, 241792
M v,240,a,248,v,200
T a,238,17,10752
buffer_load_dwordx4 v23, s[40:43], s61 offen offset:0 nt lds
T a,240,14,12288
M v,244,a,252,v,200
T a,242,14,14336
T a,244,17,12288
W 7
s_mov_b32 m0, 45056
s_mov_b32 s61, 245888
M v,224,a,224,v,208
T a,246,17,14336
buffer_load_dwordx4 v23, s[40:43], s61 offen offset:0 nt lds
T a,248,14,12800
M v,228,a,228,v,208
T a,250,14,14848
T a,252,17,12800
W 7
s_mov_b32 m0, 46080
s_mov_b32 s61, 249984
M v,240,a,232,v,208
T a,254,17,14848
buffer_load_dwordx4 v23, s[40:43], s61 offen offset:0 nt lds
s_waitcnt vmcnt(63)
s_barrier
T a,224,8,16384
M v,244,a,236,v,208
T a,226,8,24576
T a,228,11,16384
W 7
s_mov_b32 m0, 47104
s_mov_b32 s61, 254080
M v,224,a,240,v,216
T a,230,11,24576
buffer_load_dwordx4 v23, s[40:43], s61 offen offset:0 nt lds
T a,232,8,16896
M v,228,a,244,v,216
T a,234,8,25088
T a,236,11,16896
W 7
s_mov_b32 m0, 48128
s_mov_b32 s61, 258176
M v,240,a,248,v,216
T a,238,11,25088
buffer_load_dwordx4 v23, s[40:43], s61 offen offset:0 nt lds
T a,240,8,17408
M v,244,a,252,v,216
T a,242,8,25600
s_nop 7
v_mul_f32 v224, v28, v224
v_mul_f32 v225, v28, v225
s_mov_b32 m0, 49152
s_mov_b32 s61, 57344
buffer_load_dwordx4 v24, s[52:55], s61 offen offset:0 lds
v_mul_f32 v226, v28, v226
v_mul_f32 v227, v28, v227
s_mov_b32 m0, 50048
s_nop 0
buffer_load_dwordx4 v24, s[52:55], s61 offen offset:128 lds
v_mul_f32 v228, v28, v228
v_mul_f32 v229, v28, v229
s_mov_b32 m0, 50944
s_nop 0
buffer_load_dwordx4 v24, s[52:55], s61 offen offset:256 lds
v_mul_f32 v230, v28, v230
v_mul_f32 v231, v28, v231
s_mov_b32 m0, 51840
s_nop 0
buffer_load_dwordx4 v24, s[52:55], s61 offen offset:384 lds
v_cvt_pk_bf16_f32 v224, v224, v225
v_cvt_pk_bf16_f32 v225, v226, v227
s_mov_b32 m0, 53248
s_mov_b32 s61, 64512
buffer_load_dwordx4 v24, s[52:55], s61 offen offset:0 lds
v_cvt_pk_bf16_f32 v226, v228, v229
v_cvt_pk_bf16_f32 v227, v230, v231
s_mov_b32 m0, 54144
s_nop 0
buffer_load_dwordx4 v24, s[52:55], s61 offen offset:128 lds
v_mul_f32 v240, v28, v240
v_mul_f32 v241, v28, v241
s_mov_b32 m0, 55040
s_nop 0
buffer_load_dwordx4 v24, s[52:55], s61 offen offset:256 lds
v_mul_f32 v242, v28, v242
v_mul_f32 v243, v28, v243
s_mov_b32 m0, 55936
s_nop 0
buffer_load_dwordx4 v24, s[52:55], s61 offen offset:384 lds
v_mul_f32 v244, v28, v244
v_mul_f32 v245, v28, v245
s_mov_b32 m0, 57344
s_mov_b32 s61, 71680
buffer_load_dwordx4 v24, s[52:55], s61 offen offset:0 lds
v_mul_f32 v246, v28, v246
v_mul_f32 v247, v28, v247
s_mov_b32 m0, 58240
s_nop 0
buffer_load_dwordx4 v24, s[52:55], s61 offen offset:128 lds
v_cvt_pk_bf16_f32 v240, v240, v241
v_cvt_pk_bf16_f32 v241, v242, v243
s_mov_b32 m0, 59136
s_nop 0
buffer_load_dwordx4 v24, s[52:55], s61 offen offset:256 lds
v_cvt_pk_bf16_f32 v242, v244, v245
v_cvt_pk_bf16_f32 v243, v246, v247
s_mov_b32 m0, 60032
s_nop 0
buffer_load_dwordx4 v24, s[52:55], s61 offen offset:384 lds
s_nop 1
T a,244,11,17408
W 7
s_mov_b32 m0, 61440
s_mov_b32 s61, 78848
v_mfma_f32_16x16x32_bf16 a[0:3], a[224:227], v[224:227], 0
T a,246,11,25600
buffer_load_dwordx4 v24, s[52:55], s61 offen offset:0 lds
s_mov_b32 m0, 62336
T a,248,8,17920
buffer_load_dwordx4 v24, s[52:55], s61 offen offset:128 lds
s_mov_b32 m0, 63232
v_mfma_f32_16x16x32_bf16 a[4:7], a[228:231], v[224:227], 0
T a,250,8,26112
buffer_load_dwordx4 v24, s[52:55], s61 offen offset:256 lds
s_mov_b32 m0, 64128
T a,252,11,17920
W 7
buffer_load_dwordx4 v24, s[52:55], s61 offen offset:384 lds
v_mfma_f32_16x16x32_bf16 a[16:19], a[232:235], v[224:227], 0
T a,254,11,26112
T a,224,8,18432
v_mfma_f32_16x16x32_bf16 a[20:23], a[236:239], v[224:227], 0
T a,226,8,26624
T a,228,11,18432
W 7
v_mfma_f32_16x16x32_bf16 a[32:35], a[240:243], v[224:227], 0
T a,230,11,26624
T a,232,8,18944
v_mfma_f32_16x16x32_bf16 a[36:39], a[244:247], v[224:227], 0
T a,234,8,27136
T a,236,11,18944
W 7
v_mfma_f32_16x16x32_bf16 a[48:51], a[248:251], v[224:227], 0
T a,238,11,27136
T a,240,8,19456
v_mfma_f32_16x16x32_bf16 a[52:55], a[252:255], v[224:227], 0
T a,242,8,27648
T a,244,11,19456
W 7
v_mfma_f32_16x16x32_bf16 a[64:67], a[224:227], v[224:227], 0
T a,246,11,27648
T a,248,8,19968
v_mfma_f32_16x16x32_bf16 a[68:71], a[228:231], v[224:227], 0
T a,250,8,28160
T a,252,11,19968
W 7
v_mfma_f32_16x16x32_bf16 a[80:83], a[232:235], v[224:227], 0
T a,254,11,28160
s_waitcnt vmcnt(63)
s_barrier
.Lc1_64_%=:
T a,224,8,32768
s_mov_b32 s61, .Lc0_64_%=-.Lpf_start%=+8192
buffer_load_dword v29, v20, s[92:95], s61 offen
v_mfma_f32_16x16x32_bf16 a[84:87], a[236:239], v[224:227], 0
T a,226,8,40960
s_mov_b32 s61, .Lc1_64_%=-.Lpf_start%=+8192
buffer_load_dword v29, v20, s[92:95], s61 offen
T a,228,11,32768
W 7
s_mov_b32 m0, 65024
s_mov_b32 s61, 57344
v_mfma_f32_16x16x32_bf16 a[96:99], a[240:243], v[224:227], 0
T a,230,11,40960
buffer_load_dwordx4 v24, s[52:55], s61 offen offset:512 lds
s_mov_b32 m0, 65920
T a,232,8,33280
buffer_load_dwordx4 v24, s[52:55], s61 offen offset:640 lds
s_mov_b32 m0, 66816
v_mfma_f32_16x16x32_bf16 a[100:103], a[244:247], v[224:227], 0
T a,234,8,41472
buffer_load_dwordx4 v24, s[52:55], s61 offen offset:768 lds
s_mov_b32 m0, 69120
T a,236,11,33280
W 7
s_mov_b32 s61, 64512
buffer_load_dwordx4 v24, s[52:55], s61 offen offset:512 lds
v_mfma_f32_16x16x32_bf16 a[112:115], a[248:251], v[224:227], 0
T a,238,11,41472
s_mov_b32 m0, 70016
T a,240,8,33792
buffer_load_dwordx4 v24, s[52:55], s61 offen offset:640 lds
s_mov_b32 m0, 70912
v_mfma_f32_16x16x32_bf16 a[116:119], a[252:255], v[224:227], 0
T a,242,8,41984
buffer_load_dwordx4 v24, s[52:55], s61 offen offset:768 lds
s_mov_b32 m0, 73216
T a,244,11,33792
W 7
s_mov_b32 s61, 71680
buffer_load_dwordx4 v24, s[52:55], s61 offen offset:512 lds
v_mfma_f32_16x16x32_bf16 a[128:131], a[224:227], v[224:227], 0
T a,246,11,41984
s_mov_b32 m0, 74112
T a,248,8,34304
buffer_load_dwordx4 v24, s[52:55], s61 offen offset:640 lds
s_mov_b32 m0, 75008
v_mfma_f32_16x16x32_bf16 a[132:135], a[228:231], v[224:227], 0
T a,250,8,42496
buffer_load_dwordx4 v24, s[52:55], s61 offen offset:768 lds
s_mov_b32 m0, 77312
T a,252,11,34304
W 7
s_mov_b32 s61, 78848
buffer_load_dwordx4 v24, s[52:55], s61 offen offset:512 lds
v_mfma_f32_16x16x32_bf16 a[144:147], a[232:235], v[224:227], 0
T a,254,11,42496
s_mov_b32 m0, 78208
T a,224,8,34816
buffer_load_dwordx4 v24, s[52:55], s61 offen offset:640 lds
s_mov_b32 m0, 79104
v_mfma_f32_16x16x32_bf16 a[148:151], a[236:239], v[224:227], 0
T a,226,8,43008
buffer_load_dwordx4 v24, s[52:55], s61 offen offset:768 lds
T a,228,11,34816
W 7
v_mfma_f32_16x16x32_bf16 a[160:163], a[240:243], v[224:227], 0
T a,230,11,43008
T a,232,8,35328
v_mfma_f32_16x16x32_bf16 a[164:167], a[244:247], v[224:227], 0
T a,234,8,43520
T a,236,11,35328
W 7
v_mfma_f32_16x16x32_bf16 a[176:179], a[248:251], v[224:227], 0
T a,238,11,43520
s_waitcnt vmcnt(63)
s_barrier
T a,240,8,49152
s_mov_b32 m0, 81920
s_mov_b32 s61, 86016
v_mfma_f32_16x16x32_bf16 a[180:183], a[252:255], v[224:227], 0
T a,242,8,57344
buffer_load_dwordx4 v24, s[52:55], s61 offen offset:0 lds
s_mov_b32 m0, 82816
T a,244,11,49152
W 7
buffer_load_dwordx4 v24, s[52:55], s61 offen offset:128 lds
s_mov_b32 m0, 83712
v_mfma_f32_16x16x32_bf16 a[192:195], a[224:227], v[224:227], 0
T a,246,11,57344
buffer_load_dwordx4 v24, s[52:55], s61 offen offset:256 lds
s_mov_b32 m0, 84608
T a,248,8,49664
buffer_load_dwordx4 v24, s[52:55], s61 offen offset:384 lds
s_mov_b32 m0, 86016
v_mfma_f32_16x16x32_bf16 a[196:199], a[228:231], v[224:227], 0
T a,250,8,57856
s_mov_b32 s61, 93184
buffer_load_dwordx4 v24, s[52:55], s61 offen offset:0 lds
T a,252,11,49664
W 7
s_mov_b32 m0, 86912
v_mfma_f32_16x16x32_bf16 a[208:211], a[232:235], v[224:227], 0
T a,254,11,57856
buffer_load_dwordx4 v24, s[52:55], s61 offen offset:128 lds
s_mov_b32 m0, 87808
T a,224,8,50176
buffer_load_dwordx4 v24, s[52:55], s61 offen offset:256 lds
s_mov_b32 m0, 88704
)x"
R"x(v_mfma_f32_16x16x32_bf16 a[212:215], a[236:239], v[224:227], 0
T a,226,8,58368
buffer_load_dwordx4 v24, s[52:55], s61 offen offset:384 lds
s_mov_b32 m0, 90112
T a,228,11,50176
W 7
s_mov_b32 s61, 100352
buffer_load_dwordx4 v24, s[52:55], s61 offen offset:0 lds
M a,0,a,240,v,240
T a,230,11,58368
s_mov_b32 m0, 91008
T a,232,8,50688
buffer_load_dwordx4 v24, s[52:55], s61 offen offset:128 lds
s_mov_b32 m0, 91904
M a,4,a,244,v,240
T a,234,8,58880
buffer_load_dwordx4 v24, s[52:55], s61 offen offset:256 lds
s_mov_b32 m0, 92800
T a,236,11,50688
W 7
buffer_load_dwordx4 v24, s[52:55], s61 offen offset:384 lds
s_mov_b32 m0, 94208
M a,16,a,248,v,240
T a,238,11,58880
s_mov_b32 s61, 107520
buffer_load_dwordx4 v24, s[52:55], s61 offen offset:0 lds
T a,240,8,51200
s_mov_b32 m0, 95104
M a,20,a,252,v,240
T a,242,8,59392
buffer_load_dwordx4 v24, s[52:55], s61 offen offset:128 lds
s_mov_b32 m0, 96000
T a,244,11,51200
W 7
buffer_load_dwordx4 v24, s[52:55], s61 offen offset:256 lds
s_mov_b32 m0, 96896
M a,32,a,224,v,240
T a,246,11,59392
buffer_load_dwordx4 v24, s[52:55], s61 offen offset:384 lds
T a,248,8,51712
M a,36,a,228,v,240
T a,250,8,59904
T a,252,11,51712
W 7
M a,48,a,232,v,240
T a,254,11,59904
T a,224,8,52224
M a,52,a,236,v,240
T a,226,8,60416
T a,228,11,52224
W 7
M a,64,a,240,v,240
T a,230,11,60416
T a,232,8,52736
M a,68,a,244,v,240
T a,234,8,60928
T a,236,11,52736
W 7
M a,80,a,248,v,240
T a,238,11,60928
s_waitcnt vmcnt(63)
s_barrier
T a,240,9,0
s_mov_b32 m0, 97792
s_mov_b32 s61, 86016
M a,84,a,252,v,240
T a,242,9,8192
buffer_load_dwordx4 v24, s[52:55], s61 offen offset:512 lds
s_mov_b32 m0, 98688
T a,244,12,0
W 7
buffer_load_dwordx4 v24, s[52:55], s61 offen offset:640 lds
s_mov_b32 m0, 99584
M a,96,a,224,v,240
T a,246,12,8192
buffer_load_dwordx4 v24, s[52:55], s61 offen offset:768 lds
s_mov_b32 m0, 101888
T a,248,9,512
s_mov_b32 s61, 93184
buffer_load_dwordx4 v24, s[52:55], s61 offen offset:512 lds
M a,100,a,228,v,240
T a,250,9,8704
s_mov_b32 m0, 102784
T a,252,12,512
W 7
buffer_load_dwordx4 v24, s[52:55], s61 offen offset:640 lds
s_mov_b32 m0, 103680
M a,112,a,232,v,240
T a,254,12,8704
buffer_load_dwordx4 v24, s[52:55], s61 offen offset:768 lds
s_mov_b32 m0, 105984
T a,224,9,1024
s_mov_b32 s61, 100352
buffer_load_dwordx4 v24, s[52:55], s61 offen offset:512 lds
M a,116,a,236,v,240
T a,226,9,9216
s_mov_b32 m0, 106880
T a,228,12,1024
W 7
buffer_load_dwordx4 v24, s[52:55], s61 offen offset:640 lds
s_mov_b32 m0, 107776
M a,128,a,240,v,240
T a,230,12,9216
buffer_load_dwordx4 v24, s[52:55], s61 offen offset:768 lds
s_mov_b32 m0, 110080
T a,232,9,1536
s_mov_b32 s61, 107520
buffer_load_dwordx4 v24, s[52:55], s61 offen offset:512 lds
M a,132,a,244,v,240
T a,234,9,9728
s_mov_b32 m0, 110976
T a,236,12,1536
W 7
buffer_load_dwordx4 v24, s[52:55], s61 offen offset:640 lds
s_mov_b32 m0, 111872
M a,144,a,248,v,240
T a,238,12,9728
buffer_load_dwordx4 v24, s[52:55], s61 offen offset:768 lds
T a,240,9,2048
M a,148,a,252,v,240
T a,242,9,10240
T a,244,12,2048
W 7
M a,160,a,224,v,240
T a,246,12,10240
T a,248,9,2560
M a,164,a,228,v,240
T a,250,9,10752
T a,252,12,2560
W 7
M a,176,a,232,v,240
T a,254,12,10752
s_waitcnt vmcnt(63)
s_barrier
T a,224,15,16384
s_mov_b32 m0, 114688
s_mov_b32 s61, 256
M a,180,a,236,v,240
T a,226,15,18432
buffer_load_dwordx4 v23, s[40:43], s61 offen offset:0 nt lds
s_mov_b32 m0, 115712
T a,228,18,16384
W 7
s_mov_b32 s61, 4352
buffer_load_dwordx4 v23, s[40:43], s61 offen offset:0 nt lds
M a,192,a,240,v,240
T a,230,18,18432
s_mov_b32 m0, 116736
s_mov_b32 s61, 8448
T a,232,15,16896
buffer_load_dwordx4 v23, s[40:43], s61 offen offset:0 nt lds
s_mov_b32 m0, 117760
M a,196,a,244,v,240
T a,234,15,18944
s_mov_b32 s61, 12544
buffer_load_dwordx4 v23, s[40:43], s61 offen offset:0 nt lds
T a,236,18,16896
W 7
s_mov_b32 m0, 118784
s_mov_b32 s61, 16640
M a,208,a,248,v,240
T a,238,18,18944
buffer_load_dwordx4 v23, s[40:43], s61 offen offset:0 nt lds
s_mov_b32 m0, 119808
T a,240,15,20480
s_mov_b32 s61, 20736
buffer_load_dwordx4 v23, s[40:43], s61 offen offset:0 nt lds
M a,212,a,252,v,240
T a,242,15,22528
s_mov_b32 m0, 120832
s_mov_b32 s61, 24832
T a,244,18,20480
W 7
buffer_load_dwordx4 v23, s[40:43], s61 offen offset:0 nt lds
s_mov_b32 m0, 121856
v_mfma_f32_16x16x32_bf16 v[224:227], a[224:227], v[96:99], 0
T a,246,18,22528
s_mov_b32 s61, 28928
buffer_load_dwordx4 v23, s[40:43], s61 offen offset:0 nt lds
T a,248,15,20992
s_mov_b32 m0, 122880
s_mov_b32 s61, 33024
v_mfma_f32_16x16x32_bf16 v[228:231], a[228:231], v[96:99], 0
T a,250,15,23040
buffer_load_dwordx4 v23, s[40:43], s61 offen offset:0 nt lds
s_mov_b32 m0, 123904
T a,252,18,20992
W 7
s_mov_b32 s61, 37120
buffer_load_dwordx4 v23, s[40:43], s61 offen offset:0 nt lds
v_mfma_f32_16x16x32_bf16 v[240:243], a[232:235], v[96:99], 0
T a,254,18,23040
s_mov_b32 m0, 124928
s_mov_b32 s61, 41216
T a,224,15,24576
buffer_load_dwordx4 v23, s[40:43], s61 offen offset:0 nt lds
s_mov_b32 m0, 125952
v_mfma_f32_16x16x32_bf16 v[244:247], a[236:239], v[96:99], 0
T a,226,15,26624
s_mov_b32 s61, 45312
buffer_load_dwordx4 v23, s[40:43], s61 offen offset:0 nt lds
T a,228,18,24576
W 7
s_mov_b32 m0, 126976
s_mov_b32 s61, 49408
M v,224,a,240,v,104
T a,230,18,26624
buffer_load_dwordx4 v23, s[40:43], s61 offen offset:0 nt lds
s_mov_b32 m0, 128000
T a,232,15,25088
s_mov_b32 s61, 53504
buffer_load_dwordx4 v23, s[40:43], s61 offen offset:0 nt lds
M v,228,a,244,v,104
T a,234,15,27136
s_mov_b32 m0, 129024
s_mov_b32 s61, 57600
T a,236,18,25088
W 7
buffer_load_dwordx4 v23, s[40:43], s61 offen offset:0 nt lds
s_mov_b32 m0, 130048
M v,240,a,248,v,104
T a,238,18,27136
s_mov_b32 s61, 61696
buffer_load_dwordx4 v23, s[40:43], s61 offen offset:0 nt lds
T a,240,15,28672
M v,244,a,252,v,104
T a,242,15,30720
T a,244,18,28672
W 7
M v,224,a,224,v,112
T a,246,18,30720
T a,248,15,29184
M v,228,a,228,v,112
T a,250,15,31232
T a,252,18,29184
W 7
M v,240,a,232,v,112
T a,254,18,31232
s_waitcnt vmcnt(63)
s_barrier
T a,224,13,0
s_mov_b32 m0, 131072
s_mov_b32 s61, 65792
M v,244,a,236,v,112
T a,226,13,2048
buffer_load_dwordx4 v23, s[40:43], s61 offen offset:0 nt lds
s_mov_b32 m0, 132096
T a,228,16,0
W 7
s_mov_b32 s61, 69888
buffer_load_dwordx4 v23, s[40:43], s61 offen offset:0 nt lds
M v,224,a,240,v,120
T a,230,16,2048
s_mov_b32 m0, 133120
s_mov_b32 s61, 73984
T a,232,13,512
buffer_load_dwordx4 v23, s[40:43], s61 offen offset:0 nt lds
s_mov_b32 m0, 134144
M v,228,a,244,v,120
T a,234,13,2560
s_mov_b32 s61, 78080
buffer_load_dwordx4 v23, s[40:43], s61 offen offset:0 nt lds
T a,236,16,512
W 7
s_mov_b32 m0, 135168
s_mov_b32 s61, 82176
M v,240,a,248,v,120
T a,238,16,2560
buffer_load_dwordx4 v23, s[40:43], s61 offen offset:0 nt lds
s_mov_b32 m0, 136192
T a,240,13,4096
s_mov_b32 s61, 86272
buffer_load_dwordx4 v23, s[40:43], s61 offen offset:0 nt lds
M v,244,a,252,v,120
T a,242,13,6144
s_mov_b32 m0, 137216
s_mov_b32 s61, 90368
T a,244,16,4096
W 7
buffer_load_dwordx4 v23, s[40:43], s61 offen offset:0 nt lds
s_mov_b32 m0, 138240
M v,224,a,224,v,128
T a,246,16,6144
s_mov_b32 s61, 94464
buffer_load_dwordx4 v23, s[40:43], s61 offen offset:0 nt lds
T a,248,13,4608
s_mov_b32 m0, 139264
s_mov_b32 s61, 98560
M v,228,a,228,v,128
T a,250,13,6656
buffer_load_dwordx4 v23, s[40:43], s61 offen offset:0 nt lds
s_mov_b32 m0, 140288
T a,252,16,4608
W 7
s_mov_b32 s61, 102656
buffer_load_dwordx4 v23, s[40:43], s61 offen offset:0 nt lds
M v,240,a,232,v,128
T a,254,16,6656
s_mov_b32 m0, 141312
s_mov_b32 s61, 106752
T a,224,13,8192
buffer_load_dwordx4 v23, s[40:43], s61 offen offset:0 nt lds
s_mov_b32 m0, 142336
M v,244,a,236,v,128
T a,226,13,10240
s_mov_b32 s61, 110848
buffer_load_dwordx4 v23, s[40:43], s61 offen offset:0 nt lds
T a,228,16,8192
W 7
s_mov_b32 m0, 143360
s_mov_b32 s61, 114944
M v,224,a,240,v,136
T a,230,16,10240
buffer_load_dwordx4 v23, s[40:43], s61 offen offset:0 nt lds
s_mov_b32 m0, 144384
T a,232,13,8704
s_mov_b32 s61, 119040
buffer_load_dwordx4 v23, s[40:43], s61 offen offset:0 nt lds
M v,228,a,244,v,136
T a,234,13,10752
s_mov_b32 m0, 145408
s_mov_b32 s61, 123136
T a,236,16,8704
W 7
buffer_load_dwordx4 v23, s[40:43], s61 offen offset:0 nt lds
s_mov_b32 m0, 146432
M v,240,a,248,v,136
T a,238,16,10752
s_mov_b32 s61, 127232
buffer_load_dwordx4 v23, s[40:43], s61 offen offset:0 nt lds
T a,240,13,12288
M v,244,a,252,v,136
T a,242,13,14336
T a,244,16,12288
W 7
M v,224,a,224,v,144
T a,246,16,14336
T a,248,13,12800
M v,228,a,228,v,144
T a,250,13,14848
T a,252,16,12800
W 7
M v,240,a,232,v,144
T a,254,16,14848
s_waitcnt vmcnt(63)
s_barrier
T a,224,13,16384
s_mov_b32 m0, 147456
s_mov_b32 s61, 131328
M v,244,a,236,v,144
T a,226,13,18432
buffer_load_dwordx4 v23, s[40:43], s61 offen offset:0 nt lds
s_mov_b32 m0, 148480
T a,228,16,16384
W 7
s_mov_b32 s61, 135424
buffer_load_dwordx4 v23, s[40:43], s61 offen offset:0 nt lds
M v,224,a,240,v,152
T a,230,16,18432
s_mov_b32 m0, 149504
s_mov_b32 s61, 139520
T a,232,13,16896
buffer_load_dwordx4 v23, s[40:43], s61 offen offset:0 nt lds
s_mov_b32 m0, 150528
M v,228,a,244,v,152
T a,234,13,18944
s_mov_b32 s61, 143616
buffer_load_dwordx4 v23, s[40:43], s61 offen offset:0 nt lds
T a,236,16,16896
W 7
M v,240,a,248,v,152
T a,238,16,18944
T a,240,13,20480
s_mov_b32 m0, 151552
s_mov_b32 s61, 147712
M v,244,a,252,v,152
T a,242,13,22528
buffer_load_dwordx4 v23, s[40:43], s61 offen offset:0 nt lds
T a,244,16,20480
W 7
M v,224,a,224,v,160
T a,246,16,22528
T a,248,13,20992
s_mov_b32 m0, 152576
s_mov_b32 s61, 151808
M v,228,a,228,v,160
T a,250,13,23040
buffer_load_dwordx4 v23, s[40:43], s61 offen offset:0 nt lds
T a,252,16,20992
W 7
M v,240,a,232,v,160
T a,254,16,23040
T a,224,13,24576
s_mov_b32 m0, 153600
s_mov_b32 s61, 155904
M v,244,a,236,v,160
T a,226,13,26624
buffer_load_dwordx4 v23, s[40:43], s61 offen offset:0 nt lds
T a,228,16,24576
W 7
)x"
R"x(s_mov_b32 m0, 154624
s_mov_b32 s61, 160000
M v,224,a,240,v,168
T a,230,16,26624
buffer_load_dwordx4 v23, s[40:43], s61 offen offset:0 nt lds
T a,232,13,25088
M v,228,a,244,v,168
T a,234,13,27136
T a,236,16,25088
W 7
s_mov_b32 m0, 155648
s_mov_b32 s61, 164096
M v,240,a,248,v,168
T a,238,16,27136
buffer_load_dwordx4 v23, s[40:43], s61 offen offset:0 nt lds
T a,240,13,28672
M v,244,a,252,v,168
T a,242,13,30720
T a,244,16,28672
W 7
s_mov_b32 m0, 156672
s_mov_b32 s61, 168192
M v,224,a,224,v,176
T a,246,16,30720
buffer_load_dwordx4 v23, s[40:43], s61 offen offset:0 nt lds
T a,248,13,29184
M v,228,a,228,v,176
T a,250,13,31232
T a,252,16,29184
W 7
s_mov_b32 m0, 157696
s_mov_b32 s61, 172288
M v,240,a,232,v,176
T a,254,16,31232
buffer_load_dwordx4 v23, s[40:43], s61 offen offset:0 nt lds
s_waitcnt vmcnt(63)
s_barrier
.Lc1_70_%=:
T a,224,13,32768
M v,244,a,236,v,176
T a,226,13,34816
T a,228,16,32768
W 7
s_mov_b32 m0, 158720
s_mov_b32 s61, 176384
M v,224,a,240,v,184
T a,230,16,34816
buffer_load_dwordx4 v23, s[40:43], s61 offen offset:0 nt lds
T a,232,13,33280
M v,228,a,244,v,184
T a,234,13,35328
T a,236,16,33280
W 7
s_mov_b32 m0, 159744
s_mov_b32 s61, 180480
M v,240,a,248,v,184
T a,238,16,35328
buffer_load_dwordx4 v23, s[40:43], s61 offen offset:0 nt lds
T a,240,13,36864
M v,244,a,252,v,184
T a,242,13,38912
T a,244,16,36864
W 7
s_mov_b32 m0, 160768
s_mov_b32 s61, 184576
M v,224,a,224,v,192
T a,246,16,38912
buffer_load_dwordx4 v23, s[40:43], s61 offen offset:0 nt lds
T a,248,13,37376
M v,228,a,228,v,192
T a,250,13,39424
T a,252,16,37376
W 7
s_mov_b32 m0, 161792
s_mov_b32 s61, 188672
M v,240,a,232,v,192
T a,254,16,39424
buffer_load_dwordx4 v23, s[40:43], s61 offen offset:0 nt lds
T a,224,13,40960
M v,244,a,236,v,192
T a,226,13,43008
T a,228,16,40960
W 7
s_mov_b32 m0, 162816
s_mov_b32 s61, 192768
M v,224,a,240,v,200
T a,230,16,43008
buffer_load_dwordx4 v23, s[40:43], s61 offen offset:0 nt lds
T a,232,13,41472
M v,228,a,244,v,200
T a,234,13,43520
T a,236,16,41472
W 7
s_mov_b32 s61, .Lc0_70_%=-.Lpf_start%=+8192
buffer_load_dword v29, v20, s[92:95], s61 offen
M v,240,a,248,v,200
T a,238,16,43520
T a,240,13,45056
s_mov_b32 s61, .Lc1_70_%=-.Lpf_start%=+8192
buffer_load_dword v29, v20, s[92:95], s61 offen
M v,244,a,252,v,200
T a,242,13,47104
T a,244,16,45056
W 7
M v,224,a,224,v,208
T a,246,16,47104
T a,248,13,45568
s_mov_b32 m0, 0
s_mov_b32 s61, 196864
M v,228,a,228,v,208
T a,250,13,47616
buffer_load_dwordx4 v23, s[40:43], s61 offen offset:0 nt lds
T a,252,16,45568
W 7
M v,240,a,232,v,208
T a,254,16,47616
s_waitcnt vmcnt(63)
s_barrier
T a,224,7,49152
s_mov_b32 m0, 1024
s_mov_b32 s61, 200960
M v,244,a,236,v,208
T a,226,7,57344
buffer_load_dwordx4 v23, s[40:43], s61 offen offset:0 nt lds
T a,228,10,49152
W 7
M v,224,a,240,v,216
T a,230,10,57344
T a,232,7,49664
s_mov_b32 m0, 2048
s_mov_b32 s61, 205056
M v,228,a,244,v,216
T a,234,7,57856
buffer_load_dwordx4 v23, s[40:43], s61 offen offset:0 nt lds
T a,236,10,49664
W 7
M v,240,a,248,v,216
T a,238,10,57856
T a,240,7,50176
s_mov_b32 m0, 3072
s_mov_b32 s61, 209152
M v,244,a,252,v,216
T a,242,7,58368
buffer_load_dwordx4 v23, s[40:43], s61 offen offset:0 nt lds
s_nop 7
v_mul_f32 v224, v28, v224
v_mul_f32 v225, v28, v225
v_mul_f32 v226, v28, v226
v_mul_f32 v227, v28, v227
s_mov_b32 m0, 4096
s_mov_b32 s61, 213248
buffer_load_dwordx4 v23, s[40:43], s61 offen offset:0 nt lds
v_mul_f32 v228, v28, v228
v_mul_f32 v229, v28, v229
v_mul_f32 v230, v28, v230
v_mul_f32 v231, v28, v231
v_cvt_pk_bf16_f32 v224, v224, v225
v_cvt_pk_bf16_f32 v225, v226, v227
v_cvt_pk_bf16_f32 v226, v228, v229
v_cvt_pk_bf16_f32 v227, v230, v231
s_mov_b32 m0, 5120
s_mov_b32 s61, 217344
buffer_load_dwordx4 v23, s[40:43], s61 offen offset:0 nt lds
v_mul_f32 v240, v28, v240
v_mul_f32 v241, v28, v241
v_mul_f32 v242, v28, v242
v_mul_f32 v243, v28, v243
v_mul_f32 v244, v28, v244
v_mul_f32 v245, v28, v245
v_mul_f32 v246, v28, v246
v_mul_f32 v247, v28, v247
s_mov_b32 m0, 6144
s_mov_b32 s61, 221440
buffer_load_dwordx4 v23, s[40:43], s61 offen offset:0 nt lds
v_cvt_pk_bf16_f32 v240, v240, v241
v_cvt_pk_bf16_f32 v241, v242, v243
v_cvt_pk_bf16_f32 v242, v244, v245
v_cvt_pk_bf16_f32 v243, v246, v247
s_nop 1
T a,244,10,50176
W 7
M a,0,a,224,v,224
T a,246,10,58368
T a,248,7,50688
s_mov_b32 m0, 7168
s_mov_b32 s61, 225536
M a,4,a,228,v,224
T a,250,7,58880
buffer_load_dwordx4 v23, s[40:43], s61 offen offset:0 nt lds
T a,252,10,50688
W 7
M a,16,a,232,v,224
T a,254,10,58880
T a,224,7,51200
s_mov_b32 m0, 8192
s_mov_b32 s61, 229632
M a,20,a,236,v,224
T a,226,7,59392
buffer_load_dwordx4 v23, s[40:43], s61 offen offset:0 nt lds
T a,228,10,51200
W 7
M a,32,a,240,v,224
T a,230,10,59392
T a,232,7,51712
s_mov_b32 m0, 9216
s_mov_b32 s61, 233728
M a,36,a,244,v,224
T a,234,7,59904
buffer_load_dwordx4 v23, s[40:43], s61 offen offset:0 nt lds
T a,236,10,51712
W 7
M a,48,a,248,v,224
T a,238,10,59904
T a,240,7,52224
s_mov_b32 m0, 10240
s_mov_b32 s61, 237824
M a,52,a,252,v,224
T a,242,7,60416
buffer_load_dwordx4 v23, s[40:43], s61 offen offset:0 nt lds
T a,244,10,52224
W 7
M a,64,a,224,v,224
T a,246,10,60416
T a,248,7,52736
s_mov_b32 m0, 11264
s_mov_b32 s61, 241920
M a,68,a,228,v,224
T a,250,7,60928
buffer_load_dwordx4 v23, s[40:43], s61 offen offset:0 nt lds
T a,252,10,52736
W 7
M a,80,a,232,v,224
T a,254,10,60928
s_waitcnt vmcnt(63)
s_barrier
T a,224,8,0
s_mov_b32 m0, 12288
s_mov_b32 s61, 246016
M a,84,a,236,v,224
T a,226,8,8192
buffer_load_dwordx4 v23, s[40:43], s61 offen offset:0 nt lds
T a,228,11,0
W 7
s_mov_b32 m0, 13312
s_mov_b32 s61, 250112
M a,96,a,240,v,224
T a,230,11,8192
buffer_load_dwordx4 v23, s[40:43], s61 offen offset:0 nt lds
T a,232,8,512
M a,100,a,244,v,224
T a,234,8,8704
T a,236,11,512
W 7
s_mov_b32 m0, 14336
s_mov_b32 s61, 254208
M a,112,a,248,v,224
T a,238,11,8704
buffer_load_dwordx4 v23, s[40:43], s61 offen offset:0 nt lds
T a,240,8,1024
M a,116,a,252,v,224
T a,242,8,9216
T a,244,11,1024
W 7
s_mov_b32 m0, 15360
s_mov_b32 s61, 258304
M a,128,a,224,v,224
T a,246,11,9216
buffer_load_dwordx4 v23, s[40:43], s61 offen offset:0 nt lds
T a,248,8,1536
M a,132,a,228,v,224
T a,250,8,9728
T a,252,11,1536
W 7
s_mov_b32 m0, 16384
s_mov_b32 s61, 114688
M a,144,a,232,v,224
T a,254,11,9728
buffer_load_dwordx4 v24, s[52:55], s61 offen offset:0 lds
s_mov_b32 m0, 17280
T a,224,8,2048
buffer_load_dwordx4 v24, s[52:55], s61 offen offset:128 lds
s_mov_b32 m0, 18176
M a,148,a,236,v,224
T a,226,8,10240
buffer_load_dwordx4 v24, s[52:55], s61 offen offset:256 lds
s_mov_b32 m0, 19072
T a,228,11,2048
W 7
buffer_load_dwordx4 v24, s[52:55], s61 offen offset:384 lds
s_mov_b32 m0, 20480
M a,160,a,240,v,224
T a,230,11,10240
s_mov_b32 s61, 121856
buffer_load_dwordx4 v24, s[52:55], s61 offen offset:0 lds
T a,232,8,2560
s_mov_b32 m0, 21376
M a,164,a,244,v,224
T a,234,8,10752
buffer_load_dwordx4 v24, s[52:55], s61 offen offset:128 lds
s_mov_b32 m0, 22272
T a,236,11,2560
W 7
buffer_load_dwordx4 v24, s[52:55], s61 offen offset:256 lds
s_mov_b32 m0, 23168
M a,176,a,248,v,224
T a,238,11,10752
buffer_load_dwordx4 v24, s[52:55], s61 offen offset:384 lds
s_mov_b32 m0, 24576
s_waitcnt vmcnt(63)
s_barrier
T a,240,8,16384
s_mov_b32 s61, 129024
buffer_load_dwordx4 v24, s[52:55], s61 offen offset:0 lds
M a,180,a,252,v,224
T a,242,8,24576
s_mov_b32 m0, 25472
T a,244,11,16384
W 7
buffer_load_dwordx4 v24, s[52:55], s61 offen offset:128 lds
s_mov_b32 m0, 26368
M a,192,a,224,v,224
T a,246,11,24576
buffer_load_dwordx4 v24, s[52:55], s61 offen offset:256 lds
s_mov_b32 m0, 27264
T a,248,8,16896
buffer_load_dwordx4 v24, s[52:55], s61 offen offset:384 lds
s_mov_b32 m0, 28672
M a,196,a,228,v,224
T a,250,8,25088
s_mov_b32 s61, 136192
buffer_load_dwordx4 v24, s[52:55], s61 offen offset:0 lds
T a,252,11,16896
W 7
s_mov_b32 m0, 29568
M a,208,a,232,v,224
T a,254,11,25088
buffer_load_dwordx4 v24, s[52:55], s61 offen offset:128 lds
s_mov_b32 m0, 30464
T a,224,8,17408
buffer_load_dwordx4 v24, s[52:55], s61 offen offset:256 lds
s_mov_b32 m0, 31360
M a,212,a,236,v,224
T a,226,8,25600
buffer_load_dwordx4 v24, s[52:55], s61 offen offset:384 lds
s_mov_b32 m0, 32256
T a,228,11,17408
W 7
s_mov_b32 s61, 114688
buffer_load_dwordx4 v24, s[52:55], s61 offen offset:512 lds
M a,0,a,240,v,240
T a,230,11,25600
s_mov_b32 m0, 33152
T a,232,8,17920
buffer_load_dwordx4 v24, s[52:55], s61 offen offset:640 lds
s_mov_b32 m0, 34048
M a,4,a,244,v,240
T a,234,8,26112
buffer_load_dwordx4 v24, s[52:55], s61 offen offset:768 lds
s_mov_b32 m0, 36352
T a,236,11,17920
W 7
s_mov_b32 s61, 121856
buffer_load_dwordx4 v24, s[52:55], s61 offen offset:512 lds
M a,16,a,248,v,240
T a,238,11,26112
s_mov_b32 m0, 37248
T a,240,8,18432
buffer_load_dwordx4 v24, s[52:55], s61 offen offset:640 lds
s_mov_b32 m0, 38144
M a,20,a,252,v,240
T a,242,8,26624
buffer_load_dwordx4 v24, s[52:55], s61 offen offset:768 lds
s_mov_b32 m0, 40448
T a,244,11,18432
W 7
s_mov_b32 s61, 129024
buffer_load_dwordx4 v24, s[52:55], s61 offen offset:512 lds
M a,32,a,224,v,240
T a,246,11,26624
s_mov_b32 m0, 41344
T a,248,8,18944
buffer_load_dwordx4 v24, s[52:55], s61 offen offset:640 lds
s_mov_b32 m0, 42240
M a,36,a,228,v,240
T a,250,8,27136
buffer_load_dwordx4 v24, s[52:55], s61 offen offset:768 lds
s_mov_b32 m0, 44544
T a,252,11,18944
W 7
s_mov_b32 s61, 136192
buffer_load_dwordx4 v24, s[52:55], s61 offen offset:512 lds
M a,48,a,232,v,240
T a,254,11,27136
s_mov_b32 m0, 45440
T a,224,8,19456
buffer_load_dwordx4 v24, s[52:55], s61 offen offset:640 lds
s_mov_b32 m0, 46336
M a,52,a,236,v,240
T a,226,8,27648
buffer_load_dwordx4 v24, s[52:55], s61 offen offset:768 lds
)x"
R"x(s_mov_b32 m0, 49152
T a,228,11,19456
W 7
s_mov_b32 s61, 143360
buffer_load_dwordx4 v24, s[52:55], s61 offen offset:0 lds
M a,64,a,240,v,240
T a,230,11,27648
s_mov_b32 m0, 50048
T a,232,8,19968
buffer_load_dwordx4 v24, s[52:55], s61 offen offset:128 lds
s_mov_b32 m0, 50944
M a,68,a,244,v,240
T a,234,8,28160
buffer_load_dwordx4 v24, s[52:55], s61 offen offset:256 lds
s_mov_b32 m0, 51840
T a,236,11,19968
W 7
buffer_load_dwordx4 v24, s[52:55], s61 offen offset:384 lds
s_mov_b32 m0, 53248
M a,80,a,248,v,240
T a,238,11,28160
s_mov_b32 s61, 150528
buffer_load_dwordx4 v24, s[52:55], s61 offen offset:0 lds
s_waitcnt vmcnt(63)
s_barrier
T a,240,8,32768
s_mov_b32 m0, 54144
M a,84,a,252,v,240
T a,242,8,40960
buffer_load_dwordx4 v24, s[52:55], s61 offen offset:128 lds
s_mov_b32 m0, 55040
T a,244,11,32768
W 7
buffer_load_dwordx4 v24, s[52:55], s61 offen offset:256 lds
s_mov_b32 m0, 55936
M a,96,a,224,v,240
T a,246,11,40960
buffer_load_dwordx4 v24, s[52:55], s61 offen offset:384 lds
s_mov_b32 m0, 57344
T a,248,8,33280
s_mov_b32 s61, 157696
buffer_load_dwordx4 v24, s[52:55], s61 offen offset:0 lds
M a,100,a,228,v,240
T a,250,8,41472
s_mov_b32 m0, 58240
T a,252,11,33280
W 7
buffer_load_dwordx4 v24, s[52:55], s61 offen offset:128 lds
s_mov_b32 m0, 59136
M a,112,a,232,v,240
T a,254,11,41472
buffer_load_dwordx4 v24, s[52:55], s61 offen offset:256 lds
s_mov_b32 m0, 60032
T a,224,8,33792
buffer_load_dwordx4 v24, s[52:55], s61 offen offset:384 lds
s_mov_b32 m0, 61440
M a,116,a,236,v,240
T a,226,8,41984
s_mov_b32 s61, 164864
buffer_load_dwordx4 v24, s[52:55], s61 offen offset:0 lds
T a,228,11,33792
W 7
s_mov_b32 m0, 62336
M a,128,a,240,v,240
T a,230,11,41984
buffer_load_dwordx4 v24, s[52:55], s61 offen offset:128 lds
s_mov_b32 m0, 63232
T a,232,8,34304
buffer_load_dwordx4 v24, s[52:55], s61 offen offset:256 lds
s_mov_b32 m0, 64128
M a,132,a,244,v,240
T a,234,8,42496
buffer_load_dwordx4 v24, s[52:55], s61 offen offset:384 lds
s_mov_b32 m0, 65024
T a,236,11,34304
W 7
s_mov_b32 s61, 143360
buffer_load_dwordx4 v24, s[52:55], s61 offen offset:512 lds
M a,144,a,248,v,240
T a,238,11,42496
s_mov_b32 m0, 65920
T a,240,8,34816
buffer_load_dwordx4 v24, s[52:55], s61 offen offset:640 lds
s_mov_b32 m0, 66816
M a,148,a,252,v,240
T a,242,8,43008
buffer_load_dwordx4 v24, s[52:55], s61 offen offset:768 lds
s_mov_b32 m0, 69120
T a,244,11,34816
W 7
s_mov_b32 s61, 150528
buffer_load_dwordx4 v24, s[52:55], s61 offen offset:512 lds
M a,160,a,224,v,240
T a,246,11,43008
s_mov_b32 m0, 70016
T a,248,8,35328
buffer_load_dwordx4 v24, s[52:55], s61 offen offset:640 lds
s_mov_b32 m0, 70912
M a,164,a,228,v,240
T a,250,8,43520
buffer_load_dwordx4 v24, s[52:55], s61 offen offset:768 lds
s_mov_b32 m0, 73216
T a,252,11,35328
W 7
s_mov_b32 s61, 157696
buffer_load_dwordx4 v24, s[52:55], s61 offen offset:512 lds
M a,176,a,232,v,240
T a,254,11,43520
s_mov_b32 m0, 74112
s_waitcnt vmcnt(63)
s_barrier
T a,224,14,49152
buffer_load_dwordx4 v24, s[52:55], s61 offen offset:640 lds
s_mov_b32 m0, 75008
M a,180,a,236,v,240
T a,226,14,51200
buffer_load_dwordx4 v24, s[52:55], s61 offen offset:768 lds
s_mov_b32 m0, 77312
T a,228,17,49152
W 7
s_mov_b32 s61, 164864
buffer_load_dwordx4 v24, s[52:55], s61 offen offset:512 lds
M a,192,a,240,v,240
T a,230,17,51200
s_mov_b32 m0, 78208
T a,232,14,49664
buffer_load_dwordx4 v24, s[52:55], s61 offen offset:640 lds
s_mov_b32 m0, 79104
M a,196,a,244,v,240
T a,234,14,51712
buffer_load_dwordx4 v24, s[52:55], s61 offen offset:768 lds
s_mov_b32 m0, 81920
T a,236,17,49664
W 7
s_mov_b32 s61, 384
buffer_load_dwordx4 v23, s[40:43], s61 offen offset:0 nt lds
M a,208,a,248,v,240
T a,238,17,51712
s_mov_b32 m0, 82944
s_mov_b32 s61, 4480
T a,240,14,53248
buffer_load_dwordx4 v23, s[40:43], s61 offen offset:0 nt lds
s_mov_b32 m0, 83968
M a,212,a,252,v,240
T a,242,14,55296
s_mov_b32 s61, 8576
buffer_load_dwordx4 v23, s[40:43], s61 offen offset:0 nt lds
T a,244,17,53248
W 7
s_mov_b32 m0, 84992
s_mov_b32 s61, 12672
v_mfma_f32_16x16x32_bf16 v[224:227], a[224:227], v[96:99], 0
T a,246,17,55296
buffer_load_dwordx4 v23, s[40:43], s61 offen offset:0 nt lds
s_mov_b32 m0, 86016
T a,248,14,53760
s_mov_b32 s61, 16768
buffer_load_dwordx4 v23, s[40:43], s61 offen offset:0 nt lds
v_mfma_f32_16x16x32_bf16 v[228:231], a[228:231], v[96:99], 0
T a,250,14,55808
s_mov_b32 m0, 87040
s_mov_b32 s61, 20864
T a,252,17,53760
W 7
buffer_load_dwordx4 v23, s[40:43], s61 offen offset:0 nt lds
s_mov_b32 m0, 88064
v_mfma_f32_16x16x32_bf16 v[240:243], a[232:235], v[96:99], 0
T a,254,17,55808
s_mov_b32 s61, 24960
buffer_load_dwordx4 v23, s[40:43], s61 offen offset:0 nt lds
T a,224,14,57344
s_mov_b32 m0, 89088
s_mov_b32 s61, 29056
v_mfma_f32_16x16x32_bf16 v[244:247], a[236:239], v[96:99], 0
T a,226,14,59392
buffer_load_dwordx4 v23, s[40:43], s61 offen offset:0 nt lds
s_mov_b32 m0, 90112
T a,228,17,57344
W 7
s_mov_b32 s61, 33152
buffer_load_dwordx4 v23, s[40:43], s61 offen offset:0 nt lds
M v,224,a,240,v,104
T a,230,17,59392
s_mov_b32 m0, 91136
s_mov_b32 s61, 37248
T a,232,14,57856
buffer_load_dwordx4 v23, s[40:43], s61 offen offset:0 nt lds
s_mov_b32 m0, 92160
M v,228,a,244,v,104
T a,234,14,59904
s_mov_b32 s61, 41344
buffer_load_dwordx4 v23, s[40:43], s61 offen offset:0 nt lds
T a,236,17,57856
W 7
s_mov_b32 m0, 93184
s_mov_b32 s61, 45440
M v,240,a,248,v,104
T a,238,17,59904
buffer_load_dwordx4 v23, s[40:43], s61 offen offset:0 nt lds
s_mov_b32 m0, 94208
T a,240,14,61440
s_mov_b32 s61, 49536
buffer_load_dwordx4 v23, s[40:43], s61 offen offset:0 nt lds
M v,244,a,252,v,104
T a,242,14,63488
s_mov_b32 m0, 95232
s_mov_b32 s61, 53632
T a,244,17,61440
W 7
buffer_load_dwordx4 v23, s[40:43], s61 offen offset:0 nt lds
s_mov_b32 m0, 96256
M v,224,a,224,v,112
T a,246,17,63488
s_mov_b32 s61, 57728
buffer_load_dwordx4 v23, s[40:43], s61 offen offset:0 nt lds
T a,248,14,61952
s_mov_b32 m0, 97280
s_mov_b32 s61, 61824
M v,228,a,228,v,112
T a,250,14,64000
buffer_load_dwordx4 v23, s[40:43], s61 offen offset:0 nt lds
T a,252,17,61952
W 7
M v,240,a,232,v,112
T a,254,17,64000
s_waitcnt vmcnt(63)
s_barrier
.Lc1_76_%=:
T a,224,15,0
s_mov_b32 s61, .Lc0_76_%=-.Lpf_start%=+8192
buffer_load_dword v29, v20, s[92:95], s61 offen
M v,244,a,236,v,112
T a,226,15,2048
s_mov_b32 s61, .Lc1_76_%=-.Lpf_start%=+8192
buffer_load_dword v29, v20, s[92:95], s61 offen
T a,228,18,0
W 7
s_mov_b32 m0, 98304
s_mov_b32 s61, 65920
M v,224,a,240,v,120
T a,230,18,2048
buffer_load_dwordx4 v23, s[40:43], s61 offen offset:0 nt lds
s_mov_b32 m0, 99328
T a,232,15,512
s_mov_b32 s61, 70016
buffer_load_dwordx4 v23, s[40:43], s61 offen offset:0 nt lds
M v,228,a,244,v,120
T a,234,15,2560
s_mov_b32 m0, 100352
s_mov_b32 s61, 74112
T a,236,18,512
W 7
buffer_load_dwordx4 v23, s[40:43], s61 offen offset:0 nt lds
s_mov_b32 m0, 101376
M v,240,a,248,v,120
T a,238,18,2560
s_mov_b32 s61, 78208
buffer_load_dwordx4 v23, s[40:43], s61 offen offset:0 nt lds
T a,240,15,4096
s_mov_b32 m0, 102400
s_mov_b32 s61, 82304
M v,244,a,252,v,120
T a,242,15,6144
buffer_load_dwordx4 v23, s[40:43], s61 offen offset:0 nt lds
s_mov_b32 m0, 103424
T a,244,18,4096
W 7
s_mov_b32 s61, 86400
buffer_load_dwordx4 v23, s[40:43], s61 offen offset:0 nt lds
M v,224,a,224,v,128
T a,246,18,6144
s_mov_b32 m0, 104448
s_mov_b32 s61, 90496
T a,248,15,4608
buffer_load_dwordx4 v23, s[40:43], s61 offen offset:0 nt lds
M v,228,a,228,v,128
T a,250,15,6656
T a,252,18,4608
W 7
s_mov_b32 m0, 105472
s_mov_b32 s61, 94592
M v,240,a,232,v,128
T a,254,18,6656
buffer_load_dwordx4 v23, s[40:43], s61 offen offset:0 nt lds
T a,224,15,8192
M v,244,a,236,v,128
T a,226,15,10240
T a,228,18,8192
W 7
s_mov_b32 m0, 106496
s_mov_b32 s61, 98688
M v,224,a,240,v,136
T a,230,18,10240
buffer_load_dwordx4 v23, s[40:43], s61 offen offset:0 nt lds
T a,232,15,8704
M v,228,a,244,v,136
T a,234,15,10752
T a,236,18,8704
W 7
s_mov_b32 m0, 107520
s_mov_b32 s61, 102784
M v,240,a,248,v,136
T a,238,18,10752
buffer_load_dwordx4 v23, s[40:43], s61 offen offset:0 nt lds
T a,240,15,12288
s_mov_b32 m0, 108544
s_mov_b32 s61, 106880
M v,244,a,252,v,136
T a,242,15,14336
buffer_load_dwordx4 v23, s[40:43], s61 offen offset:0 nt lds
T a,244,18,12288
W 7
M v,224,a,224,v,144
T a,246,18,14336
T a,248,15,12800
s_mov_b32 m0, 109568
s_mov_b32 s61, 110976
M v,228,a,228,v,144
T a,250,15,14848
buffer_load_dwordx4 v23, s[40:43], s61 offen offset:0 nt lds
T a,252,18,12800
W 7
M v,240,a,232,v,144
T a,254,18,14848
s_waitcnt vmcnt(63)
s_barrier
T a,224,15,16384
s_mov_b32 m0, 110592
s_mov_b32 s61, 115072
M v,244,a,236,v,144
T a,226,15,18432
buffer_load_dwordx4 v23, s[40:43], s61 offen offset:0 nt lds
T a,228,18,16384
W 7
M v,224,a,240,v,152
T a,230,18,18432
T a,232,15,16896
s_mov_b32 m0, 111616
s_mov_b32 s61, 119168
M v,228,a,244,v,152
T a,234,15,18944
buffer_load_dwordx4 v23, s[40:43], s61 offen offset:0 nt lds
T a,236,18,16896
W 7
M v,240,a,248,v,152
T a,238,18,18944
T a,240,15,20480
s_mov_b32 m0, 112640
s_mov_b32 s61, 123264
M v,244,a,252,v,152
T a,242,15,22528
buffer_load_dwordx4 v23, s[40:43], s61 offen offset:0 nt lds
T a,244,18,20480
W 7
M v,224,a,224,v,160
T a,246,18,22528
T a,248,15,20992
s_mov_b32 m0, 113664
s_mov_b32 s61, 127360
M v,228,a,228,v,160
T a,250,15,23040
buffer_load_dwordx4 v23, s[40:43], s61 offen offset:0 nt lds
T a,252,18,20992
W 7
M v,240,a,232,v,160
T a,254,18,23040
T a,224,15,24576
s_mov_b32 m0, 114688
s_mov_b32 s61, 131456
M v,244,a,236,v,160
T a,226,15,26624
buffer_load_dwordx4 v23, s[40:43], s61 offen offset:0 nt lds
T a,228,18,24576
W 7
M v,224,a,240,v,168
T a,230,18,26624
T a,232,15,25088
s_mov_b32 m0, 115712
s_mov_b32 s61, 135552
M v,228,a,244,v,168
T a,234,15,27136
buffer_load_dwordx4 v23, s[40:43], s61 offen offset:0 nt lds
T a,236,18,25088
W 7
M v,240,a,248,v,168
T a,238,18,27136
T a,240,15,28672
s_mov_b32 m0, 116736
s_mov_b32 s61, 139648
M v,244,a,252,v,168
T a,242,15,30720
buffer_load_dwordx4 v23, s[40:43], s61 offen offset:0 nt lds
T a,244,18,28672
W 7
M v,224,a,224,v,176
T a,246,18,30720
T a,248,15,29184
s_mov_b32 m0, 117760
s_mov_b32 s61, 143744
M v,228,a,228,v,176
)x"
R"x(T a,250,15,31232
buffer_load_dwordx4 v23, s[40:43], s61 offen offset:0 nt lds
T a,252,18,29184
W 7
M v,240,a,232,v,176
T a,254,18,31232
s_waitcnt vmcnt(63)
s_barrier
T a,224,13,0
s_mov_b32 m0, 118784
s_mov_b32 s61, 147840
M v,244,a,236,v,176
T a,226,13,2048
buffer_load_dwordx4 v23, s[40:43], s61 offen offset:0 nt lds
T a,228,16,0
W 7
s_mov_b32 m0, 119808
s_mov_b32 s61, 151936
M v,224,a,240,v,184
T a,230,16,2048
buffer_load_dwordx4 v23, s[40:43], s61 offen offset:0 nt lds
T a,232,13,512
M v,228,a,244,v,184
T a,234,13,2560
T a,236,16,512
W 7
s_mov_b32 m0, 120832
s_mov_b32 s61, 156032
M v,240,a,248,v,184
T a,238,16,2560
buffer_load_dwordx4 v23, s[40:43], s61 offen offset:0 nt lds
T a,240,13,4096
M v,244,a,252,v,184
T a,242,13,6144
T a,244,16,4096
W 7
s_mov_b32 m0, 121856
s_mov_b32 s61, 160128
M v,224,a,224,v,192
T a,246,16,6144
buffer_load_dwordx4 v23, s[40:43], s61 offen offset:0 nt lds
T a,248,13,4608
M v,228,a,228,v,192
T a,250,13,6656
T a,252,16,4608
W 7
s_mov_b32 m0, 122880
s_mov_b32 s61, 164224
M v,240,a,232,v,192
T a,254,16,6656
buffer_load_dwordx4 v23, s[40:43], s61 offen offset:0 nt lds
T a,224,13,8192
M v,244,a,236,v,192
T a,226,13,10240
T a,228,16,8192
W 7
s_mov_b32 m0, 123904
s_mov_b32 s61, 168320
M v,224,a,240,v,200
T a,230,16,10240
buffer_load_dwordx4 v23, s[40:43], s61 offen offset:0 nt lds
T a,232,13,8704
M v,228,a,244,v,200
T a,234,13,10752
T a,236,16,8704
W 7
s_mov_b32 m0, 124928
s_mov_b32 s61, 172416
M v,240,a,248,v,200
T a,238,16,10752
buffer_load_dwordx4 v23, s[40:43], s61 offen offset:0 nt lds
T a,240,13,12288
M v,244,a,252,v,200
T a,242,13,14336
T a,244,16,12288
W 7
s_mov_b32 m0, 125952
s_mov_b32 s61, 176512
M v,224,a,224,v,208
T a,246,16,14336
buffer_load_dwordx4 v23, s[40:43], s61 offen offset:0 nt lds
T a,248,13,12800
M v,228,a,228,v,208
T a,250,13,14848
T a,252,16,12800
W 7
s_mov_b32 m0, 126976
s_mov_b32 s61, 180608
M v,240,a,232,v,208
T a,254,16,14848
buffer_load_dwordx4 v23, s[40:43], s61 offen offset:0 nt lds
s_waitcnt vmcnt(63)
s_barrier
T a,224,7,16384
M v,244,a,236,v,208
T a,226,7,24576
T a,228,10,16384
W 7
s_mov_b32 m0, 128000
s_mov_b32 s61, 184704
M v,224,a,240,v,216
T a,230,10,24576
buffer_load_dwordx4 v23, s[40:43], s61 offen offset:0 nt lds
T a,232,7,16896
M v,228,a,244,v,216
T a,234,7,25088
T a,236,10,16896
W 7
s_mov_b32 m0, 129024
s_mov_b32 s61, 188800
M v,240,a,248,v,216
T a,238,10,25088
buffer_load_dwordx4 v23, s[40:43], s61 offen offset:0 nt lds
T a,240,7,17408
s_mov_b32 m0, 130048
s_mov_b32 s61, 192896
M v,244,a,252,v,216
T a,242,7,25600
buffer_load_dwordx4 v23, s[40:43], s61 offen offset:0 nt lds
s_nop 7
v_mul_f32 v224, v28, v224
v_mul_f32 v225, v28, v225
v_mul_f32 v226, v28, v226
v_mul_f32 v227, v28, v227
s_mov_b32 m0, 131072
s_mov_b32 s61, 196992
buffer_load_dwordx4 v23, s[40:43], s61 offen offset:0 nt lds
v_mul_f32 v228, v28, v228
v_mul_f32 v229, v28, v229
v_mul_f32 v230, v28, v230
v_mul_f32 v231, v28, v231
v_cvt_pk_bf16_f32 v224, v224, v225
v_cvt_pk_bf16_f32 v225, v226, v227
v_cvt_pk_bf16_f32 v226, v228, v229
v_cvt_pk_bf16_f32 v227, v230, v231
s_mov_b32 m0, 132096
s_mov_b32 s61, 201088
buffer_load_dwordx4 v23, s[40:43], s61 offen offset:0 nt lds
v_mul_f32 v240, v28, v240
v_mul_f32 v241, v28, v241
v_mul_f32 v242, v28, v242
v_mul_f32 v243, v28, v243
v_mul_f32 v244, v28, v244
v_mul_f32 v245, v28, v245
v_mul_f32 v246, v28, v246
v_mul_f32 v247, v28, v247
s_mov_b32 m0, 133120
s_mov_b32 s61, 205184
buffer_load_dwordx4 v23, s[40:43], s61 offen offset:0 nt lds
v_cvt_pk_bf16_f32 v240, v240, v241
v_cvt_pk_bf16_f32 v241, v242, v243
v_cvt_pk_bf16_f32 v242, v244, v245
v_cvt_pk_bf16_f32 v243, v246, v247
s_nop 1
T a,244,10,17408
W 7
M a,0,a,224,v,224
T a,246,10,25600
T a,248,7,17920
s_mov_b32 m0, 134144
s_mov_b32 s61, 209280
M a,4,a,228,v,224
T a,250,7,26112
buffer_load_dwordx4 v23, s[40:43], s61 offen offset:0 nt lds
T a,252,10,17920
W 7
M a,16,a,232,v,224
T a,254,10,26112
T a,224,7,18432
s_mov_b32 m0, 135168
s_mov_b32 s61, 213376
M a,20,a,236,v,224
T a,226,7,26624
buffer_load_dwordx4 v23, s[40:43], s61 offen offset:0 nt lds
T a,228,10,18432
W 7
M a,32,a,240,v,224
T a,230,10,26624
T a,232,7,18944
s_mov_b32 m0, 136192
s_mov_b32 s61, 217472
M a,36,a,244,v,224
T a,234,7,27136
buffer_load_dwordx4 v23, s[40:43], s61 offen offset:0 nt lds
T a,236,10,18944
W 7
M a,48,a,248,v,224
T a,238,10,27136
T a,240,7,19456
s_mov_b32 m0, 137216
s_mov_b32 s61, 221568
M a,52,a,252,v,224
T a,242,7,27648
buffer_load_dwordx4 v23, s[40:43], s61 offen offset:0 nt lds
T a,244,10,19456
W 7
M a,64,a,224,v,224
T a,246,10,27648
T a,248,7,19968
s_mov_b32 m0, 138240
s_mov_b32 s61, 225664
M a,68,a,228,v,224
T a,250,7,28160
buffer_load_dwordx4 v23, s[40:43], s61 offen offset:0 nt lds
T a,252,10,19968
W 7
M a,80,a,232,v,224
T a,254,10,28160
s_waitcnt vmcnt(63)
s_barrier
T a,224,7,32768
s_mov_b32 m0, 139264
s_mov_b32 s61, 229760
M a,84,a,236,v,224
T a,226,7,40960
buffer_load_dwordx4 v23, s[40:43], s61 offen offset:0 nt lds
T a,228,10,32768
W 7
M a,96,a,240,v,224
T a,230,10,40960
T a,232,7,33280
s_mov_b32 m0, 140288
s_mov_b32 s61, 233856
M a,100,a,244,v,224
T a,234,7,41472
buffer_load_dwordx4 v23, s[40:43], s61 offen offset:0 nt lds
T a,236,10,33280
W 7
M a,112,a,248,v,224
T a,238,10,41472
T a,240,7,33792
s_mov_b32 m0, 141312
s_mov_b32 s61, 237952
M a,116,a,252,v,224
T a,242,7,41984
buffer_load_dwordx4 v23, s[40:43], s61 offen offset:0 nt lds
T a,244,10,33792
W 7
M a,128,a,224,v,224
T a,246,10,41984
T a,248,7,34304
s_mov_b32 m0, 142336
s_mov_b32 s61, 242048
M a,132,a,228,v,224
T a,250,7,42496
buffer_load_dwordx4 v23, s[40:43], s61 offen offset:0 nt lds
T a,252,10,34304
W 7
M a,144,a,232,v,224
T a,254,10,42496
T a,224,7,34816
s_mov_b32 m0, 143360
s_mov_b32 s61, 246144
M a,148,a,236,v,224
T a,226,7,43008
buffer_load_dwordx4 v23, s[40:43], s61 offen offset:0 nt lds
T a,228,10,34816
W 7
s_mov_b32 m0, 144384
s_mov_b32 s61, 250240
M a,160,a,240,v,224
T a,230,10,43008
buffer_load_dwordx4 v23, s[40:43], s61 offen offset:0 nt lds
T a,232,7,35328
M a,164,a,244,v,224
T a,234,7,43520
T a,236,10,35328
W 7
s_mov_b32 m0, 145408
s_mov_b32 s61, 254336
M a,176,a,248,v,224
T a,238,10,43520
buffer_load_dwordx4 v23, s[40:43], s61 offen offset:0 nt lds
s_waitcnt vmcnt(63)
s_barrier
T a,240,7,49152
M a,180,a,252,v,224
T a,242,7,57344
T a,244,10,49152
W 7
s_mov_b32 m0, 146432
s_mov_b32 s61, 258432
M a,192,a,224,v,224
T a,246,10,57344
buffer_load_dwordx4 v23, s[40:43], s61 offen offset:0 nt lds
T a,248,7,49664
M a,196,a,228,v,224
T a,250,7,57856
T a,252,10,49664
W 7
s_mov_b32 m0, 147456
s_mov_b32 s61, 172032
M a,208,a,232,v,224
T a,254,10,57856
buffer_load_dwordx4 v24, s[52:55], s61 offen offset:0 lds
s_mov_b32 m0, 148352
T a,224,7,50176
buffer_load_dwordx4 v24, s[52:55], s61 offen offset:128 lds
s_mov_b32 m0, 149248
M a,212,a,236,v,224
T a,226,7,58368
buffer_load_dwordx4 v24, s[52:55], s61 offen offset:256 lds
s_mov_b32 m0, 150144
T a,228,10,50176
W 7
buffer_load_dwordx4 v24, s[52:55], s61 offen offset:384 lds
s_mov_b32 m0, 151552
M a,0,a,240,v,240
T a,230,10,58368
s_mov_b32 s61, 179200
buffer_load_dwordx4 v24, s[52:55], s61 offen offset:0 lds
T a,232,7,50688
s_mov_b32 m0, 152448
M a,4,a,244,v,240
T a,234,7,58880
buffer_load_dwordx4 v24, s[52:55], s61 offen offset:128 lds
s_mov_b32 m0, 153344
T a,236,10,50688
W 7
buffer_load_dwordx4 v24, s[52:55], s61 offen offset:256 lds
s_mov_b32 m0, 154240
M a,16,a,248,v,240
T a,238,10,58880
buffer_load_dwordx4 v24, s[52:55], s61 offen offset:384 lds
s_mov_b32 m0, 155648
T a,240,7,51200
s_mov_b32 s61, 186368
buffer_load_dwordx4 v24, s[52:55], s61 offen offset:0 lds
M a,20,a,252,v,240
T a,242,7,59392
s_mov_b32 m0, 156544
T a,244,10,51200
W 7
buffer_load_dwordx4 v24, s[52:55], s61 offen offset:128 lds
s_mov_b32 m0, 157440
M a,32,a,224,v,240
T a,246,10,59392
buffer_load_dwordx4 v24, s[52:55], s61 offen offset:256 lds
s_mov_b32 m0, 158336
T a,248,7,51712
buffer_load_dwordx4 v24, s[52:55], s61 offen offset:384 lds
s_mov_b32 m0, 159744
M a,36,a,228,v,240
T a,250,7,59904
s_mov_b32 s61, 193536
buffer_load_dwordx4 v24, s[52:55], s61 offen offset:0 lds
T a,252,10,51712
W 7
s_mov_b32 m0, 160640
M a,48,a,232,v,240
T a,254,10,59904
buffer_load_dwordx4 v24, s[52:55], s61 offen offset:128 lds
s_mov_b32 m0, 161536
T a,224,7,52224
buffer_load_dwordx4 v24, s[52:55], s61 offen offset:256 lds
s_mov_b32 m0, 162432
M a,52,a,236,v,240
T a,226,7,60416
buffer_load_dwordx4 v24, s[52:55], s61 offen offset:384 lds
s_mov_b32 m0, 0
T a,228,10,52224
W 7
s_mov_b32 s61, 172544
buffer_load_dwordx4 v24, s[52:55], s61 offen offset:0 lds
M a,64,a,240,v,240
T a,230,10,60416
s_mov_b32 m0, 384
s_mov_b32 s61, 172032
T a,232,7,52736
buffer_load_dwordx4 v24, s[52:55], s61 offen offset:640 lds
s_mov_b32 m0, 1280
M a,68,a,244,v,240
T a,234,7,60928
buffer_load_dwordx4 v24, s[52:55], s61 offen offset:768 lds
s_mov_b32 m0, 3584
T a,236,10,52736
W 7
s_mov_b32 s61, 179200
buffer_load_dwordx4 v24, s[52:55], s61 offen offset:512 lds
M a,80,a,248,v,240
T a,238,10,60928
s_mov_b32 m0, 4480
s_waitcnt vmcnt(63)
s_barrier
.Lc1_82_%=:
T a,240,8,0
buffer_load_dwordx4 v24, s[52:55], s61 offen offset:640 lds
s_mov_b32 m0, 5376
M a,84,a,252,v,240
T a,242,8,8192
buffer_load_dwordx4 v24, s[52:55], s61 offen offset:768 lds
s_mov_b32 m0, 7680
T a,244,11,0
W 7
s_mov_b32 s61, 186368
buffer_load_dwordx4 v24, s[52:55], s61 offen offset:512 lds
M a,96,a,224,v,240
T a,246,11,8192
s_mov_b32 m0, 8576
T a,248,8,512
)x"
R"x(buffer_load_dwordx4 v24, s[52:55], s61 offen offset:640 lds
s_mov_b32 m0, 9472
M a,100,a,228,v,240
T a,250,8,8704
buffer_load_dwordx4 v24, s[52:55], s61 offen offset:768 lds
s_mov_b32 m0, 11776
T a,252,11,512
W 7
s_mov_b32 s61, 193536
buffer_load_dwordx4 v24, s[52:55], s61 offen offset:512 lds
M a,112,a,232,v,240
T a,254,11,8704
s_mov_b32 m0, 12672
T a,224,8,1024
buffer_load_dwordx4 v24, s[52:55], s61 offen offset:640 lds
s_mov_b32 m0, 13568
M a,116,a,236,v,240
T a,226,8,9216
buffer_load_dwordx4 v24, s[52:55], s61 offen offset:768 lds
s_mov_b32 m0, 16384
T a,228,11,1024
W 7
s_mov_b32 s61, 200704
buffer_load_dwordx4 v24, s[52:55], s61 offen offset:0 lds
M a,128,a,240,v,240
T a,230,11,9216
s_mov_b32 m0, 17280
T a,232,8,1536
buffer_load_dwordx4 v24, s[52:55], s61 offen offset:128 lds
s_mov_b32 m0, 18176
M a,132,a,244,v,240
T a,234,8,9728
buffer_load_dwordx4 v24, s[52:55], s61 offen offset:256 lds
s_mov_b32 m0, 19072
T a,236,11,1536
W 7
buffer_load_dwordx4 v24, s[52:55], s61 offen offset:384 lds
s_mov_b32 m0, 20480
M a,144,a,248,v,240
T a,238,11,9728
s_mov_b32 s61, 207872
buffer_load_dwordx4 v24, s[52:55], s61 offen offset:0 lds
T a,240,8,2048
s_mov_b32 m0, 21376
M a,148,a,252,v,240
T a,242,8,10240
buffer_load_dwordx4 v24, s[52:55], s61 offen offset:128 lds
s_mov_b32 m0, 22272
T a,244,11,2048
W 7
buffer_load_dwordx4 v24, s[52:55], s61 offen offset:256 lds
s_mov_b32 m0, 23168
M a,160,a,224,v,240
T a,246,11,10240
buffer_load_dwordx4 v24, s[52:55], s61 offen offset:384 lds
s_mov_b32 m0, 24576
T a,248,8,2560
s_mov_b32 s61, 215040
buffer_load_dwordx4 v24, s[52:55], s61 offen offset:0 lds
M a,164,a,228,v,240
T a,250,8,10752
s_mov_b32 m0, 25472
T a,252,11,2560
W 7
buffer_load_dwordx4 v24, s[52:55], s61 offen offset:128 lds
s_mov_b32 m0, 26368
M a,176,a,232,v,240
T a,254,11,10752
buffer_load_dwordx4 v24, s[52:55], s61 offen offset:256 lds
s_mov_b32 m0, 27264
s_waitcnt vmcnt(63)
s_barrier
T a,224,14,16384
buffer_load_dwordx4 v24, s[52:55], s61 offen offset:384 lds
s_mov_b32 m0, 28672
M a,180,a,236,v,240
T a,226,14,18432
s_mov_b32 s61, 222208
buffer_load_dwordx4 v24, s[52:55], s61 offen offset:0 lds
T a,228,17,16384
W 7
s_mov_b32 m0, 29568
M a,192,a,240,v,240
T a,230,17,18432
buffer_load_dwordx4 v24, s[52:55], s61 offen offset:128 lds
s_mov_b32 m0, 30464
T a,232,14,16896
buffer_load_dwordx4 v24, s[52:55], s61 offen offset:256 lds
s_mov_b32 m0, 31360
M a,196,a,244,v,240
T a,234,14,18944
buffer_load_dwordx4 v24, s[52:55], s61 offen offset:384 lds
s_mov_b32 s61, .Lc0_82_%=-.Lpf_start%=+8192
T a,236,17,16896
W 7
buffer_load_dword v29, v20, s[92:95], s61 offen
s_mov_b32 s61, .Lc1_82_%=-.Lpf_start%=+8192
M a,208,a,248,v,240
T a,238,17,18944
buffer_load_dword v29, v20, s[92:95], s61 offen
s_mov_b32 m0, 32256
T a,240,14,20480
s_mov_b32 s61, 200704
buffer_load_dwordx4 v24, s[52:55], s61 offen offset:512 lds
M a,212,a,252,v,240
T a,242,14,22528
s_mov_b32 m0, 33152
T a,244,17,20480
W 7
buffer_load_dwordx4 v24, s[52:55], s61 offen offset:640 lds
s_mov_b32 m0, 34048
v_mfma_f32_16x16x32_bf16 v[224:227], a[224:227], v[96:99], 0
T a,246,17,22528
buffer_load_dwordx4 v24, s[52:55], s61 offen offset:768 lds
s_mov_b32 m0, 36352
T a,248,14,20992
s_mov_b32 s61, 207872
buffer_load_dwordx4 v24, s[52:55], s61 offen offset:512 lds
v_mfma_f32_16x16x32_bf16 v[228:231], a[228:231], v[96:99], 0
T a,250,14,23040
s_mov_b32 m0, 37248
T a,252,17,20992
W 7
buffer_load_dwordx4 v24, s[52:55], s61 offen offset:640 lds
s_mov_b32 m0, 38144
v_mfma_f32_16x16x32_bf16 v[240:243], a[232:235], v[96:99], 0
T a,254,17,23040
buffer_load_dwordx4 v24, s[52:55], s61 offen offset:768 lds
s_mov_b32 m0, 40448
T a,224,14,24576
s_mov_b32 s61, 215040
buffer_load_dwordx4 v24, s[52:55], s61 offen offset:512 lds
v_mfma_f32_16x16x32_bf16 v[244:247], a[236:239], v[96:99], 0
T a,226,14,26624
s_mov_b32 m0, 41344
T a,228,17,24576
W 7
buffer_load_dwordx4 v24, s[52:55], s61 offen offset:640 lds
s_mov_b32 m0, 42240
M v,224,a,240,v,104
T a,230,17,26624
buffer_load_dwordx4 v24, s[52:55], s61 offen offset:768 lds
s_mov_b32 m0, 44544
T a,232,14,25088
s_mov_b32 s61, 222208
buffer_load_dwordx4 v24, s[52:55], s61 offen offset:512 lds
M v,228,a,244,v,104
T a,234,14,27136
s_mov_b32 m0, 45440
T a,236,17,25088
W 7
buffer_load_dwordx4 v24, s[52:55], s61 offen offset:640 lds
s_mov_b32 m0, 46336
M v,240,a,248,v,104
T a,238,17,27136
buffer_load_dwordx4 v24, s[52:55], s61 offen offset:768 lds
s_mov_b32 m0, 49152
T a,240,14,28672
s_mov_b32 s61, 262144
buffer_load_dwordx4 v19, s[36:39], s61 offen offset:0 nt lds
M v,244,a,252,v,104
T a,242,14,30720
s_mov_b32 m0, 50048
T a,244,17,28672
W 7
buffer_load_dwordx4 v19, s[36:39], s61 offen offset:128 nt lds
s_mov_b32 m0, 50944
M v,224,a,224,v,112
T a,246,17,30720
buffer_load_dwordx4 v19, s[36:39], s61 offen offset:256 nt lds
s_mov_b32 m0, 51840
T a,248,14,29184
buffer_load_dwordx4 v19, s[36:39], s61 offen offset:384 nt lds
s_mov_b32 m0, 53248
M v,228,a,228,v,112
T a,250,14,31232
s_mov_b32 s61, 266240
buffer_load_dwordx4 v27, s[36:39], s61 offen offset:0 nt lds
T a,252,17,29184
W 7
s_mov_b32 m0, 54144
M v,240,a,232,v,112
T a,254,17,31232
buffer_load_dwordx4 v27, s[36:39], s61 offen offset:128 nt lds
s_mov_b32 m0, 55040
s_waitcnt vmcnt(63)
s_barrier
T a,224,14,32768
buffer_load_dwordx4 v27, s[36:39], s61 offen offset:256 nt lds
s_mov_b32 m0, 55936
M v,244,a,236,v,112
T a,226,14,34816
buffer_load_dwordx4 v27, s[36:39], s61 offen offset:384 nt lds
s_mov_b32 m0, 57344
T a,228,17,32768
W 7
s_mov_b32 s61, 270336
buffer_load_dwordx4 v19, s[36:39], s61 offen offset:0 nt lds
M v,224,a,240,v,120
T a,230,17,34816
s_mov_b32 m0, 58240
T a,232,14,33280
buffer_load_dwordx4 v19, s[36:39], s61 offen offset:128 nt lds
s_mov_b32 m0, 59136
M v,228,a,244,v,120
T a,234,14,35328
buffer_load_dwordx4 v19, s[36:39], s61 offen offset:256 nt lds
s_mov_b32 m0, 60032
T a,236,17,33280
W 7
buffer_load_dwordx4 v19, s[36:39], s61 offen offset:384 nt lds
s_mov_b32 m0, 61440
M v,240,a,248,v,120
T a,238,17,35328
s_mov_b32 s61, 274432
buffer_load_dwordx4 v27, s[36:39], s61 offen offset:0 nt lds
T a,240,14,36864
s_mov_b32 m0, 62336
M v,244,a,252,v,120
T a,242,14,38912
buffer_load_dwordx4 v27, s[36:39], s61 offen offset:128 nt lds
s_mov_b32 m0, 63232
T a,244,17,36864
W 7
buffer_load_dwordx4 v27, s[36:39], s61 offen offset:256 nt lds
s_mov_b32 m0, 64128
M v,224,a,224,v,128
T a,246,17,38912
buffer_load_dwordx4 v27, s[36:39], s61 offen offset:384 nt lds
s_mov_b32 m0, 65536
T a,248,14,37376
s_mov_b32 s61, 278528
buffer_load_dwordx4 v19, s[36:39], s61 offen offset:0 nt lds
M v,228,a,228,v,128
T a,250,14,39424
s_mov_b32 m0, 66432
T a,252,17,37376
W 7
buffer_load_dwordx4 v19, s[36:39], s61 offen offset:128 nt lds
s_mov_b32 m0, 67328
M v,240,a,232,v,128
T a,254,17,39424
buffer_load_dwordx4 v19, s[36:39], s61 offen offset:256 nt lds
s_mov_b32 m0, 68224
T a,224,14,40960
buffer_load_dwordx4 v19, s[36:39], s61 offen offset:384 nt lds
M v,244,a,236,v,128
T a,226,14,43008
T a,228,17,40960
W 7
s_mov_b32 m0, 69632
s_mov_b32 s61, 282624
M v,224,a,240,v,136
T a,230,17,43008
buffer_load_dwordx4 v27, s[36:39], s61 offen offset:0 nt lds
T a,232,14,41472
M v,228,a,244,v,136
T a,234,14,43520
T a,236,17,41472
W 7
s_mov_b32 m0, 70528
M v,240,a,248,v,136
T a,238,17,43520
buffer_load_dwordx4 v27, s[36:39], s61 offen offset:128 nt lds
T a,240,14,45056
M v,244,a,252,v,136
T a,242,14,47104
T a,244,17,45056
W 7
s_mov_b32 m0, 71424
M v,224,a,224,v,144
T a,246,17,47104
buffer_load_dwordx4 v27, s[36:39], s61 offen offset:256 nt lds
T a,248,14,45568
M v,228,a,228,v,144
T a,250,14,47616
T a,252,17,45568
W 7
s_mov_b32 m0, 72320
M v,240,a,232,v,144
T a,254,17,47616
buffer_load_dwordx4 v27, s[36:39], s61 offen offset:384 nt lds
s_waitcnt vmcnt(63)
s_barrier
T a,224,14,49152
M v,244,a,236,v,144
T a,226,14,51200
T a,228,17,49152
W 7
s_mov_b32 m0, 73728
s_mov_b32 s61, 286720
M v,224,a,240,v,152
T a,230,17,51200
buffer_load_dwordx4 v19, s[36:39], s61 offen offset:0 nt lds
T a,232,14,49664
M v,228,a,244,v,152
T a,234,14,51712
T a,236,17,49664
W 7
s_mov_b32 m0, 74624
M v,240,a,248,v,152
T a,238,17,51712
buffer_load_dwordx4 v19, s[36:39], s61 offen offset:128 nt lds
T a,240,14,53248
s_mov_b32 m0, 75520
M v,244,a,252,v,152
T a,242,14,55296
buffer_load_dwordx4 v19, s[36:39], s61 offen offset:256 nt lds
T a,244,17,53248
W 7
M v,224,a,224,v,160
T a,246,17,55296
T a,248,14,53760
s_mov_b32 m0, 76416
M v,228,a,228,v,160
T a,250,14,55808
buffer_load_dwordx4 v19, s[36:39], s61 offen offset:384 nt lds
T a,252,17,53760
W 7
M v,240,a,232,v,160
T a,254,17,55808
T a,224,14,57344
s_mov_b32 m0, 77824
s_mov_b32 s61, 290816
M v,244,a,236,v,160
T a,226,14,59392
buffer_load_dwordx4 v27, s[36:39], s61 offen offset:0 nt lds
T a,228,17,57344
W 7
M v,224,a,240,v,168
T a,230,17,59392
T a,232,14,57856
s_mov_b32 m0, 78720
M v,228,a,244,v,168
T a,234,14,59904
buffer_load_dwordx4 v27, s[36:39], s61 offen offset:128 nt lds
T a,236,17,57856
W 7
M v,240,a,248,v,168
T a,238,17,59904
T a,240,14,61440
s_mov_b32 m0, 79616
M v,244,a,252,v,168
T a,242,14,63488
buffer_load_dwordx4 v27, s[36:39], s61 offen offset:256 nt lds
T a,244,17,61440
W 7
M v,224,a,224,v,176
T a,246,17,63488
T a,248,14,61952
s_mov_b32 m0, 80512
M v,228,a,228,v,176
T a,250,14,64000
buffer_load_dwordx4 v27, s[36:39], s61 offen offset:384 nt lds
T a,252,17,61952
W 7
M v,240,a,232,v,176
T a,254,17,64000
s_waitcnt vmcnt(63)
s_barrier
T a,224,15,0
s_mov_b32 m0, 81920
s_mov_b32 s61, 294912
M v,244,a,236,v,176
T a,226,15,2048
buffer_load_dwordx4 v19, s[36:39], s61 offen offset:0 nt lds
T a,228,18,0
W 7
M v,224,a,240,v,184
T a,230,18,2048
T a,232,15,512
s_mov_b32 m0, 82816
M v,228,a,244,v,184
T a,234,15,2560
buffer_load_dwordx4 v19, s[36:39], s61 offen offset:128 nt lds
T a,236,18,512
W 7
M v,240,a,248,v,184
T a,238,18,2560
T a,240,15,4096
s_mov_b32 m0, 83712
M v,244,a,252,v,184
T a,242,15,6144
buffer_load_dwordx4 v19, s[36:39], s61 offen offset:256 nt lds
T a,244,18,4096
W 7
M v,224,a,224,v,192
T a,246,18,6144
T a,248,15,4608
s_mov_b32 m0, 84608
M v,228,a,228,v,192
T a,250,15,6656
buffer_load_dwordx4 v19, s[36:39], s61 offen offset:384 nt lds
T a,252,18,4608
W 7
M v,240,a,232,v,192
T a,254,18,6656
T a,224,15,8192
s_mov_b32 m0, 86016
s_mov_b32 s61, 299008
)x"
R"x(M v,244,a,236,v,192
T a,226,15,10240
buffer_load_dwordx4 v27, s[36:39], s61 offen offset:0 nt lds
T a,228,18,8192
W 7
s_mov_b32 m0, 86912
M v,224,a,240,v,200
T a,230,18,10240
buffer_load_dwordx4 v27, s[36:39], s61 offen offset:128 nt lds
T a,232,15,8704
M v,228,a,244,v,200
T a,234,15,10752
T a,236,18,8704
W 7
s_mov_b32 m0, 87808
M v,240,a,248,v,200
T a,238,18,10752
buffer_load_dwordx4 v27, s[36:39], s61 offen offset:256 nt lds
T a,240,15,12288
M v,244,a,252,v,200
T a,242,15,14336
T a,244,18,12288
W 7
s_mov_b32 m0, 88704
M v,224,a,224,v,208
T a,246,18,14336
buffer_load_dwordx4 v27, s[36:39], s61 offen offset:384 nt lds
T a,248,15,12800
M v,228,a,228,v,208
T a,250,15,14848
T a,252,18,12800
W 7
s_mov_b32 m0, 90112
s_mov_b32 s61, 303104
M v,240,a,232,v,208
T a,254,18,14848
buffer_load_dwordx4 v19, s[36:39], s61 offen offset:0 nt lds
s_waitcnt vmcnt(63)
s_barrier
T a,224,9,16384
M v,244,a,236,v,208
T a,226,9,24576
T a,228,12,16384
W 7
s_mov_b32 m0, 91008
M v,224,a,240,v,216
T a,230,12,24576
buffer_load_dwordx4 v19, s[36:39], s61 offen offset:128 nt lds
T a,232,9,16896
M v,228,a,244,v,216
T a,234,9,25088
T a,236,12,16896
W 7
s_mov_b32 m0, 91904
M v,240,a,248,v,216
T a,238,12,25088
buffer_load_dwordx4 v19, s[36:39], s61 offen offset:256 nt lds
T a,240,9,17408
M v,244,a,252,v,216
T a,242,9,25600
s_nop 7
v_mul_f32 v224, v28, v224
v_mul_f32 v225, v28, v225
s_mov_b32 m0, 92800
s_nop 0
buffer_load_dwordx4 v19, s[36:39], s61 offen offset:384 nt lds
v_mul_f32 v226, v28, v226
v_mul_f32 v227, v28, v227
v_mul_f32 v228, v28, v228
v_mul_f32 v229, v28, v229
s_mov_b32 m0, 94208
s_mov_b32 s61, 307200
buffer_load_dwordx4 v27, s[36:39], s61 offen offset:0 nt lds
v_mul_f32 v230, v28, v230
v_mul_f32 v231, v28, v231
v_cvt_pk_bf16_f32 v224, v224, v225
v_cvt_pk_bf16_f32 v225, v226, v227
v_cvt_pk_bf16_f32 v226, v228, v229
v_cvt_pk_bf16_f32 v227, v230, v231
v_mul_f32 v240, v28, v240
v_mul_f32 v241, v28, v241
s_mov_b32 m0, 95104
s_nop 0
buffer_load_dwordx4 v27, s[36:39], s61 offen offset:128 nt lds
v_mul_f32 v242, v28, v242
v_mul_f32 v243, v28, v243
v_mul_f32 v244, v28, v244
v_mul_f32 v245, v28, v245
v_mul_f32 v246, v28, v246
v_mul_f32 v247, v28, v247
v_cvt_pk_bf16_f32 v240, v240, v241
v_cvt_pk_bf16_f32 v241, v242, v243
s_mov_b32 m0, 96000
s_nop 0
buffer_load_dwordx4 v27, s[36:39], s61 offen offset:256 nt lds
v_cvt_pk_bf16_f32 v242, v244, v245
v_cvt_pk_bf16_f32 v243, v246, v247
s_nop 1
T a,244,12,17408
W 7
M a,0,a,224,v,224
T a,246,12,25600
T a,248,9,17920
M a,4,a,228,v,224
T a,250,9,26112
T a,252,12,17920
W 7
s_mov_b32 m0, 96896
M a,16,a,232,v,224
T a,254,12,26112
buffer_load_dwordx4 v27, s[36:39], s61 offen offset:384 nt lds
T a,224,9,18432
M a,20,a,236,v,224
T a,226,9,26624
T a,228,12,18432
W 7
s_mov_b32 m0, 98304
s_mov_b32 s61, 311296
M a,32,a,240,v,224
T a,230,12,26624
buffer_load_dwordx4 v19, s[36:39], s61 offen offset:0 nt lds
T a,232,9,18944
M a,36,a,244,v,224
T a,234,9,27136
T a,236,12,18944
W 7
s_mov_b32 m0, 99200
M a,48,a,248,v,224
T a,238,12,27136
buffer_load_dwordx4 v19, s[36:39], s61 offen offset:128 nt lds
T a,240,9,19456
s_mov_b32 m0, 100096
M a,52,a,252,v,224
T a,242,9,27648
buffer_load_dwordx4 v19, s[36:39], s61 offen offset:256 nt lds
T a,244,12,19456
W 7
M a,64,a,224,v,224
T a,246,12,27648
T a,248,9,19968
s_mov_b32 m0, 100992
M a,68,a,228,v,224
T a,250,9,28160
buffer_load_dwordx4 v19, s[36:39], s61 offen offset:384 nt lds
T a,252,12,19968
W 7
M a,80,a,232,v,224
T a,254,12,28160
s_waitcnt vmcnt(63)
s_barrier
.Lc1_88_%=:
T a,224,7,0
s_mov_b32 m0, 102400
s_mov_b32 s61, 315392
M a,84,a,236,v,224
T a,226,7,8192
buffer_load_dwordx4 v27, s[36:39], s61 offen offset:0 nt lds
T a,228,10,0
W 7
M a,96,a,240,v,224
T a,230,10,8192
T a,232,7,512
s_mov_b32 m0, 103296
M a,100,a,244,v,224
T a,234,7,8704
buffer_load_dwordx4 v27, s[36:39], s61 offen offset:128 nt lds
T a,236,10,512
W 7
M a,112,a,248,v,224
T a,238,10,8704
T a,240,7,1024
s_mov_b32 m0, 104192
M a,116,a,252,v,224
T a,242,7,9216
buffer_load_dwordx4 v27, s[36:39], s61 offen offset:256 nt lds
T a,244,10,1024
W 7
M a,128,a,224,v,224
T a,246,10,9216
T a,248,7,1536
s_mov_b32 m0, 105088
M a,132,a,228,v,224
T a,250,7,9728
buffer_load_dwordx4 v27, s[36:39], s61 offen offset:384 nt lds
T a,252,10,1536
W 7
M a,144,a,232,v,224
T a,254,10,9728
T a,224,7,2048
s_mov_b32 m0, 106496
s_mov_b32 s61, 319488
M a,148,a,236,v,224
T a,226,7,10240
buffer_load_dwordx4 v19, s[36:39], s61 offen offset:0 nt lds
T a,228,10,2048
W 7
M a,160,a,240,v,224
T a,230,10,10240
T a,232,7,2560
s_mov_b32 m0, 107392
M a,164,a,244,v,224
T a,234,7,10752
buffer_load_dwordx4 v19, s[36:39], s61 offen offset:128 nt lds
T a,236,10,2560
W 7
M a,176,a,248,v,224
T a,238,10,10752
s_waitcnt vmcnt(63)
s_barrier
T a,240,7,16384
s_mov_b32 m0, 108288
M a,180,a,252,v,224
T a,242,7,24576
buffer_load_dwordx4 v19, s[36:39], s61 offen offset:256 nt lds
T a,244,10,16384
W 7
M a,192,a,224,v,224
T a,246,10,24576
T a,248,7,16896
s_mov_b32 m0, 109184
M a,196,a,228,v,224
T a,250,7,25088
buffer_load_dwordx4 v19, s[36:39], s61 offen offset:384 nt lds
T a,252,10,16896
W 7
M a,208,a,232,v,224
T a,254,10,25088
T a,224,7,17408
s_mov_b32 m0, 110592
s_mov_b32 s61, 323584
M a,212,a,236,v,224
T a,226,7,25600
buffer_load_dwordx4 v27, s[36:39], s61 offen offset:0 nt lds
T a,228,10,17408
W 7
s_mov_b32 m0, 111488
M a,0,a,240,v,240
T a,230,10,25600
buffer_load_dwordx4 v27, s[36:39], s61 offen offset:128 nt lds
T a,232,7,17920
M a,4,a,244,v,240
T a,234,7,26112
T a,236,10,17920
W 7
s_mov_b32 m0, 112384
M a,16,a,248,v,240
T a,238,10,26112
buffer_load_dwordx4 v27, s[36:39], s61 offen offset:256 nt lds
T a,240,7,18432
M a,20,a,252,v,240
T a,242,7,26624
T a,244,10,18432
W 7
s_mov_b32 m0, 113280
M a,32,a,224,v,240
T a,246,10,26624
buffer_load_dwordx4 v27, s[36:39], s61 offen offset:384 nt lds
T a,248,7,18944
M a,36,a,228,v,240
T a,250,7,27136
T a,252,10,18944
W 7
s_mov_b32 m0, 114688
s_mov_b32 s61, 327680
M a,48,a,232,v,240
T a,254,10,27136
buffer_load_dwordx4 v19, s[36:39], s61 offen offset:0 nt lds
T a,224,7,19456
M a,52,a,236,v,240
T a,226,7,27648
T a,228,10,19456
W 7
s_mov_b32 m0, 115584
M a,64,a,240,v,240
T a,230,10,27648
buffer_load_dwordx4 v19, s[36:39], s61 offen offset:128 nt lds
T a,232,7,19968
M a,68,a,244,v,240
T a,234,7,28160
T a,236,10,19968
W 7
s_mov_b32 m0, 116480
M a,80,a,248,v,240
T a,238,10,28160
buffer_load_dwordx4 v19, s[36:39], s61 offen offset:256 nt lds
s_waitcnt vmcnt(63)
s_barrier
T a,240,7,32768
M a,84,a,252,v,240
T a,242,7,40960
T a,244,10,32768
W 7
s_mov_b32 m0, 117376
M a,96,a,224,v,240
T a,246,10,40960
buffer_load_dwordx4 v19, s[36:39], s61 offen offset:384 nt lds
T a,248,7,33280
M a,100,a,228,v,240
T a,250,7,41472
T a,252,10,33280
W 7
s_mov_b32 m0, 118784
s_mov_b32 s61, 331776
M a,112,a,232,v,240
T a,254,10,41472
buffer_load_dwordx4 v27, s[36:39], s61 offen offset:0 nt lds
T a,224,7,33792
M a,116,a,236,v,240
T a,226,7,41984
T a,228,10,33792
W 7
s_mov_b32 m0, 119680
M a,128,a,240,v,240
T a,230,10,41984
buffer_load_dwordx4 v27, s[36:39], s61 offen offset:128 nt lds
T a,232,7,34304
M a,132,a,244,v,240
T a,234,7,42496
T a,236,10,34304
W 7
s_mov_b32 m0, 120576
M a,144,a,248,v,240
T a,238,10,42496
buffer_load_dwordx4 v27, s[36:39], s61 offen offset:256 nt lds
T a,240,7,34816
s_mov_b32 m0, 121472
M a,148,a,252,v,240
T a,242,7,43008
buffer_load_dwordx4 v27, s[36:39], s61 offen offset:384 nt lds
T a,244,10,34816
W 7
M a,160,a,224,v,240
T a,246,10,43008
T a,248,7,35328
s_mov_b32 m0, 122880
s_mov_b32 s61, 335872
M a,164,a,228,v,240
T a,250,7,43520
buffer_load_dwordx4 v19, s[36:39], s61 offen offset:0 nt lds
T a,252,10,35328
W 7
M a,176,a,232,v,240
T a,254,10,43520
s_mov_b32 m0, 123776
M a,180,a,236,v,240
buffer_load_dwordx4 v19, s[36:39], s61 offen offset:128 nt lds
W 6
M a,192,a,240,v,240
W 4
s_mov_b32 m0, 124672
M a,196,a,244,v,240
buffer_load_dwordx4 v19, s[36:39], s61 offen offset:256 nt lds
W 2
M a,208,a,248,v,240
W 0
s_mov_b32 m0, 125568
M a,212,a,252,v,240
buffer_load_dwordx4 v19, s[36:39], s61 offen offset:384 nt lds
s_barrier
s_nop 7
A 224,0
A 225,1
v_cvt_pk_bf16_f32 v96, v224, v225
A 226,2
A 227,3
v_cvt_pk_bf16_f32 v97, v226, v227
A 228,4
A 229,5
v_cvt_pk_bf16_f32 v98, v228, v229
A 230,6
A 231,7
v_cvt_pk_bf16_f32 v99, v230, v231
A 224,16
A 225,17
v_cvt_pk_bf16_f32 v104, v224, v225
A 226,18
A 227,19
v_cvt_pk_bf16_f32 v105, v226, v227
A 228,20
A 229,21
v_cvt_pk_bf16_f32 v106, v228, v229
A 230,22
A 231,23
v_cvt_pk_bf16_f32 v107, v230, v231
A 224,32
A 225,33
v_cvt_pk_bf16_f32 v112, v224, v225
A 226,34
A 227,35
v_cvt_pk_bf16_f32 v113, v226, v227
A 228,36
A 229,37
s_mov_b32 m0, 126976
s_mov_b32 s61, 339968
buffer_load_dwordx4 v27, s[36:39], s61 offen offset:0 nt lds
v_cvt_pk_bf16_f32 v114, v228, v229
A 230,38
s_mov_b32 m0, 127872
s_nop 0
buffer_load_dwordx4 v27, s[36:39], s61 offen offset:128 nt lds
)x"
R"x(A 231,39
v_cvt_pk_bf16_f32 v115, v230, v231
s_mov_b32 m0, 128768
s_nop 0
buffer_load_dwordx4 v27, s[36:39], s61 offen offset:256 nt lds
A 224,48
A 225,49
s_mov_b32 m0, 129664
s_nop 0
buffer_load_dwordx4 v27, s[36:39], s61 offen offset:384 nt lds
v_cvt_pk_bf16_f32 v120, v224, v225
A 226,50
s_mov_b32 s61, .Lc0_88_%=-.Lpf_start%=+8192
buffer_load_dword v29, v20, s[92:95], s61 offen
A 227,51
v_cvt_pk_bf16_f32 v121, v226, v227
A 228,52
A 229,53
s_mov_b32 s61, .Lc1_88_%=-.Lpf_start%=+8192
buffer_load_dword v29, v20, s[92:95], s61 offen
v_cvt_pk_bf16_f32 v122, v228, v229
A 230,54
A 231,55
v_cvt_pk_bf16_f32 v123, v230, v231
A 224,64
A 225,65
v_cvt_pk_bf16_f32 v128, v224, v225
A 226,66
s_mov_b32 m0, 131072
s_mov_b32 s61, 344064
buffer_load_dwordx4 v19, s[36:39], s61 offen offset:0 nt lds
A 227,67
v_cvt_pk_bf16_f32 v129, v226, v227
A 228,68
A 229,69
v_cvt_pk_bf16_f32 v130, v228, v229
A 230,70
A 231,71
v_cvt_pk_bf16_f32 v131, v230, v231
s_mov_b32 m0, 131968
s_nop 0
buffer_load_dwordx4 v19, s[36:39], s61 offen offset:128 nt lds
A 224,80
A 225,81
v_cvt_pk_bf16_f32 v136, v224, v225
A 226,82
A 227,83
v_cvt_pk_bf16_f32 v137, v226, v227
A 228,84
A 229,85
s_mov_b32 m0, 132864
s_nop 0
buffer_load_dwordx4 v19, s[36:39], s61 offen offset:256 nt lds
v_cvt_pk_bf16_f32 v138, v228, v229
A 230,86
A 231,87
v_cvt_pk_bf16_f32 v139, v230, v231
A 224,96
A 225,97
v_cvt_pk_bf16_f32 v144, v224, v225
A 226,98
s_mov_b32 m0, 133760
s_nop 0
buffer_load_dwordx4 v19, s[36:39], s61 offen offset:384 nt lds
A 227,99
v_cvt_pk_bf16_f32 v145, v226, v227
A 228,100
A 229,101
v_cvt_pk_bf16_f32 v146, v228, v229
A 230,102
A 231,103
v_cvt_pk_bf16_f32 v147, v230, v231
s_mov_b32 m0, 135168
s_mov_b32 s61, 348160
buffer_load_dwordx4 v27, s[36:39], s61 offen offset:0 nt lds
A 224,112
A 225,113
v_cvt_pk_bf16_f32 v152, v224, v225
A 226,114
A 227,115
v_cvt_pk_bf16_f32 v153, v226, v227
A 228,116
A 229,117
s_mov_b32 m0, 136064
s_nop 0
buffer_load_dwordx4 v27, s[36:39], s61 offen offset:128 nt lds
v_cvt_pk_bf16_f32 v154, v228, v229
A 230,118
A 231,119
v_cvt_pk_bf16_f32 v155, v230, v231
A 224,128
A 225,129
v_cvt_pk_bf16_f32 v160, v224, v225
A 226,130
s_mov_b32 m0, 136960
s_nop 0
buffer_load_dwordx4 v27, s[36:39], s61 offen offset:256 nt lds
A 227,131
v_cvt_pk_bf16_f32 v161, v226, v227
A 228,132
A 229,133
v_cvt_pk_bf16_f32 v162, v228, v229
A 230,134
A 231,135
v_cvt_pk_bf16_f32 v163, v230, v231
s_mov_b32 m0, 137856
s_nop 0
buffer_load_dwordx4 v27, s[36:39], s61 offen offset:384 nt lds
A 224,144
A 225,145
v_cvt_pk_bf16_f32 v168, v224, v225
A 226,146
A 227,147
v_cvt_pk_bf16_f32 v169, v226, v227
A 228,148
A 229,149
s_mov_b32 m0, 139264
s_mov_b32 s61, 352256
buffer_load_dwordx4 v19, s[36:39], s61 offen offset:0 nt lds
v_cvt_pk_bf16_f32 v170, v228, v229
A 230,150
A 231,151
v_cvt_pk_bf16_f32 v171, v230, v231
A 224,160
A 225,161
v_cvt_pk_bf16_f32 v176, v224, v225
A 226,162
s_mov_b32 m0, 140160
s_nop 0
buffer_load_dwordx4 v19, s[36:39], s61 offen offset:128 nt lds
A 227,163
v_cvt_pk_bf16_f32 v177, v226, v227
A 228,164
A 229,165
v_cvt_pk_bf16_f32 v178, v228, v229
A 230,166
A 231,167
v_cvt_pk_bf16_f32 v179, v230, v231
s_mov_b32 m0, 141056
s_nop 0
buffer_load_dwordx4 v19, s[36:39], s61 offen offset:256 nt lds
A 224,176
A 225,177
v_cvt_pk_bf16_f32 v184, v224, v225
A 226,178
A 227,179
v_cvt_pk_bf16_f32 v185, v226, v227
A 228,180
A 229,181
s_mov_b32 m0, 141952
s_nop 0
buffer_load_dwordx4 v19, s[36:39], s61 offen offset:384 nt lds
v_cvt_pk_bf16_f32 v186, v228, v229
A 230,182
A 231,183
v_cvt_pk_bf16_f32 v187, v230, v231
A 224,192
A 225,193
v_cvt_pk_bf16_f32 v192, v224, v225
A 226,194
s_mov_b32 m0, 143360
s_mov_b32 s61, 356352
buffer_load_dwordx4 v27, s[36:39], s61 offen offset:0 nt lds
A 227,195
v_cvt_pk_bf16_f32 v193, v226, v227
A 228,196
A 229,197
v_cvt_pk_bf16_f32 v194, v228, v229
A 230,198
A 231,199
v_cvt_pk_bf16_f32 v195, v230, v231
s_mov_b32 m0, 144256
s_nop 0
buffer_load_dwordx4 v27, s[36:39], s61 offen offset:128 nt lds
A 224,208
A 225,209
v_cvt_pk_bf16_f32 v200, v224, v225
A 226,210
A 227,211
v_cvt_pk_bf16_f32 v201, v226, v227
A 228,212
A 229,213
s_mov_b32 m0, 145152
s_nop 0
buffer_load_dwordx4 v27, s[36:39], s61 offen offset:256 nt lds
v_cvt_pk_bf16_f32 v202, v228, v229
A 230,214
A 231,215
v_cvt_pk_bf16_f32 v203, v230, v231
s_waitcnt vmcnt(63)
s_barrier
R v,224,1,49152
R v,226,4,49152
R v,228,1,57344
R v,230,4,57344
R v,232,1,49664
R v,234,4,49664
R v,236,1,57856
R v,238,4,57856
R v,240,1,50176
R v,242,4,50176
R v,244,1,58368
W 7
v_mfma_f32_16x16x32_bf16 a[0:3], v[224:227], v[32:35], 0
R v,246,4,58368
R v,248,1,50688
s_mov_b32 m0, 146048
v_mfma_f32_16x16x32_bf16 a[4:7], v[228:231], v[32:35], 0
R v,250,4,50688
buffer_load_dwordx4 v27, s[36:39], s61 offen offset:384 nt lds
R v,252,1,58880
W 7
M a,0,v,232,v,40
R v,254,4,58880
R v,224,1,51200
s_mul_i32 s61, %9, 56
v_add_u32 v25, s61, v0
v_cmp_gt_u32 vcc, 56, v0
v_lshlrev_b32 v25, 7, v25
M a,4,v,236,v,40
R v,226,4,51200
s_mov_b32 s61, 229376
v_cndmask_b32 v25, 2.0, v25, vcc
buffer_load_dword v29, v25, s[52:55], s61 offen
R v,228,1,59392
W 7
M a,0,v,240,v,48
R v,230,4,59392
R v,232,1,51712
s_mov_b32 m0, 147456
s_mov_b32 s61, 360448
buffer_load_dwordx4 v19, s[36:39], s61 offen offset:0 nt lds
M a,4,v,244,v,48
R v,234,4,51712
R v,236,1,59904
W 7
M a,0,v,248,v,56
R v,238,4,59904
R v,240,1,52224
s_mov_b32 m0, 148352
M a,4,v,252,v,56
R v,242,4,52224
buffer_load_dwordx4 v19, s[36:39], s61 offen offset:128 nt lds
R v,244,1,60416
W 7
M a,0,v,224,v,64
R v,246,4,60416
R v,248,1,52736
s_mov_b32 m0, 149248
M a,4,v,228,v,64
R v,250,4,52736
buffer_load_dwordx4 v19, s[36:39], s61 offen offset:256 nt lds
R v,252,1,60928
W 7
M a,0,v,232,v,72
R v,254,4,60928
s_waitcnt vmcnt(63)
s_barrier
R v,224,2,0
s_mov_b32 m0, 150144
M a,4,v,236,v,72
R v,226,5,0
buffer_load_dwordx4 v19, s[36:39], s61 offen offset:384 nt lds
R v,228,2,8192
W 7
s_mov_b32 m0, 151552
s_mov_b32 s61, 364544
buffer_load_dwordx4 v27, s[36:39], s61 offen offset:0 nt lds
M a,0,v,240,v,80
R v,230,5,8192
R v,232,2,512
M a,4,v,244,v,80
R v,234,5,512
R v,236,2,8704
W 7
s_mov_b32 m0, 152448
M a,0,v,248,v,88
R v,238,5,8704
buffer_load_dwordx4 v27, s[36:39], s61 offen offset:128 nt lds
R v,240,2,1024
M a,4,v,252,v,88
R v,242,5,1024
R v,244,2,9216
W 7
s_mov_b32 m0, 153344
v_mfma_f32_16x16x32_bf16 a[16:19], v[224:227], v[32:35], 0
R v,246,5,9216
buffer_load_dwordx4 v27, s[36:39], s61 offen offset:256 nt lds
R v,248,2,1536
v_mfma_f32_16x16x32_bf16 a[20:23], v[228:231], v[32:35], 0
R v,250,5,1536
R v,252,2,9728
W 7
s_mov_b32 m0, 154240
M a,16,v,232,v,40
R v,254,5,9728
buffer_load_dwordx4 v27, s[36:39], s61 offen offset:384 nt lds
R v,224,2,2048
M a,20,v,236,v,40
R v,226,5,2048
R v,228,2,10240
W 7
s_mov_b32 m0, 155648
A 30,0
s_mov_b32 s61, 368640
buffer_load_dwordx4 v19, s[36:39], s61 offen offset:0 nt lds
M a,16,v,240,v,48
R v,230,5,10240
R v,232,2,2560
M a,20,v,244,v,48
R v,234,5,2560
R v,236,2,10752
W 7
s_mov_b32 m0, 156544
A 31,1
buffer_load_dwordx4 v19, s[36:39], s61 offen offset:128 nt lds
M a,16,v,248,v,56
R v,238,5,10752
R v,240,2,3072
M a,20,v,252,v,56
R v,242,5,3072
R v,244,2,11264
W 7
s_mov_b32 m0, 157440
v_max_f32 v26, v30, v31
buffer_load_dwordx4 v19, s[36:39], s61 offen offset:256 nt lds
M a,16,v,224,v,64
R v,246,5,11264
R v,248,2,3584
M a,20,v,228,v,64
R v,250,5,3584
R v,252,2,11776
W 7
s_mov_b32 m0, 158336
A 30,2
buffer_load_dwordx4 v19, s[36:39], s61 offen offset:384 nt lds
M a,16,v,232,v,72
R v,254,5,11776
s_waitcnt vmcnt(63)
s_barrier
R v,224,2,16384
M a,20,v,236,v,72
R v,226,5,16384
R v,228,2,24576
W 7
s_mov_b32 m0, 159744
A 31,3
s_mov_b32 s61, 372736
buffer_load_dwordx4 v27, s[36:39], s61 offen offset:0 nt lds
M a,16,v,240,v,80
R v,230,5,24576
R v,232,2,16896
M a,20,v,244,v,80
R v,234,5,16896
R v,236,2,25088
W 7
s_mov_b32 m0, 160640
v_max3_f32 v26, v30, v31, v26
buffer_load_dwordx4 v27, s[36:39], s61 offen offset:128 nt lds
M a,16,v,248,v,88
R v,238,5,25088
R v,240,2,17408
s_mov_b32 m0, 161536
A 30,4
buffer_load_dwordx4 v27, s[36:39], s61 offen offset:256 nt lds
M a,20,v,252,v,88
R v,242,5,17408
R v,244,2,25600
W 7
v_mfma_f32_16x16x32_bf16 a[32:35], v[224:227], v[32:35], 0
R v,246,5,25600
R v,248,2,17920
s_mov_b32 m0, 162432
A 31,5
buffer_load_dwordx4 v27, s[36:39], s61 offen offset:384 nt lds
v_mfma_f32_16x16x32_bf16 a[36:39], v[228:231], v[32:35], 0
R v,250,5,17920
R v,252,2,26112
W 7
M a,32,v,232,v,40
R v,254,5,26112
R v,224,2,18432
s_mov_b32 m0, 0
v_max3_f32 v26, v30, v31, v26
s_mov_b32 s61, 376832
buffer_load_dwordx4 v19, s[36:39], s61 offen offset:0 nt lds
M a,36,v,236,v,40
R v,226,5,18432
R v,228,2,26624
W 7
M a,32,v,240,v,48
R v,230,5,26624
R v,232,2,18944
s_mov_b32 m0, 896
A 30,6
buffer_load_dwordx4 v19, s[36:39], s61 offen offset:128 nt lds
M a,36,v,244,v,48
R v,234,5,18944
R v,236,2,27136
W 7
)x"
R"x(M a,32,v,248,v,56
R v,238,5,27136
R v,240,2,19456
s_mov_b32 m0, 1792
A 31,7
buffer_load_dwordx4 v19, s[36:39], s61 offen offset:256 nt lds
M a,36,v,252,v,56
R v,242,5,19456
R v,244,2,27648
W 7
M a,32,v,224,v,64
R v,246,5,27648
R v,248,2,19968
s_mov_b32 m0, 2688
v_max3_f32 v26, v30, v31, v26
buffer_load_dwordx4 v19, s[36:39], s61 offen offset:384 nt lds
M a,36,v,228,v,64
R v,250,5,19968
R v,252,2,28160
W 7
M a,32,v,232,v,72
R v,254,5,28160
s_waitcnt vmcnt(55)
s_barrier
.Lc1_94_%=:
R v,224,2,32768
s_mov_b32 m0, 4096
A 30,16
s_mov_b32 s61, 380928
buffer_load_dwordx4 v27, s[36:39], s61 offen offset:0 nt lds
M a,36,v,236,v,72
R v,226,5,32768
R v,228,2,40960
W 7
M a,32,v,240,v,80
R v,230,5,40960
R v,232,2,33280
s_mov_b32 m0, 4992
A 31,17
buffer_load_dwordx4 v27, s[36:39], s61 offen offset:128 nt lds
M a,36,v,244,v,80
R v,234,5,33280
R v,236,2,41472
W 7
M a,32,v,248,v,88
R v,238,5,41472
R v,240,2,33792
s_mov_b32 m0, 5888
v_max3_f32 v26, v30, v31, v26
buffer_load_dwordx4 v27, s[36:39], s61 offen offset:256 nt lds
M a,36,v,252,v,88
R v,242,5,33792
R v,244,2,41984
W 7
v_mfma_f32_16x16x32_bf16 a[48:51], v[224:227], v[32:35], 0
R v,246,5,41984
R v,248,2,34304
s_mov_b32 m0, 6784
A 30,18
buffer_load_dwordx4 v27, s[36:39], s61 offen offset:384 nt lds
v_mfma_f32_16x16x32_bf16 a[52:55], v[228:231], v[32:35], 0
R v,250,5,34304
R v,252,2,42496
W 7
M a,48,v,232,v,40
R v,254,5,42496
R v,224,2,34816
s_mov_b32 m0, 8192
A 31,19
s_mov_b32 s61, 385024
buffer_load_dwordx4 v19, s[36:39], s61 offen offset:0 nt lds
M a,52,v,236,v,40
R v,226,5,34816
R v,228,2,43008
W 7
s_mov_b32 m0, 9088
v_max3_f32 v26, v30, v31, v26
buffer_load_dwordx4 v19, s[36:39], s61 offen offset:128 nt lds
M a,48,v,240,v,48
R v,230,5,43008
R v,232,2,35328
M a,52,v,244,v,48
R v,234,5,35328
R v,236,2,43520
W 7
s_mov_b32 m0, 9984
A 30,20
buffer_load_dwordx4 v19, s[36:39], s61 offen offset:256 nt lds
M a,48,v,248,v,56
R v,238,5,43520
R v,240,2,35840
M a,52,v,252,v,56
R v,242,5,35840
R v,244,2,44032
W 7
s_mov_b32 m0, 10880
A 31,21
buffer_load_dwordx4 v19, s[36:39], s61 offen offset:384 nt lds
M a,48,v,224,v,64
R v,246,5,44032
R v,248,2,36352
M a,52,v,228,v,64
R v,250,5,36352
R v,252,2,44544
W 7
s_mov_b32 m0, 12288
v_max3_f32 v26, v30, v31, v26
s_mov_b32 s61, 389120
buffer_load_dwordx4 v27, s[36:39], s61 offen offset:0 nt lds
M a,48,v,232,v,72
R v,254,5,44544
s_waitcnt vmcnt(48)
s_barrier
R v,224,2,49152
M a,52,v,236,v,72
R v,226,5,49152
R v,228,2,57344
W 7
s_mov_b32 m0, 13184
A 30,22
buffer_load_dwordx4 v27, s[36:39], s61 offen offset:128 nt lds
M a,48,v,240,v,80
R v,230,5,57344
R v,232,2,49664
M a,52,v,244,v,80
R v,234,5,49664
R v,236,2,57856
W 7
s_mov_b32 m0, 14080
A 31,23
buffer_load_dwordx4 v27, s[36:39], s61 offen offset:256 nt lds
M a,48,v,248,v,88
R v,238,5,57856
R v,240,2,50176
M a,52,v,252,v,88
R v,242,5,50176
R v,244,2,58368
W 7
s_mov_b32 m0, 14976
v_max3_f32 v26, v30, v31, v26
buffer_load_dwordx4 v27, s[36:39], s61 offen offset:384 nt lds
v_mfma_f32_16x16x32_bf16 a[64:67], v[224:227], v[32:35], 0
R v,246,5,58368
R v,248,2,50688
v_mfma_f32_16x16x32_bf16 a[68:71], v[228:231], v[32:35], 0
R v,250,5,50688
R v,252,2,58880
W 7
s_mul_i32 s61, %9, 56
A 30,32
v_add_u32 v25, s61, v0
v_cmp_gt_u32 vcc, 56, v0
M a,64,v,232,v,40
R v,254,5,58880
v_lshlrev_b32 v25, 7, v25
A 31,33
s_mov_b32 s61, 344064
v_cndmask_b32 v25, 2.0, v25, vcc
R v,224,2,51200
v_max3_f32 v26, v30, v31, v26
A 30,34
buffer_load_dword v29, v25, s[52:55], s61 offen
M a,68,v,236,v,40
R v,226,5,51200
R v,228,2,59392
W 7
s_mov_b32 m0, 16384
A 31,35
s_mov_b32 s61, 393216
buffer_load_dwordx4 v19, s[36:39], s61 offen offset:0 nt lds
M a,64,v,240,v,48
R v,230,5,59392
R v,232,2,51712
M a,68,v,244,v,48
R v,234,5,51712
R v,236,2,59904
W 7
s_mov_b32 m0, 17280
v_max3_f32 v26, v30, v31, v26
buffer_load_dwordx4 v19, s[36:39], s61 offen offset:128 nt lds
M a,64,v,248,v,56
R v,238,5,59904
R v,240,2,52224
s_mov_b32 m0, 18176
A 30,36
buffer_load_dwordx4 v19, s[36:39], s61 offen offset:256 nt lds
M a,68,v,252,v,56
R v,242,5,52224
R v,244,2,60416
W 7
M a,64,v,224,v,64
R v,246,5,60416
R v,248,2,52736
s_mov_b32 m0, 19072
A 31,37
buffer_load_dwordx4 v19, s[36:39], s61 offen offset:384 nt lds
M a,68,v,228,v,64
R v,250,5,52736
R v,252,2,60928
W 7
M a,64,v,232,v,72
R v,254,5,60928
s_waitcnt vmcnt(38)
s_barrier
R v,224,3,0
s_mov_b32 m0, 20480
v_max3_f32 v26, v30, v31, v26
s_mov_b32 s61, 397312
buffer_load_dwordx4 v27, s[36:39], s61 offen offset:0 nt lds
M a,68,v,236,v,72
R v,226,6,0
R v,228,3,8192
W 7
M a,64,v,240,v,80
R v,230,6,8192
R v,232,3,512
s_mov_b32 m0, 21376
A 30,38
buffer_load_dwordx4 v27, s[36:39], s61 offen offset:128 nt lds
M a,68,v,244,v,80
R v,234,6,512
R v,236,3,8704
W 7
M a,64,v,248,v,88
R v,238,6,8704
R v,240,3,1024
s_mov_b32 m0, 22272
A 31,39
buffer_load_dwordx4 v27, s[36:39], s61 offen offset:256 nt lds
M a,68,v,252,v,88
R v,242,6,1024
R v,244,3,9216
W 7
v_mfma_f32_16x16x32_bf16 a[80:83], v[224:227], v[32:35], 0
R v,246,6,9216
R v,248,3,1536
s_mov_b32 m0, 23168
v_max3_f32 v26, v30, v31, v26
buffer_load_dwordx4 v27, s[36:39], s61 offen offset:384 nt lds
v_mfma_f32_16x16x32_bf16 a[84:87], v[228:231], v[32:35], 0
R v,250,6,1536
R v,252,3,9728
W 7
M a,80,v,232,v,40
R v,254,6,9728
R v,224,3,2048
A 30,48
A 31,49
v_max3_f32 v26, v30, v31, v26
A 30,50
M a,84,v,236,v,40
R v,226,6,2048
s_mov_b32 m0, 24576
A 31,51
s_mov_b32 s61, 401408
buffer_load_dwordx4 v19, s[36:39], s61 offen offset:0 nt lds
R v,228,3,10240
W 7
M a,80,v,240,v,48
R v,230,6,10240
R v,232,3,2560
s_mov_b32 m0, 25472
v_max3_f32 v26, v30, v31, v26
buffer_load_dwordx4 v19, s[36:39], s61 offen offset:128 nt lds
M a,84,v,244,v,48
R v,234,6,2560
R v,236,3,10752
W 7
M a,80,v,248,v,56
R v,238,6,10752
R v,240,3,3072
s_mov_b32 m0, 26368
A 30,52
buffer_load_dwordx4 v19, s[36:39], s61 offen offset:256 nt lds
M a,84,v,252,v,56
R v,242,6,3072
R v,244,3,11264
W 7
M a,80,v,224,v,64
R v,246,6,11264
R v,248,3,3584
s_mov_b32 m0, 27264
A 31,53
buffer_load_dwordx4 v19, s[36:39], s61 offen offset:384 nt lds
M a,84,v,228,v,64
R v,250,6,3584
R v,252,3,11776
W 7
M a,80,v,232,v,72
R v,254,6,11776
s_waitcnt vmcnt(29)
s_barrier
R v,224,3,16384
s_mov_b32 m0, 28672
v_max3_f32 v26, v30, v31, v26
s_mov_b32 s61, 405504
buffer_load_dwordx4 v27, s[36:39], s61 offen offset:0 nt lds
M a,84,v,236,v,72
R v,226,6,16384
s_mov_b32 m0, 29568
A 30,54
buffer_load_dwordx4 v27, s[36:39], s61 offen offset:128 nt lds
s_mov_b32 m0, 30464
R v,228,3,24576
W 7
buffer_load_dwordx4 v27, s[36:39], s61 offen offset:256 nt lds
A 31,55
s_mov_b32 m0, 31360
M a,80,v,240,v,80
R v,230,6,24576
buffer_load_dwordx4 v27, s[36:39], s61 offen offset:384 nt lds
v_max3_f32 v26, v30, v31, v26
s_mov_b32 m0, 32768
s_mov_b32 s61, 409600
R v,232,3,16896
buffer_load_dwordx4 v19, s[36:39], s61 offen offset:0 nt lds
A 30,64
M a,84,v,244,v,80
R v,234,6,16896
R v,236,3,25088
W 7
s_mov_b32 m0, 33664
A 31,65
buffer_load_dwordx4 v19, s[36:39], s61 offen offset:128 nt lds
M a,80,v,248,v,88
R v,238,6,25088
R v,240,3,17408
M a,84,v,252,v,88
R v,242,6,17408
R v,244,3,25600
W 7
s_mov_b32 m0, 34560
v_max3_f32 v26, v30, v31, v26
buffer_load_dwordx4 v19, s[36:39], s61 offen offset:256 nt lds
v_mfma_f32_16x16x32_bf16 a[96:99], v[224:227], v[32:35], 0
R v,246,6,25600
R v,248,3,17920
v_mfma_f32_16x16x32_bf16 a[100:103], v[228:231], v[32:35], 0
R v,250,6,17920
R v,252,3,26112
W 7
s_mov_b32 m0, 35456
A 30,66
buffer_load_dwordx4 v19, s[36:39], s61 offen offset:384 nt lds
M a,96,v,232,v,40
R v,254,6,26112
R v,224,3,18432
M a,100,v,236,v,40
R v,226,6,18432
R v,228,3,26624
W 7
s_mov_b32 m0, 36864
A 31,67
s_mov_b32 s61, 413696
buffer_load_dwordx4 v27, s[36:39], s61 offen offset:0 nt lds
M a,96,v,240,v,48
R v,230,6,26624
R v,232,3,18944
M a,100,v,244,v,48
R v,234,6,18944
R v,236,3,27136
W 7
s_mov_b32 m0, 37760
v_max3_f32 v26, v30, v31, v26
buffer_load_dwordx4 v27, s[36:39], s61 offen offset:128 nt lds
M a,96,v,248,v,56
R v,238,6,27136
R v,240,3,19456
M a,100,v,252,v,56
R v,242,6,19456
R v,244,3,27648
W 7
s_mov_b32 m0, 38656
A 30,68
buffer_load_dwordx4 v27, s[36:39], s61 offen offset:256 nt lds
M a,96,v,224,v,64
R v,246,6,27648
R v,248,3,19968
M a,100,v,228,v,64
R v,250,6,19968
R v,252,3,28160
W 7
s_mov_b32 m0, 39552
A 31,69
buffer_load_dwordx4 v27, s[36:39], s61 offen offset:384 nt lds
M a,96,v,232,v,72
R v,254,6,28160
s_waitcnt vmcnt(25)
s_barrier
R v,224,1,0
s_mov_b32 m0, 40960
v_max3_f32 v26, v30, v31, v26
s_mov_b32 s61, 417792
buffer_load_dwordx4 v19, s[36:39], s61 offen offset:0 nt lds
M a,100,v,236,v,72
R v,226,4,0
s_mov_b32 m0, 41856
A 30,70
buffer_load_dwordx4 v19, s[36:39], s61 offen offset:128 nt lds
s_mov_b32 m0, 42752
)x"
R"x(R v,228,1,8192
W 7
buffer_load_dwordx4 v19, s[36:39], s61 offen offset:256 nt lds
A 31,71
s_mov_b32 m0, 43648
M a,96,v,240,v,80
R v,230,4,8192
buffer_load_dwordx4 v19, s[36:39], s61 offen offset:384 nt lds
v_max3_f32 v26, v30, v31, v26
s_mov_b32 m0, 45056
s_mov_b32 s61, 421888
R v,232,1,512
buffer_load_dwordx4 v27, s[36:39], s61 offen offset:0 nt lds
A 30,80
s_mov_b32 m0, 45952
M a,100,v,244,v,80
R v,234,4,512
buffer_load_dwordx4 v27, s[36:39], s61 offen offset:128 nt lds
A 31,81
s_mov_b32 m0, 46848
R v,236,1,8704
W 7
buffer_load_dwordx4 v27, s[36:39], s61 offen offset:256 nt lds
v_max3_f32 v26, v30, v31, v26
s_mov_b32 m0, 47744
M a,96,v,248,v,88
R v,238,4,8704
buffer_load_dwordx4 v27, s[36:39], s61 offen offset:384 nt lds
A 30,82
s_mov_b32 m0, 49152
s_mov_b32 s61, 425984
R v,240,1,1024
buffer_load_dwordx4 v19, s[36:39], s61 offen offset:0 nt lds
A 31,83
s_mov_b32 m0, 50048
M a,100,v,252,v,88
R v,242,4,1024
buffer_load_dwordx4 v19, s[36:39], s61 offen offset:128 nt lds
v_max3_f32 v26, v30, v31, v26
R v,244,1,9216
W 7
s_mov_b32 m0, 50944
A 30,84
buffer_load_dwordx4 v19, s[36:39], s61 offen offset:256 nt lds
v_mfma_f32_16x16x32_bf16 a[112:115], v[224:227], v[32:35], 0
R v,246,4,9216
R v,248,1,1536
v_mfma_f32_16x16x32_bf16 a[116:119], v[228:231], v[32:35], 0
R v,250,4,1536
R v,252,1,9728
W 7
s_mov_b32 m0, 51840
A 31,85
buffer_load_dwordx4 v19, s[36:39], s61 offen offset:384 nt lds
M a,112,v,232,v,40
R v,254,4,9728
R v,224,1,2048
M a,116,v,236,v,40
R v,226,4,2048
R v,228,1,10240
W 7
s_mov_b32 m0, 53248
v_max3_f32 v26, v30, v31, v26
s_mov_b32 s61, 430080
buffer_load_dwordx4 v27, s[36:39], s61 offen offset:0 nt lds
M a,112,v,240,v,48
R v,230,4,10240
R v,232,1,2560
M a,116,v,244,v,48
R v,234,4,2560
R v,236,1,10752
W 7
s_mov_b32 m0, 54144
A 30,86
buffer_load_dwordx4 v27, s[36:39], s61 offen offset:128 nt lds
M a,112,v,248,v,56
R v,238,4,10752
R v,240,1,3072
M a,116,v,252,v,56
R v,242,4,3072
R v,244,1,11264
W 7
s_mov_b32 m0, 55040
A 31,87
buffer_load_dwordx4 v27, s[36:39], s61 offen offset:256 nt lds
M a,112,v,224,v,64
R v,246,4,11264
R v,248,1,3584
M a,116,v,228,v,64
R v,250,4,3584
R v,252,1,11776
W 7
s_mov_b32 m0, 55936
v_max3_f32 v26, v30, v31, v26
buffer_load_dwordx4 v27, s[36:39], s61 offen offset:384 nt lds
M a,112,v,232,v,72
R v,254,4,11776
s_waitcnt vmcnt(24)
s_barrier
R v,224,1,16384
s_mov_b32 m0, 57344
A 30,96
s_mov_b32 s61, 434176
buffer_load_dwordx4 v19, s[36:39], s61 offen offset:0 nt lds
M a,116,v,236,v,72
R v,226,4,16384
s_mov_b32 m0, 58240
A 31,97
buffer_load_dwordx4 v19, s[36:39], s61 offen offset:128 nt lds
s_mov_b32 m0, 59136
R v,228,1,24576
W 7
buffer_load_dwordx4 v19, s[36:39], s61 offen offset:256 nt lds
v_max3_f32 v26, v30, v31, v26
s_mov_b32 m0, 60032
M a,112,v,240,v,80
R v,230,4,24576
buffer_load_dwordx4 v19, s[36:39], s61 offen offset:384 nt lds
A 30,98
s_mov_b32 m0, 61440
s_mov_b32 s61, 438272
R v,232,1,16896
buffer_load_dwordx4 v27, s[36:39], s61 offen offset:0 nt lds
A 31,99
s_mov_b32 m0, 62336
M a,116,v,244,v,80
R v,234,4,16896
buffer_load_dwordx4 v27, s[36:39], s61 offen offset:128 nt lds
v_max3_f32 v26, v30, v31, v26
s_mov_b32 m0, 63232
R v,236,1,25088
W 7
buffer_load_dwordx4 v27, s[36:39], s61 offen offset:256 nt lds
A 30,100
s_mov_b32 m0, 64128
M a,112,v,248,v,88
R v,238,4,25088
buffer_load_dwordx4 v27, s[36:39], s61 offen offset:384 nt lds
A 31,101
s_mov_b32 s61, .Lc0_94_%=-.Lpf_start%=+8192
buffer_load_dword v29, v20, s[92:95], s61 offen
R v,240,1,17408
s_mov_b32 s61, .Lc1_94_%=-.Lpf_start%=+8192
v_max3_f32 v26, v30, v31, v26
buffer_load_dword v29, v20, s[92:95], s61 offen
s_mov_b32 m0, 65536
M a,116,v,252,v,88
R v,242,4,17408
s_mov_b32 s61, 442368
A 30,102
buffer_load_dwordx4 v19, s[36:39], s61 offen offset:0 nt lds
R v,244,1,25600
W 7
s_mov_b32 m0, 66432
A 31,103
buffer_load_dwordx4 v19, s[36:39], s61 offen offset:128 nt lds
v_mfma_f32_16x16x32_bf16 a[128:131], v[224:227], v[32:35], 0
R v,246,4,25600
R v,248,1,17920
v_mfma_f32_16x16x32_bf16 a[132:135], v[228:231], v[32:35], 0
R v,250,4,17920
R v,252,1,26112
W 7
s_mov_b32 m0, 67328
v_max3_f32 v26, v30, v31, v26
buffer_load_dwordx4 v19, s[36:39], s61 offen offset:256 nt lds
M a,128,v,232,v,40
R v,254,4,26112
R v,224,1,18432
M a,132,v,236,v,40
R v,226,4,18432
R v,228,1,26624
W 7
s_mov_b32 m0, 68224
A 30,112
buffer_load_dwordx4 v19, s[36:39], s61 offen offset:384 nt lds
M a,128,v,240,v,48
R v,230,4,26624
R v,232,1,18944
M a,132,v,244,v,48
R v,234,4,18944
R v,236,1,27136
W 7
s_mov_b32 m0, 69632
A 31,113
s_mov_b32 s61, 446464
buffer_load_dwordx4 v27, s[36:39], s61 offen offset:0 nt lds
M a,128,v,248,v,56
R v,238,4,27136
R v,240,1,19456
M a,132,v,252,v,56
R v,242,4,19456
R v,244,1,27648
W 7
s_mov_b32 m0, 70528
v_max3_f32 v26, v30, v31, v26
buffer_load_dwordx4 v27, s[36:39], s61 offen offset:128 nt lds
M a,128,v,224,v,64
R v,246,4,27648
R v,248,1,19968
M a,132,v,228,v,64
R v,250,4,19968
R v,252,1,28160
W 7
s_mov_b32 m0, 71424
A 30,114
buffer_load_dwordx4 v27, s[36:39], s61 offen offset:256 nt lds
M a,128,v,232,v,72
R v,254,4,28160
s_waitcnt vmcnt(25)
s_barrier
.Lc1_100_%=:
R v,224,1,32768
s_mov_b32 m0, 72320
A 31,115
buffer_load_dwordx4 v27, s[36:39], s61 offen offset:384 nt lds
s_mov_b32 m0, 73728
M a,132,v,236,v,72
R v,226,4,32768
s_mov_b32 s61, 450560
v_max3_f32 v26, v30, v31, v26
buffer_load_dwordx4 v19, s[36:39], s61 offen offset:0 nt lds
s_mov_b32 m0, 74624
R v,228,1,40960
W 7
buffer_load_dwordx4 v19, s[36:39], s61 offen offset:128 nt lds
A 30,116
s_mov_b32 m0, 75520
M a,128,v,240,v,80
R v,230,4,40960
buffer_load_dwordx4 v19, s[36:39], s61 offen offset:256 nt lds
A 31,117
s_mov_b32 m0, 76416
R v,232,1,33280
buffer_load_dwordx4 v19, s[36:39], s61 offen offset:384 nt lds
v_max3_f32 v26, v30, v31, v26
s_mov_b32 m0, 77824
s_mov_b32 s61, 454656
M a,132,v,244,v,80
R v,234,4,33280
buffer_load_dwordx4 v27, s[36:39], s61 offen offset:0 nt lds
A 30,118
s_mov_b32 m0, 78720
R v,236,1,41472
W 7
buffer_load_dwordx4 v27, s[36:39], s61 offen offset:128 nt lds
A 31,119
s_mov_b32 m0, 79616
M a,128,v,248,v,88
R v,238,4,41472
buffer_load_dwordx4 v27, s[36:39], s61 offen offset:256 nt lds
v_max3_f32 v26, v30, v31, v26
s_mov_b32 m0, 80512
R v,240,1,33792
buffer_load_dwordx4 v27, s[36:39], s61 offen offset:384 nt lds
s_mov_b32 m0, 81920
s_mov_b32 s61, 458752
buffer_load_dwordx4 v19, s[36:39], s61 offen offset:0 nt lds
M a,132,v,252,v,88
R v,242,4,33792
R v,244,1,41984
W 7
s_mov_b32 m0, 82816
v_mfma_f32_16x16x32_bf16 a[144:147], v[224:227], v[32:35], 0
R v,246,4,41984
buffer_load_dwordx4 v19, s[36:39], s61 offen offset:128 nt lds
R v,248,1,34304
v_mfma_f32_16x16x32_bf16 a[148:151], v[228:231], v[32:35], 0
R v,250,4,34304
R v,252,1,42496
W 7
s_mov_b32 m0, 83712
M a,144,v,232,v,40
R v,254,4,42496
buffer_load_dwordx4 v19, s[36:39], s61 offen offset:256 nt lds
R v,224,1,34816
M a,148,v,236,v,40
R v,226,4,34816
R v,228,1,43008
W 7
s_mov_b32 m0, 84608
A 30,128
buffer_load_dwordx4 v19, s[36:39], s61 offen offset:384 nt lds
M a,144,v,240,v,48
R v,230,4,43008
R v,232,1,35328
M a,148,v,244,v,48
R v,234,4,35328
R v,236,1,43520
W 7
s_mov_b32 m0, 86016
A 31,129
s_mov_b32 s61, 462848
buffer_load_dwordx4 v27, s[36:39], s61 offen offset:0 nt lds
M a,144,v,248,v,56
R v,238,4,43520
R v,240,1,35840
M a,148,v,252,v,56
R v,242,4,35840
R v,244,1,44032
W 7
s_mov_b32 m0, 86912
v_max3_f32 v26, v30, v31, v26
buffer_load_dwordx4 v27, s[36:39], s61 offen offset:128 nt lds
M a,144,v,224,v,64
R v,246,4,44032
R v,248,1,36352
M a,148,v,228,v,64
R v,250,4,36352
R v,252,1,44544
W 7
s_mov_b32 m0, 87808
A 30,130
buffer_load_dwordx4 v27, s[36:39], s61 offen offset:256 nt lds
M a,144,v,232,v,72
R v,254,4,44544
s_waitcnt vmcnt(25)
s_barrier
R v,224,1,49152
s_mov_b32 m0, 88704
A 31,131
buffer_load_dwordx4 v27, s[36:39], s61 offen offset:384 nt lds
s_mov_b32 m0, 90112
M a,148,v,236,v,72
R v,226,4,49152
s_mov_b32 s61, 466944
v_max3_f32 v26, v30, v31, v26
buffer_load_dwordx4 v19, s[36:39], s61 offen offset:0 nt lds
s_mov_b32 m0, 91008
R v,228,1,57344
W 7
buffer_load_dwordx4 v19, s[36:39], s61 offen offset:128 nt lds
A 30,132
s_mov_b32 m0, 91904
M a,144,v,240,v,80
R v,230,4,57344
buffer_load_dwordx4 v19, s[36:39], s61 offen offset:256 nt lds
A 31,133
s_mov_b32 m0, 92800
R v,232,1,49664
buffer_load_dwordx4 v19, s[36:39], s61 offen offset:384 nt lds
v_max3_f32 v26, v30, v31, v26
s_mov_b32 m0, 94208
s_mov_b32 s61, 471040
M a,148,v,244,v,80
R v,234,4,49664
buffer_load_dwordx4 v27, s[36:39], s61 offen offset:0 nt lds
A 30,134
s_mov_b32 m0, 95104
R v,236,1,57856
W 7
buffer_load_dwordx4 v27, s[36:39], s61 offen offset:128 nt lds
A 31,135
s_mov_b32 m0, 96000
M a,144,v,248,v,88
R v,238,4,57856
buffer_load_dwordx4 v27, s[36:39], s61 offen offset:256 nt lds
v_max3_f32 v26, v30, v31, v26
s_mov_b32 m0, 96896
R v,240,1,50176
buffer_load_dwordx4 v27, s[36:39], s61 offen offset:384 nt lds
s_mov_b32 m0, 98304
s_mov_b32 s61, 475136
buffer_load_dwordx4 v19, s[36:39], s61 offen offset:0 nt lds
M a,148,v,252,v,88
R v,242,4,50176
R v,244,1,58368
W 7
s_mov_b32 m0, 99200
v_mfma_f32_16x16x32_bf16 a[160:163], v[224:227], v[32:35], 0
R v,246,4,58368
buffer_load_dwordx4 v19, s[36:39], s61 offen offset:128 nt lds
R v,248,1,50688
v_mfma_f32_16x16x32_bf16 a[164:167], v[228:231], v[32:35], 0
R v,250,4,50688
R v,252,1,58880
W 7
s_mov_b32 m0, 100096
M a,160,v,232,v,40
R v,254,4,58880
buffer_load_dwordx4 v19, s[36:39], s61 offen offset:256 nt lds
R v,224,1,51200
M a,164,v,236,v,40
R v,226,4,51200
R v,228,1,59392
W 7
s_mov_b32 m0, 100992
A 30,144
buffer_load_dwordx4 v19, s[36:39], s61 offen offset:384 nt lds
M a,160,v,240,v,48
R v,230,4,59392
R v,232,1,51712
M a,164,v,244,v,48
R v,234,4,51712
R v,236,1,59904
W 7
s_mov_b32 m0, 102400
A 31,145
s_mov_b32 s61, 479232
buffer_load_dwordx4 v27, s[36:39], s61 offen offset:0 nt lds
)x"
R"x(M a,160,v,248,v,56
R v,238,4,59904
R v,240,1,52224
M a,164,v,252,v,56
R v,242,4,52224
R v,244,1,60416
W 7
s_mov_b32 m0, 103296
v_max3_f32 v26, v30, v31, v26
buffer_load_dwordx4 v27, s[36:39], s61 offen offset:128 nt lds
M a,160,v,224,v,64
R v,246,4,60416
R v,248,1,52736
M a,164,v,228,v,64
R v,250,4,52736
R v,252,1,60928
W 7
s_mov_b32 m0, 104192
A 30,146
buffer_load_dwordx4 v27, s[36:39], s61 offen offset:256 nt lds
M a,160,v,232,v,72
R v,254,4,60928
s_waitcnt vmcnt(23)
s_barrier
R v,224,2,0
s_mov_b32 m0, 105088
A 31,147
buffer_load_dwordx4 v27, s[36:39], s61 offen offset:384 nt lds
s_mov_b32 m0, 106496
M a,164,v,236,v,72
R v,226,5,0
s_mov_b32 s61, 483328
v_max3_f32 v26, v30, v31, v26
buffer_load_dwordx4 v19, s[36:39], s61 offen offset:0 nt lds
s_mov_b32 m0, 107392
R v,228,2,8192
W 7
buffer_load_dwordx4 v19, s[36:39], s61 offen offset:128 nt lds
A 30,148
s_mov_b32 m0, 108288
M a,160,v,240,v,80
R v,230,5,8192
buffer_load_dwordx4 v19, s[36:39], s61 offen offset:256 nt lds
A 31,149
s_mov_b32 m0, 109184
R v,232,2,512
buffer_load_dwordx4 v19, s[36:39], s61 offen offset:384 nt lds
v_max3_f32 v26, v30, v31, v26
s_mov_b32 m0, 110592
s_mov_b32 s61, 487424
M a,164,v,244,v,80
R v,234,5,512
buffer_load_dwordx4 v27, s[36:39], s61 offen offset:0 nt lds
A 30,150
s_mov_b32 m0, 111488
R v,236,2,8704
W 7
buffer_load_dwordx4 v27, s[36:39], s61 offen offset:128 nt lds
A 31,151
s_mov_b32 m0, 112384
M a,160,v,248,v,88
R v,238,5,8704
buffer_load_dwordx4 v27, s[36:39], s61 offen offset:256 nt lds
v_max3_f32 v26, v30, v31, v26
s_mov_b32 m0, 113280
R v,240,2,1024
buffer_load_dwordx4 v27, s[36:39], s61 offen offset:384 nt lds
s_mov_b32 m0, 114688
s_mov_b32 s61, 491520
buffer_load_dwordx4 v19, s[36:39], s61 offen offset:0 nt lds
M a,164,v,252,v,88
R v,242,5,1024
s_mov_b32 m0, 115584
R v,244,2,9216
W 7
buffer_load_dwordx4 v19, s[36:39], s61 offen offset:128 nt lds
s_mov_b32 m0, 116480
v_mfma_f32_16x16x32_bf16 a[176:179], v[224:227], v[32:35], 0
R v,246,5,9216
buffer_load_dwordx4 v19, s[36:39], s61 offen offset:256 nt lds
s_mov_b32 m0, 117376
R v,248,2,1536
buffer_load_dwordx4 v19, s[36:39], s61 offen offset:384 nt lds
v_mfma_f32_16x16x32_bf16 a[180:183], v[228:231], v[32:35], 0
R v,250,5,1536
R v,252,2,9728
W 7
s_mov_b32 m0, 118784
s_mov_b32 s61, 495616
buffer_load_dwordx4 v27, s[36:39], s61 offen offset:0 nt lds
M a,176,v,232,v,40
R v,254,5,9728
R v,224,2,2048
M a,180,v,236,v,40
R v,226,5,2048
R v,228,2,10240
W 7
s_mov_b32 m0, 119680
A 30,160
buffer_load_dwordx4 v27, s[36:39], s61 offen offset:128 nt lds
M a,176,v,240,v,48
R v,230,5,10240
R v,232,2,2560
M a,180,v,244,v,48
R v,234,5,2560
R v,236,2,10752
W 7
s_mov_b32 m0, 120576
A 31,161
buffer_load_dwordx4 v27, s[36:39], s61 offen offset:256 nt lds
M a,176,v,248,v,56
R v,238,5,10752
R v,240,2,3072
M a,180,v,252,v,56
R v,242,5,3072
R v,244,2,11264
W 7
s_mov_b32 m0, 121472
v_max3_f32 v26, v30, v31, v26
buffer_load_dwordx4 v27, s[36:39], s61 offen offset:384 nt lds
M a,176,v,224,v,64
R v,246,5,11264
R v,248,2,3584
M a,180,v,228,v,64
R v,250,5,3584
R v,252,2,11776
W 7
s_mov_b32 m0, 122880
A 30,162
s_mov_b32 s61, 499712
buffer_load_dwordx4 v19, s[36:39], s61 offen offset:0 nt lds
M a,176,v,232,v,72
R v,254,5,11776
s_waitcnt vmcnt(25)
s_barrier
R v,224,2,16384
s_mov_b32 m0, 123776
A 31,163
buffer_load_dwordx4 v19, s[36:39], s61 offen offset:128 nt lds
s_mov_b32 m0, 124672
M a,180,v,236,v,72
R v,226,5,16384
buffer_load_dwordx4 v19, s[36:39], s61 offen offset:256 nt lds
v_max3_f32 v26, v30, v31, v26
s_mov_b32 m0, 125568
R v,228,2,24576
W 7
buffer_load_dwordx4 v19, s[36:39], s61 offen offset:384 nt lds
A 30,164
s_mov_b32 m0, 126976
s_mov_b32 s61, 503808
M a,176,v,240,v,80
R v,230,5,24576
buffer_load_dwordx4 v27, s[36:39], s61 offen offset:0 nt lds
A 31,165
s_mov_b32 m0, 127872
R v,232,2,16896
buffer_load_dwordx4 v27, s[36:39], s61 offen offset:128 nt lds
v_max3_f32 v26, v30, v31, v26
s_mov_b32 m0, 128768
M a,180,v,244,v,80
R v,234,5,16896
buffer_load_dwordx4 v27, s[36:39], s61 offen offset:256 nt lds
A 30,166
s_mov_b32 m0, 129664
R v,236,2,25088
W 7
buffer_load_dwordx4 v27, s[36:39], s61 offen offset:384 nt lds
A 31,167
s_mov_b32 m0, 131072
s_mov_b32 s61, 507904
M a,176,v,248,v,88
R v,238,5,25088
buffer_load_dwordx4 v19, s[36:39], s61 offen offset:0 nt lds
v_max3_f32 v26, v30, v31, v26
s_mov_b32 m0, 131968
R v,240,2,17408
buffer_load_dwordx4 v19, s[36:39], s61 offen offset:128 nt lds
s_mov_b32 m0, 132864
M a,180,v,252,v,88
R v,242,5,17408
buffer_load_dwordx4 v19, s[36:39], s61 offen offset:256 nt lds
R v,244,2,25600
W 7
s_mov_b32 m0, 133760
v_mfma_f32_16x16x32_bf16 a[192:195], v[224:227], v[32:35], 0
R v,246,5,25600
buffer_load_dwordx4 v19, s[36:39], s61 offen offset:384 nt lds
R v,248,2,17920
v_mfma_f32_16x16x32_bf16 a[196:199], v[228:231], v[32:35], 0
R v,250,5,17920
R v,252,2,26112
W 7
s_mov_b32 m0, 135168
s_mov_b32 s61, 512000
buffer_load_dwordx4 v27, s[36:39], s61 offen offset:0 nt lds
M a,192,v,232,v,40
R v,254,5,26112
R v,224,2,18432
M a,196,v,236,v,40
R v,226,5,18432
R v,228,2,26624
W 7
s_mov_b32 m0, 136064
A 30,176
buffer_load_dwordx4 v27, s[36:39], s61 offen offset:128 nt lds
M a,192,v,240,v,48
R v,230,5,26624
R v,232,2,18944
M a,196,v,244,v,48
R v,234,5,18944
R v,236,2,27136
W 7
s_mov_b32 m0, 136960
A 31,177
buffer_load_dwordx4 v27, s[36:39], s61 offen offset:256 nt lds
M a,192,v,248,v,56
R v,238,5,27136
R v,240,2,19456
M a,196,v,252,v,56
R v,242,5,19456
R v,244,2,27648
W 7
s_mov_b32 m0, 137856
v_max3_f32 v26, v30, v31, v26
buffer_load_dwordx4 v27, s[36:39], s61 offen offset:384 nt lds
M a,192,v,224,v,64
R v,246,5,27648
R v,248,2,19968
M a,196,v,228,v,64
R v,250,5,19968
R v,252,2,28160
W 7
s_mov_b32 m0, 139264
A 30,178
s_mov_b32 s61, 516096
buffer_load_dwordx4 v19, s[36:39], s61 offen offset:0 nt lds
M a,192,v,232,v,72
R v,254,5,28160
s_waitcnt vmcnt(25)
s_barrier
R v,224,2,32768
s_mov_b32 m0, 140160
A 31,179
buffer_load_dwordx4 v19, s[36:39], s61 offen offset:128 nt lds
s_mov_b32 m0, 141056
M a,196,v,236,v,72
R v,226,5,32768
buffer_load_dwordx4 v19, s[36:39], s61 offen offset:256 nt lds
v_max3_f32 v26, v30, v31, v26
s_mov_b32 m0, 141952
R v,228,2,40960
W 7
buffer_load_dwordx4 v19, s[36:39], s61 offen offset:384 nt lds
A 30,180
s_mov_b32 m0, 143360
s_mov_b32 s61, 520192
M a,192,v,240,v,80
R v,230,5,40960
buffer_load_dwordx4 v27, s[36:39], s61 offen offset:0 nt lds
A 31,181
s_mov_b32 m0, 144256
R v,232,2,33280
buffer_load_dwordx4 v27, s[36:39], s61 offen offset:128 nt lds
v_max3_f32 v26, v30, v31, v26
s_mov_b32 m0, 145152
M a,196,v,244,v,80
R v,234,5,33280
buffer_load_dwordx4 v27, s[36:39], s61 offen offset:256 nt lds
A 30,182
s_mov_b32 m0, 146048
R v,236,2,41472
W 7
buffer_load_dwordx4 v27, s[36:39], s61 offen offset:384 nt lds
A 31,183
s_mov_b32 m0, 147456
s_mov_b32 s61, 262144
M a,192,v,248,v,88
R v,238,5,41472
buffer_load_dwordx4 v23, s[40:43], s61 offen offset:0 nt lds
v_max3_f32 v26, v30, v31, v26
s_mov_b32 m0, 148480
s_mov_b32 s61, 266240
R v,240,2,33792
buffer_load_dwordx4 v23, s[40:43], s61 offen offset:0 nt lds
s_mov_b32 m0, 149504
s_mov_b32 s61, 270336
buffer_load_dwordx4 v23, s[40:43], s61 offen offset:0 nt lds
M a,196,v,252,v,88
R v,242,5,33792
R v,244,2,41984
W 7
s_mov_b32 m0, 150528
s_mov_b32 s61, 274432
buffer_load_dwordx4 v23, s[40:43], s61 offen offset:0 nt lds
v_mfma_f32_16x16x32_bf16 a[208:211], v[224:227], v[32:35], 0
R v,246,5,41984
R v,248,2,34304
v_mfma_f32_16x16x32_bf16 a[212:215], v[228:231], v[32:35], 0
R v,250,5,34304
R v,252,2,42496
W 7
s_mov_b32 m0, 151552
s_mov_b32 s61, 278528
buffer_load_dwordx4 v23, s[40:43], s61 offen offset:0 nt lds
M a,208,v,232,v,40
R v,254,5,42496
R v,224,2,34816
M a,212,v,236,v,40
R v,226,5,34816
R v,228,2,43008
W 7
s_mov_b32 m0, 152576
A 30,192
s_mov_b32 s61, 282624
buffer_load_dwordx4 v23, s[40:43], s61 offen offset:0 nt lds
M a,208,v,240,v,48
R v,230,5,43008
R v,232,2,35328
M a,212,v,244,v,48
R v,234,5,35328
R v,236,2,43520
W 7
s_mov_b32 m0, 153600
A 31,193
s_mov_b32 s61, 286720
buffer_load_dwordx4 v23, s[40:43], s61 offen offset:0 nt lds
M a,208,v,248,v,56
R v,238,5,43520
R v,240,2,35840
M a,212,v,252,v,56
R v,242,5,35840
R v,244,2,44032
W 7
s_mov_b32 m0, 154624
v_max3_f32 v26, v30, v31, v26
s_mov_b32 s61, 290816
buffer_load_dwordx4 v23, s[40:43], s61 offen offset:0 nt lds
M a,208,v,224,v,64
R v,246,5,44032
R v,248,2,36352
M a,212,v,228,v,64
R v,250,5,36352
R v,252,2,44544
W 7
s_mov_b32 m0, 155648
A 30,194
s_mov_b32 s61, 294912
buffer_load_dwordx4 v23, s[40:43], s61 offen offset:0 nt lds
M a,208,v,232,v,72
R v,254,5,44544
s_waitcnt vmcnt(25)
s_barrier
R v,224,2,49152
s_mov_b32 m0, 156672
A 31,195
s_mov_b32 s61, 299008
buffer_load_dwordx4 v23, s[40:43], s61 offen offset:0 nt lds
M a,212,v,236,v,72
R v,226,5,49152
s_mov_b32 m0, 157696
v_max3_f32 v26, v30, v31, v26
s_mov_b32 s61, 303104
buffer_load_dwordx4 v23, s[40:43], s61 offen offset:0 nt lds
R v,228,2,57344
W 7
s_mov_b32 m0, 158720
A 30,196
s_mov_b32 s61, 307200
buffer_load_dwordx4 v23, s[40:43], s61 offen offset:0 nt lds
M a,208,v,240,v,80
R v,230,5,57344
s_mov_b32 m0, 159744
A 31,197
s_mov_b32 s61, 311296
buffer_load_dwordx4 v23, s[40:43], s61 offen offset:0 nt lds
R v,232,2,49664
s_mov_b32 m0, 160768
v_max3_f32 v26, v30, v31, v26
s_mov_b32 s61, 315392
buffer_load_dwordx4 v23, s[40:43], s61 offen offset:0 nt lds
M a,212,v,244,v,80
R v,234,5,49664
s_mov_b32 m0, 161792
A 30,198
s_mov_b32 s61, 319488
buffer_load_dwordx4 v23, s[40:43], s61 offen offset:0 nt lds
R v,236,2,57856
W 7
s_mov_b32 m0, 162816
A 31,199
s_mov_b32 s61, 323584
buffer_load_dwordx4 v23, s[40:43], s61 offen offset:0 nt lds
M a,208,v,248,v,88
R v,238,5,57856
s_mov_b32 s61, .Lc0_100_%=-.Lpf_start%=+8192
v_max3_f32 v26, v30, v31, v26
buffer_load_dword v29, v20, s[92:95], s61 offen
s_mov_b32 s61, .Lc1_100_%=-.Lpf_start%=+8192
R v,240,2,50176
)x"
R"x(buffer_load_dword v29, v20, s[92:95], s61 offen
s_mov_b32 m0, 0
s_mov_b32 s61, 327680
buffer_load_dwordx4 v23, s[40:43], s61 offen offset:0 nt lds
M a,212,v,252,v,88
R v,242,5,50176
R v,244,2,58368
W 7
s_mov_b32 m0, 1024
s_mov_b32 s61, 331776
buffer_load_dwordx4 v23, s[40:43], s61 offen offset:0 nt lds
v_mfma_f32_16x16x32_bf16 a[224:227], v[224:227], v[32:35], 0
R v,246,5,58368
R v,248,2,50688
v_mfma_f32_16x16x32_bf16 a[228:231], v[228:231], v[32:35], 0
R v,250,5,50688
R v,252,2,58880
W 7
s_mov_b32 m0, 2048
s_mov_b32 s61, 335872
buffer_load_dwordx4 v23, s[40:43], s61 offen offset:0 nt lds
M a,224,v,232,v,40
R v,254,5,58880
R v,224,2,51200
M a,228,v,236,v,40
R v,226,5,51200
R v,228,2,59392
W 7
s_mov_b32 m0, 3072
A 30,208
s_mov_b32 s61, 339968
buffer_load_dwordx4 v23, s[40:43], s61 offen offset:0 nt lds
M a,224,v,240,v,48
R v,230,5,59392
R v,232,2,51712
M a,228,v,244,v,48
R v,234,5,51712
R v,236,2,59904
W 7
s_mov_b32 m0, 4096
A 31,209
s_mov_b32 s61, 344064
buffer_load_dwordx4 v23, s[40:43], s61 offen offset:0 nt lds
M a,224,v,248,v,56
R v,238,5,59904
R v,240,2,52224
M a,228,v,252,v,56
R v,242,5,52224
R v,244,2,60416
W 7
s_mov_b32 m0, 5120
v_max3_f32 v26, v30, v31, v26
s_mov_b32 s61, 348160
buffer_load_dwordx4 v23, s[40:43], s61 offen offset:0 nt lds
M a,224,v,224,v,64
R v,246,5,60416
R v,248,2,52736
M a,228,v,228,v,64
R v,250,5,52736
R v,252,2,60928
W 7
s_mov_b32 m0, 6144
A 30,210
s_mov_b32 s61, 352256
buffer_load_dwordx4 v23, s[40:43], s61 offen offset:0 nt lds
M a,224,v,232,v,72
R v,254,5,60928
s_waitcnt vmcnt(25)
s_barrier
.Lc1_106_%=:
R v,224,3,0
s_mov_b32 m0, 7168
A 31,211
s_mov_b32 s61, 356352
buffer_load_dwordx4 v23, s[40:43], s61 offen offset:0 nt lds
M a,228,v,236,v,72
R v,226,6,0
s_mov_b32 m0, 8192
v_max3_f32 v26, v30, v31, v26
s_mov_b32 s61, 360448
buffer_load_dwordx4 v23, s[40:43], s61 offen offset:0 nt lds
R v,228,3,8192
W 7
s_mov_b32 m0, 9216
A 30,212
s_mov_b32 s61, 364544
buffer_load_dwordx4 v23, s[40:43], s61 offen offset:0 nt lds
M a,224,v,240,v,80
R v,230,6,8192
s_mov_b32 m0, 10240
A 31,213
s_mov_b32 s61, 368640
buffer_load_dwordx4 v23, s[40:43], s61 offen offset:0 nt lds
R v,232,3,512
s_mov_b32 m0, 11264
v_max3_f32 v26, v30, v31, v26
s_mov_b32 s61, 372736
buffer_load_dwordx4 v23, s[40:43], s61 offen offset:0 nt lds
M a,228,v,244,v,80
R v,234,6,512
s_mov_b32 m0, 12288
A 30,214
s_mov_b32 s61, 376832
buffer_load_dwordx4 v23, s[40:43], s61 offen offset:0 nt lds
R v,236,3,8704
W 7
s_mov_b32 m0, 13312
A 31,215
s_mov_b32 s61, 380928
buffer_load_dwordx4 v23, s[40:43], s61 offen offset:0 nt lds
M a,224,v,248,v,88
R v,238,6,8704
s_mov_b32 m0, 14336
v_max3_f32 v26, v30, v31, v26
s_mov_b32 s61, 385024
buffer_load_dwordx4 v23, s[40:43], s61 offen offset:0 nt lds
R v,240,3,1024
s_mov_b32 m0, 15360
s_mov_b32 s61, 389120
buffer_load_dwordx4 v23, s[40:43], s61 offen offset:0 nt lds
s_mov_b32 m0, 16384
M a,228,v,252,v,88
R v,242,6,1024
s_mov_b32 s61, 393216
buffer_load_dwordx4 v23, s[40:43], s61 offen offset:0 nt lds
R v,244,3,9216
W 7
s_mov_b32 m0, 17408
s_mov_b32 s61, 397312
buffer_load_dwordx4 v23, s[40:43], s61 offen offset:0 nt lds
v_mfma_f32_16x16x32_bf16 a[240:243], v[224:227], v[32:35], 0
R v,246,6,9216
R v,248,3,1536
v_mfma_f32_16x16x32_bf16 a[244:247], v[228:231], v[32:35], 0
R v,250,6,1536
R v,252,3,9728
W 7
s_mov_b32 m0, 18432
s_mov_b32 s61, 401408
buffer_load_dwordx4 v23, s[40:43], s61 offen offset:0 nt lds
M a,240,v,232,v,40
R v,254,6,9728
R v,224,3,2048
M a,244,v,236,v,40
R v,226,6,2048
R v,228,3,10240
W 7
s_mov_b32 m0, 19456
A 30,224
s_mov_b32 s61, 405504
buffer_load_dwordx4 v23, s[40:43], s61 offen offset:0 nt lds
M a,240,v,240,v,48
R v,230,6,10240
R v,232,3,2560
M a,244,v,244,v,48
R v,234,6,2560
R v,236,3,10752
W 7
s_mov_b32 m0, 20480
A 31,225
s_mov_b32 s61, 409600
buffer_load_dwordx4 v23, s[40:43], s61 offen offset:0 nt lds
M a,240,v,248,v,56
R v,238,6,10752
R v,240,3,3072
M a,244,v,252,v,56
R v,242,6,3072
R v,244,3,11264
W 7
s_mov_b32 m0, 21504
v_max3_f32 v26, v30, v31, v26
s_mov_b32 s61, 413696
buffer_load_dwordx4 v23, s[40:43], s61 offen offset:0 nt lds
M a,240,v,224,v,64
R v,246,6,11264
R v,248,3,3584
M a,244,v,228,v,64
R v,250,6,3584
R v,252,3,11776
W 7
s_mov_b32 m0, 22528
A 30,226
s_mov_b32 s61, 417792
buffer_load_dwordx4 v23, s[40:43], s61 offen offset:0 nt lds
M a,240,v,232,v,72
R v,254,6,11776
M a,244,v,236,v,72
W 6
s_mov_b32 m0, 23552
A 31,227
s_mov_b32 s61, 421888
buffer_load_dwordx4 v23, s[40:43], s61 offen offset:0 nt lds
M a,240,v,240,v,80
W 4
M a,244,v,244,v,80
W 2
s_mov_b32 m0, 24576
v_max3_f32 v26, v30, v31, v26
s_mov_b32 s61, 425984
buffer_load_dwordx4 v23, s[40:43], s61 offen offset:0 nt lds
M a,240,v,248,v,88
W 0
s_mov_b32 m0, 25600
A 30,228
s_mov_b32 s61, 430080
buffer_load_dwordx4 v23, s[40:43], s61 offen offset:0 nt lds
M a,244,v,252,v,88
A 31,229
v_max3_f32 v26, v30, v31, v26
A 30,230
A 31,231
v_max3_f32 v26, v30, v31, v26
s_barrier
s_nop 7
v_and_b32 v30, 1, v0
v_lshlrev_b32 v30, 4, v30
v_mov_b32 v31, 0x3f80
v_lshlrev_b32 v31, v30, v31
v_bfe_u32 v30, v0, 2, 2
v_lshrrev_b32 v248, 4, v0
v_xor_b32 v30, v30, v248
v_bfe_u32 v248, v0, 1, 1
v_lshl_add_u32 v30, v30, 1, v248
v_mov_b32 v248, 0
v_mov_b32 v249, 0
v_mov_b32 v252, 0
v_mov_b32 v253, 0
v_cmp_eq_u32 vcc, 0, v30
s_nop 1
v_cndmask_b32 v250, v248, v31, vcc
v_cmp_eq_u32 vcc, 1, v30
s_nop 1
v_cndmask_b32 v251, v248, v31, vcc
A 30,240
A 31,241
v_max3_f32 v26, v30, v31, v26
A 30,242
A 31,243
v_max3_f32 v26, v30, v31, v26
A 30,244
A 31,245
v_max3_f32 v26, v30, v31, v26
A 30,246
A 31,247
v_max3_f32 v26, v30, v31, v26
v_mov_b32 v244, 0x3f803f80
s_mov_b32 m0, 26624
s_mov_b32 s61, 434176
buffer_load_dwordx4 v23, s[40:43], s61 offen offset:0 nt lds
v_mov_b32 v245, 0x3f803f80
v_mov_b32 v246, 0x3f803f80
s_mov_b32 m0, 27648
s_mov_b32 s61, 438272
buffer_load_dwordx4 v23, s[40:43], s61 offen offset:0 nt lds
v_mov_b32 v247, 0x3f803f80
v_mov_b32 v30, v26
s_mov_b32 m0, 28672
s_mov_b32 s61, 442368
buffer_load_dwordx4 v23, s[40:43], s61 offen offset:0 nt lds
s_nop 1
v_permlane32_swap_b32 v30, v26
s_mov_b32 m0, 29696
s_mov_b32 s61, 446464
buffer_load_dwordx4 v23, s[40:43], s61 offen offset:0 nt lds
s_nop 1
v_max_f32 v26, v30, v26
s_mov_b32 m0, 30720
s_mov_b32 s61, 450560
buffer_load_dwordx4 v23, s[40:43], s61 offen offset:0 nt lds
s_nop 0
v_mov_b32 v30, v26
s_nop 1
v_permlane16_swap_b32 v30, v26
s_mov_b32 m0, 31744
s_mov_b32 s61, 454656
buffer_load_dwordx4 v23, s[40:43], s61 offen offset:0 nt lds
s_nop 1
v_max_f32 v26, v30, v26
v_mul_f32 v30, s62, v26
v_sub_f32 v30, 0, v30
v_mov_b32 v31, v30
A 224,0
A 225,1
A 226,2
s_mov_b32 m0, 32768
s_mov_b32 s61, 458752
buffer_load_dwordx4 v23, s[40:43], s61 offen offset:0 nt lds
A 227,3
A 228,4
A 229,5
A 230,6
A 231,7
v_mfma_f32_16x16x32_bf16 a[0:3], v[250:253], v[96:99], 0
v_pk_fma_f32 v[224:225], v[224:225], s[62:63], v[30:31]
v_pk_fma_f32 v[226:227], v[226:227], s[62:63], v[30:31]
s_mov_b32 m0, 33792
s_mov_b32 s61, 462848
buffer_load_dwordx4 v23, s[40:43], s61 offen offset:0 nt lds
v_mfma_f32_16x16x32_bf16 a[4:7], v[248:251], v[96:99], 0
v_pk_fma_f32 v[228:229], v[228:229], s[62:63], v[30:31]
v_pk_fma_f32 v[230:231], v[230:231], s[62:63], v[30:31]
v_exp_f32 v224, v224
v_exp_f32 v225, v225
v_exp_f32 v226, v226
v_exp_f32 v227, v227
v_exp_f32 v228, v228
s_mov_b32 m0, 34816
s_mov_b32 s61, 466944
buffer_load_dwordx4 v23, s[40:43], s61 offen offset:0 nt lds
v_exp_f32 v229, v229
v_exp_f32 v230, v230
v_exp_f32 v231, v231
A 232,16
A 233,17
A 234,18
A 235,19
A 236,20
s_mov_b32 m0, 35840
s_mov_b32 s61, 471040
buffer_load_dwordx4 v23, s[40:43], s61 offen offset:0 nt lds
A 237,21
A 238,22
A 239,23
v_mfma_f32_16x16x32_bf16 a[16:19], v[250:253], v[104:107], 0
v_pk_fma_f32 v[232:233], v[232:233], s[62:63], v[30:31]
v_pk_fma_f32 v[234:235], v[234:235], s[62:63], v[30:31]
v_mfma_f32_16x16x32_bf16 a[20:23], v[248:251], v[104:107], 0
v_pk_fma_f32 v[236:237], v[236:237], s[62:63], v[30:31]
s_mov_b32 m0, 36864
s_mov_b32 s61, 475136
buffer_load_dwordx4 v23, s[40:43], s61 offen offset:0 nt lds
v_pk_fma_f32 v[238:239], v[238:239], s[62:63], v[30:31]
v_cvt_pk_bf16_f32 v32, v224, v225
v_cvt_pk_bf16_f32 v33, v226, v227
v_cvt_pk_bf16_f32 v34, v228, v229
v_cvt_pk_bf16_f32 v35, v230, v231
v_exp_f32 v232, v232
v_exp_f32 v233, v233
v_exp_f32 v234, v234
s_mov_b32 m0, 37888
s_mov_b32 s61, 479232
buffer_load_dwordx4 v23, s[40:43], s61 offen offset:0 nt lds
v_exp_f32 v235, v235
v_mfma_f32_16x16x32_bf16 v[240:243], v[244:247], v[32:35], 0
v_exp_f32 v236, v236
v_exp_f32 v237, v237
v_exp_f32 v238, v238
v_exp_f32 v239, v239
A 224,32
A 225,33
s_mov_b32 m0, 38912
s_mov_b32 s61, 483328
buffer_load_dwordx4 v23, s[40:43], s61 offen offset:0 nt lds
A 226,34
A 227,35
A 228,36
A 229,37
A 230,38
A 231,39
v_mfma_f32_16x16x32_bf16 a[32:35], v[250:253], v[112:115], 0
v_pk_fma_f32 v[224:225], v[224:225], s[62:63], v[30:31]
s_mov_b32 m0, 39936
s_mov_b32 s61, 487424
buffer_load_dwordx4 v23, s[40:43], s61 offen offset:0 nt lds
v_pk_fma_f32 v[226:227], v[226:227], s[62:63], v[30:31]
v_mfma_f32_16x16x32_bf16 a[36:39], v[248:251], v[112:115], 0
v_pk_fma_f32 v[228:229], v[228:229], s[62:63], v[30:31]
v_pk_fma_f32 v[230:231], v[230:231], s[62:63], v[30:31]
v_cvt_pk_bf16_f32 v40, v232, v233
v_cvt_pk_bf16_f32 v41, v234, v235
v_cvt_pk_bf16_f32 v42, v236, v237
v_cvt_pk_bf16_f32 v43, v238, v239
s_mov_b32 m0, 40960
s_mov_b32 s61, 491520
buffer_load_dwordx4 v23, s[40:43], s61 offen offset:0 nt lds
v_exp_f32 v224, v224
v_exp_f32 v225, v225
v_exp_f32 v226, v226
v_exp_f32 v227, v227
M v,240,v,244,v,40
v_exp_f32 v228, v228
v_exp_f32 v229, v229
v_exp_f32 v230, v230
s_mov_b32 m0, 41984
s_mov_b32 s61, 495616
buffer_load_dwordx4 v23, s[40:43], s61 offen offset:0 nt lds
v_exp_f32 v231, v231
A 232,48
A 233,49
A 234,50
A 235,51
A 236,52
A 237,53
A 238,54
s_mov_b32 m0, 43008
s_mov_b32 s61, 499712
buffer_load_dwordx4 v23, s[40:43], s61 offen offset:0 nt lds
A 239,55
v_mfma_f32_16x16x32_bf16 a[48:51], v[250:253], v[120:123], 0
v_pk_fma_f32 v[232:233], v[232:233], s[62:63], v[30:31]
)x"
R"x(v_pk_fma_f32 v[234:235], v[234:235], s[62:63], v[30:31]
v_mfma_f32_16x16x32_bf16 a[52:55], v[248:251], v[120:123], 0
v_pk_fma_f32 v[236:237], v[236:237], s[62:63], v[30:31]
v_pk_fma_f32 v[238:239], v[238:239], s[62:63], v[30:31]
v_cvt_pk_bf16_f32 v48, v224, v225
s_mov_b32 m0, 44032
s_mov_b32 s61, 503808
buffer_load_dwordx4 v23, s[40:43], s61 offen offset:0 nt lds
v_cvt_pk_bf16_f32 v49, v226, v227
v_cvt_pk_bf16_f32 v50, v228, v229
v_cvt_pk_bf16_f32 v51, v230, v231
v_exp_f32 v232, v232
v_exp_f32 v233, v233
v_exp_f32 v234, v234
v_exp_f32 v235, v235
M v,240,v,244,v,48
s_mov_b32 m0, 45056
s_mov_b32 s61, 507904
buffer_load_dwordx4 v23, s[40:43], s61 offen offset:0 nt lds
v_exp_f32 v236, v236
v_exp_f32 v237, v237
v_exp_f32 v238, v238
v_exp_f32 v239, v239
A 224,64
A 225,65
A 226,66
A 227,67
s_mov_b32 m0, 46080
s_mov_b32 s61, 512000
buffer_load_dwordx4 v23, s[40:43], s61 offen offset:0 nt lds
A 228,68
A 229,69
A 230,70
A 231,71
v_mfma_f32_16x16x32_bf16 a[64:67], v[250:253], v[128:131], 0
v_pk_fma_f32 v[224:225], v[224:225], s[62:63], v[30:31]
v_pk_fma_f32 v[226:227], v[226:227], s[62:63], v[30:31]
v_mfma_f32_16x16x32_bf16 a[68:71], v[248:251], v[128:131], 0
s_mov_b32 m0, 47104
s_mov_b32 s61, 516096
buffer_load_dwordx4 v23, s[40:43], s61 offen offset:0 nt lds
v_pk_fma_f32 v[228:229], v[228:229], s[62:63], v[30:31]
v_pk_fma_f32 v[230:231], v[230:231], s[62:63], v[30:31]
v_cvt_pk_bf16_f32 v56, v232, v233
v_cvt_pk_bf16_f32 v57, v234, v235
v_cvt_pk_bf16_f32 v58, v236, v237
v_cvt_pk_bf16_f32 v59, v238, v239
v_exp_f32 v224, v224
v_exp_f32 v225, v225
s_mov_b32 m0, 48128
s_mov_b32 s61, 520192
buffer_load_dwordx4 v23, s[40:43], s61 offen offset:0 nt lds
v_exp_f32 v226, v226
v_exp_f32 v227, v227
M v,240,v,244,v,56
v_exp_f32 v228, v228
v_exp_f32 v229, v229
v_exp_f32 v230, v230
v_exp_f32 v231, v231
A 232,80
s_mov_b32 m0, 49152
s_mov_b32 s61, 229376
buffer_load_dwordx4 v24, s[52:55], s61 offen offset:0 lds
A 233,81
A 234,82
s_mov_b32 m0, 50048
s_nop 0
buffer_load_dwordx4 v24, s[52:55], s61 offen offset:128 lds
A 235,83
A 236,84
s_mov_b32 m0, 50944
s_nop 0
buffer_load_dwordx4 v24, s[52:55], s61 offen offset:256 lds
A 237,85
A 238,86
s_mov_b32 m0, 51840
s_nop 0
buffer_load_dwordx4 v24, s[52:55], s61 offen offset:384 lds
A 239,87
v_mfma_f32_16x16x32_bf16 a[80:83], v[250:253], v[136:139], 0
s_mov_b32 m0, 53248
s_mov_b32 s61, 236544
buffer_load_dwordx4 v24, s[52:55], s61 offen offset:0 lds
v_pk_fma_f32 v[232:233], v[232:233], s[62:63], v[30:31]
v_pk_fma_f32 v[234:235], v[234:235], s[62:63], v[30:31]
s_mov_b32 m0, 54144
s_nop 0
buffer_load_dwordx4 v24, s[52:55], s61 offen offset:128 lds
v_mfma_f32_16x16x32_bf16 a[84:87], v[248:251], v[136:139], 0
v_pk_fma_f32 v[236:237], v[236:237], s[62:63], v[30:31]
s_mov_b32 m0, 55040
s_nop 0
buffer_load_dwordx4 v24, s[52:55], s61 offen offset:256 lds
v_pk_fma_f32 v[238:239], v[238:239], s[62:63], v[30:31]
v_cvt_pk_bf16_f32 v64, v224, v225
s_mov_b32 m0, 55936
s_nop 0
buffer_load_dwordx4 v24, s[52:55], s61 offen offset:384 lds
v_cvt_pk_bf16_f32 v65, v226, v227
v_cvt_pk_bf16_f32 v66, v228, v229
s_mov_b32 m0, 57344
s_mov_b32 s61, 243712
buffer_load_dwordx4 v24, s[52:55], s61 offen offset:0 lds
v_cvt_pk_bf16_f32 v67, v230, v231
v_exp_f32 v232, v232
s_mov_b32 m0, 58240
s_nop 0
buffer_load_dwordx4 v24, s[52:55], s61 offen offset:128 lds
v_exp_f32 v233, v233
v_exp_f32 v234, v234
s_mov_b32 m0, 59136
s_nop 0
buffer_load_dwordx4 v24, s[52:55], s61 offen offset:256 lds
v_exp_f32 v235, v235
M v,240,v,244,v,64
s_mov_b32 m0, 60032
s_nop 0
buffer_load_dwordx4 v24, s[52:55], s61 offen offset:384 lds
v_exp_f32 v236, v236
v_exp_f32 v237, v237
s_mov_b32 m0, 61440
s_mov_b32 s61, 250880
buffer_load_dwordx4 v24, s[52:55], s61 offen offset:0 lds
v_exp_f32 v238, v238
v_exp_f32 v239, v239
s_mov_b32 m0, 62336
s_nop 0
buffer_load_dwordx4 v24, s[52:55], s61 offen offset:128 lds
A 224,96
A 225,97
s_mov_b32 m0, 63232
s_nop 0
buffer_load_dwordx4 v24, s[52:55], s61 offen offset:256 lds
A 226,98
A 227,99
s_mov_b32 m0, 64128
s_nop 0
buffer_load_dwordx4 v24, s[52:55], s61 offen offset:384 lds
A 228,100
A 229,101
s_mov_b32 m0, 65024
s_mov_b32 s61, 229376
buffer_load_dwordx4 v24, s[52:55], s61 offen offset:512 lds
A 230,102
A 231,103
s_mov_b32 m0, 65920
s_nop 0
buffer_load_dwordx4 v24, s[52:55], s61 offen offset:640 lds
v_mfma_f32_16x16x32_bf16 a[96:99], v[250:253], v[144:147], 0
v_pk_fma_f32 v[224:225], v[224:225], s[62:63], v[30:31]
s_mov_b32 m0, 66816
s_nop 0
buffer_load_dwordx4 v24, s[52:55], s61 offen offset:768 lds
v_pk_fma_f32 v[226:227], v[226:227], s[62:63], v[30:31]
v_mfma_f32_16x16x32_bf16 a[100:103], v[248:251], v[144:147], 0
s_mov_b32 m0, 69120
s_mov_b32 s61, 236544
buffer_load_dwordx4 v24, s[52:55], s61 offen offset:512 lds
v_pk_fma_f32 v[228:229], v[228:229], s[62:63], v[30:31]
v_pk_fma_f32 v[230:231], v[230:231], s[62:63], v[30:31]
s_mov_b32 m0, 70016
s_nop 0
buffer_load_dwordx4 v24, s[52:55], s61 offen offset:640 lds
v_cvt_pk_bf16_f32 v72, v232, v233
v_cvt_pk_bf16_f32 v73, v234, v235
s_mov_b32 m0, 70912
s_nop 0
buffer_load_dwordx4 v24, s[52:55], s61 offen offset:768 lds
v_cvt_pk_bf16_f32 v74, v236, v237
v_cvt_pk_bf16_f32 v75, v238, v239
s_mov_b32 m0, 73216
s_mov_b32 s61, 243712
buffer_load_dwordx4 v24, s[52:55], s61 offen offset:512 lds
v_exp_f32 v224, v224
v_exp_f32 v225, v225
s_mov_b32 m0, 74112
s_nop 0
buffer_load_dwordx4 v24, s[52:55], s61 offen offset:640 lds
v_exp_f32 v226, v226
v_exp_f32 v227, v227
s_mov_b32 m0, 75008
s_nop 0
buffer_load_dwordx4 v24, s[52:55], s61 offen offset:768 lds
M v,240,v,244,v,72
v_exp_f32 v228, v228
s_mov_b32 m0, 77312
s_mov_b32 s61, 250880
buffer_load_dwordx4 v24, s[52:55], s61 offen offset:512 lds
v_exp_f32 v229, v229
v_exp_f32 v230, v230
s_mov_b32 m0, 78208
s_nop 0
buffer_load_dwordx4 v24, s[52:55], s61 offen offset:640 lds
v_exp_f32 v231, v231
A 232,112
s_mov_b32 m0, 79104
s_nop 0
buffer_load_dwordx4 v24, s[52:55], s61 offen offset:768 lds
A 233,113
A 234,114
s_mov_b32 m0, 81920
s_mov_b32 s61, 258048
buffer_load_dwordx4 v24, s[52:55], s61 offen offset:0 lds
A 235,115
A 236,116
s_mov_b32 m0, 82816
s_nop 0
buffer_load_dwordx4 v24, s[52:55], s61 offen offset:128 lds
A 237,117
A 238,118
s_mov_b32 m0, 83712
s_nop 0
buffer_load_dwordx4 v24, s[52:55], s61 offen offset:256 lds
A 239,119
v_mfma_f32_16x16x32_bf16 a[112:115], v[250:253], v[152:155], 0
s_mov_b32 m0, 84608
s_nop 0
buffer_load_dwordx4 v24, s[52:55], s61 offen offset:384 lds
v_pk_fma_f32 v[232:233], v[232:233], s[62:63], v[30:31]
v_pk_fma_f32 v[234:235], v[234:235], s[62:63], v[30:31]
s_mov_b32 m0, 86016
s_mov_b32 s61, 265216
buffer_load_dwordx4 v24, s[52:55], s61 offen offset:0 lds
v_mfma_f32_16x16x32_bf16 a[116:119], v[248:251], v[152:155], 0
v_pk_fma_f32 v[236:237], v[236:237], s[62:63], v[30:31]
s_mov_b32 m0, 86912
s_nop 0
buffer_load_dwordx4 v24, s[52:55], s61 offen offset:128 lds
v_pk_fma_f32 v[238:239], v[238:239], s[62:63], v[30:31]
v_cvt_pk_bf16_f32 v80, v224, v225
s_mov_b32 m0, 87808
s_nop 0
buffer_load_dwordx4 v24, s[52:55], s61 offen offset:256 lds
v_cvt_pk_bf16_f32 v81, v226, v227
v_cvt_pk_bf16_f32 v82, v228, v229
s_mov_b32 m0, 88704
s_nop 0
buffer_load_dwordx4 v24, s[52:55], s61 offen offset:384 lds
v_cvt_pk_bf16_f32 v83, v230, v231
v_exp_f32 v232, v232
s_mov_b32 m0, 90112
s_mov_b32 s61, 272384
buffer_load_dwordx4 v24, s[52:55], s61 offen offset:0 lds
v_exp_f32 v233, v233
v_exp_f32 v234, v234
s_mov_b32 m0, 91008
s_nop 0
buffer_load_dwordx4 v24, s[52:55], s61 offen offset:128 lds
v_exp_f32 v235, v235
M v,240,v,244,v,80
s_mov_b32 m0, 91904
s_nop 0
buffer_load_dwordx4 v24, s[52:55], s61 offen offset:256 lds
v_exp_f32 v236, v236
v_exp_f32 v237, v237
s_mov_b32 m0, 92800
s_nop 0
buffer_load_dwordx4 v24, s[52:55], s61 offen offset:384 lds
v_exp_f32 v238, v238
v_exp_f32 v239, v239
s_mov_b32 m0, 94208
s_mov_b32 s61, 279552
buffer_load_dwordx4 v24, s[52:55], s61 offen offset:0 lds
A 224,128
A 225,129
s_mov_b32 m0, 95104
s_nop 0
buffer_load_dwordx4 v24, s[52:55], s61 offen offset:128 lds
A 226,130
A 227,131
s_mov_b32 m0, 96000
s_nop 0
buffer_load_dwordx4 v24, s[52:55], s61 offen offset:256 lds
A 228,132
A 229,133
s_mov_b32 m0, 96896
s_nop 0
buffer_load_dwordx4 v24, s[52:55], s61 offen offset:384 lds
A 230,134
A 231,135
s_mov_b32 s61, .Lc0_106_%=-.Lpf_start%=+8192
buffer_load_dword v29, v20, s[92:95], s61 offen
v_mfma_f32_16x16x32_bf16 a[128:131], v[250:253], v[160:163], 0
v_pk_fma_f32 v[224:225], v[224:225], s[62:63], v[30:31]
s_mov_b32 s61, .Lc1_106_%=-.Lpf_start%=+8192
buffer_load_dword v29, v20, s[92:95], s61 offen
v_pk_fma_f32 v[226:227], v[226:227], s[62:63], v[30:31]
v_mfma_f32_16x16x32_bf16 a[132:135], v[248:251], v[160:163], 0
s_mov_b32 m0, 97792
s_mov_b32 s61, 258048
buffer_load_dwordx4 v24, s[52:55], s61 offen offset:512 lds
v_pk_fma_f32 v[228:229], v[228:229], s[62:63], v[30:31]
v_pk_fma_f32 v[230:231], v[230:231], s[62:63], v[30:31]
s_mov_b32 m0, 98688
s_nop 0
buffer_load_dwordx4 v24, s[52:55], s61 offen offset:640 lds
v_cvt_pk_bf16_f32 v88, v232, v233
v_cvt_pk_bf16_f32 v89, v234, v235
s_mov_b32 m0, 99584
s_nop 0
buffer_load_dwordx4 v24, s[52:55], s61 offen offset:768 lds
v_cvt_pk_bf16_f32 v90, v236, v237
v_cvt_pk_bf16_f32 v91, v238, v239
s_mov_b32 m0, 101888
s_mov_b32 s61, 265216
buffer_load_dwordx4 v24, s[52:55], s61 offen offset:512 lds
v_exp_f32 v224, v224
v_exp_f32 v225, v225
s_mov_b32 m0, 102784
s_nop 0
buffer_load_dwordx4 v24, s[52:55], s61 offen offset:640 lds
v_exp_f32 v226, v226
v_exp_f32 v227, v227
s_mov_b32 m0, 103680
s_nop 0
buffer_load_dwordx4 v24, s[52:55], s61 offen offset:768 lds
M v,240,v,244,v,88
v_exp_f32 v228, v228
s_mov_b32 m0, 105984
s_mov_b32 s61, 272384
buffer_load_dwordx4 v24, s[52:55], s61 offen offset:512 lds
v_exp_f32 v229, v229
v_exp_f32 v230, v230
s_mov_b32 m0, 106880
s_nop 0
buffer_load_dwordx4 v24, s[52:55], s61 offen offset:640 lds
v_exp_f32 v231, v231
A 232,144
s_mov_b32 m0, 107776
s_nop 0
buffer_load_dwordx4 v24, s[52:55], s61 offen offset:768 lds
A 233,145
A 234,146
s_mov_b32 m0, 110080
s_mov_b32 s61, 279552
buffer_load_dwordx4 v24, s[52:55], s61 offen offset:512 lds
A 235,147
A 236,148
s_mov_b32 m0, 110976
s_nop 0
buffer_load_dwordx4 v24, s[52:55], s61 offen offset:640 lds
A 237,149
A 238,150
s_mov_b32 m0, 111872
s_nop 0
buffer_load_dwordx4 v24, s[52:55], s61 offen offset:768 lds
A 239,151
v_mfma_f32_16x16x32_bf16 a[144:147], v[250:253], v[168:171], 0
s_mov_b32 m0, 114688
s_mov_b32 s61, 262272
buffer_load_dwordx4 v23, s[40:43], s61 offen offset:0 nt lds
v_pk_fma_f32 v[232:233], v[232:233], s[62:63], v[30:31]
v_pk_fma_f32 v[234:235], v[234:235], s[62:63], v[30:31]
s_mov_b32 m0, 115712
s_mov_b32 s61, 266368
buffer_load_dwordx4 v23, s[40:43], s61 offen offset:0 nt lds
v_mfma_f32_16x16x32_bf16 a[148:151], v[248:251], v[168:171], 0
v_pk_fma_f32 v[236:237], v[236:237], s[62:63], v[30:31]
s_mov_b32 m0, 116736
s_mov_b32 s61, 270464
buffer_load_dwordx4 v23, s[40:43], s61 offen offset:0 nt lds
v_pk_fma_f32 v[238:239], v[238:239], s[62:63], v[30:31]
v_cvt_pk_bf16_f32 v96, v224, v225
s_mov_b32 m0, 117760
s_mov_b32 s61, 274560
buffer_load_dwordx4 v23, s[40:43], s61 offen offset:0 nt lds
v_cvt_pk_bf16_f32 v97, v226, v227
v_cvt_pk_bf16_f32 v98, v228, v229
s_mov_b32 m0, 118784
s_mov_b32 s61, 278656
buffer_load_dwordx4 v23, s[40:43], s61 offen offset:0 nt lds
v_cvt_pk_bf16_f32 v99, v230, v231
v_exp_f32 v232, v232
s_mov_b32 m0, 119808
s_mov_b32 s61, 282752
buffer_load_dwordx4 v23, s[40:43], s61 offen offset:0 nt lds
v_exp_f32 v233, v233
v_exp_f32 v234, v234
s_mov_b32 m0, 120832
s_mov_b32 s61, 286848
buffer_load_dwordx4 v23, s[40:43], s61 offen offset:0 nt lds
v_exp_f32 v235, v235
M v,240,v,244,v,96
s_mov_b32 m0, 121856
s_mov_b32 s61, 290944
buffer_load_dwordx4 v23, s[40:43], s61 offen offset:0 nt lds
v_exp_f32 v236, v236
v_exp_f32 v237, v237
s_mov_b32 m0, 122880
s_mov_b32 s61, 295040
buffer_load_dwordx4 v23, s[40:43], s61 offen offset:0 nt lds
v_exp_f32 v238, v238
v_exp_f32 v239, v239
s_mov_b32 m0, 123904
s_mov_b32 s61, 299136
buffer_load_dwordx4 v23, s[40:43], s61 offen offset:0 nt lds
A 224,160
A 225,161
s_mov_b32 m0, 124928
s_mov_b32 s61, 303232
)x"
R"x(buffer_load_dwordx4 v23, s[40:43], s61 offen offset:0 nt lds
A 226,162
A 227,163
s_mov_b32 m0, 125952
s_mov_b32 s61, 307328
buffer_load_dwordx4 v23, s[40:43], s61 offen offset:0 nt lds
A 228,164
A 229,165
A 230,166
A 231,167
v_mfma_f32_16x16x32_bf16 a[160:163], v[250:253], v[176:179], 0
v_pk_fma_f32 v[224:225], v[224:225], s[62:63], v[30:31]
v_pk_fma_f32 v[226:227], v[226:227], s[62:63], v[30:31]
v_mfma_f32_16x16x32_bf16 a[164:167], v[248:251], v[176:179], 0
s_mov_b32 m0, 126976
s_mov_b32 s61, 311424
buffer_load_dwordx4 v23, s[40:43], s61 offen offset:0 nt lds
v_pk_fma_f32 v[228:229], v[228:229], s[62:63], v[30:31]
v_pk_fma_f32 v[230:231], v[230:231], s[62:63], v[30:31]
v_cvt_pk_bf16_f32 v104, v232, v233
v_cvt_pk_bf16_f32 v105, v234, v235
v_cvt_pk_bf16_f32 v106, v236, v237
v_cvt_pk_bf16_f32 v107, v238, v239
v_exp_f32 v224, v224
v_exp_f32 v225, v225
s_mov_b32 m0, 128000
s_mov_b32 s61, 315520
buffer_load_dwordx4 v23, s[40:43], s61 offen offset:0 nt lds
v_exp_f32 v226, v226
v_exp_f32 v227, v227
M v,240,v,244,v,104
v_exp_f32 v228, v228
v_exp_f32 v229, v229
v_exp_f32 v230, v230
v_exp_f32 v231, v231
A 232,176
s_mov_b32 m0, 129024
s_mov_b32 s61, 319616
buffer_load_dwordx4 v23, s[40:43], s61 offen offset:0 nt lds
A 233,177
A 234,178
A 235,179
A 236,180
A 237,181
A 238,182
A 239,183
v_mfma_f32_16x16x32_bf16 a[176:179], v[250:253], v[184:187], 0
s_mov_b32 m0, 130048
s_mov_b32 s61, 323712
buffer_load_dwordx4 v23, s[40:43], s61 offen offset:0 nt lds
v_pk_fma_f32 v[232:233], v[232:233], s[62:63], v[30:31]
v_pk_fma_f32 v[234:235], v[234:235], s[62:63], v[30:31]
v_mfma_f32_16x16x32_bf16 a[180:183], v[248:251], v[184:187], 0
v_pk_fma_f32 v[236:237], v[236:237], s[62:63], v[30:31]
v_pk_fma_f32 v[238:239], v[238:239], s[62:63], v[30:31]
v_cvt_pk_bf16_f32 v112, v224, v225
v_cvt_pk_bf16_f32 v113, v226, v227
v_cvt_pk_bf16_f32 v114, v228, v229
s_mov_b32 m0, 131072
s_mov_b32 s61, 327808
buffer_load_dwordx4 v23, s[40:43], s61 offen offset:0 nt lds
v_cvt_pk_bf16_f32 v115, v230, v231
v_exp_f32 v232, v232
v_exp_f32 v233, v233
v_exp_f32 v234, v234
v_exp_f32 v235, v235
M v,240,v,244,v,112
v_exp_f32 v236, v236
v_exp_f32 v237, v237
s_mov_b32 m0, 132096
s_mov_b32 s61, 331904
buffer_load_dwordx4 v23, s[40:43], s61 offen offset:0 nt lds
v_exp_f32 v238, v238
v_exp_f32 v239, v239
A 224,192
A 225,193
A 226,194
A 227,195
A 228,196
A 229,197
s_mov_b32 m0, 133120
s_mov_b32 s61, 336000
buffer_load_dwordx4 v23, s[40:43], s61 offen offset:0 nt lds
A 230,198
A 231,199
v_mfma_f32_16x16x32_bf16 a[192:195], v[250:253], v[192:195], 0
v_pk_fma_f32 v[224:225], v[224:225], s[62:63], v[30:31]
v_pk_fma_f32 v[226:227], v[226:227], s[62:63], v[30:31]
v_mfma_f32_16x16x32_bf16 a[196:199], v[248:251], v[192:195], 0
v_pk_fma_f32 v[228:229], v[228:229], s[62:63], v[30:31]
v_pk_fma_f32 v[230:231], v[230:231], s[62:63], v[30:31]
s_mov_b32 m0, 134144
s_mov_b32 s61, 340096
buffer_load_dwordx4 v23, s[40:43], s61 offen offset:0 nt lds
v_cvt_pk_bf16_f32 v120, v232, v233
v_cvt_pk_bf16_f32 v121, v234, v235
v_cvt_pk_bf16_f32 v122, v236, v237
v_cvt_pk_bf16_f32 v123, v238, v239
v_exp_f32 v224, v224
v_exp_f32 v225, v225
v_exp_f32 v226, v226
v_exp_f32 v227, v227
s_mov_b32 m0, 135168
s_mov_b32 s61, 344192
buffer_load_dwordx4 v23, s[40:43], s61 offen offset:0 nt lds
M v,240,v,244,v,120
v_exp_f32 v228, v228
v_exp_f32 v229, v229
v_exp_f32 v230, v230
v_exp_f32 v231, v231
A 232,208
A 233,209
A 234,210
s_mov_b32 m0, 136192
s_mov_b32 s61, 348288
buffer_load_dwordx4 v23, s[40:43], s61 offen offset:0 nt lds
A 235,211
A 236,212
A 237,213
A 238,214
A 239,215
v_mfma_f32_16x16x32_bf16 a[208:211], v[250:253], v[200:203], 0
v_pk_fma_f32 v[232:233], v[232:233], s[62:63], v[30:31]
v_pk_fma_f32 v[234:235], v[234:235], s[62:63], v[30:31]
s_mov_b32 m0, 137216
s_mov_b32 s61, 352384
buffer_load_dwordx4 v23, s[40:43], s61 offen offset:0 nt lds
v_mfma_f32_16x16x32_bf16 a[212:215], v[248:251], v[200:203], 0
v_pk_fma_f32 v[236:237], v[236:237], s[62:63], v[30:31]
v_pk_fma_f32 v[238:239], v[238:239], s[62:63], v[30:31]
v_cvt_pk_bf16_f32 v128, v224, v225
v_cvt_pk_bf16_f32 v129, v226, v227
v_cvt_pk_bf16_f32 v130, v228, v229
v_cvt_pk_bf16_f32 v131, v230, v231
v_exp_f32 v232, v232
s_mov_b32 m0, 138240
s_mov_b32 s61, 356480
buffer_load_dwordx4 v23, s[40:43], s61 offen offset:0 nt lds
v_exp_f32 v233, v233
v_exp_f32 v234, v234
v_exp_f32 v235, v235
M v,240,v,244,v,128
v_exp_f32 v236, v236
v_exp_f32 v237, v237
v_exp_f32 v238, v238
v_exp_f32 v239, v239
s_mov_b32 m0, 139264
s_mov_b32 s61, 360576
buffer_load_dwordx4 v23, s[40:43], s61 offen offset:0 nt lds
A 224,224
A 225,225
A 226,226
A 227,227
A 228,228
A 229,229
A 230,230
A 231,231
s_mov_b32 m0, 140288
s_mov_b32 s61, 364672
buffer_load_dwordx4 v23, s[40:43], s61 offen offset:0 nt lds
v_pk_fma_f32 v[224:225], v[224:225], s[62:63], v[30:31]
v_pk_fma_f32 v[226:227], v[226:227], s[62:63], v[30:31]
v_pk_fma_f32 v[228:229], v[228:229], s[62:63], v[30:31]
v_pk_fma_f32 v[230:231], v[230:231], s[62:63], v[30:31]
v_cvt_pk_bf16_f32 v136, v232, v233
v_cvt_pk_bf16_f32 v137, v234, v235
v_cvt_pk_bf16_f32 v138, v236, v237
v_cvt_pk_bf16_f32 v139, v238, v239
s_mov_b32 m0, 141312
s_mov_b32 s61, 368768
buffer_load_dwordx4 v23, s[40:43], s61 offen offset:0 nt lds
v_exp_f32 v224, v224
v_exp_f32 v225, v225
v_exp_f32 v226, v226
v_exp_f32 v227, v227
M v,240,v,244,v,136
v_exp_f32 v228, v228
v_exp_f32 v229, v229
v_exp_f32 v230, v230
s_mov_b32 m0, 142336
s_mov_b32 s61, 372864
buffer_load_dwordx4 v23, s[40:43], s61 offen offset:0 nt lds
v_exp_f32 v231, v231
A 232,240
A 233,241
A 234,242
A 235,243
A 236,244
A 237,245
A 238,246
s_mov_b32 m0, 143360
s_mov_b32 s61, 376960
buffer_load_dwordx4 v23, s[40:43], s61 offen offset:0 nt lds
A 239,247
v_pk_fma_f32 v[232:233], v[232:233], s[62:63], v[30:31]
v_pk_fma_f32 v[234:235], v[234:235], s[62:63], v[30:31]
v_pk_fma_f32 v[236:237], v[236:237], s[62:63], v[30:31]
v_pk_fma_f32 v[238:239], v[238:239], s[62:63], v[30:31]
v_cvt_pk_bf16_f32 v144, v224, v225
v_cvt_pk_bf16_f32 v145, v226, v227
v_cvt_pk_bf16_f32 v146, v228, v229
s_mov_b32 m0, 144384
s_mov_b32 s61, 381056
buffer_load_dwordx4 v23, s[40:43], s61 offen offset:0 nt lds
v_cvt_pk_bf16_f32 v147, v230, v231
v_exp_f32 v232, v232
v_exp_f32 v233, v233
v_exp_f32 v234, v234
v_exp_f32 v235, v235
M v,240,v,244,v,144
v_exp_f32 v236, v236
v_exp_f32 v237, v237
s_mov_b32 m0, 145408
s_mov_b32 s61, 385152
buffer_load_dwordx4 v23, s[40:43], s61 offen offset:0 nt lds
v_exp_f32 v238, v238
v_exp_f32 v239, v239
s_nop 0
v_cvt_pk_bf16_f32 v152, v232, v233
v_cvt_pk_bf16_f32 v153, v234, v235
v_cvt_pk_bf16_f32 v154, v236, v237
v_cvt_pk_bf16_f32 v155, v238, v239
s_nop 1
s_mov_b32 m0, 146432
s_mov_b32 s61, 389248
buffer_load_dwordx4 v23, s[40:43], s61 offen offset:0 nt lds
M v,240,v,244,v,152
s_nop 7
v_rcp_f32 v28, v240
s_nop 1
s_waitcnt vmcnt(63)
s_barrier
T a,224,15,16384
T a,226,15,18432
T a,228,18,16384
T a,230,18,18432
T a,232,15,16896
T a,234,15,18944
T a,236,18,16896
T a,238,18,18944
T a,240,15,20480
T a,242,15,22528
T a,244,18,20480
W 7
v_mfma_f32_16x16x32_bf16 v[224:227], a[224:227], v[32:35], 0
T a,246,18,22528
T a,248,15,20992
v_mfma_f32_16x16x32_bf16 v[228:231], a[228:231], v[32:35], 0
T a,250,15,23040
T a,252,18,20992
W 7
v_mfma_f32_16x16x32_bf16 v[240:243], a[232:235], v[32:35], 0
T a,254,18,23040
T a,224,15,24576
v_mfma_f32_16x16x32_bf16 v[244:247], a[236:239], v[32:35], 0
T a,226,15,26624
T a,228,18,24576
W 7
M v,224,a,240,v,40
T a,230,18,26624
T a,232,15,25088
M v,228,a,244,v,40
T a,234,15,27136
T a,236,18,25088
W 7
M v,240,a,248,v,40
T a,238,18,27136
T a,240,15,28672
M v,244,a,252,v,40
T a,242,15,30720
T a,244,18,28672
W 7
M v,224,a,224,v,48
T a,246,18,30720
T a,248,15,29184
M v,228,a,228,v,48
T a,250,15,31232
T a,252,18,29184
W 7
M v,240,a,232,v,48
T a,254,18,31232
s_waitcnt vmcnt(63)
s_barrier
T a,224,13,0
M v,244,a,236,v,48
T a,226,13,2048
T a,228,16,0
W 7
M v,224,a,240,v,56
T a,230,16,2048
T a,232,13,512
M v,228,a,244,v,56
T a,234,13,2560
T a,236,16,512
W 7
M v,240,a,248,v,56
T a,238,16,2560
T a,240,13,4096
M v,244,a,252,v,56
T a,242,13,6144
T a,244,16,4096
W 7
M v,224,a,224,v,64
T a,246,16,6144
T a,248,13,4608
M v,228,a,228,v,64
T a,250,13,6656
T a,252,16,4608
W 7
M v,240,a,232,v,64
T a,254,16,6656
T a,224,13,8192
M v,244,a,236,v,64
T a,226,13,10240
T a,228,16,8192
W 7
M v,224,a,240,v,72
T a,230,16,10240
T a,232,13,8704
M v,228,a,244,v,72
T a,234,13,10752
T a,236,16,8704
W 7
M v,240,a,248,v,72
T a,238,16,10752
T a,240,13,12288
M v,244,a,252,v,72
T a,242,13,14336
T a,244,16,12288
W 7
M v,224,a,224,v,80
T a,246,16,14336
T a,248,13,12800
M v,228,a,228,v,80
T a,250,13,14848
T a,252,16,12800
W 7
M v,240,a,232,v,80
T a,254,16,14848
s_waitcnt vmcnt(63)
s_barrier
T a,224,13,16384
s_mov_b32 m0, 147456
s_mov_b32 s61, 393344
M v,244,a,236,v,80
T a,226,13,18432
buffer_load_dwordx4 v23, s[40:43], s61 offen offset:0 nt lds
s_mov_b32 m0, 148480
T a,228,16,16384
W 7
s_mov_b32 s61, 397440
buffer_load_dwordx4 v23, s[40:43], s61 offen offset:0 nt lds
M v,224,a,240,v,88
T a,230,16,18432
s_mov_b32 m0, 149504
s_mov_b32 s61, 401536
T a,232,13,16896
buffer_load_dwordx4 v23, s[40:43], s61 offen offset:0 nt lds
s_mov_b32 m0, 150528
M v,228,a,244,v,88
T a,234,13,18944
s_mov_b32 s61, 405632
buffer_load_dwordx4 v23, s[40:43], s61 offen offset:0 nt lds
T a,236,16,16896
W 7
s_mov_b32 m0, 151552
s_mov_b32 s61, 409728
M v,240,a,248,v,88
T a,238,16,18944
buffer_load_dwordx4 v23, s[40:43], s61 offen offset:0 nt lds
s_mov_b32 m0, 152576
T a,240,13,20480
s_mov_b32 s61, 413824
buffer_load_dwordx4 v23, s[40:43], s61 offen offset:0 nt lds
M v,244,a,252,v,88
T a,242,13,22528
s_mov_b32 m0, 153600
s_mov_b32 s61, 417920
T a,244,16,20480
W 7
buffer_load_dwordx4 v23, s[40:43], s61 offen offset:0 nt lds
s_mov_b32 m0, 154624
M v,224,a,224,v,96
T a,246,16,22528
s_mov_b32 s61, 422016
buffer_load_dwordx4 v23, s[40:43], s61 offen offset:0 nt lds
T a,248,13,20992
s_mov_b32 m0, 155648
s_mov_b32 s61, 426112
M v,228,a,228,v,96
T a,250,13,23040
buffer_load_dwordx4 v23, s[40:43], s61 offen offset:0 nt lds
s_mov_b32 m0, 156672
T a,252,16,20992
W 7
s_mov_b32 s61, 430208
buffer_load_dwordx4 v23, s[40:43], s61 offen offset:0 nt lds
M v,240,a,232,v,96
T a,254,16,23040
s_mov_b32 m0, 157696
)x"
R"x(s_mov_b32 s61, 434304
T a,224,13,24576
buffer_load_dwordx4 v23, s[40:43], s61 offen offset:0 nt lds
s_mov_b32 m0, 158720
M v,244,a,236,v,96
T a,226,13,26624
s_mov_b32 s61, 438400
buffer_load_dwordx4 v23, s[40:43], s61 offen offset:0 nt lds
T a,228,16,24576
W 7
s_mov_b32 m0, 159744
s_mov_b32 s61, 442496
M v,224,a,240,v,104
T a,230,16,26624
buffer_load_dwordx4 v23, s[40:43], s61 offen offset:0 nt lds
s_mov_b32 m0, 160768
T a,232,13,25088
s_mov_b32 s61, 446592
buffer_load_dwordx4 v23, s[40:43], s61 offen offset:0 nt lds
M v,228,a,244,v,104
T a,234,13,27136
s_mov_b32 m0, 161792
s_mov_b32 s61, 450688
T a,236,16,25088
W 7
buffer_load_dwordx4 v23, s[40:43], s61 offen offset:0 nt lds
s_mov_b32 m0, 162816
M v,240,a,248,v,104
T a,238,16,27136
s_mov_b32 s61, 454784
buffer_load_dwordx4 v23, s[40:43], s61 offen offset:0 nt lds
T a,240,13,28672
M v,244,a,252,v,104
T a,242,13,30720
T a,244,16,28672
W 7
M v,224,a,224,v,112
T a,246,16,30720
T a,248,13,29184
M v,228,a,228,v,112
T a,250,13,31232
T a,252,16,29184
W 7
M v,240,a,232,v,112
T a,254,16,31232
s_waitcnt vmcnt(63)
s_barrier
T a,224,13,32768
s_mov_b32 m0, 0
s_mov_b32 s61, 458880
M v,244,a,236,v,112
T a,226,13,34816
buffer_load_dwordx4 v23, s[40:43], s61 offen offset:0 nt lds
s_mov_b32 m0, 1024
T a,228,16,32768
W 7
s_mov_b32 s61, 462976
buffer_load_dwordx4 v23, s[40:43], s61 offen offset:0 nt lds
M v,224,a,240,v,120
T a,230,16,34816
s_mov_b32 m0, 2048
s_mov_b32 s61, 467072
T a,232,13,33280
buffer_load_dwordx4 v23, s[40:43], s61 offen offset:0 nt lds
s_mov_b32 m0, 3072
M v,228,a,244,v,120
T a,234,13,35328
s_mov_b32 s61, 471168
buffer_load_dwordx4 v23, s[40:43], s61 offen offset:0 nt lds
T a,236,16,33280
W 7
s_mov_b32 m0, 4096
s_mov_b32 s61, 475264
M v,240,a,248,v,120
T a,238,16,35328
buffer_load_dwordx4 v23, s[40:43], s61 offen offset:0 nt lds
s_mov_b32 m0, 5120
T a,240,13,36864
s_mov_b32 s61, 479360
buffer_load_dwordx4 v23, s[40:43], s61 offen offset:0 nt lds
M v,244,a,252,v,120
T a,242,13,38912
s_mov_b32 m0, 6144
s_mov_b32 s61, 483456
T a,244,16,36864
W 7
buffer_load_dwordx4 v23, s[40:43], s61 offen offset:0 nt lds
s_mov_b32 m0, 7168
M v,224,a,224,v,128
T a,246,16,38912
s_mov_b32 s61, 487552
buffer_load_dwordx4 v23, s[40:43], s61 offen offset:0 nt lds
T a,248,13,37376
s_mov_b32 m0, 8192
s_mov_b32 s61, 491648
M v,228,a,228,v,128
T a,250,13,39424
buffer_load_dwordx4 v23, s[40:43], s61 offen offset:0 nt lds
s_mov_b32 m0, 9216
T a,252,16,37376
W 7
s_mov_b32 s61, 495744
buffer_load_dwordx4 v23, s[40:43], s61 offen offset:0 nt lds
M v,240,a,232,v,128
T a,254,16,39424
T a,224,13,40960
s_mov_b32 m0, 10240
s_mov_b32 s61, 499840
M v,244,a,236,v,128
T a,226,13,43008
buffer_load_dwordx4 v23, s[40:43], s61 offen offset:0 nt lds
T a,228,16,40960
W 7
M v,224,a,240,v,136
T a,230,16,43008
T a,232,13,41472
s_mov_b32 m0, 11264
s_mov_b32 s61, 503936
M v,228,a,244,v,136
T a,234,13,43520
buffer_load_dwordx4 v23, s[40:43], s61 offen offset:0 nt lds
T a,236,16,41472
W 7
M v,240,a,248,v,136
T a,238,16,43520
T a,240,13,45056
s_mov_b32 m0, 12288
s_mov_b32 s61, 508032
M v,244,a,252,v,136
T a,242,13,47104
buffer_load_dwordx4 v23, s[40:43], s61 offen offset:0 nt lds
T a,244,16,45056
W 7
M v,224,a,224,v,144
T a,246,16,47104
T a,248,13,45568
s_mov_b32 m0, 13312
s_mov_b32 s61, 512128
M v,228,a,228,v,144
T a,250,13,47616
buffer_load_dwordx4 v23, s[40:43], s61 offen offset:0 nt lds
T a,252,16,45568
W 7
s_mov_b32 m0, 14336
s_mov_b32 s61, 516224
M v,240,a,232,v,144
T a,254,16,47616
buffer_load_dwordx4 v23, s[40:43], s61 offen offset:0 nt lds
s_waitcnt vmcnt(63)
s_barrier
T a,224,7,49152
M v,244,a,236,v,144
T a,226,7,57344
T a,228,10,49152
W 7
s_mov_b32 m0, 15360
s_mov_b32 s61, 520320
M v,224,a,240,v,152
T a,230,10,57344
buffer_load_dwordx4 v23, s[40:43], s61 offen offset:0 nt lds
T a,232,7,49664
M v,228,a,244,v,152
T a,234,7,57856
T a,236,10,49664
W 7
s_mov_b32 m0, 16384
s_mov_b32 s61, 286720
M v,240,a,248,v,152
T a,238,10,57856
buffer_load_dwordx4 v24, s[52:55], s61 offen offset:0 lds
s_mov_b32 m0, 17280
T a,240,7,50176
buffer_load_dwordx4 v24, s[52:55], s61 offen offset:128 lds
s_mov_b32 m0, 18176
M v,244,a,252,v,152
T a,242,7,58368
buffer_load_dwordx4 v24, s[52:55], s61 offen offset:256 lds
s_mov_b32 m0, 19072
s_nop 0
buffer_load_dwordx4 v24, s[52:55], s61 offen offset:384 lds
s_nop 7
v_mul_f32 v224, v28, v224
v_mul_f32 v225, v28, v225
s_mov_b32 m0, 20480
s_mov_b32 s61, 293888
buffer_load_dwordx4 v24, s[52:55], s61 offen offset:0 lds
v_mul_f32 v226, v28, v226
v_mul_f32 v227, v28, v227
s_mov_b32 m0, 21376
s_nop 0
buffer_load_dwordx4 v24, s[52:55], s61 offen offset:128 lds
v_mul_f32 v228, v28, v228
v_mul_f32 v229, v28, v229
s_mov_b32 m0, 22272
s_nop 0
buffer_load_dwordx4 v24, s[52:55], s61 offen offset:256 lds
v_mul_f32 v230, v28, v230
v_mul_f32 v231, v28, v231
s_mov_b32 m0, 23168
s_nop 0
buffer_load_dwordx4 v24, s[52:55], s61 offen offset:384 lds
v_cvt_pk_bf16_f32 v224, v224, v225
v_cvt_pk_bf16_f32 v225, v226, v227
s_mov_b32 m0, 24576
s_mov_b32 s61, 301056
buffer_load_dwordx4 v24, s[52:55], s61 offen offset:0 lds
v_cvt_pk_bf16_f32 v226, v228, v229
v_cvt_pk_bf16_f32 v227, v230, v231
s_mov_b32 m0, 25472
s_nop 0
buffer_load_dwordx4 v24, s[52:55], s61 offen offset:128 lds
v_mul_f32 v240, v28, v240
v_mul_f32 v241, v28, v241
s_mov_b32 m0, 26368
s_nop 0
buffer_load_dwordx4 v24, s[52:55], s61 offen offset:256 lds
v_mul_f32 v242, v28, v242
v_mul_f32 v243, v28, v243
s_mov_b32 m0, 27264
s_nop 0
buffer_load_dwordx4 v24, s[52:55], s61 offen offset:384 lds
v_mul_f32 v244, v28, v244
v_mul_f32 v245, v28, v245
s_mov_b32 m0, 28672
s_mov_b32 s61, 308224
buffer_load_dwordx4 v24, s[52:55], s61 offen offset:0 lds
v_mul_f32 v246, v28, v246
v_mul_f32 v247, v28, v247
s_mov_b32 m0, 29568
s_nop 0
buffer_load_dwordx4 v24, s[52:55], s61 offen offset:128 lds
v_cvt_pk_bf16_f32 v240, v240, v241
v_cvt_pk_bf16_f32 v241, v242, v243
s_mov_b32 m0, 30464
s_nop 0
buffer_load_dwordx4 v24, s[52:55], s61 offen offset:256 lds
v_cvt_pk_bf16_f32 v242, v244, v245
v_cvt_pk_bf16_f32 v243, v246, v247
s_mov_b32 m0, 31360
s_nop 0
buffer_load_dwordx4 v24, s[52:55], s61 offen offset:384 lds
s_nop 1
T a,244,10,50176
W 7
M a,0,a,224,v,224
T a,246,10,58368
T a,248,7,50688
M a,4,a,228,v,224
T a,250,7,58880
T a,252,10,50688
W 7
M a,16,a,232,v,224
T a,254,10,58880
T a,224,7,51200
M a,20,a,236,v,224
T a,226,7,59392
T a,228,10,51200
W 7
M a,32,a,240,v,224
T a,230,10,59392
T a,232,7,51712
M a,36,a,244,v,224
T a,234,7,59904
T a,236,10,51712
W 7
M a,48,a,248,v,224
T a,238,10,59904
T a,240,7,52224
M a,52,a,252,v,224
T a,242,7,60416
T a,244,10,52224
W 7
M a,64,a,224,v,224
T a,246,10,60416
T a,248,7,52736
M a,68,a,228,v,224
T a,250,7,60928
T a,252,10,52736
W 7
M a,80,a,232,v,224
T a,254,10,60928
s_waitcnt vmcnt(63)
s_barrier
.Lc1_112_%=:
T a,224,8,0
s_mov_b32 s61, .Lc0_112_%=-.Lpf_start%=+8192
buffer_load_dword v29, v20, s[92:95], s61 offen
M a,84,a,236,v,224
T a,226,8,8192
s_mov_b32 s61, .Lc1_112_%=-.Lpf_start%=+8192
buffer_load_dword v29, v20, s[92:95], s61 offen
T a,228,11,0
W 7
s_mov_b32 m0, 32256
s_mov_b32 s61, 286720
M a,96,a,240,v,224
T a,230,11,8192
buffer_load_dwordx4 v24, s[52:55], s61 offen offset:512 lds
s_mov_b32 m0, 33152
T a,232,8,512
buffer_load_dwordx4 v24, s[52:55], s61 offen offset:640 lds
s_mov_b32 m0, 34048
M a,100,a,244,v,224
T a,234,8,8704
buffer_load_dwordx4 v24, s[52:55], s61 offen offset:768 lds
s_mov_b32 m0, 36352
T a,236,11,512
W 7
s_mov_b32 s61, 293888
buffer_load_dwordx4 v24, s[52:55], s61 offen offset:512 lds
M a,112,a,248,v,224
T a,238,11,8704
s_mov_b32 m0, 37248
T a,240,8,1024
buffer_load_dwordx4 v24, s[52:55], s61 offen offset:640 lds
s_mov_b32 m0, 38144
M a,116,a,252,v,224
T a,242,8,9216
buffer_load_dwordx4 v24, s[52:55], s61 offen offset:768 lds
s_mov_b32 m0, 40448
T a,244,11,1024
W 7
s_mov_b32 s61, 301056
buffer_load_dwordx4 v24, s[52:55], s61 offen offset:512 lds
M a,128,a,224,v,224
T a,246,11,9216
s_mov_b32 m0, 41344
T a,248,8,1536
buffer_load_dwordx4 v24, s[52:55], s61 offen offset:640 lds
s_mov_b32 m0, 42240
M a,132,a,228,v,224
T a,250,8,9728
buffer_load_dwordx4 v24, s[52:55], s61 offen offset:768 lds
s_mov_b32 m0, 44544
T a,252,11,1536
W 7
s_mov_b32 s61, 308224
buffer_load_dwordx4 v24, s[52:55], s61 offen offset:512 lds
M a,144,a,232,v,224
T a,254,11,9728
s_mov_b32 m0, 45440
T a,224,8,2048
buffer_load_dwordx4 v24, s[52:55], s61 offen offset:640 lds
s_mov_b32 m0, 46336
M a,148,a,236,v,224
T a,226,8,10240
buffer_load_dwordx4 v24, s[52:55], s61 offen offset:768 lds
T a,228,11,2048
W 7
M a,160,a,240,v,224
T a,230,11,10240
T a,232,8,2560
M a,164,a,244,v,224
T a,234,8,10752
T a,236,11,2560
W 7
M a,176,a,248,v,224
T a,238,11,10752
s_waitcnt vmcnt(63)
s_barrier
T a,240,8,16384
s_mov_b32 m0, 49152
s_mov_b32 s61, 315392
M a,180,a,252,v,224
T a,242,8,24576
buffer_load_dwordx4 v24, s[52:55], s61 offen offset:0 lds
s_mov_b32 m0, 50048
T a,244,11,16384
W 7
buffer_load_dwordx4 v24, s[52:55], s61 offen offset:128 lds
s_mov_b32 m0, 50944
M a,192,a,224,v,224
T a,246,11,24576
buffer_load_dwordx4 v24, s[52:55], s61 offen offset:256 lds
s_mov_b32 m0, 51840
T a,248,8,16896
buffer_load_dwordx4 v24, s[52:55], s61 offen offset:384 lds
s_mov_b32 m0, 53248
M a,196,a,228,v,224
T a,250,8,25088
s_mov_b32 s61, 322560
buffer_load_dwordx4 v24, s[52:55], s61 offen offset:0 lds
T a,252,11,16896
W 7
s_mov_b32 m0, 54144
M a,208,a,232,v,224
T a,254,11,25088
buffer_load_dwordx4 v24, s[52:55], s61 offen offset:128 lds
s_mov_b32 m0, 55040
T a,224,8,17408
buffer_load_dwordx4 v24, s[52:55], s61 offen offset:256 lds
s_mov_b32 m0, 55936
M a,212,a,236,v,224
T a,226,8,25600
buffer_load_dwordx4 v24, s[52:55], s61 offen offset:384 lds
s_mov_b32 m0, 57344
T a,228,11,17408
W 7
s_mov_b32 s61, 329728
buffer_load_dwordx4 v24, s[52:55], s61 offen offset:0 lds
)x"
R"x(M a,0,a,240,v,240
T a,230,11,25600
s_mov_b32 m0, 58240
T a,232,8,17920
buffer_load_dwordx4 v24, s[52:55], s61 offen offset:128 lds
s_mov_b32 m0, 59136
M a,4,a,244,v,240
T a,234,8,26112
buffer_load_dwordx4 v24, s[52:55], s61 offen offset:256 lds
s_mov_b32 m0, 60032
T a,236,11,17920
W 7
buffer_load_dwordx4 v24, s[52:55], s61 offen offset:384 lds
s_mov_b32 m0, 61440
M a,16,a,248,v,240
T a,238,11,26112
s_mov_b32 s61, 336896
buffer_load_dwordx4 v24, s[52:55], s61 offen offset:0 lds
T a,240,8,18432
s_mov_b32 m0, 62336
M a,20,a,252,v,240
T a,242,8,26624
buffer_load_dwordx4 v24, s[52:55], s61 offen offset:128 lds
s_mov_b32 m0, 63232
T a,244,11,18432
W 7
buffer_load_dwordx4 v24, s[52:55], s61 offen offset:256 lds
s_mov_b32 m0, 64128
M a,32,a,224,v,240
T a,246,11,26624
buffer_load_dwordx4 v24, s[52:55], s61 offen offset:384 lds
T a,248,8,18944
M a,36,a,228,v,240
T a,250,8,27136
T a,252,11,18944
W 7
M a,48,a,232,v,240
T a,254,11,27136
T a,224,8,19456
M a,52,a,236,v,240
T a,226,8,27648
T a,228,11,19456
W 7
M a,64,a,240,v,240
T a,230,11,27648
T a,232,8,19968
M a,68,a,244,v,240
T a,234,8,28160
T a,236,11,19968
W 7
M a,80,a,248,v,240
T a,238,11,28160
s_waitcnt vmcnt(63)
s_barrier
T a,240,8,32768
s_mov_b32 m0, 65024
s_mov_b32 s61, 315392
M a,84,a,252,v,240
T a,242,8,40960
buffer_load_dwordx4 v24, s[52:55], s61 offen offset:512 lds
s_mov_b32 m0, 65920
T a,244,11,32768
W 7
buffer_load_dwordx4 v24, s[52:55], s61 offen offset:640 lds
s_mov_b32 m0, 66816
M a,96,a,224,v,240
T a,246,11,40960
buffer_load_dwordx4 v24, s[52:55], s61 offen offset:768 lds
s_mov_b32 m0, 69120
T a,248,8,33280
s_mov_b32 s61, 322560
buffer_load_dwordx4 v24, s[52:55], s61 offen offset:512 lds
M a,100,a,228,v,240
T a,250,8,41472
s_mov_b32 m0, 70016
T a,252,11,33280
W 7
buffer_load_dwordx4 v24, s[52:55], s61 offen offset:640 lds
s_mov_b32 m0, 70912
M a,112,a,232,v,240
T a,254,11,41472
buffer_load_dwordx4 v24, s[52:55], s61 offen offset:768 lds
s_mov_b32 m0, 73216
T a,224,8,33792
s_mov_b32 s61, 329728
buffer_load_dwordx4 v24, s[52:55], s61 offen offset:512 lds
M a,116,a,236,v,240
T a,226,8,41984
s_mov_b32 m0, 74112
T a,228,11,33792
W 7
buffer_load_dwordx4 v24, s[52:55], s61 offen offset:640 lds
s_mov_b32 m0, 75008
M a,128,a,240,v,240
T a,230,11,41984
buffer_load_dwordx4 v24, s[52:55], s61 offen offset:768 lds
s_mov_b32 m0, 77312
T a,232,8,34304
s_mov_b32 s61, 336896
buffer_load_dwordx4 v24, s[52:55], s61 offen offset:512 lds
M a,132,a,244,v,240
T a,234,8,42496
s_mov_b32 m0, 78208
T a,236,11,34304
W 7
buffer_load_dwordx4 v24, s[52:55], s61 offen offset:640 lds
s_mov_b32 m0, 79104
M a,144,a,248,v,240
T a,238,11,42496
buffer_load_dwordx4 v24, s[52:55], s61 offen offset:768 lds
T a,240,8,34816
M a,148,a,252,v,240
T a,242,8,43008
T a,244,11,34816
W 7
M a,160,a,224,v,240
T a,246,11,43008
T a,248,8,35328
M a,164,a,228,v,240
T a,250,8,43520
T a,252,11,35328
W 7
M a,176,a,232,v,240
T a,254,11,43520
s_waitcnt vmcnt(63)
s_barrier
T a,224,14,49152
s_mov_b32 m0, 81920
s_mov_b32 s61, 262400
M a,180,a,236,v,240
T a,226,14,51200
buffer_load_dwordx4 v23, s[40:43], s61 offen offset:0 nt lds
s_mov_b32 m0, 82944
T a,228,17,49152
W 7
s_mov_b32 s61, 266496
buffer_load_dwordx4 v23, s[40:43], s61 offen offset:0 nt lds
M a,192,a,240,v,240
T a,230,17,51200
s_mov_b32 m0, 83968
s_mov_b32 s61, 270592
T a,232,14,49664
buffer_load_dwordx4 v23, s[40:43], s61 offen offset:0 nt lds
s_mov_b32 m0, 84992
M a,196,a,244,v,240
T a,234,14,51712
s_mov_b32 s61, 274688
buffer_load_dwordx4 v23, s[40:43], s61 offen offset:0 nt lds
T a,236,17,49664
W 7
s_mov_b32 m0, 86016
s_mov_b32 s61, 278784
M a,208,a,248,v,240
T a,238,17,51712
buffer_load_dwordx4 v23, s[40:43], s61 offen offset:0 nt lds
s_mov_b32 m0, 87040
T a,240,14,53248
s_mov_b32 s61, 282880
buffer_load_dwordx4 v23, s[40:43], s61 offen offset:0 nt lds
M a,212,a,252,v,240
T a,242,14,55296
s_mov_b32 m0, 88064
s_mov_b32 s61, 286976
T a,244,17,53248
W 7
buffer_load_dwordx4 v23, s[40:43], s61 offen offset:0 nt lds
s_mov_b32 m0, 89088
v_mfma_f32_16x16x32_bf16 v[224:227], a[224:227], v[32:35], 0
T a,246,17,55296
s_mov_b32 s61, 291072
buffer_load_dwordx4 v23, s[40:43], s61 offen offset:0 nt lds
T a,248,14,53760
s_mov_b32 m0, 90112
s_mov_b32 s61, 295168
v_mfma_f32_16x16x32_bf16 v[228:231], a[228:231], v[32:35], 0
T a,250,14,55808
buffer_load_dwordx4 v23, s[40:43], s61 offen offset:0 nt lds
s_mov_b32 m0, 91136
T a,252,17,53760
W 7
s_mov_b32 s61, 299264
buffer_load_dwordx4 v23, s[40:43], s61 offen offset:0 nt lds
v_mfma_f32_16x16x32_bf16 v[240:243], a[232:235], v[32:35], 0
T a,254,17,55808
s_mov_b32 m0, 92160
s_mov_b32 s61, 303360
T a,224,14,57344
buffer_load_dwordx4 v23, s[40:43], s61 offen offset:0 nt lds
s_mov_b32 m0, 93184
v_mfma_f32_16x16x32_bf16 v[244:247], a[236:239], v[32:35], 0
T a,226,14,59392
s_mov_b32 s61, 307456
buffer_load_dwordx4 v23, s[40:43], s61 offen offset:0 nt lds
T a,228,17,57344
W 7
s_mov_b32 m0, 94208
s_mov_b32 s61, 311552
M v,224,a,240,v,40
T a,230,17,59392
buffer_load_dwordx4 v23, s[40:43], s61 offen offset:0 nt lds
s_mov_b32 m0, 95232
T a,232,14,57856
s_mov_b32 s61, 315648
buffer_load_dwordx4 v23, s[40:43], s61 offen offset:0 nt lds
M v,228,a,244,v,40
T a,234,14,59904
s_mov_b32 m0, 96256
s_mov_b32 s61, 319744
T a,236,17,57856
W 7
buffer_load_dwordx4 v23, s[40:43], s61 offen offset:0 nt lds
s_mov_b32 m0, 97280
M v,240,a,248,v,40
T a,238,17,59904
s_mov_b32 s61, 323840
buffer_load_dwordx4 v23, s[40:43], s61 offen offset:0 nt lds
T a,240,14,61440
M v,244,a,252,v,40
T a,242,14,63488
T a,244,17,61440
W 7
M v,224,a,224,v,48
T a,246,17,63488
T a,248,14,61952
M v,228,a,228,v,48
T a,250,14,64000
T a,252,17,61952
W 7
M v,240,a,232,v,48
T a,254,17,64000
s_waitcnt vmcnt(63)
s_barrier
T a,224,15,0
s_mov_b32 m0, 98304
s_mov_b32 s61, 327936
M v,244,a,236,v,48
T a,226,15,2048
buffer_load_dwordx4 v23, s[40:43], s61 offen offset:0 nt lds
s_mov_b32 m0, 99328
T a,228,18,0
W 7
s_mov_b32 s61, 332032
buffer_load_dwordx4 v23, s[40:43], s61 offen offset:0 nt lds
M v,224,a,240,v,56
T a,230,18,2048
s_mov_b32 m0, 100352
s_mov_b32 s61, 336128
T a,232,15,512
buffer_load_dwordx4 v23, s[40:43], s61 offen offset:0 nt lds
s_mov_b32 m0, 101376
M v,228,a,244,v,56
T a,234,15,2560
s_mov_b32 s61, 340224
buffer_load_dwordx4 v23, s[40:43], s61 offen offset:0 nt lds
T a,236,18,512
W 7
s_mov_b32 m0, 102400
s_mov_b32 s61, 344320
M v,240,a,248,v,56
T a,238,18,2560
buffer_load_dwordx4 v23, s[40:43], s61 offen offset:0 nt lds
s_mov_b32 m0, 103424
T a,240,15,4096
s_mov_b32 s61, 348416
buffer_load_dwordx4 v23, s[40:43], s61 offen offset:0 nt lds
M v,244,a,252,v,56
T a,242,15,6144
s_mov_b32 m0, 104448
s_mov_b32 s61, 352512
T a,244,18,4096
W 7
buffer_load_dwordx4 v23, s[40:43], s61 offen offset:0 nt lds
s_mov_b32 m0, 105472
M v,224,a,224,v,64
T a,246,18,6144
s_mov_b32 s61, 356608
buffer_load_dwordx4 v23, s[40:43], s61 offen offset:0 nt lds
T a,248,15,4608
s_mov_b32 m0, 106496
s_mov_b32 s61, 360704
M v,228,a,228,v,64
T a,250,15,6656
buffer_load_dwordx4 v23, s[40:43], s61 offen offset:0 nt lds
s_mov_b32 m0, 107520
T a,252,18,4608
W 7
s_mov_b32 s61, 364800
buffer_load_dwordx4 v23, s[40:43], s61 offen offset:0 nt lds
M v,240,a,232,v,64
T a,254,18,6656
s_mov_b32 m0, 108544
s_mov_b32 s61, 368896
T a,224,15,8192
buffer_load_dwordx4 v23, s[40:43], s61 offen offset:0 nt lds
s_mov_b32 m0, 109568
M v,244,a,236,v,64
T a,226,15,10240
s_mov_b32 s61, 372992
buffer_load_dwordx4 v23, s[40:43], s61 offen offset:0 nt lds
T a,228,18,8192
W 7
s_mov_b32 m0, 110592
s_mov_b32 s61, 377088
M v,224,a,240,v,72
T a,230,18,10240
buffer_load_dwordx4 v23, s[40:43], s61 offen offset:0 nt lds
s_mov_b32 m0, 111616
T a,232,15,8704
s_mov_b32 s61, 381184
buffer_load_dwordx4 v23, s[40:43], s61 offen offset:0 nt lds
M v,228,a,244,v,72
T a,234,15,10752
s_mov_b32 m0, 112640
s_mov_b32 s61, 385280
T a,236,18,8704
W 7
buffer_load_dwordx4 v23, s[40:43], s61 offen offset:0 nt lds
s_mov_b32 m0, 113664
M v,240,a,248,v,72
T a,238,18,10752
s_mov_b32 s61, 389376
buffer_load_dwordx4 v23, s[40:43], s61 offen offset:0 nt lds
T a,240,15,12288
M v,244,a,252,v,72
T a,242,15,14336
T a,244,18,12288
W 7
M v,224,a,224,v,80
T a,246,18,14336
T a,248,15,12800
M v,228,a,228,v,80
T a,250,15,14848
T a,252,18,12800
W 7
M v,240,a,232,v,80
T a,254,18,14848
s_waitcnt vmcnt(63)
s_barrier
T a,224,15,16384
s_mov_b32 m0, 114688
s_mov_b32 s61, 393472
M v,244,a,236,v,80
T a,226,15,18432
buffer_load_dwordx4 v23, s[40:43], s61 offen offset:0 nt lds
s_mov_b32 m0, 115712
T a,228,18,16384
W 7
s_mov_b32 s61, 397568
buffer_load_dwordx4 v23, s[40:43], s61 offen offset:0 nt lds
M v,224,a,240,v,88
T a,230,18,18432
s_mov_b32 m0, 116736
s_mov_b32 s61, 401664
T a,232,15,16896
buffer_load_dwordx4 v23, s[40:43], s61 offen offset:0 nt lds
s_mov_b32 m0, 117760
M v,228,a,244,v,88
T a,234,15,18944
s_mov_b32 s61, 405760
buffer_load_dwordx4 v23, s[40:43], s61 offen offset:0 nt lds
T a,236,18,16896
W 7
M v,240,a,248,v,88
T a,238,18,18944
T a,240,15,20480
s_mov_b32 m0, 118784
s_mov_b32 s61, 409856
M v,244,a,252,v,88
T a,242,15,22528
buffer_load_dwordx4 v23, s[40:43], s61 offen offset:0 nt lds
T a,244,18,20480
W 7
M v,224,a,224,v,96
T a,246,18,22528
T a,248,15,20992
s_mov_b32 m0, 119808
s_mov_b32 s61, 413952
M v,228,a,228,v,96
T a,250,15,23040
buffer_load_dwordx4 v23, s[40:43], s61 offen offset:0 nt lds
T a,252,18,20992
W 7
M v,240,a,232,v,96
T a,254,18,23040
T a,224,15,24576
s_mov_b32 m0, 120832
s_mov_b32 s61, 418048
M v,244,a,236,v,96
T a,226,15,26624
buffer_load_dwordx4 v23, s[40:43], s61 offen offset:0 nt lds
T a,228,18,24576
W 7
s_mov_b32 m0, 121856
s_mov_b32 s61, 422144
M v,224,a,240,v,104
T a,230,18,26624
buffer_load_dwordx4 v23, s[40:43], s61 offen offset:0 nt lds
T a,232,15,25088
M v,228,a,244,v,104
T a,234,15,27136
)x"
R"x(T a,236,18,25088
W 7
s_mov_b32 m0, 122880
s_mov_b32 s61, 426240
M v,240,a,248,v,104
T a,238,18,27136
buffer_load_dwordx4 v23, s[40:43], s61 offen offset:0 nt lds
T a,240,15,28672
M v,244,a,252,v,104
T a,242,15,30720
T a,244,18,28672
W 7
s_mov_b32 m0, 123904
s_mov_b32 s61, 430336
M v,224,a,224,v,112
T a,246,18,30720
buffer_load_dwordx4 v23, s[40:43], s61 offen offset:0 nt lds
T a,248,15,29184
M v,228,a,228,v,112
T a,250,15,31232
T a,252,18,29184
W 7
s_mov_b32 m0, 124928
s_mov_b32 s61, 434432
M v,240,a,232,v,112
T a,254,18,31232
buffer_load_dwordx4 v23, s[40:43], s61 offen offset:0 nt lds
s_waitcnt vmcnt(63)
s_barrier
.Lc1_118_%=:
T a,224,13,0
M v,244,a,236,v,112
T a,226,13,2048
T a,228,16,0
W 7
s_mov_b32 m0, 125952
s_mov_b32 s61, 438528
M v,224,a,240,v,120
T a,230,16,2048
buffer_load_dwordx4 v23, s[40:43], s61 offen offset:0 nt lds
T a,232,13,512
M v,228,a,244,v,120
T a,234,13,2560
T a,236,16,512
W 7
s_mov_b32 m0, 126976
s_mov_b32 s61, 442624
M v,240,a,248,v,120
T a,238,16,2560
buffer_load_dwordx4 v23, s[40:43], s61 offen offset:0 nt lds
T a,240,13,4096
M v,244,a,252,v,120
T a,242,13,6144
T a,244,16,4096
W 7
s_mov_b32 m0, 128000
s_mov_b32 s61, 446720
M v,224,a,224,v,128
T a,246,16,6144
buffer_load_dwordx4 v23, s[40:43], s61 offen offset:0 nt lds
T a,248,13,4608
M v,228,a,228,v,128
T a,250,13,6656
T a,252,16,4608
W 7
s_mov_b32 m0, 129024
s_mov_b32 s61, 450816
M v,240,a,232,v,128
T a,254,16,6656
buffer_load_dwordx4 v23, s[40:43], s61 offen offset:0 nt lds
T a,224,13,8192
M v,244,a,236,v,128
T a,226,13,10240
T a,228,16,8192
W 7
s_mov_b32 m0, 130048
s_mov_b32 s61, 454912
M v,224,a,240,v,136
T a,230,16,10240
buffer_load_dwordx4 v23, s[40:43], s61 offen offset:0 nt lds
T a,232,13,8704
M v,228,a,244,v,136
T a,234,13,10752
T a,236,16,8704
W 7
s_mov_b32 s61, .Lc0_118_%=-.Lpf_start%=+8192
buffer_load_dword v29, v20, s[92:95], s61 offen
M v,240,a,248,v,136
T a,238,16,10752
T a,240,13,12288
s_mov_b32 s61, .Lc1_118_%=-.Lpf_start%=+8192
buffer_load_dword v29, v20, s[92:95], s61 offen
M v,244,a,252,v,136
T a,242,13,14336
T a,244,16,12288
W 7
M v,224,a,224,v,144
T a,246,16,14336
T a,248,13,12800
s_mov_b32 m0, 131072
s_mov_b32 s61, 459008
M v,228,a,228,v,144
T a,250,13,14848
buffer_load_dwordx4 v23, s[40:43], s61 offen offset:0 nt lds
T a,252,16,12800
W 7
M v,240,a,232,v,144
T a,254,16,14848
s_waitcnt vmcnt(63)
s_barrier
T a,224,7,16384
s_mov_b32 m0, 132096
s_mov_b32 s61, 463104
M v,244,a,236,v,144
T a,226,7,24576
buffer_load_dwordx4 v23, s[40:43], s61 offen offset:0 nt lds
T a,228,10,16384
W 7
M v,224,a,240,v,152
T a,230,10,24576
T a,232,7,16896
s_mov_b32 m0, 133120
s_mov_b32 s61, 467200
M v,228,a,244,v,152
T a,234,7,25088
buffer_load_dwordx4 v23, s[40:43], s61 offen offset:0 nt lds
T a,236,10,16896
W 7
M v,240,a,248,v,152
T a,238,10,25088
T a,240,7,17408
s_mov_b32 m0, 134144
s_mov_b32 s61, 471296
M v,244,a,252,v,152
T a,242,7,25600
buffer_load_dwordx4 v23, s[40:43], s61 offen offset:0 nt lds
s_nop 7
v_mul_f32 v224, v28, v224
v_mul_f32 v225, v28, v225
v_mul_f32 v226, v28, v226
v_mul_f32 v227, v28, v227
s_mov_b32 m0, 135168
s_mov_b32 s61, 475392
buffer_load_dwordx4 v23, s[40:43], s61 offen offset:0 nt lds
v_mul_f32 v228, v28, v228
v_mul_f32 v229, v28, v229
v_mul_f32 v230, v28, v230
v_mul_f32 v231, v28, v231
v_cvt_pk_bf16_f32 v224, v224, v225
v_cvt_pk_bf16_f32 v225, v226, v227
v_cvt_pk_bf16_f32 v226, v228, v229
v_cvt_pk_bf16_f32 v227, v230, v231
s_mov_b32 m0, 136192
s_mov_b32 s61, 479488
buffer_load_dwordx4 v23, s[40:43], s61 offen offset:0 nt lds
v_mul_f32 v240, v28, v240
v_mul_f32 v241, v28, v241
v_mul_f32 v242, v28, v242
v_mul_f32 v243, v28, v243
v_mul_f32 v244, v28, v244
v_mul_f32 v245, v28, v245
v_mul_f32 v246, v28, v246
v_mul_f32 v247, v28, v247
s_mov_b32 m0, 137216
s_mov_b32 s61, 483584
buffer_load_dwordx4 v23, s[40:43], s61 offen offset:0 nt lds
v_cvt_pk_bf16_f32 v240, v240, v241
v_cvt_pk_bf16_f32 v241, v242, v243
v_cvt_pk_bf16_f32 v242, v244, v245
v_cvt_pk_bf16_f32 v243, v246, v247
s_nop 1
T a,244,10,17408
W 7
M a,0,a,224,v,224
T a,246,10,25600
T a,248,7,17920
s_mov_b32 m0, 138240
s_mov_b32 s61, 487680
M a,4,a,228,v,224
T a,250,7,26112
buffer_load_dwordx4 v23, s[40:43], s61 offen offset:0 nt lds
T a,252,10,17920
W 7
M a,16,a,232,v,224
T a,254,10,26112
T a,224,7,18432
s_mov_b32 m0, 139264
s_mov_b32 s61, 491776
M a,20,a,236,v,224
T a,226,7,26624
buffer_load_dwordx4 v23, s[40:43], s61 offen offset:0 nt lds
T a,228,10,18432
W 7
M a,32,a,240,v,224
T a,230,10,26624
T a,232,7,18944
s_mov_b32 m0, 140288
s_mov_b32 s61, 495872
M a,36,a,244,v,224
T a,234,7,27136
buffer_load_dwordx4 v23, s[40:43], s61 offen offset:0 nt lds
T a,236,10,18944
W 7
M a,48,a,248,v,224
T a,238,10,27136
T a,240,7,19456
s_mov_b32 m0, 141312
s_mov_b32 s61, 499968
M a,52,a,252,v,224
T a,242,7,27648
buffer_load_dwordx4 v23, s[40:43], s61 offen offset:0 nt lds
T a,244,10,19456
W 7
M a,64,a,224,v,224
T a,246,10,27648
T a,248,7,19968
s_mov_b32 m0, 142336
s_mov_b32 s61, 504064
M a,68,a,228,v,224
T a,250,7,28160
buffer_load_dwordx4 v23, s[40:43], s61 offen offset:0 nt lds
T a,252,10,19968
W 7
M a,80,a,232,v,224
T a,254,10,28160
s_waitcnt vmcnt(63)
s_barrier
T a,224,7,32768
s_mov_b32 m0, 143360
s_mov_b32 s61, 508160
M a,84,a,236,v,224
T a,226,7,40960
buffer_load_dwordx4 v23, s[40:43], s61 offen offset:0 nt lds
T a,228,10,32768
W 7
s_mov_b32 m0, 144384
s_mov_b32 s61, 512256
M a,96,a,240,v,224
T a,230,10,40960
buffer_load_dwordx4 v23, s[40:43], s61 offen offset:0 nt lds
T a,232,7,33280
M a,100,a,244,v,224
T a,234,7,41472
T a,236,10,33280
W 7
s_mov_b32 m0, 145408
s_mov_b32 s61, 516352
M a,112,a,248,v,224
T a,238,10,41472
buffer_load_dwordx4 v23, s[40:43], s61 offen offset:0 nt lds
T a,240,7,33792
M a,116,a,252,v,224
T a,242,7,41984
T a,244,10,33792
W 7
s_mov_b32 m0, 146432
s_mov_b32 s61, 520448
M a,128,a,224,v,224
T a,246,10,41984
buffer_load_dwordx4 v23, s[40:43], s61 offen offset:0 nt lds
T a,248,7,34304
M a,132,a,228,v,224
T a,250,7,42496
T a,252,10,34304
W 7
s_mov_b32 m0, 147456
s_mov_b32 s61, 344064
M a,144,a,232,v,224
T a,254,10,42496
buffer_load_dwordx4 v24, s[52:55], s61 offen offset:0 lds
s_mov_b32 m0, 148352
T a,224,7,34816
buffer_load_dwordx4 v24, s[52:55], s61 offen offset:128 lds
s_mov_b32 m0, 149248
M a,148,a,236,v,224
T a,226,7,43008
buffer_load_dwordx4 v24, s[52:55], s61 offen offset:256 lds
s_mov_b32 m0, 150144
T a,228,10,34816
W 7
buffer_load_dwordx4 v24, s[52:55], s61 offen offset:384 lds
s_mov_b32 m0, 151552
M a,160,a,240,v,224
T a,230,10,43008
s_mov_b32 s61, 351232
buffer_load_dwordx4 v24, s[52:55], s61 offen offset:0 lds
T a,232,7,35328
s_mov_b32 m0, 152448
M a,164,a,244,v,224
T a,234,7,43520
buffer_load_dwordx4 v24, s[52:55], s61 offen offset:128 lds
s_mov_b32 m0, 153344
T a,236,10,35328
W 7
buffer_load_dwordx4 v24, s[52:55], s61 offen offset:256 lds
s_mov_b32 m0, 154240
M a,176,a,248,v,224
T a,238,10,43520
buffer_load_dwordx4 v24, s[52:55], s61 offen offset:384 lds
s_mov_b32 m0, 155648
s_waitcnt vmcnt(63)
s_barrier
T a,240,7,49152
s_mov_b32 s61, 358400
buffer_load_dwordx4 v24, s[52:55], s61 offen offset:0 lds
M a,180,a,252,v,224
T a,242,7,57344
s_mov_b32 m0, 156544
T a,244,10,49152
W 7
buffer_load_dwordx4 v24, s[52:55], s61 offen offset:128 lds
s_mov_b32 m0, 157440
M a,192,a,224,v,224
T a,246,10,57344
buffer_load_dwordx4 v24, s[52:55], s61 offen offset:256 lds
s_mov_b32 m0, 158336
T a,248,7,49664
buffer_load_dwordx4 v24, s[52:55], s61 offen offset:384 lds
s_mov_b32 m0, 159744
M a,196,a,228,v,224
T a,250,7,57856
s_mov_b32 s61, 365568
buffer_load_dwordx4 v24, s[52:55], s61 offen offset:0 lds
T a,252,10,49664
W 7
s_mov_b32 m0, 160640
M a,208,a,232,v,224
T a,254,10,57856
buffer_load_dwordx4 v24, s[52:55], s61 offen offset:128 lds
s_mov_b32 m0, 161536
T a,224,7,50176
buffer_load_dwordx4 v24, s[52:55], s61 offen offset:256 lds
s_mov_b32 m0, 162432
M a,212,a,236,v,224
T a,226,7,58368
buffer_load_dwordx4 v24, s[52:55], s61 offen offset:384 lds
s_mov_b32 m0, 0
T a,228,10,50176
W 7
s_mov_b32 s61, 344576
buffer_load_dwordx4 v24, s[52:55], s61 offen offset:0 lds
M a,0,a,240,v,240
T a,230,10,58368
s_mov_b32 m0, 384
s_mov_b32 s61, 344064
T a,232,7,50688
buffer_load_dwordx4 v24, s[52:55], s61 offen offset:640 lds
s_mov_b32 m0, 1280
M a,4,a,244,v,240
T a,234,7,58880
buffer_load_dwordx4 v24, s[52:55], s61 offen offset:768 lds
s_mov_b32 m0, 3584
T a,236,10,50688
W 7
s_mov_b32 s61, 351232
buffer_load_dwordx4 v24, s[52:55], s61 offen offset:512 lds
M a,16,a,248,v,240
T a,238,10,58880
s_mov_b32 m0, 4480
T a,240,7,51200
buffer_load_dwordx4 v24, s[52:55], s61 offen offset:640 lds
s_mov_b32 m0, 5376
M a,20,a,252,v,240
T a,242,7,59392
buffer_load_dwordx4 v24, s[52:55], s61 offen offset:768 lds
s_mov_b32 m0, 7680
T a,244,10,51200
W 7
s_mov_b32 s61, 358400
buffer_load_dwordx4 v24, s[52:55], s61 offen offset:512 lds
M a,32,a,224,v,240
T a,246,10,59392
s_mov_b32 m0, 8576
T a,248,7,51712
buffer_load_dwordx4 v24, s[52:55], s61 offen offset:640 lds
s_mov_b32 m0, 9472
M a,36,a,228,v,240
T a,250,7,59904
buffer_load_dwordx4 v24, s[52:55], s61 offen offset:768 lds
s_mov_b32 m0, 11776
T a,252,10,51712
W 7
s_mov_b32 s61, 365568
buffer_load_dwordx4 v24, s[52:55], s61 offen offset:512 lds
M a,48,a,232,v,240
T a,254,10,59904
s_mov_b32 m0, 12672
T a,224,7,52224
buffer_load_dwordx4 v24, s[52:55], s61 offen offset:640 lds
s_mov_b32 m0, 13568
M a,52,a,236,v,240
T a,226,7,60416
buffer_load_dwordx4 v24, s[52:55], s61 offen offset:768 lds
s_mov_b32 m0, 16384
T a,228,10,52224
W 7
s_mov_b32 s61, 372736
buffer_load_dwordx4 v24, s[52:55], s61 offen offset:0 lds
M a,64,a,240,v,240
T a,230,10,60416
)x"
R"x(s_mov_b32 m0, 17280
T a,232,7,52736
buffer_load_dwordx4 v24, s[52:55], s61 offen offset:128 lds
s_mov_b32 m0, 18176
M a,68,a,244,v,240
T a,234,7,60928
buffer_load_dwordx4 v24, s[52:55], s61 offen offset:256 lds
s_mov_b32 m0, 19072
T a,236,10,52736
W 7
buffer_load_dwordx4 v24, s[52:55], s61 offen offset:384 lds
s_mov_b32 m0, 20480
M a,80,a,248,v,240
T a,238,10,60928
s_mov_b32 s61, 379904
buffer_load_dwordx4 v24, s[52:55], s61 offen offset:0 lds
s_waitcnt vmcnt(63)
s_barrier
T a,240,8,0
s_mov_b32 m0, 21376
M a,84,a,252,v,240
T a,242,8,8192
buffer_load_dwordx4 v24, s[52:55], s61 offen offset:128 lds
s_mov_b32 m0, 22272
T a,244,11,0
W 7
buffer_load_dwordx4 v24, s[52:55], s61 offen offset:256 lds
s_mov_b32 m0, 23168
M a,96,a,224,v,240
T a,246,11,8192
buffer_load_dwordx4 v24, s[52:55], s61 offen offset:384 lds
s_mov_b32 m0, 24576
T a,248,8,512
s_mov_b32 s61, 387072
buffer_load_dwordx4 v24, s[52:55], s61 offen offset:0 lds
M a,100,a,228,v,240
T a,250,8,8704
s_mov_b32 m0, 25472
T a,252,11,512
W 7
buffer_load_dwordx4 v24, s[52:55], s61 offen offset:128 lds
s_mov_b32 m0, 26368
M a,112,a,232,v,240
T a,254,11,8704
buffer_load_dwordx4 v24, s[52:55], s61 offen offset:256 lds
s_mov_b32 m0, 27264
T a,224,8,1024
buffer_load_dwordx4 v24, s[52:55], s61 offen offset:384 lds
s_mov_b32 m0, 28672
M a,116,a,236,v,240
T a,226,8,9216
s_mov_b32 s61, 394240
buffer_load_dwordx4 v24, s[52:55], s61 offen offset:0 lds
T a,228,11,1024
W 7
s_mov_b32 m0, 29568
M a,128,a,240,v,240
T a,230,11,9216
buffer_load_dwordx4 v24, s[52:55], s61 offen offset:128 lds
s_mov_b32 m0, 30464
T a,232,8,1536
buffer_load_dwordx4 v24, s[52:55], s61 offen offset:256 lds
s_mov_b32 m0, 31360
M a,132,a,244,v,240
T a,234,8,9728
buffer_load_dwordx4 v24, s[52:55], s61 offen offset:384 lds
s_mov_b32 m0, 32256
T a,236,11,1536
W 7
s_mov_b32 s61, 372736
buffer_load_dwordx4 v24, s[52:55], s61 offen offset:512 lds
M a,144,a,248,v,240
T a,238,11,9728
s_mov_b32 m0, 33152
T a,240,8,2048
buffer_load_dwordx4 v24, s[52:55], s61 offen offset:640 lds
s_mov_b32 m0, 34048
M a,148,a,252,v,240
T a,242,8,10240
buffer_load_dwordx4 v24, s[52:55], s61 offen offset:768 lds
s_mov_b32 m0, 36352
T a,244,11,2048
W 7
s_mov_b32 s61, 379904
buffer_load_dwordx4 v24, s[52:55], s61 offen offset:512 lds
M a,160,a,224,v,240
T a,246,11,10240
s_mov_b32 m0, 37248
T a,248,8,2560
buffer_load_dwordx4 v24, s[52:55], s61 offen offset:640 lds
s_mov_b32 m0, 38144
M a,164,a,228,v,240
T a,250,8,10752
buffer_load_dwordx4 v24, s[52:55], s61 offen offset:768 lds
s_mov_b32 m0, 40448
T a,252,11,2560
W 7
s_mov_b32 s61, 387072
buffer_load_dwordx4 v24, s[52:55], s61 offen offset:512 lds
M a,176,a,232,v,240
T a,254,11,10752
s_mov_b32 m0, 41344
s_waitcnt vmcnt(63)
s_barrier
T a,224,14,16384
buffer_load_dwordx4 v24, s[52:55], s61 offen offset:640 lds
s_mov_b32 m0, 42240
M a,180,a,236,v,240
T a,226,14,18432
buffer_load_dwordx4 v24, s[52:55], s61 offen offset:768 lds
s_mov_b32 m0, 44544
T a,228,17,16384
W 7
s_mov_b32 s61, 394240
buffer_load_dwordx4 v24, s[52:55], s61 offen offset:512 lds
M a,192,a,240,v,240
T a,230,17,18432
s_mov_b32 m0, 45440
T a,232,14,16896
buffer_load_dwordx4 v24, s[52:55], s61 offen offset:640 lds
s_mov_b32 m0, 46336
M a,196,a,244,v,240
T a,234,14,18944
buffer_load_dwordx4 v24, s[52:55], s61 offen offset:768 lds
s_mov_b32 m0, 49152
T a,236,17,16896
W 7
s_mov_b32 s61, 262528
buffer_load_dwordx4 v23, s[40:43], s61 offen offset:0 nt lds
M a,208,a,248,v,240
T a,238,17,18944
s_mov_b32 m0, 50176
s_mov_b32 s61, 266624
T a,240,14,20480
buffer_load_dwordx4 v23, s[40:43], s61 offen offset:0 nt lds
s_mov_b32 m0, 51200
M a,212,a,252,v,240
T a,242,14,22528
s_mov_b32 s61, 270720
buffer_load_dwordx4 v23, s[40:43], s61 offen offset:0 nt lds
T a,244,17,20480
W 7
s_mov_b32 m0, 52224
s_mov_b32 s61, 274816
v_mfma_f32_16x16x32_bf16 v[224:227], a[224:227], v[32:35], 0
T a,246,17,22528
buffer_load_dwordx4 v23, s[40:43], s61 offen offset:0 nt lds
s_mov_b32 m0, 53248
T a,248,14,20992
s_mov_b32 s61, 278912
buffer_load_dwordx4 v23, s[40:43], s61 offen offset:0 nt lds
v_mfma_f32_16x16x32_bf16 v[228:231], a[228:231], v[32:35], 0
T a,250,14,23040
s_mov_b32 m0, 54272
s_mov_b32 s61, 283008
T a,252,17,20992
W 7
buffer_load_dwordx4 v23, s[40:43], s61 offen offset:0 nt lds
s_mov_b32 m0, 55296
v_mfma_f32_16x16x32_bf16 v[240:243], a[232:235], v[32:35], 0
T a,254,17,23040
s_mov_b32 s61, 287104
buffer_load_dwordx4 v23, s[40:43], s61 offen offset:0 nt lds
T a,224,14,24576
s_mov_b32 m0, 56320
s_mov_b32 s61, 291200
v_mfma_f32_16x16x32_bf16 v[244:247], a[236:239], v[32:35], 0
T a,226,14,26624
buffer_load_dwordx4 v23, s[40:43], s61 offen offset:0 nt lds
s_mov_b32 m0, 57344
T a,228,17,24576
W 7
s_mov_b32 s61, 295296
buffer_load_dwordx4 v23, s[40:43], s61 offen offset:0 nt lds
M v,224,a,240,v,40
T a,230,17,26624
s_mov_b32 m0, 58368
s_mov_b32 s61, 299392
T a,232,14,25088
buffer_load_dwordx4 v23, s[40:43], s61 offen offset:0 nt lds
s_mov_b32 m0, 59392
M v,228,a,244,v,40
T a,234,14,27136
s_mov_b32 s61, 303488
buffer_load_dwordx4 v23, s[40:43], s61 offen offset:0 nt lds
T a,236,17,25088
W 7
s_mov_b32 m0, 60416
s_mov_b32 s61, 307584
M v,240,a,248,v,40
T a,238,17,27136
buffer_load_dwordx4 v23, s[40:43], s61 offen offset:0 nt lds
s_mov_b32 m0, 61440
T a,240,14,28672
s_mov_b32 s61, 311680
buffer_load_dwordx4 v23, s[40:43], s61 offen offset:0 nt lds
M v,244,a,252,v,40
T a,242,14,30720
s_mov_b32 m0, 62464
s_mov_b32 s61, 315776
T a,244,17,28672
W 7
buffer_load_dwordx4 v23, s[40:43], s61 offen offset:0 nt lds
s_mov_b32 m0, 63488
M v,224,a,224,v,48
T a,246,17,30720
s_mov_b32 s61, 319872
buffer_load_dwordx4 v23, s[40:43], s61 offen offset:0 nt lds
T a,248,14,29184
s_mov_b32 m0, 64512
s_mov_b32 s61, 323968
M v,228,a,228,v,48
T a,250,14,31232
buffer_load_dwordx4 v23, s[40:43], s61 offen offset:0 nt lds
T a,252,17,29184
W 7
M v,240,a,232,v,48
T a,254,17,31232
s_waitcnt vmcnt(63)
s_barrier
.Lc1_124_%=:
T a,224,14,32768
s_mov_b32 s61, .Lc0_124_%=-.Lpf_start%=+8192
buffer_load_dword v29, v20, s[92:95], s61 offen
M v,244,a,236,v,48
T a,226,14,34816
s_mov_b32 s61, .Lc1_124_%=-.Lpf_start%=+8192
buffer_load_dword v29, v20, s[92:95], s61 offen
T a,228,17,32768
W 7
s_mov_b32 m0, 65536
s_mov_b32 s61, 328064
M v,224,a,240,v,56
T a,230,17,34816
buffer_load_dwordx4 v23, s[40:43], s61 offen offset:0 nt lds
s_mov_b32 m0, 66560
T a,232,14,33280
s_mov_b32 s61, 332160
buffer_load_dwordx4 v23, s[40:43], s61 offen offset:0 nt lds
M v,228,a,244,v,56
T a,234,14,35328
s_mov_b32 m0, 67584
s_mov_b32 s61, 336256
T a,236,17,33280
W 7
buffer_load_dwordx4 v23, s[40:43], s61 offen offset:0 nt lds
s_mov_b32 m0, 68608
M v,240,a,248,v,56
T a,238,17,35328
s_mov_b32 s61, 340352
buffer_load_dwordx4 v23, s[40:43], s61 offen offset:0 nt lds
T a,240,14,36864
s_mov_b32 m0, 69632
s_mov_b32 s61, 344448
M v,244,a,252,v,56
T a,242,14,38912
buffer_load_dwordx4 v23, s[40:43], s61 offen offset:0 nt lds
s_mov_b32 m0, 70656
T a,244,17,36864
W 7
s_mov_b32 s61, 348544
buffer_load_dwordx4 v23, s[40:43], s61 offen offset:0 nt lds
M v,224,a,224,v,64
T a,246,17,38912
s_mov_b32 m0, 71680
s_mov_b32 s61, 352640
T a,248,14,37376
buffer_load_dwordx4 v23, s[40:43], s61 offen offset:0 nt lds
M v,228,a,228,v,64
T a,250,14,39424
T a,252,17,37376
W 7
s_mov_b32 m0, 72704
s_mov_b32 s61, 356736
M v,240,a,232,v,64
T a,254,17,39424
buffer_load_dwordx4 v23, s[40:43], s61 offen offset:0 nt lds
T a,224,14,40960
M v,244,a,236,v,64
T a,226,14,43008
T a,228,17,40960
W 7
s_mov_b32 m0, 73728
s_mov_b32 s61, 360832
M v,224,a,240,v,72
T a,230,17,43008
buffer_load_dwordx4 v23, s[40:43], s61 offen offset:0 nt lds
T a,232,14,41472
M v,228,a,244,v,72
T a,234,14,43520
T a,236,17,41472
W 7
s_mov_b32 m0, 74752
s_mov_b32 s61, 364928
M v,240,a,248,v,72
T a,238,17,43520
buffer_load_dwordx4 v23, s[40:43], s61 offen offset:0 nt lds
T a,240,14,45056
s_mov_b32 m0, 75776
s_mov_b32 s61, 369024
M v,244,a,252,v,72
T a,242,14,47104
buffer_load_dwordx4 v23, s[40:43], s61 offen offset:0 nt lds
T a,244,17,45056
W 7
M v,224,a,224,v,80
T a,246,17,47104
T a,248,14,45568
s_mov_b32 m0, 76800
s_mov_b32 s61, 373120
M v,228,a,228,v,80
T a,250,14,47616
buffer_load_dwordx4 v23, s[40:43], s61 offen offset:0 nt lds
T a,252,17,45568
W 7
M v,240,a,232,v,80
T a,254,17,47616
s_waitcnt vmcnt(63)
s_barrier
T a,224,14,49152
s_mov_b32 m0, 77824
s_mov_b32 s61, 377216
M v,244,a,236,v,80
T a,226,14,51200
buffer_load_dwordx4 v23, s[40:43], s61 offen offset:0 nt lds
T a,228,17,49152
W 7
M v,224,a,240,v,88
T a,230,17,51200
T a,232,14,49664
s_mov_b32 m0, 78848
s_mov_b32 s61, 381312
M v,228,a,244,v,88
T a,234,14,51712
buffer_load_dwordx4 v23, s[40:43], s61 offen offset:0 nt lds
T a,236,17,49664
W 7
M v,240,a,248,v,88
T a,238,17,51712
T a,240,14,53248
s_mov_b32 m0, 79872
s_mov_b32 s61, 385408
M v,244,a,252,v,88
T a,242,14,55296
buffer_load_dwordx4 v23, s[40:43], s61 offen offset:0 nt lds
T a,244,17,53248
W 7
M v,224,a,224,v,96
T a,246,17,55296
T a,248,14,53760
s_mov_b32 m0, 80896
s_mov_b32 s61, 389504
M v,228,a,228,v,96
T a,250,14,55808
buffer_load_dwordx4 v23, s[40:43], s61 offen offset:0 nt lds
T a,252,17,53760
W 7
M v,240,a,232,v,96
T a,254,17,55808
T a,224,14,57344
s_mov_b32 m0, 81920
s_mov_b32 s61, 393600
M v,244,a,236,v,96
T a,226,14,59392
buffer_load_dwordx4 v23, s[40:43], s61 offen offset:0 nt lds
T a,228,17,57344
W 7
M v,224,a,240,v,104
T a,230,17,59392
T a,232,14,57856
s_mov_b32 m0, 82944
s_mov_b32 s61, 397696
M v,228,a,244,v,104
T a,234,14,59904
buffer_load_dwordx4 v23, s[40:43], s61 offen offset:0 nt lds
T a,236,17,57856
W 7
M v,240,a,248,v,104
T a,238,17,59904
T a,240,14,61440
s_mov_b32 m0, 83968
s_mov_b32 s61, 401792
M v,244,a,252,v,104
T a,242,14,63488
buffer_load_dwordx4 v23, s[40:43], s61 offen offset:0 nt lds
T a,244,17,61440
W 7
M v,224,a,224,v,112
T a,246,17,63488
T a,248,14,61952
s_mov_b32 m0, 84992
s_mov_b32 s61, 405888
M v,228,a,228,v,112
T a,250,14,64000
buffer_load_dwordx4 v23, s[40:43], s61 offen offset:0 nt lds
T a,252,17,61952
W 7
M v,240,a,232,v,112
T a,254,17,64000
s_waitcnt vmcnt(63)
)x"
R"x(s_barrier
T a,224,15,0
s_mov_b32 m0, 86016
s_mov_b32 s61, 409984
M v,244,a,236,v,112
T a,226,15,2048
buffer_load_dwordx4 v23, s[40:43], s61 offen offset:0 nt lds
T a,228,18,0
W 7
s_mov_b32 m0, 87040
s_mov_b32 s61, 414080
M v,224,a,240,v,120
T a,230,18,2048
buffer_load_dwordx4 v23, s[40:43], s61 offen offset:0 nt lds
T a,232,15,512
M v,228,a,244,v,120
T a,234,15,2560
T a,236,18,512
W 7
s_mov_b32 m0, 88064
s_mov_b32 s61, 418176
M v,240,a,248,v,120
T a,238,18,2560
buffer_load_dwordx4 v23, s[40:43], s61 offen offset:0 nt lds
T a,240,15,4096
M v,244,a,252,v,120
T a,242,15,6144
T a,244,18,4096
W 7
s_mov_b32 m0, 89088
s_mov_b32 s61, 422272
M v,224,a,224,v,128
T a,246,18,6144
buffer_load_dwordx4 v23, s[40:43], s61 offen offset:0 nt lds
T a,248,15,4608
M v,228,a,228,v,128
T a,250,15,6656
T a,252,18,4608
W 7
s_mov_b32 m0, 90112
s_mov_b32 s61, 426368
M v,240,a,232,v,128
T a,254,18,6656
buffer_load_dwordx4 v23, s[40:43], s61 offen offset:0 nt lds
T a,224,15,8192
M v,244,a,236,v,128
T a,226,15,10240
T a,228,18,8192
W 7
s_mov_b32 m0, 91136
s_mov_b32 s61, 430464
M v,224,a,240,v,136
T a,230,18,10240
buffer_load_dwordx4 v23, s[40:43], s61 offen offset:0 nt lds
T a,232,15,8704
M v,228,a,244,v,136
T a,234,15,10752
T a,236,18,8704
W 7
s_mov_b32 m0, 92160
s_mov_b32 s61, 434560
M v,240,a,248,v,136
T a,238,18,10752
buffer_load_dwordx4 v23, s[40:43], s61 offen offset:0 nt lds
T a,240,15,12288
M v,244,a,252,v,136
T a,242,15,14336
T a,244,18,12288
W 7
s_mov_b32 m0, 93184
s_mov_b32 s61, 438656
M v,224,a,224,v,144
T a,246,18,14336
buffer_load_dwordx4 v23, s[40:43], s61 offen offset:0 nt lds
T a,248,15,12800
M v,228,a,228,v,144
T a,250,15,14848
T a,252,18,12800
W 7
s_mov_b32 m0, 94208
s_mov_b32 s61, 442752
M v,240,a,232,v,144
T a,254,18,14848
buffer_load_dwordx4 v23, s[40:43], s61 offen offset:0 nt lds
s_waitcnt vmcnt(63)
s_barrier
T a,224,9,16384
M v,244,a,236,v,144
T a,226,9,24576
T a,228,12,16384
W 7
s_mov_b32 m0, 95232
s_mov_b32 s61, 446848
M v,224,a,240,v,152
T a,230,12,24576
buffer_load_dwordx4 v23, s[40:43], s61 offen offset:0 nt lds
T a,232,9,16896
M v,228,a,244,v,152
T a,234,9,25088
T a,236,12,16896
W 7
s_mov_b32 m0, 96256
s_mov_b32 s61, 450944
M v,240,a,248,v,152
T a,238,12,25088
buffer_load_dwordx4 v23, s[40:43], s61 offen offset:0 nt lds
T a,240,9,17408
s_mov_b32 m0, 97280
s_mov_b32 s61, 455040
M v,244,a,252,v,152
T a,242,9,25600
buffer_load_dwordx4 v23, s[40:43], s61 offen offset:0 nt lds
s_nop 7
v_mul_f32 v224, v28, v224
v_mul_f32 v225, v28, v225
v_mul_f32 v226, v28, v226
v_mul_f32 v227, v28, v227
s_mov_b32 m0, 98304
s_mov_b32 s61, 459136
buffer_load_dwordx4 v23, s[40:43], s61 offen offset:0 nt lds
v_mul_f32 v228, v28, v228
v_mul_f32 v229, v28, v229
v_mul_f32 v230, v28, v230
v_mul_f32 v231, v28, v231
v_cvt_pk_bf16_f32 v224, v224, v225
v_cvt_pk_bf16_f32 v225, v226, v227
v_cvt_pk_bf16_f32 v226, v228, v229
v_cvt_pk_bf16_f32 v227, v230, v231
s_mov_b32 m0, 99328
s_mov_b32 s61, 463232
buffer_load_dwordx4 v23, s[40:43], s61 offen offset:0 nt lds
v_mul_f32 v240, v28, v240
v_mul_f32 v241, v28, v241
v_mul_f32 v242, v28, v242
v_mul_f32 v243, v28, v243
v_mul_f32 v244, v28, v244
v_mul_f32 v245, v28, v245
v_mul_f32 v246, v28, v246
v_mul_f32 v247, v28, v247
s_mov_b32 m0, 100352
s_mov_b32 s61, 467328
buffer_load_dwordx4 v23, s[40:43], s61 offen offset:0 nt lds
v_cvt_pk_bf16_f32 v240, v240, v241
v_cvt_pk_bf16_f32 v241, v242, v243
v_cvt_pk_bf16_f32 v242, v244, v245
v_cvt_pk_bf16_f32 v243, v246, v247
s_nop 1
T a,244,12,17408
W 7
M a,0,a,224,v,224
T a,246,12,25600
T a,248,9,17920
s_mov_b32 m0, 101376
s_mov_b32 s61, 471424
M a,4,a,228,v,224
T a,250,9,26112
buffer_load_dwordx4 v23, s[40:43], s61 offen offset:0 nt lds
T a,252,12,17920
W 7
M a,16,a,232,v,224
T a,254,12,26112
T a,224,9,18432
s_mov_b32 m0, 102400
s_mov_b32 s61, 475520
M a,20,a,236,v,224
T a,226,9,26624
buffer_load_dwordx4 v23, s[40:43], s61 offen offset:0 nt lds
T a,228,12,18432
W 7
M a,32,a,240,v,224
T a,230,12,26624
T a,232,9,18944
s_mov_b32 m0, 103424
s_mov_b32 s61, 479616
M a,36,a,244,v,224
T a,234,9,27136
buffer_load_dwordx4 v23, s[40:43], s61 offen offset:0 nt lds
T a,236,12,18944
W 7
M a,48,a,248,v,224
T a,238,12,27136
T a,240,9,19456
s_mov_b32 m0, 104448
s_mov_b32 s61, 483712
M a,52,a,252,v,224
T a,242,9,27648
buffer_load_dwordx4 v23, s[40:43], s61 offen offset:0 nt lds
T a,244,12,19456
W 7
M a,64,a,224,v,224
T a,246,12,27648
T a,248,9,19968
s_mov_b32 m0, 105472
s_mov_b32 s61, 487808
M a,68,a,228,v,224
T a,250,9,28160
buffer_load_dwordx4 v23, s[40:43], s61 offen offset:0 nt lds
T a,252,12,19968
W 7
M a,80,a,232,v,224
T a,254,12,28160
s_waitcnt vmcnt(63)
s_barrier
T a,224,7,0
s_mov_b32 m0, 106496
s_mov_b32 s61, 491904
M a,84,a,236,v,224
T a,226,7,8192
buffer_load_dwordx4 v23, s[40:43], s61 offen offset:0 nt lds
T a,228,10,0
W 7
M a,96,a,240,v,224
T a,230,10,8192
T a,232,7,512
s_mov_b32 m0, 107520
s_mov_b32 s61, 496000
M a,100,a,244,v,224
T a,234,7,8704
buffer_load_dwordx4 v23, s[40:43], s61 offen offset:0 nt lds
T a,236,10,512
W 7
M a,112,a,248,v,224
T a,238,10,8704
T a,240,7,1024
s_mov_b32 m0, 108544
s_mov_b32 s61, 500096
M a,116,a,252,v,224
T a,242,7,9216
buffer_load_dwordx4 v23, s[40:43], s61 offen offset:0 nt lds
T a,244,10,1024
W 7
M a,128,a,224,v,224
T a,246,10,9216
T a,248,7,1536
s_mov_b32 m0, 109568
s_mov_b32 s61, 504192
M a,132,a,228,v,224
T a,250,7,9728
buffer_load_dwordx4 v23, s[40:43], s61 offen offset:0 nt lds
T a,252,10,1536
W 7
M a,144,a,232,v,224
T a,254,10,9728
T a,224,7,2048
s_mov_b32 m0, 110592
s_mov_b32 s61, 508288
M a,148,a,236,v,224
T a,226,7,10240
buffer_load_dwordx4 v23, s[40:43], s61 offen offset:0 nt lds
T a,228,10,2048
W 7
s_mov_b32 m0, 111616
s_mov_b32 s61, 512384
M a,160,a,240,v,224
T a,230,10,10240
buffer_load_dwordx4 v23, s[40:43], s61 offen offset:0 nt lds
T a,232,7,2560
M a,164,a,244,v,224
T a,234,7,10752
T a,236,10,2560
W 7
s_mov_b32 m0, 112640
s_mov_b32 s61, 516480
M a,176,a,248,v,224
T a,238,10,10752
buffer_load_dwordx4 v23, s[40:43], s61 offen offset:0 nt lds
s_waitcnt vmcnt(63)
s_barrier
T a,240,7,16384
M a,180,a,252,v,224
T a,242,7,24576
T a,244,10,16384
W 7
s_mov_b32 m0, 113664
s_mov_b32 s61, 520576
M a,192,a,224,v,224
T a,246,10,24576
buffer_load_dwordx4 v23, s[40:43], s61 offen offset:0 nt lds
T a,248,7,16896
M a,196,a,228,v,224
T a,250,7,25088
T a,252,10,16896
W 7
s_mov_b32 m0, 114688
s_mov_b32 s61, 401408
M a,208,a,232,v,224
T a,254,10,25088
buffer_load_dwordx4 v24, s[52:55], s61 offen offset:0 lds
s_mov_b32 m0, 115584
T a,224,7,17408
buffer_load_dwordx4 v24, s[52:55], s61 offen offset:128 lds
s_mov_b32 m0, 116480
M a,212,a,236,v,224
T a,226,7,25600
buffer_load_dwordx4 v24, s[52:55], s61 offen offset:256 lds
s_mov_b32 m0, 117376
T a,228,10,17408
W 7
buffer_load_dwordx4 v24, s[52:55], s61 offen offset:384 lds
s_mov_b32 m0, 118784
M a,0,a,240,v,240
T a,230,10,25600
s_mov_b32 s61, 408576
buffer_load_dwordx4 v24, s[52:55], s61 offen offset:0 lds
T a,232,7,17920
s_mov_b32 m0, 119680
M a,4,a,244,v,240
T a,234,7,26112
buffer_load_dwordx4 v24, s[52:55], s61 offen offset:128 lds
s_mov_b32 m0, 120576
T a,236,10,17920
W 7
buffer_load_dwordx4 v24, s[52:55], s61 offen offset:256 lds
s_mov_b32 m0, 121472
M a,16,a,248,v,240
T a,238,10,26112
buffer_load_dwordx4 v24, s[52:55], s61 offen offset:384 lds
s_mov_b32 m0, 122880
T a,240,7,18432
s_mov_b32 s61, 415744
buffer_load_dwordx4 v24, s[52:55], s61 offen offset:0 lds
M a,20,a,252,v,240
T a,242,7,26624
s_mov_b32 m0, 123776
T a,244,10,18432
W 7
buffer_load_dwordx4 v24, s[52:55], s61 offen offset:128 lds
s_mov_b32 m0, 124672
M a,32,a,224,v,240
T a,246,10,26624
buffer_load_dwordx4 v24, s[52:55], s61 offen offset:256 lds
s_mov_b32 m0, 125568
T a,248,7,18944
buffer_load_dwordx4 v24, s[52:55], s61 offen offset:384 lds
s_mov_b32 m0, 126976
M a,36,a,228,v,240
T a,250,7,27136
s_mov_b32 s61, 422912
buffer_load_dwordx4 v24, s[52:55], s61 offen offset:0 lds
T a,252,10,18944
W 7
s_mov_b32 m0, 127872
M a,48,a,232,v,240
T a,254,10,27136
buffer_load_dwordx4 v24, s[52:55], s61 offen offset:128 lds
s_mov_b32 m0, 128768
T a,224,7,19456
buffer_load_dwordx4 v24, s[52:55], s61 offen offset:256 lds
s_mov_b32 m0, 129664
M a,52,a,236,v,240
T a,226,7,27648
buffer_load_dwordx4 v24, s[52:55], s61 offen offset:384 lds
s_mov_b32 m0, 131072
T a,228,10,19456
W 7
s_mov_b32 s61, 430080
buffer_load_dwordx4 v24, s[52:55], s61 offen offset:0 lds
M a,64,a,240,v,240
T a,230,10,27648
s_mov_b32 m0, 131968
T a,232,7,19968
buffer_load_dwordx4 v24, s[52:55], s61 offen offset:128 lds
s_mov_b32 m0, 132864
M a,68,a,244,v,240
T a,234,7,28160
buffer_load_dwordx4 v24, s[52:55], s61 offen offset:256 lds
s_mov_b32 m0, 133760
T a,236,10,19968
W 7
buffer_load_dwordx4 v24, s[52:55], s61 offen offset:384 lds
s_mov_b32 m0, 135168
M a,80,a,248,v,240
T a,238,10,28160
s_mov_b32 s61, 437248
buffer_load_dwordx4 v24, s[52:55], s61 offen offset:0 lds
s_waitcnt vmcnt(63)
s_barrier
.Lc1_130_%=:
T a,240,7,32768
s_mov_b32 m0, 136064
M a,84,a,252,v,240
T a,242,7,40960
buffer_load_dwordx4 v24, s[52:55], s61 offen offset:128 lds
s_mov_b32 m0, 136960
T a,244,10,32768
W 7
buffer_load_dwordx4 v24, s[52:55], s61 offen offset:256 lds
s_mov_b32 m0, 137856
M a,96,a,224,v,240
T a,246,10,40960
buffer_load_dwordx4 v24, s[52:55], s61 offen offset:384 lds
s_mov_b32 m0, 139264
T a,248,7,33280
s_mov_b32 s61, 444416
buffer_load_dwordx4 v24, s[52:55], s61 offen offset:0 lds
M a,100,a,228,v,240
T a,250,7,41472
s_mov_b32 m0, 140160
T a,252,10,33280
W 7
)x"
R"x(buffer_load_dwordx4 v24, s[52:55], s61 offen offset:128 lds
s_mov_b32 m0, 141056
M a,112,a,232,v,240
T a,254,10,41472
buffer_load_dwordx4 v24, s[52:55], s61 offen offset:256 lds
s_mov_b32 m0, 141952
T a,224,7,33792
buffer_load_dwordx4 v24, s[52:55], s61 offen offset:384 lds
s_mov_b32 m0, 143360
M a,116,a,236,v,240
T a,226,7,41984
s_mov_b32 s61, 451584
buffer_load_dwordx4 v24, s[52:55], s61 offen offset:0 lds
T a,228,10,33792
W 7
s_mov_b32 m0, 144256
M a,128,a,240,v,240
T a,230,10,41984
buffer_load_dwordx4 v24, s[52:55], s61 offen offset:128 lds
s_mov_b32 m0, 145152
T a,232,7,34304
buffer_load_dwordx4 v24, s[52:55], s61 offen offset:256 lds
s_mov_b32 m0, 146048
M a,132,a,244,v,240
T a,234,7,42496
buffer_load_dwordx4 v24, s[52:55], s61 offen offset:384 lds
s_mov_b32 m0, 146944
T a,236,10,34304
W 7
s_mov_b32 s61, 401408
buffer_load_dwordx4 v24, s[52:55], s61 offen offset:512 lds
M a,144,a,248,v,240
T a,238,10,42496
s_mov_b32 m0, 147840
T a,240,7,34816
buffer_load_dwordx4 v24, s[52:55], s61 offen offset:640 lds
s_mov_b32 m0, 148736
M a,148,a,252,v,240
T a,242,7,43008
buffer_load_dwordx4 v24, s[52:55], s61 offen offset:768 lds
s_mov_b32 m0, 151040
T a,244,10,34816
W 7
s_mov_b32 s61, 408576
buffer_load_dwordx4 v24, s[52:55], s61 offen offset:512 lds
M a,160,a,224,v,240
T a,246,10,43008
s_mov_b32 m0, 151936
T a,248,7,35328
buffer_load_dwordx4 v24, s[52:55], s61 offen offset:640 lds
s_mov_b32 m0, 152832
M a,164,a,228,v,240
T a,250,7,43520
buffer_load_dwordx4 v24, s[52:55], s61 offen offset:768 lds
s_mov_b32 m0, 155136
T a,252,10,35328
W 7
s_mov_b32 s61, 415744
buffer_load_dwordx4 v24, s[52:55], s61 offen offset:512 lds
M a,176,a,232,v,240
T a,254,10,43520
s_mov_b32 m0, 156032
s_waitcnt vmcnt(63)
s_barrier
T a,224,13,49152
buffer_load_dwordx4 v24, s[52:55], s61 offen offset:640 lds
s_mov_b32 m0, 156928
M a,180,a,236,v,240
T a,226,13,51200
buffer_load_dwordx4 v24, s[52:55], s61 offen offset:768 lds
s_mov_b32 m0, 159232
T a,228,16,49152
W 7
s_mov_b32 s61, 422912
buffer_load_dwordx4 v24, s[52:55], s61 offen offset:512 lds
M a,192,a,240,v,240
T a,230,16,51200
s_mov_b32 m0, 160128
T a,232,13,49664
buffer_load_dwordx4 v24, s[52:55], s61 offen offset:640 lds
s_mov_b32 m0, 161024
M a,196,a,244,v,240
T a,234,13,51712
buffer_load_dwordx4 v24, s[52:55], s61 offen offset:768 lds
s_mov_b32 s61, .Lc0_130_%=-.Lpf_start%=+8192
T a,236,16,49664
W 7
buffer_load_dword v29, v20, s[92:95], s61 offen
s_mov_b32 s61, .Lc1_130_%=-.Lpf_start%=+8192
M a,208,a,248,v,240
T a,238,16,51712
buffer_load_dword v29, v20, s[92:95], s61 offen
s_mov_b32 m0, 0
T a,240,13,53248
s_mov_b32 s61, 430592
buffer_load_dwordx4 v24, s[52:55], s61 offen offset:0 lds
M a,212,a,252,v,240
T a,242,13,55296
s_mov_b32 m0, 384
s_mov_b32 s61, 430080
T a,244,16,53248
W 7
buffer_load_dwordx4 v24, s[52:55], s61 offen offset:640 lds
s_mov_b32 m0, 1280
v_mfma_f32_16x16x32_bf16 v[224:227], a[224:227], v[32:35], 0
T a,246,16,55296
buffer_load_dwordx4 v24, s[52:55], s61 offen offset:768 lds
s_mov_b32 m0, 3584
T a,248,13,53760
s_mov_b32 s61, 437248
buffer_load_dwordx4 v24, s[52:55], s61 offen offset:512 lds
v_mfma_f32_16x16x32_bf16 v[228:231], a[228:231], v[32:35], 0
T a,250,13,55808
s_mov_b32 m0, 4480
T a,252,16,53760
W 7
buffer_load_dwordx4 v24, s[52:55], s61 offen offset:640 lds
s_mov_b32 m0, 5376
v_mfma_f32_16x16x32_bf16 v[240:243], a[232:235], v[32:35], 0
T a,254,16,55808
buffer_load_dwordx4 v24, s[52:55], s61 offen offset:768 lds
s_mov_b32 m0, 7680
T a,224,13,57344
s_mov_b32 s61, 444416
buffer_load_dwordx4 v24, s[52:55], s61 offen offset:512 lds
v_mfma_f32_16x16x32_bf16 v[244:247], a[236:239], v[32:35], 0
T a,226,13,59392
s_mov_b32 m0, 8576
T a,228,16,57344
W 7
buffer_load_dwordx4 v24, s[52:55], s61 offen offset:640 lds
s_mov_b32 m0, 9472
M v,224,a,240,v,40
T a,230,16,59392
buffer_load_dwordx4 v24, s[52:55], s61 offen offset:768 lds
s_mov_b32 m0, 11776
T a,232,13,57856
s_mov_b32 s61, 451584
buffer_load_dwordx4 v24, s[52:55], s61 offen offset:512 lds
M v,228,a,244,v,40
T a,234,13,59904
s_mov_b32 m0, 12672
T a,236,16,57856
W 7
buffer_load_dwordx4 v24, s[52:55], s61 offen offset:640 lds
s_mov_b32 m0, 13568
M v,240,a,248,v,40
T a,238,16,59904
buffer_load_dwordx4 v24, s[52:55], s61 offen offset:768 lds
T a,240,13,61440
M v,244,a,252,v,40
T a,242,13,63488
T a,244,16,61440
W 7
M v,224,a,224,v,48
T a,246,16,63488
T a,248,13,61952
M v,228,a,228,v,48
T a,250,13,64000
T a,252,16,61952
W 7
M v,240,a,232,v,48
T a,254,16,64000
s_waitcnt vmcnt(63)
s_barrier
T a,224,14,0
M v,244,a,236,v,48
T a,226,14,2048
T a,228,17,0
W 7
M v,224,a,240,v,56
T a,230,17,2048
T a,232,14,512
M v,228,a,244,v,56
T a,234,14,2560
T a,236,17,512
W 7
M v,240,a,248,v,56
T a,238,17,2560
T a,240,14,4096
M v,244,a,252,v,56
T a,242,14,6144
T a,244,17,4096
W 7
M v,224,a,224,v,64
T a,246,17,6144
T a,248,14,4608
M v,228,a,228,v,64
T a,250,14,6656
T a,252,17,4608
W 7
M v,240,a,232,v,64
T a,254,17,6656
T a,224,14,8192
M v,244,a,236,v,64
T a,226,14,10240
T a,228,17,8192
W 7
M v,224,a,240,v,72
T a,230,17,10240
T a,232,14,8704
M v,228,a,244,v,72
T a,234,14,10752
T a,236,17,8704
W 7
M v,240,a,248,v,72
T a,238,17,10752
T a,240,14,12288
M v,244,a,252,v,72
T a,242,14,14336
T a,244,17,12288
W 7
M v,224,a,224,v,80
T a,246,17,14336
T a,248,14,12800
M v,228,a,228,v,80
T a,250,14,14848
T a,252,17,12800
W 7
M v,240,a,232,v,80
T a,254,17,14848
s_waitcnt vmcnt(63)
s_barrier
T a,224,14,16384
M v,244,a,236,v,80
T a,226,14,18432
T a,228,17,16384
W 7
M v,224,a,240,v,88
T a,230,17,18432
T a,232,14,16896
M v,228,a,244,v,88
T a,234,14,18944
T a,236,17,16896
W 7
M v,240,a,248,v,88
T a,238,17,18944
T a,240,14,20480
M v,244,a,252,v,88
T a,242,14,22528
T a,244,17,20480
W 7
M v,224,a,224,v,96
T a,246,17,22528
T a,248,14,20992
M v,228,a,228,v,96
T a,250,14,23040
T a,252,17,20992
W 7
M v,240,a,232,v,96
T a,254,17,23040
T a,224,14,24576
M v,244,a,236,v,96
T a,226,14,26624
T a,228,17,24576
W 7
M v,224,a,240,v,104
T a,230,17,26624
T a,232,14,25088
M v,228,a,244,v,104
T a,234,14,27136
T a,236,17,25088
W 7
M v,240,a,248,v,104
T a,238,17,27136
T a,240,14,28672
M v,244,a,252,v,104
T a,242,14,30720
T a,244,17,28672
W 7
M v,224,a,224,v,112
T a,246,17,30720
T a,248,14,29184
M v,228,a,228,v,112
T a,250,14,31232
T a,252,17,29184
W 7
M v,240,a,232,v,112
T a,254,17,31232
s_waitcnt vmcnt(58)
s_barrier
T a,224,14,32768
M v,244,a,236,v,112
T a,226,14,34816
T a,228,17,32768
W 7
M v,224,a,240,v,120
T a,230,17,34816
T a,232,14,33280
M v,228,a,244,v,120
T a,234,14,35328
T a,236,17,33280
W 7
M v,240,a,248,v,120
T a,238,17,35328
T a,240,14,36864
M v,244,a,252,v,120
T a,242,14,38912
T a,244,17,36864
W 7
M v,224,a,224,v,128
T a,246,17,38912
T a,248,14,37376
M v,228,a,228,v,128
T a,250,14,39424
T a,252,17,37376
W 7
M v,240,a,232,v,128
T a,254,17,39424
T a,224,14,40960
M v,244,a,236,v,128
T a,226,14,43008
T a,228,17,40960
W 7
M v,224,a,240,v,136
T a,230,17,43008
T a,232,14,41472
M v,228,a,244,v,136
T a,234,14,43520
T a,236,17,41472
W 7
M v,240,a,248,v,136
T a,238,17,43520
T a,240,14,45056
M v,244,a,252,v,136
T a,242,14,47104
T a,244,17,45056
W 7
M v,224,a,224,v,144
T a,246,17,47104
T a,248,14,45568
M v,228,a,228,v,144
T a,250,14,47616
T a,252,17,45568
W 7
M v,240,a,232,v,144
T a,254,17,47616
s_waitcnt vmcnt(42)
s_barrier
T a,224,8,49152
M v,244,a,236,v,144
T a,226,8,57344
T a,228,11,49152
W 7
M v,224,a,240,v,152
T a,230,11,57344
T a,232,8,49664
M v,228,a,244,v,152
T a,234,8,57856
T a,236,11,49664
W 7
M v,240,a,248,v,152
T a,238,11,57856
T a,240,8,50176
M v,244,a,252,v,152
T a,242,8,58368
s_nop 7
v_mul_f32 v224, v28, v224
v_mul_f32 v225, v28, v225
v_mul_f32 v226, v28, v226
v_mul_f32 v227, v28, v227
v_mul_f32 v228, v28, v228
v_mul_f32 v229, v28, v229
v_mul_f32 v230, v28, v230
v_mul_f32 v231, v28, v231
v_cvt_pk_bf16_f32 v224, v224, v225
v_cvt_pk_bf16_f32 v225, v226, v227
v_cvt_pk_bf16_f32 v226, v228, v229
v_cvt_pk_bf16_f32 v227, v230, v231
v_mul_f32 v240, v28, v240
v_mul_f32 v241, v28, v241
v_mul_f32 v242, v28, v242
v_mul_f32 v243, v28, v243
v_mul_f32 v244, v28, v244
v_mul_f32 v245, v28, v245
v_mul_f32 v246, v28, v246
v_mul_f32 v247, v28, v247
v_cvt_pk_bf16_f32 v240, v240, v241
v_cvt_pk_bf16_f32 v241, v242, v243
v_cvt_pk_bf16_f32 v242, v244, v245
v_cvt_pk_bf16_f32 v243, v246, v247
s_nop 1
T a,244,11,50176
W 7
M a,0,a,224,v,224
T a,246,11,58368
T a,248,8,50688
M a,4,a,228,v,224
T a,250,8,58880
T a,252,11,50688
W 7
M a,16,a,232,v,224
T a,254,11,58880
T a,224,8,51200
M a,20,a,236,v,224
T a,226,8,59392
T a,228,11,51200
W 7
M a,32,a,240,v,224
T a,230,11,59392
)x"
R"x(T a,232,8,51712
M a,36,a,244,v,224
T a,234,8,59904
T a,236,11,51712
W 7
M a,48,a,248,v,224
T a,238,11,59904
T a,240,8,52224
M a,52,a,252,v,224
T a,242,8,60416
T a,244,11,52224
W 7
M a,64,a,224,v,224
T a,246,11,60416
T a,248,8,52736
M a,68,a,228,v,224
T a,250,8,60928
T a,252,11,52736
W 7
M a,80,a,232,v,224
T a,254,11,60928
s_waitcnt vmcnt(26)
s_barrier
T a,224,9,0
M a,84,a,236,v,224
T a,226,9,8192
T a,228,12,0
W 7
M a,96,a,240,v,224
T a,230,12,8192
T a,232,9,512
M a,100,a,244,v,224
T a,234,9,8704
T a,236,12,512
W 7
M a,112,a,248,v,224
T a,238,12,8704
T a,240,9,1024
v_lshlrev_b32 v25, 4, v0
s_mul_i32 s27, s60, 7
M a,116,a,252,v,224
T a,242,9,9216
v_add_u32 v25, s27, v25
T a,244,12,1024
W 7
M a,0,a,224,v,240
T a,246,12,9216
T a,248,9,1536
M a,4,a,228,v,240
T a,250,9,9728
T a,252,12,1536
W 7
M a,16,a,232,v,240
T a,254,12,9728
T a,224,9,2048
M a,20,a,236,v,240
T a,226,9,10240
T a,228,12,2048
W 7
A 32,0
A 33,1
M a,32,a,240,v,240
T a,230,12,10240
A 34,2
A 35,3
A 36,4
A 37,5
A 38,6
A 39,7
T a,232,9,2560
v_cvt_pk_bf16_f32 v64, v32, v33
v_cvt_pk_bf16_f32 v65, v34, v35
M a,36,a,244,v,240
T a,234,9,10752
v_cvt_pk_bf16_f32 v66, v36, v37
v_cvt_pk_bf16_f32 v67, v38, v39
s_mov_b32 s27, 0
buffer_store_dwordx4 v[64:67], v25, s[56:59], s27 offen sc1
T a,236,12,2560
W 7
A 48,16
A 49,17
M a,48,a,248,v,240
T a,238,12,10752
A 50,18
A 51,19
A 52,20
A 53,21
A 54,22
A 55,23
T a,240,9,3072
v_cvt_pk_bf16_f32 v72, v48, v49
v_cvt_pk_bf16_f32 v73, v50, v51
M a,52,a,252,v,240
T a,242,9,11264
v_cvt_pk_bf16_f32 v74, v52, v53
v_cvt_pk_bf16_f32 v75, v54, v55
s_mov_b32 s27, 2048
buffer_store_dwordx4 v[72:75], v25, s[56:59], s27 offen sc1
T a,244,12,3072
W 7
A 32,32
A 33,33
M a,64,a,224,v,240
T a,246,12,11264
A 34,34
A 35,35
A 36,36
A 37,37
A 38,38
A 39,39
T a,248,9,3584
v_cvt_pk_bf16_f32 v80, v32, v33
v_cvt_pk_bf16_f32 v81, v34, v35
M a,68,a,228,v,240
T a,250,9,11776
v_cvt_pk_bf16_f32 v82, v36, v37
v_cvt_pk_bf16_f32 v83, v38, v39
s_mov_b32 s27, 4096
buffer_store_dwordx4 v[80:83], v25, s[56:59], s27 offen sc1
T a,252,12,3584
W 7
A 48,48
A 49,49
M a,80,a,232,v,240
T a,254,12,11776
A 50,50
A 51,51
A 52,52
A 53,53
A 54,54
A 55,55
s_waitcnt vmcnt(14)
s_barrier
T a,224,9,16384
v_cvt_pk_bf16_f32 v88, v48, v49
v_cvt_pk_bf16_f32 v89, v50, v51
M a,84,a,236,v,240
T a,226,9,24576
v_cvt_pk_bf16_f32 v90, v52, v53
v_cvt_pk_bf16_f32 v91, v54, v55
s_mov_b32 s27, 6144
buffer_store_dwordx4 v[88:91], v25, s[56:59], s27 offen sc1
T a,228,12,16384
W 7
A 32,64
A 33,65
M a,96,a,240,v,240
T a,230,12,24576
A 34,66
A 35,67
A 36,68
A 37,69
A 38,70
A 39,71
T a,232,9,16896
v_cvt_pk_bf16_f32 v64, v32, v33
v_cvt_pk_bf16_f32 v65, v34, v35
M a,100,a,244,v,240
T a,234,9,25088
v_cvt_pk_bf16_f32 v66, v36, v37
v_cvt_pk_bf16_f32 v67, v38, v39
s_mov_b32 s27, 8192
buffer_store_dwordx4 v[64:67], v25, s[56:59], s27 offen sc1
T a,236,12,16896
W 7
A 48,80
A 49,81
M a,112,a,248,v,240
T a,238,12,25088
A 50,82
A 51,83
A 52,84
A 53,85
A 54,86
A 55,87
T a,240,9,17408
v_cvt_pk_bf16_f32 v72, v48, v49
v_cvt_pk_bf16_f32 v73, v50, v51
M a,116,a,252,v,240
T a,242,9,25600
v_cvt_pk_bf16_f32 v74, v52, v53
v_cvt_pk_bf16_f32 v75, v54, v55
s_mov_b32 s27, 10240
buffer_store_dwordx4 v[72:75], v25, s[56:59], s27 offen sc1
T a,244,12,17408
W 7
A 32,96
A 33,97
M a,128,a,224,v,224
T a,246,12,25600
A 34,98
A 35,99
A 36,100
A 37,101
A 38,102
A 39,103
T a,248,9,17920
v_cvt_pk_bf16_f32 v80, v32, v33
v_cvt_pk_bf16_f32 v81, v34, v35
M a,132,a,228,v,224
T a,250,9,26112
v_cvt_pk_bf16_f32 v82, v36, v37
v_cvt_pk_bf16_f32 v83, v38, v39
s_mov_b32 s27, 12288
buffer_store_dwordx4 v[80:83], v25, s[56:59], s27 offen sc1
T a,252,12,17920
W 7
A 48,112
A 49,113
M a,144,a,232,v,224
T a,254,12,26112
A 50,114
A 51,115
A 52,116
A 53,117
A 54,118
A 55,119
T a,224,9,18432
v_cvt_pk_bf16_f32 v88, v48, v49
v_cvt_pk_bf16_f32 v89, v50, v51
M a,148,a,236,v,224
T a,226,9,26624
v_cvt_pk_bf16_f32 v90, v52, v53
v_cvt_pk_bf16_f32 v91, v54, v55
s_mov_b32 s27, 14336
buffer_store_dwordx4 v[88:91], v25, s[56:59], s27 offen sc1
T a,228,12,18432
W 7
M a,160,a,240,v,224
T a,230,12,26624
T a,232,9,18944
M a,164,a,244,v,224
T a,234,9,27136
T a,236,12,18944
W 7
M a,176,a,248,v,224
T a,238,12,27136
s_waitcnt vmcnt(0)
s_barrier
T a,240,7,0
M a,180,a,252,v,224
T a,242,7,8192
T a,244,10,0
W 7
M a,192,a,224,v,224
T a,246,10,8192
T a,248,7,512
M a,196,a,228,v,224
T a,250,7,8704
T a,252,10,512
W 7
M a,208,a,232,v,224
T a,254,10,8704
T a,224,7,1024
M a,212,a,236,v,224
T a,226,7,9216
T a,228,10,1024
W 7
M a,128,a,240,v,240
T a,230,10,9216
T a,232,7,1536
M a,132,a,244,v,240
T a,234,7,9728
T a,236,10,1536
W 7
M a,144,a,248,v,240
T a,238,10,9728
T a,240,7,2048
M a,148,a,252,v,240
T a,242,7,10240
T a,244,10,2048
W 7
A 32,128
A 33,129
M a,160,a,224,v,240
T a,246,10,10240
A 34,130
A 35,131
A 36,132
A 37,133
A 38,134
A 39,135
T a,248,7,2560
v_cvt_pk_bf16_f32 v64, v32, v33
v_cvt_pk_bf16_f32 v65, v34, v35
M a,164,a,228,v,240
T a,250,7,10752
v_cvt_pk_bf16_f32 v66, v36, v37
v_cvt_pk_bf16_f32 v67, v38, v39
s_mov_b32 s27, 16384
buffer_store_dwordx4 v[64:67], v25, s[56:59], s27 offen sc1
T a,252,10,2560
W 7
A 48,144
A 49,145
M a,176,a,232,v,240
T a,254,10,10752
A 50,146
A 51,147
A 52,148
A 53,149
A 54,150
A 55,151
v_cvt_pk_bf16_f32 v72, v48, v49
v_cvt_pk_bf16_f32 v73, v50, v51
M a,180,a,236,v,240
v_cvt_pk_bf16_f32 v74, v52, v53
v_cvt_pk_bf16_f32 v75, v54, v55
s_mov_b32 s27, 18432
buffer_store_dwordx4 v[72:75], v25, s[56:59], s27 offen sc1
W 6
A 32,160
A 33,161
M a,192,a,240,v,240
A 34,162
A 35,163
A 36,164
A 37,165
A 38,166
A 39,167
W 4
v_cvt_pk_bf16_f32 v80, v32, v33
v_cvt_pk_bf16_f32 v81, v34, v35
M a,196,a,244,v,240
v_cvt_pk_bf16_f32 v82, v36, v37
v_cvt_pk_bf16_f32 v83, v38, v39
s_mov_b32 s27, 20480
buffer_store_dwordx4 v[80:83], v25, s[56:59], s27 offen sc1
W 2
A 48,176
A 49,177
M a,208,a,248,v,240
A 50,178
A 51,179
A 52,180
A 53,181
A 54,182
A 55,183
W 0
v_cvt_pk_bf16_f32 v88, v48, v49
v_cvt_pk_bf16_f32 v89, v50, v51
M a,212,a,252,v,240
v_cvt_pk_bf16_f32 v90, v52, v53
v_cvt_pk_bf16_f32 v91, v54, v55
s_mov_b32 s27, 22528
buffer_store_dwordx4 v[88:91], v25, s[56:59], s27 offen sc1
s_nop 7
A 32,192
A 33,193
A 34,194
A 35,195
A 36,196
A 37,197
A 38,198
A 39,199
v_cvt_pk_bf16_f32 v64, v32, v33
v_cvt_pk_bf16_f32 v65, v34, v35
v_cvt_pk_bf16_f32 v66, v36, v37
v_cvt_pk_bf16_f32 v67, v38, v39
s_mov_b32 s27, 24576
buffer_store_dwordx4 v[64:67], v25, s[56:59], s27 offen sc1
A 48,208
A 49,209
A 50,210
A 51,211
A 52,212
A 53,213
A 54,214
A 55,215
v_cvt_pk_bf16_f32 v72, v48, v49
v_cvt_pk_bf16_f32 v73, v50, v51
v_cvt_pk_bf16_f32 v74, v52, v53
v_cvt_pk_bf16_f32 v75, v54, v55
s_mov_b32 s27, 26624
buffer_store_dwordx4 v[72:75], v25, s[56:59], s27 offen sc1
s_branch .Lpf_end%=
.Lw3%=:
v_lshrrev_b32 v30, 5, v0
v_bfe_u32 v31, v0, 2, 2
v_xor_b32 v31, v31, v30
v_and_b32 v26, 7, v0
v_lshlrev_b32 v26, 6, v26
v_lshl_add_u32 v26, v31, 4, v26
v_bfe_u32 v27, v0, 4, 1
v_lshl_add_u32 v26, v27, 3, v26
v_bfe_u32 v31, v0, 3, 1
v_lshl_add_u32 v1, v31, 12, v26
v_xor_b32 v4, 32, v1
v_lshl_add_u32 v26, v31, 10, v26
v_add_u32 v13, s60, v26
v_xor_b32 v16, 32, v13
v_bfe_u32 v31, v0, 2, 3
v_lshlrev_b32 v31, 6, v31
v_lshl_add_u32 v31, v30, 12, v31
v_lshl_add_u32 v31, v27, 5, v31
v_and_b32 v26, 3, v0
v_lshl_add_u32 v7, v26, 3, v31
v_xor_b32 v10, 32, v7
v_add_u32 v2, 0x10000, v1
)x"
R"x(v_add_u32 v3, 0x20000, v1
v_add_u32 v5, 0x10000, v4
v_add_u32 v6, 0x20000, v4
v_add_u32 v14, 0x10000, v13
v_add_u32 v15, 0x20000, v13
v_add_u32 v17, 0x10000, v16
v_add_u32 v18, 0x20000, v16
v_add_u32 v8, 0x10000, v7
v_add_u32 v9, 0x20000, v7
v_add_u32 v11, 0x10000, v10
v_add_u32 v12, 0x20000, v10
s_barrier
R a,144,13,0
R a,146,16,0
R a,148,13,2048
R a,150,16,2048
T v,224,7,16384
T v,226,7,24576
T v,228,10,16384
T v,230,10,24576
T v,232,7,16896
T v,234,7,25088
T v,236,10,16896
T v,238,10,25088
T v,240,7,17408
T v,242,7,25600
T v,244,10,17408
W 7
v_mfma_f32_16x16x32_bf16 v[96:99], v[224:227], a[144:147], 0
T v,246,10,25600
v_mfma_f32_16x16x32_bf16 v[104:107], v[224:227], a[148:151], 0
T v,248,7,17920
v_mfma_f32_16x16x32_bf16 v[100:103], v[228:231], a[144:147], 0
T v,250,7,26112
v_mfma_f32_16x16x32_bf16 v[108:111], v[228:231], a[148:151], 0
T v,252,10,17920
W 7
v_mfma_f32_16x16x32_bf16 v[112:115], v[232:235], a[144:147], 0
T v,254,10,26112
v_mfma_f32_16x16x32_bf16 v[120:123], v[232:235], a[148:151], 0
T v,224,7,18432
v_mfma_f32_16x16x32_bf16 v[116:119], v[236:239], a[144:147], 0
T v,226,7,26624
v_mfma_f32_16x16x32_bf16 v[124:127], v[236:239], a[148:151], 0
T v,228,10,18432
W 7
v_mfma_f32_16x16x32_bf16 v[128:131], v[240:243], a[144:147], 0
T v,230,10,26624
v_mfma_f32_16x16x32_bf16 v[136:139], v[240:243], a[148:151], 0
T v,232,7,18944
v_mfma_f32_16x16x32_bf16 v[132:135], v[244:247], a[144:147], 0
T v,234,7,27136
v_mfma_f32_16x16x32_bf16 v[140:143], v[244:247], a[148:151], 0
T v,236,10,18944
W 7
v_mfma_f32_16x16x32_bf16 v[144:147], v[248:251], a[144:147], 0
T v,238,10,27136
v_mfma_f32_16x16x32_bf16 v[152:155], v[248:251], a[148:151], 0
T v,240,7,19456
v_mfma_f32_16x16x32_bf16 v[148:151], v[252:255], a[144:147], 0
T v,242,7,27648
v_mfma_f32_16x16x32_bf16 v[156:159], v[252:255], a[148:151], 0
T v,244,10,19456
W 7
v_mfma_f32_16x16x32_bf16 v[160:163], v[224:227], a[144:147], 0
T v,246,10,27648
v_mfma_f32_16x16x32_bf16 v[168:171], v[224:227], a[148:151], 0
T v,248,7,19968
v_mfma_f32_16x16x32_bf16 v[164:167], v[228:231], a[144:147], 0
T v,250,7,28160
v_mfma_f32_16x16x32_bf16 v[172:175], v[228:231], a[148:151], 0
T v,252,10,19968
W 7
v_mfma_f32_16x16x32_bf16 v[176:179], v[232:235], a[144:147], 0
T v,254,10,28160
v_mfma_f32_16x16x32_bf16 v[184:187], v[232:235], a[148:151], 0
s_barrier
R a,152,13,512
R a,154,16,512
R a,156,13,2560
R a,158,16,2560
T v,224,7,32768
v_mfma_f32_16x16x32_bf16 v[180:183], v[236:239], a[144:147], 0
T v,226,7,40960
v_mfma_f32_16x16x32_bf16 v[188:191], v[236:239], a[148:151], 0
T v,228,10,32768
W 11
v_mfma_f32_16x16x32_bf16 v[192:195], v[240:243], a[144:147], 0
T v,230,10,40960
v_mfma_f32_16x16x32_bf16 v[200:203], v[240:243], a[148:151], 0
T v,232,7,33280
v_mfma_f32_16x16x32_bf16 v[196:199], v[244:247], a[144:147], 0
T v,234,7,41472
v_mfma_f32_16x16x32_bf16 v[204:207], v[244:247], a[148:151], 0
T v,236,10,33280
W 11
v_mfma_f32_16x16x32_bf16 v[208:211], v[248:251], a[144:147], 0
T v,238,10,41472
v_mfma_f32_16x16x32_bf16 v[216:219], v[248:251], a[148:151], 0
T v,240,7,33792
v_mfma_f32_16x16x32_bf16 v[212:215], v[252:255], a[144:147], 0
T v,242,7,41984
v_mfma_f32_16x16x32_bf16 v[220:223], v[252:255], a[148:151], 0
T v,244,10,33792
W 7
M v,96,v,224,a,152
T v,246,10,41984
M v,104,v,224,a,156
T v,248,7,34304
M v,100,v,228,a,152
T v,250,7,42496
M v,108,v,228,a,156
T v,252,10,34304
W 7
M v,112,v,232,a,152
T v,254,10,42496
M v,120,v,232,a,156
T v,224,7,34816
M v,116,v,236,a,152
T v,226,7,43008
M v,124,v,236,a,156
T v,228,10,34816
W 7
M v,128,v,240,a,152
T v,230,10,43008
M v,136,v,240,a,156
T v,232,7,35328
M v,132,v,244,a,152
T v,234,7,43520
M v,140,v,244,a,156
T v,236,10,35328
W 7
M v,144,v,248,a,152
T v,238,10,43520
M v,152,v,248,a,156
T v,240,7,35840
M v,148,v,252,a,152
T v,242,7,44032
M v,156,v,252,a,156
T v,244,10,35840
W 7
M v,160,v,224,a,152
T v,246,10,44032
M v,168,v,224,a,156
T v,248,7,36352
M v,164,v,228,a,152
T v,250,7,44544
M v,172,v,228,a,156
T v,252,10,36352
W 7
M v,176,v,232,a,152
T v,254,10,44544
M v,184,v,232,a,156
s_barrier
R a,160,13,49152
R a,162,16,49152
R a,164,13,51200
R a,166,16,51200
T v,224,8,0
M v,180,v,236,a,152
T v,226,8,8192
M v,188,v,236,a,156
T v,228,11,0
W 11
M v,192,v,240,a,152
T v,230,11,8192
M v,200,v,240,a,156
T v,232,8,512
M v,196,v,244,a,152
T v,234,8,8704
M v,204,v,244,a,156
T v,236,11,512
W 11
M v,208,v,248,a,152
T v,238,11,8704
M v,216,v,248,a,156
T v,240,8,1024
M v,212,v,252,a,152
T v,242,8,9216
M v,220,v,252,a,156
T v,244,11,1024
W 7
M v,96,v,224,a,160
T v,246,11,9216
M v,104,v,224,a,164
T v,248,8,1536
M v,100,v,228,a,160
T v,250,8,9728
M v,108,v,228,a,164
T v,252,11,1536
W 7
M v,112,v,232,a,160
T v,254,11,9728
M v,120,v,232,a,164
T v,224,8,2048
M v,116,v,236,a,160
T v,226,8,10240
M v,124,v,236,a,164
T v,228,11,2048
W 7
M v,128,v,240,a,160
T v,230,11,10240
M v,136,v,240,a,164
T v,232,8,2560
M v,132,v,244,a,160
T v,234,8,10752
M v,140,v,244,a,164
T v,236,11,2560
W 7
M v,144,v,248,a,160
T v,238,11,10752
M v,152,v,248,a,164
T v,240,8,3072
M v,148,v,252,a,160
T v,242,8,11264
M v,156,v,252,a,164
T v,244,11,3072
W 7
M v,160,v,224,a,160
T v,246,11,11264
M v,168,v,224,a,164
T v,248,8,3584
M v,164,v,228,a,160
T v,250,8,11776
M v,172,v,228,a,164
T v,252,11,3584
W 7
M v,176,v,232,a,160
T v,254,11,11776
M v,184,v,232,a,164
s_barrier
R a,168,13,49664
R a,170,16,49664
R a,172,13,51712
R a,174,16,51712
T v,224,8,16384
M v,180,v,236,a,160
T v,226,8,24576
M v,188,v,236,a,164
T v,228,11,16384
W 11
M v,192,v,240,a,160
T v,230,11,24576
M v,200,v,240,a,164
T v,232,8,16896
M v,196,v,244,a,160
T v,234,8,25088
M v,204,v,244,a,164
T v,236,11,16896
W 11
M v,208,v,248,a,160
T v,238,11,25088
M v,216,v,248,a,164
T v,240,8,17408
M v,212,v,252,a,160
T v,242,8,25600
M v,220,v,252,a,164
T v,244,11,17408
W 7
M v,96,v,224,a,168
T v,246,11,25600
M v,104,v,224,a,172
T v,248,8,17920
M v,100,v,228,a,168
T v,250,8,26112
M v,108,v,228,a,172
T v,252,11,17920
W 7
M v,112,v,232,a,168
T v,254,11,26112
M v,120,v,232,a,172
T v,224,8,18432
M v,116,v,236,a,168
T v,226,8,26624
M v,124,v,236,a,172
T v,228,11,18432
W 7
M v,128,v,240,a,168
T v,230,11,26624
M v,136,v,240,a,172
T v,232,8,18944
M v,132,v,244,a,168
T v,234,8,27136
M v,140,v,244,a,172
T v,236,11,18944
W 7
M v,144,v,248,a,168
T v,238,11,27136
M v,152,v,248,a,172
T v,240,8,19456
M v,148,v,252,a,168
T v,242,8,27648
M v,156,v,252,a,172
T v,244,11,19456
W 7
M v,160,v,224,a,168
T v,246,11,27648
M v,168,v,224,a,172
T v,248,8,19968
M v,164,v,228,a,168
T v,250,8,28160
M v,172,v,228,a,172
T v,252,11,19968
W 7
M v,176,v,232,a,168
T v,254,11,28160
M v,184,v,232,a,172
s_barrier
R a,176,14,32768
R a,178,17,32768
R a,180,14,34816
R a,182,17,34816
T v,224,8,49152
M v,180,v,236,a,168
T v,226,8,57344
M v,188,v,236,a,172
T v,228,11,49152
W 11
M v,192,v,240,a,168
T v,230,11,57344
M v,200,v,240,a,172
T v,232,8,49664
M v,196,v,244,a,168
T v,234,8,57856
M v,204,v,244,a,172
T v,236,11,49664
W 11
M v,208,v,248,a,168
T v,238,11,57856
M v,216,v,248,a,172
T v,240,8,50176
M v,212,v,252,a,168
T v,242,8,58368
M v,220,v,252,a,172
T v,244,11,50176
W 7
M v,96,v,224,a,176
T v,246,11,58368
M v,104,v,224,a,180
T v,248,8,50688
M v,100,v,228,a,176
T v,250,8,58880
M v,108,v,228,a,180
T v,252,11,50688
W 7
M v,112,v,232,a,176
T v,254,11,58880
M v,120,v,232,a,180
T v,224,8,51200
M v,116,v,236,a,176
T v,226,8,59392
M v,124,v,236,a,180
T v,228,11,51200
W 7
M v,128,v,240,a,176
T v,230,11,59392
M v,136,v,240,a,180
T v,232,8,51712
M v,132,v,244,a,176
T v,234,8,59904
M v,140,v,244,a,180
T v,236,11,51712
W 7
M v,144,v,248,a,176
T v,238,11,59904
M v,152,v,248,a,180
T v,240,8,52224
M v,148,v,252,a,176
T v,242,8,60416
M v,156,v,252,a,180
T v,244,11,52224
W 7
M v,160,v,224,a,176
T v,246,11,60416
M v,168,v,224,a,180
T v,248,8,52736
M v,164,v,228,a,176
T v,250,8,60928
M v,172,v,228,a,180
T v,252,11,52736
W 7
M v,176,v,232,a,176
T v,254,11,60928
M v,184,v,232,a,180
s_barrier
R a,184,14,33280
R a,186,17,33280
R a,188,14,35328
R a,190,17,35328
T v,224,9,0
M v,180,v,236,a,176
T v,226,9,8192
M v,188,v,236,a,180
T v,228,12,0
W 11
M v,192,v,240,a,176
T v,230,12,8192
M v,200,v,240,a,180
T v,232,9,512
M v,196,v,244,a,176
)x"
R"x(T v,234,9,8704
M v,204,v,244,a,180
T v,236,12,512
W 11
M v,208,v,248,a,176
T v,238,12,8704
M v,216,v,248,a,180
T v,240,9,1024
M v,212,v,252,a,176
T v,242,9,9216
M v,220,v,252,a,180
T v,244,12,1024
W 7
M v,96,v,224,a,184
T v,246,12,9216
M v,104,v,224,a,188
T v,248,9,1536
M v,100,v,228,a,184
T v,250,9,9728
M v,108,v,228,a,188
T v,252,12,1536
W 7
M v,112,v,232,a,184
T v,254,12,9728
M v,120,v,232,a,188
T v,224,9,2048
M v,116,v,236,a,184
T v,226,9,10240
M v,124,v,236,a,188
T v,228,12,2048
W 7
M v,128,v,240,a,184
T v,230,12,10240
M v,136,v,240,a,188
T v,232,9,2560
M v,132,v,244,a,184
T v,234,9,10752
M v,140,v,244,a,188
T v,236,12,2560
W 7
M v,144,v,248,a,184
T v,238,12,10752
M v,152,v,248,a,188
T v,240,9,3072
M v,148,v,252,a,184
T v,242,9,11264
M v,156,v,252,a,188
T v,244,12,3072
W 7
M v,160,v,224,a,184
T v,246,12,11264
M v,168,v,224,a,188
T v,248,9,3584
M v,164,v,228,a,184
T v,250,9,11776
M v,172,v,228,a,188
T v,252,12,3584
W 7
M v,176,v,232,a,184
T v,254,12,11776
M v,184,v,232,a,188
s_barrier
R a,192,15,16384
R a,194,18,16384
R a,196,15,18432
R a,198,18,18432
T v,224,7,0
M v,180,v,236,a,184
T v,226,7,8192
M v,188,v,236,a,188
T v,228,10,0
W 11
M v,192,v,240,a,184
T v,230,10,8192
M v,200,v,240,a,188
T v,232,7,512
M v,196,v,244,a,184
T v,234,7,8704
M v,204,v,244,a,188
T v,236,10,512
W 11
M v,208,v,248,a,184
T v,238,10,8704
M v,216,v,248,a,188
T v,240,7,1024
M v,212,v,252,a,184
T v,242,7,9216
M v,220,v,252,a,188
T v,244,10,1024
W 7
M v,96,v,224,a,192
T v,246,10,9216
M v,104,v,224,a,196
T v,248,7,1536
M v,100,v,228,a,192
T v,250,7,9728
M v,108,v,228,a,196
T v,252,10,1536
W 7
M v,112,v,232,a,192
T v,254,10,9728
M v,120,v,232,a,196
T v,224,7,2048
M v,116,v,236,a,192
T v,226,7,10240
M v,124,v,236,a,196
T v,228,10,2048
W 7
M v,128,v,240,a,192
T v,230,10,10240
M v,136,v,240,a,196
T v,232,7,2560
M v,132,v,244,a,192
T v,234,7,10752
M v,140,v,244,a,196
T v,236,10,2560
W 7
M v,144,v,248,a,192
T v,238,10,10752
M v,152,v,248,a,196
T v,240,7,3072
M v,148,v,252,a,192
T v,242,7,11264
M v,156,v,252,a,196
T v,244,10,3072
W 7
M v,160,v,224,a,192
T v,246,10,11264
M v,168,v,224,a,196
T v,248,7,3584
M v,164,v,228,a,192
T v,250,7,11776
M v,172,v,228,a,196
T v,252,10,3584
W 7
M v,176,v,232,a,192
T v,254,10,11776
M v,184,v,232,a,196
s_barrier
R a,200,15,16896
R a,202,18,16896
R a,204,15,18944
R a,206,18,18944
T v,224,7,16384
M v,180,v,236,a,192
T v,226,7,24576
M v,188,v,236,a,196
T v,228,10,16384
W 11
M v,192,v,240,a,192
T v,230,10,24576
M v,200,v,240,a,196
T v,232,7,16896
M v,196,v,244,a,192
T v,234,7,25088
M v,204,v,244,a,196
T v,236,10,16896
W 11
M v,208,v,248,a,192
T v,238,10,25088
M v,216,v,248,a,196
T v,240,7,17408
M v,212,v,252,a,192
T v,242,7,25600
M v,220,v,252,a,196
T v,244,10,17408
W 7
M v,96,v,224,a,200
T v,246,10,25600
M v,104,v,224,a,204
T v,248,7,17920
M v,100,v,228,a,200
T v,250,7,26112
M v,108,v,228,a,204
T v,252,10,17920
W 7
M v,112,v,232,a,200
T v,254,10,26112
M v,120,v,232,a,204
T v,224,7,18432
M v,116,v,236,a,200
T v,226,7,26624
M v,124,v,236,a,204
T v,228,10,18432
W 7
M v,128,v,240,a,200
T v,230,10,26624
M v,136,v,240,a,204
T v,232,7,18944
M v,132,v,244,a,200
T v,234,7,27136
M v,140,v,244,a,204
T v,236,10,18944
W 7
M v,144,v,248,a,200
T v,238,10,27136
M v,152,v,248,a,204
T v,240,7,19456
M v,148,v,252,a,200
T v,242,7,27648
M v,156,v,252,a,204
T v,244,10,19456
W 7
M v,160,v,224,a,200
T v,246,10,27648
M v,168,v,224,a,204
T v,248,7,19968
M v,164,v,228,a,200
T v,250,7,28160
M v,172,v,228,a,204
T v,252,10,19968
W 7
M v,176,v,232,a,200
T v,254,10,28160
M v,184,v,232,a,204
s_barrier
R a,208,13,32768
R a,210,16,32768
R a,212,13,34816
R a,214,16,34816
T v,224,7,49152
M v,180,v,236,a,200
T v,226,7,57344
M v,188,v,236,a,204
T v,228,10,49152
W 11
M v,192,v,240,a,200
T v,230,10,57344
M v,200,v,240,a,204
T v,232,7,49664
M v,196,v,244,a,200
T v,234,7,57856
M v,204,v,244,a,204
T v,236,10,49664
W 11
M v,208,v,248,a,200
T v,238,10,57856
M v,216,v,248,a,204
T v,240,7,50176
M v,212,v,252,a,200
T v,242,7,58368
M v,220,v,252,a,204
T v,244,10,50176
W 7
M v,96,v,224,a,208
T v,246,10,58368
M v,104,v,224,a,212
T v,248,7,50688
M v,100,v,228,a,208
T v,250,7,58880
M v,108,v,228,a,212
T v,252,10,50688
W 7
M v,112,v,232,a,208
T v,254,10,58880
M v,120,v,232,a,212
T v,224,7,51200
M v,116,v,236,a,208
T v,226,7,59392
M v,124,v,236,a,212
T v,228,10,51200
W 7
M v,128,v,240,a,208
T v,230,10,59392
M v,136,v,240,a,212
T v,232,7,51712
M v,132,v,244,a,208
T v,234,7,59904
M v,140,v,244,a,212
T v,236,10,51712
W 7
M v,144,v,248,a,208
T v,238,10,59904
M v,152,v,248,a,212
T v,240,7,52224
M v,148,v,252,a,208
T v,242,7,60416
M v,156,v,252,a,212
T v,244,10,52224
W 7
M v,160,v,224,a,208
T v,246,10,60416
M v,168,v,224,a,212
T v,248,7,52736
M v,164,v,228,a,208
T v,250,7,60928
M v,172,v,228,a,212
T v,252,10,52736
W 7
M v,176,v,232,a,208
T v,254,10,60928
M v,184,v,232,a,212
s_barrier
R a,216,13,33280
R a,218,16,33280
R a,220,13,35328
R a,222,16,35328
T v,224,8,0
M v,180,v,236,a,208
T v,226,8,8192
M v,188,v,236,a,212
T v,228,11,0
W 11
M v,192,v,240,a,208
T v,230,11,8192
M v,200,v,240,a,212
T v,232,8,512
M v,196,v,244,a,208
T v,234,8,8704
M v,204,v,244,a,212
T v,236,11,512
W 11
M v,208,v,248,a,208
T v,238,11,8704
M v,216,v,248,a,212
T v,240,8,1024
M v,212,v,252,a,208
T v,242,8,9216
M v,220,v,252,a,212
T v,244,11,1024
W 7
M v,96,v,224,a,216
T v,246,11,9216
M v,104,v,224,a,220
T v,248,8,1536
M v,100,v,228,a,216
T v,250,8,9728
M v,108,v,228,a,220
T v,252,11,1536
W 7
M v,112,v,232,a,216
T v,254,11,9728
M v,120,v,232,a,220
T v,224,8,2048
M v,116,v,236,a,216
T v,226,8,10240
M v,124,v,236,a,220
T v,228,11,2048
W 7
M v,128,v,240,a,216
T v,230,11,10240
M v,136,v,240,a,220
T v,232,8,2560
M v,132,v,244,a,216
T v,234,8,10752
M v,140,v,244,a,220
T v,236,11,2560
W 7
M v,144,v,248,a,216
T v,238,11,10752
M v,152,v,248,a,220
T v,240,8,3072
M v,148,v,252,a,216
T v,242,8,11264
M v,156,v,252,a,220
T v,244,11,3072
W 7
M v,160,v,224,a,216
T v,246,11,11264
M v,168,v,224,a,220
T v,248,8,3584
M v,164,v,228,a,216
T v,250,8,11776
M v,172,v,228,a,220
T v,252,11,3584
W 7
M v,176,v,232,a,216
T v,254,11,11776
M v,184,v,232,a,220
s_barrier
.Lc0_22_%=:
R a,224,14,16384
R a,226,17,16384
R a,228,14,18432
R a,230,17,18432
T v,224,8,32768
M v,180,v,236,a,216
T v,226,8,40960
M v,188,v,236,a,220
T v,228,11,32768
W 11
M v,192,v,240,a,216
T v,230,11,40960
M v,200,v,240,a,220
T v,232,8,33280
M v,196,v,244,a,216
T v,234,8,41472
M v,204,v,244,a,220
T v,236,11,33280
W 11
M v,208,v,248,a,216
T v,238,11,41472
M v,216,v,248,a,220
T v,240,8,33792
M v,212,v,252,a,216
T v,242,8,41984
M v,220,v,252,a,220
T v,244,11,33792
W 7
M v,96,v,224,a,224
)x"
R"x(T v,246,11,41984
M v,104,v,224,a,228
T v,248,8,34304
M v,100,v,228,a,224
T v,250,8,42496
M v,108,v,228,a,228
T v,252,11,34304
W 7
M v,112,v,232,a,224
T v,254,11,42496
M v,120,v,232,a,228
T v,224,8,34816
M v,116,v,236,a,224
T v,226,8,43008
M v,124,v,236,a,228
T v,228,11,34816
W 7
M v,128,v,240,a,224
T v,230,11,43008
M v,136,v,240,a,228
T v,232,8,35328
M v,132,v,244,a,224
T v,234,8,43520
M v,140,v,244,a,228
T v,236,11,35328
W 7
M v,144,v,248,a,224
T v,238,11,43520
M v,152,v,248,a,228
T v,240,8,35840
M v,148,v,252,a,224
T v,242,8,44032
M v,156,v,252,a,228
T v,244,11,35840
W 7
M v,160,v,224,a,224
T v,246,11,44032
M v,168,v,224,a,228
T v,248,8,36352
M v,164,v,228,a,224
T v,250,8,44544
M v,172,v,228,a,228
T v,252,11,36352
W 7
M v,176,v,232,a,224
T v,254,11,44544
M v,184,v,232,a,228
s_barrier
R a,232,14,16896
R a,234,17,16896
R a,236,14,18944
R a,238,17,18944
T v,224,8,49152
M v,180,v,236,a,224
T v,226,8,57344
M v,188,v,236,a,228
T v,228,11,49152
W 11
M v,192,v,240,a,224
T v,230,11,57344
M v,200,v,240,a,228
T v,232,8,49664
M v,196,v,244,a,224
T v,234,8,57856
M v,204,v,244,a,228
T v,236,11,49664
W 11
M v,208,v,248,a,224
T v,238,11,57856
M v,216,v,248,a,228
T v,240,8,50176
M v,212,v,252,a,224
T v,242,8,58368
M v,220,v,252,a,228
T v,244,11,50176
W 7
M v,96,v,224,a,232
T v,246,11,58368
M v,104,v,224,a,236
T v,248,8,50688
M v,100,v,228,a,232
T v,250,8,58880
M v,108,v,228,a,236
T v,252,11,50688
W 7
M v,112,v,232,a,232
T v,254,11,58880
M v,120,v,232,a,236
T v,224,8,51200
M v,116,v,236,a,232
T v,226,8,59392
M v,124,v,236,a,236
T v,228,11,51200
W 7
M v,128,v,240,a,232
T v,230,11,59392
M v,136,v,240,a,236
T v,232,8,51712
M v,132,v,244,a,232
T v,234,8,59904
M v,140,v,244,a,236
T v,236,11,51712
W 7
M v,144,v,248,a,232
T v,238,11,59904
M v,152,v,248,a,236
T v,240,8,52224
M v,148,v,252,a,232
T v,242,8,60416
M v,156,v,252,a,236
T v,244,11,52224
W 7
M v,160,v,224,a,232
T v,246,11,60416
M v,168,v,224,a,236
T v,248,8,52736
M v,164,v,228,a,232
T v,250,8,60928
M v,172,v,228,a,236
T v,252,11,52736
W 7
M v,176,v,232,a,232
T v,254,11,60928
M v,184,v,232,a,236
s_barrier
R a,240,15,0
R a,242,18,0
R a,244,15,2048
R a,246,18,2048
T v,224,9,16384
M v,180,v,236,a,232
T v,226,9,24576
M v,188,v,236,a,236
T v,228,12,16384
W 11
M v,192,v,240,a,232
T v,230,12,24576
M v,200,v,240,a,236
T v,232,9,16896
M v,196,v,244,a,232
T v,234,9,25088
M v,204,v,244,a,236
T v,236,12,16896
W 11
M v,208,v,248,a,232
T v,238,12,25088
M v,216,v,248,a,236
T v,240,9,17408
M v,212,v,252,a,232
T v,242,9,25600
M v,220,v,252,a,236
T v,244,12,17408
W 7
M v,96,v,224,a,240
T v,246,12,25600
M v,104,v,224,a,244
T v,248,9,17920
M v,100,v,228,a,240
T v,250,9,26112
M v,108,v,228,a,244
T v,252,12,17920
W 7
M v,112,v,232,a,240
T v,254,12,26112
M v,120,v,232,a,244
T v,224,9,18432
M v,116,v,236,a,240
T v,226,9,26624
M v,124,v,236,a,244
T v,228,12,18432
W 7
M v,128,v,240,a,240
T v,230,12,26624
M v,136,v,240,a,244
T v,232,9,18944
M v,132,v,244,a,240
T v,234,9,27136
M v,140,v,244,a,244
T v,236,12,18944
W 7
M v,144,v,248,a,240
T v,238,12,27136
M v,152,v,248,a,244
T v,240,9,19456
M v,148,v,252,a,240
T v,242,9,27648
M v,156,v,252,a,244
T v,244,12,19456
W 7
M v,160,v,224,a,240
T v,246,12,27648
M v,168,v,224,a,244
T v,248,9,19968
M v,164,v,228,a,240
T v,250,9,28160
M v,172,v,228,a,244
T v,252,12,19968
W 7
M v,176,v,232,a,240
T v,254,12,28160
M v,184,v,232,a,244
s_barrier
R a,248,15,512
R a,250,18,512
R a,252,15,2560
R a,254,18,2560
T v,224,7,0
M v,180,v,236,a,240
T v,226,7,8192
M v,188,v,236,a,244
T v,228,10,0
W 11
M v,192,v,240,a,240
T v,230,10,8192
M v,200,v,240,a,244
T v,232,7,512
M v,196,v,244,a,240
T v,234,7,8704
M v,204,v,244,a,244
T v,236,10,512
W 11
M v,208,v,248,a,240
T v,238,10,8704
M v,216,v,248,a,244
T v,240,7,1024
M v,212,v,252,a,240
T v,242,7,9216
M v,220,v,252,a,244
T v,244,10,1024
W 7
M v,96,v,224,a,248
T v,246,10,9216
M v,104,v,224,a,252
T v,248,7,1536
M v,100,v,228,a,248
T v,250,7,9728
M v,108,v,228,a,252
T v,252,10,1536
W 7
M v,112,v,232,a,248
T v,254,10,9728
M v,120,v,232,a,252
T v,224,7,2048
M v,116,v,236,a,248
T v,226,7,10240
M v,124,v,236,a,252
T v,228,10,2048
W 7
M v,128,v,240,a,248
T v,230,10,10240
M v,136,v,240,a,252
T v,232,7,2560
M v,132,v,244,a,248
T v,234,7,10752
M v,140,v,244,a,252
T v,236,10,2560
W 7
M v,144,v,248,a,248
T v,238,10,10752
M v,152,v,248,a,252
T v,240,7,3072
M v,148,v,252,a,248
T v,242,7,11264
M v,156,v,252,a,252
T v,244,10,3072
W 7
M v,160,v,224,a,248
T v,246,10,11264
M v,168,v,224,a,252
T v,248,7,3584
M v,164,v,228,a,248
T v,250,7,11776
M v,172,v,228,a,252
T v,252,10,3584
W 7
M v,176,v,232,a,248
T v,254,10,11776
M v,184,v,232,a,252
s_barrier
.Lc0_28_%=:
R v,224,1,16384
M v,180,v,236,a,248
R v,226,4,16384
M v,188,v,236,a,252
R v,228,1,24576
W 7
M v,192,v,240,a,248
R v,230,4,24576
M v,200,v,240,a,252
R v,232,1,16896
M v,196,v,244,a,248
R v,234,4,16896
M v,204,v,244,a,252
R v,236,1,25088
W 7
M v,208,v,248,a,248
R v,238,4,25088
M v,216,v,248,a,252
R v,240,1,17408
M v,212,v,252,a,248
R v,242,4,17408
M v,220,v,252,a,252
s_nop 7
v_cvt_pk_bf16_f32 v32, v96, v97
v_cvt_pk_bf16_f32 v33, v98, v99
v_cvt_pk_bf16_f32 v34, v100, v101
v_cvt_pk_bf16_f32 v35, v102, v103
v_cvt_pk_bf16_f32 v36, v104, v105
v_cvt_pk_bf16_f32 v37, v106, v107
v_cvt_pk_bf16_f32 v38, v108, v109
v_cvt_pk_bf16_f32 v39, v110, v111
v_cvt_pk_bf16_f32 v40, v112, v113
v_cvt_pk_bf16_f32 v41, v114, v115
v_cvt_pk_bf16_f32 v42, v116, v117
v_cvt_pk_bf16_f32 v43, v118, v119
v_cvt_pk_bf16_f32 v44, v120, v121
v_cvt_pk_bf16_f32 v45, v122, v123
v_cvt_pk_bf16_f32 v46, v124, v125
v_cvt_pk_bf16_f32 v47, v126, v127
v_cvt_pk_bf16_f32 v48, v128, v129
v_cvt_pk_bf16_f32 v49, v130, v131
v_cvt_pk_bf16_f32 v50, v132, v133
v_cvt_pk_bf16_f32 v51, v134, v135
v_cvt_pk_bf16_f32 v52, v136, v137
v_cvt_pk_bf16_f32 v53, v138, v139
v_cvt_pk_bf16_f32 v54, v140, v141
v_cvt_pk_bf16_f32 v55, v142, v143
v_cvt_pk_bf16_f32 v56, v144, v145
v_cvt_pk_bf16_f32 v57, v146, v147
v_cvt_pk_bf16_f32 v58, v148, v149
v_cvt_pk_bf16_f32 v59, v150, v151
v_cvt_pk_bf16_f32 v60, v152, v153
v_cvt_pk_bf16_f32 v61, v154, v155
v_cvt_pk_bf16_f32 v62, v156, v157
v_cvt_pk_bf16_f32 v63, v158, v159
v_cvt_pk_bf16_f32 v64, v160, v161
v_cvt_pk_bf16_f32 v65, v162, v163
v_cvt_pk_bf16_f32 v66, v164, v165
v_cvt_pk_bf16_f32 v67, v166, v167
v_cvt_pk_bf16_f32 v68, v168, v169
v_cvt_pk_bf16_f32 v69, v170, v171
v_cvt_pk_bf16_f32 v70, v172, v173
v_cvt_pk_bf16_f32 v71, v174, v175
v_cvt_pk_bf16_f32 v72, v176, v177
v_cvt_pk_bf16_f32 v73, v178, v179
v_cvt_pk_bf16_f32 v74, v180, v181
v_cvt_pk_bf16_f32 v75, v182, v183
v_cvt_pk_bf16_f32 v76, v184, v185
v_cvt_pk_bf16_f32 v77, v186, v187
v_cvt_pk_bf16_f32 v78, v188, v189
v_cvt_pk_bf16_f32 v79, v190, v191
v_cvt_pk_bf16_f32 v80, v192, v193
v_cvt_pk_bf16_f32 v81, v194, v195
v_cvt_pk_bf16_f32 v82, v196, v197
v_cvt_pk_bf16_f32 v83, v198, v199
v_cvt_pk_bf16_f32 v84, v200, v201
v_cvt_pk_bf16_f32 v85, v202, v203
v_cvt_pk_bf16_f32 v86, v204, v205
v_cvt_pk_bf16_f32 v87, v206, v207
v_cvt_pk_bf16_f32 v88, v208, v209
v_cvt_pk_bf16_f32 v89, v210, v211
v_cvt_pk_bf16_f32 v90, v212, v213
v_cvt_pk_bf16_f32 v91, v214, v215
v_cvt_pk_bf16_f32 v92, v216, v217
v_cvt_pk_bf16_f32 v93, v218, v219
v_cvt_pk_bf16_f32 v94, v220, v221
v_cvt_pk_bf16_f32 v95, v222, v223
s_nop 1
R v,244,1,25600
W 7
v_mfma_f32_16x16x32_bf16 a[0:3], v[224:227], v[32:35], 0
R v,246,4,25600
v_mfma_f32_16x16x32_bf16 a[8:11], v[224:227], v[36:39], 0
R v,248,1,17920
v_mfma_f32_16x16x32_bf16 a[4:7], v[228:231], v[32:35], 0
R v,250,4,17920
v_mfma_f32_16x16x32_bf16 a[12:15], v[228:231], v[36:39], 0
R v,252,1,26112
W 7
M a,0,v,232,v,40
R v,254,4,26112
M a,8,v,232,v,44
R v,224,1,18432
M a,4,v,236,v,40
R v,226,4,18432
M a,12,v,236,v,44
R v,228,1,26624
W 7
M a,0,v,240,v,48
R v,230,4,26624
M a,8,v,240,v,52
R v,232,1,18944
M a,4,v,244,v,48
R v,234,4,18944
M a,12,v,244,v,52
R v,236,1,27136
W 7
M a,0,v,248,v,56
R v,238,4,27136
M a,8,v,248,v,60
)x"
R"x(R v,240,1,19456
M a,4,v,252,v,56
R v,242,4,19456
M a,12,v,252,v,60
R v,244,1,27648
W 7
M a,0,v,224,v,64
R v,246,4,27648
M a,8,v,224,v,68
R v,248,1,19968
M a,4,v,228,v,64
R v,250,4,19968
M a,12,v,228,v,68
R v,252,1,28160
W 7
M a,0,v,232,v,72
R v,254,4,28160
M a,8,v,232,v,76
s_barrier
T v,224,7,32768
M a,4,v,236,v,72
T v,226,7,40960
M a,12,v,236,v,76
T v,228,10,32768
W 7
M a,0,v,240,v,80
T v,230,10,40960
M a,8,v,240,v,84
T v,232,7,33280
M a,4,v,244,v,80
T v,234,7,41472
M a,12,v,244,v,84
T v,236,10,33280
W 7
M a,0,v,248,v,88
T v,238,10,41472
M a,8,v,248,v,92
T v,240,7,33792
M a,4,v,252,v,88
T v,242,7,41984
M a,12,v,252,v,92
T v,244,10,33792
W 7
v_mfma_f32_16x16x32_bf16 v[96:99], v[224:227], a[144:147], 0
T v,246,10,41984
v_mfma_f32_16x16x32_bf16 v[104:107], v[224:227], a[148:151], 0
T v,248,7,34304
v_mfma_f32_16x16x32_bf16 v[100:103], v[228:231], a[144:147], 0
T v,250,7,42496
v_mfma_f32_16x16x32_bf16 v[108:111], v[228:231], a[148:151], 0
T v,252,10,34304
W 7
v_mfma_f32_16x16x32_bf16 v[112:115], v[232:235], a[144:147], 0
T v,254,10,42496
v_mfma_f32_16x16x32_bf16 v[120:123], v[232:235], a[148:151], 0
T v,224,7,34816
A 30,0
A 31,1
v_mfma_f32_16x16x32_bf16 v[116:119], v[236:239], a[144:147], 0
T v,226,7,43008
v_max_f32 v26, v30, v31
v_mfma_f32_16x16x32_bf16 v[124:127], v[236:239], a[148:151], 0
T v,228,10,34816
W 7
A 30,2
v_mfma_f32_16x16x32_bf16 v[128:131], v[240:243], a[144:147], 0
T v,230,10,43008
A 31,3
v_mfma_f32_16x16x32_bf16 v[136:139], v[240:243], a[148:151], 0
T v,232,7,35328
v_max3_f32 v26, v30, v31, v26
A 30,4
v_mfma_f32_16x16x32_bf16 v[132:135], v[244:247], a[144:147], 0
T v,234,7,43520
A 31,5
v_mfma_f32_16x16x32_bf16 v[140:143], v[244:247], a[148:151], 0
T v,236,10,35328
W 7
v_max3_f32 v26, v30, v31, v26
v_mfma_f32_16x16x32_bf16 v[144:147], v[248:251], a[144:147], 0
T v,238,10,43520
A 30,6
v_mfma_f32_16x16x32_bf16 v[152:155], v[248:251], a[148:151], 0
T v,240,7,35840
A 31,7
v_max3_f32 v26, v30, v31, v26
v_mfma_f32_16x16x32_bf16 v[148:151], v[252:255], a[144:147], 0
T v,242,7,44032
A 30,8
v_mfma_f32_16x16x32_bf16 v[156:159], v[252:255], a[148:151], 0
T v,244,10,35840
W 7
v_mfma_f32_16x16x32_bf16 v[160:163], v[224:227], a[144:147], 0
T v,246,10,44032
A 31,9
v_mfma_f32_16x16x32_bf16 v[168:171], v[224:227], a[148:151], 0
T v,248,7,36352
v_max_f32 v27, v30, v31
v_mfma_f32_16x16x32_bf16 v[164:167], v[228:231], a[144:147], 0
T v,250,7,44544
A 30,10
v_mfma_f32_16x16x32_bf16 v[172:175], v[228:231], a[148:151], 0
T v,252,10,36352
W 7
v_mfma_f32_16x16x32_bf16 v[176:179], v[232:235], a[144:147], 0
T v,254,10,44544
A 31,11
v_mfma_f32_16x16x32_bf16 v[184:187], v[232:235], a[148:151], 0
s_barrier
R v,224,1,49152
v_max3_f32 v27, v30, v31, v27
v_mfma_f32_16x16x32_bf16 v[180:183], v[236:239], a[144:147], 0
R v,226,4,49152
A 30,12
v_mfma_f32_16x16x32_bf16 v[188:191], v[236:239], a[148:151], 0
R v,228,1,57344
W 7
v_mfma_f32_16x16x32_bf16 v[192:195], v[240:243], a[144:147], 0
R v,230,4,57344
A 31,13
v_mfma_f32_16x16x32_bf16 v[200:203], v[240:243], a[148:151], 0
R v,232,1,49664
v_max3_f32 v27, v30, v31, v27
v_mfma_f32_16x16x32_bf16 v[196:199], v[244:247], a[144:147], 0
R v,234,4,49664
A 30,14
v_mfma_f32_16x16x32_bf16 v[204:207], v[244:247], a[148:151], 0
R v,236,1,57856
W 7
v_mfma_f32_16x16x32_bf16 v[208:211], v[248:251], a[144:147], 0
R v,238,4,57856
A 31,15
v_mfma_f32_16x16x32_bf16 v[216:219], v[248:251], a[148:151], 0
R v,240,1,50176
v_max3_f32 v27, v30, v31, v27
v_mfma_f32_16x16x32_bf16 v[212:215], v[252:255], a[144:147], 0
R v,242,4,50176
v_mfma_f32_16x16x32_bf16 v[220:223], v[252:255], a[148:151], 0
R v,244,1,58368
W 7
v_mfma_f32_16x16x32_bf16 a[16:19], v[224:227], v[32:35], 0
R v,246,4,58368
v_mfma_f32_16x16x32_bf16 a[24:27], v[224:227], v[36:39], 0
R v,248,1,50688
v_mfma_f32_16x16x32_bf16 a[20:23], v[228:231], v[32:35], 0
R v,250,4,50688
v_mfma_f32_16x16x32_bf16 a[28:31], v[228:231], v[36:39], 0
R v,252,1,58880
W 7
M a,16,v,232,v,40
R v,254,4,58880
M a,24,v,232,v,44
R v,224,1,51200
M a,20,v,236,v,40
R v,226,4,51200
M a,28,v,236,v,44
R v,228,1,59392
W 7
M a,16,v,240,v,48
R v,230,4,59392
M a,24,v,240,v,52
R v,232,1,51712
M a,20,v,244,v,48
R v,234,4,51712
M a,28,v,244,v,52
R v,236,1,59904
W 7
M a,16,v,248,v,56
R v,238,4,59904
M a,24,v,248,v,60
R v,240,1,52224
M a,20,v,252,v,56
R v,242,4,52224
M a,28,v,252,v,60
R v,244,1,60416
W 7
M a,16,v,224,v,64
R v,246,4,60416
M a,24,v,224,v,68
R v,248,1,52736
M a,20,v,228,v,64
R v,250,4,52736
M a,28,v,228,v,68
R v,252,1,60928
W 7
M a,16,v,232,v,72
R v,254,4,60928
M a,24,v,232,v,76
s_barrier
T v,224,8,0
M a,20,v,236,v,72
T v,226,8,8192
M a,28,v,236,v,76
T v,228,11,0
W 7
M a,16,v,240,v,80
T v,230,11,8192
M a,24,v,240,v,84
T v,232,8,512
M a,20,v,244,v,80
T v,234,8,8704
M a,28,v,244,v,84
T v,236,11,512
W 7
M a,16,v,248,v,88
T v,238,11,8704
M a,24,v,248,v,92
T v,240,8,1024
M a,20,v,252,v,88
T v,242,8,9216
M a,28,v,252,v,92
T v,244,11,1024
W 7
M v,96,v,224,a,152
T v,246,11,9216
M v,104,v,224,a,156
T v,248,8,1536
M v,100,v,228,a,152
T v,250,8,9728
M v,108,v,228,a,156
T v,252,11,1536
W 7
M v,112,v,232,a,152
T v,254,11,9728
M v,120,v,232,a,156
T v,224,8,2048
A 30,16
A 31,17
M v,116,v,236,a,152
T v,226,8,10240
v_max3_f32 v26, v30, v31, v26
M v,124,v,236,a,156
T v,228,11,2048
W 7
A 30,18
M v,128,v,240,a,152
T v,230,11,10240
A 31,19
M v,136,v,240,a,156
T v,232,8,2560
v_max3_f32 v26, v30, v31, v26
A 30,20
M v,132,v,244,a,152
T v,234,8,10752
A 31,21
M v,140,v,244,a,156
T v,236,11,2560
W 7
v_max3_f32 v26, v30, v31, v26
M v,144,v,248,a,152
T v,238,11,10752
A 30,22
M v,152,v,248,a,156
T v,240,8,3072
A 31,23
v_max3_f32 v26, v30, v31, v26
M v,148,v,252,a,152
T v,242,8,11264
A 30,24
M v,156,v,252,a,156
T v,244,11,3072
W 7
M v,160,v,224,a,152
T v,246,11,11264
A 31,25
M v,168,v,224,a,156
T v,248,8,3584
v_max3_f32 v27, v30, v31, v27
M v,164,v,228,a,152
T v,250,8,11776
A 30,26
M v,172,v,228,a,156
T v,252,11,3584
W 7
M v,176,v,232,a,152
T v,254,11,11776
A 31,27
M v,184,v,232,a,156
s_barrier
R v,224,2,16384
v_max3_f32 v27, v30, v31, v27
M v,180,v,236,a,152
R v,226,5,16384
A 30,28
M v,188,v,236,a,156
R v,228,2,24576
W 7
M v,192,v,240,a,152
R v,230,5,24576
A 31,29
M v,200,v,240,a,156
R v,232,2,16896
v_max3_f32 v27, v30, v31, v27
M v,196,v,244,a,152
R v,234,5,16896
A 30,30
M v,204,v,244,a,156
R v,236,2,25088
W 7
M v,208,v,248,a,152
R v,238,5,25088
A 31,31
M v,216,v,248,a,156
R v,240,2,17408
v_max3_f32 v27, v30, v31, v27
M v,212,v,252,a,152
R v,242,5,17408
M v,220,v,252,a,156
R v,244,2,25600
W 7
v_mfma_f32_16x16x32_bf16 a[32:35], v[224:227], v[32:35], 0
R v,246,5,25600
v_mfma_f32_16x16x32_bf16 a[40:43], v[224:227], v[36:39], 0
R v,248,2,17920
v_mfma_f32_16x16x32_bf16 a[36:39], v[228:231], v[32:35], 0
R v,250,5,17920
v_mfma_f32_16x16x32_bf16 a[44:47], v[228:231], v[36:39], 0
R v,252,2,26112
W 7
M a,32,v,232,v,40
R v,254,5,26112
M a,40,v,232,v,44
R v,224,2,18432
M a,36,v,236,v,40
R v,226,5,18432
M a,44,v,236,v,44
R v,228,2,26624
W 7
M a,32,v,240,v,48
R v,230,5,26624
M a,40,v,240,v,52
R v,232,2,18944
M a,36,v,244,v,48
R v,234,5,18944
M a,44,v,244,v,52
R v,236,2,27136
W 7
M a,32,v,248,v,56
R v,238,5,27136
M a,40,v,248,v,60
R v,240,2,19456
M a,36,v,252,v,56
R v,242,5,19456
M a,44,v,252,v,60
R v,244,2,27648
W 7
M a,32,v,224,v,64
R v,246,5,27648
M a,40,v,224,v,68
R v,248,2,19968
M a,36,v,228,v,64
R v,250,5,19968
M a,44,v,228,v,68
R v,252,2,28160
W 7
M a,32,v,232,v,72
R v,254,5,28160
M a,40,v,232,v,76
s_barrier
.Lc0_34_%=:
T v,224,8,32768
M a,36,v,236,v,72
T v,226,8,40960
M a,44,v,236,v,76
T v,228,11,32768
W 7
M a,32,v,240,v,80
T v,230,11,40960
M a,40,v,240,v,84
T v,232,8,33280
M a,36,v,244,v,80
T v,234,8,41472
M a,44,v,244,v,84
T v,236,11,33280
W 7
M a,32,v,248,v,88
T v,238,11,41472
M a,40,v,248,v,92
T v,240,8,33792
M a,36,v,252,v,88
T v,242,8,41984
M a,44,v,252,v,92
T v,244,11,33792
W 7
M v,96,v,224,a,160
T v,246,11,41984
M v,104,v,224,a,164
T v,248,8,34304
M v,100,v,228,a,160
T v,250,8,42496
M v,108,v,228,a,164
T v,252,11,34304
W 7
M v,112,v,232,a,160
T v,254,11,42496
M v,120,v,232,a,164
T v,224,8,34816
A 30,32
A 31,33
M v,116,v,236,a,160
)x"
R"x(T v,226,8,43008
v_max3_f32 v26, v30, v31, v26
M v,124,v,236,a,164
T v,228,11,34816
W 7
A 30,34
M v,128,v,240,a,160
T v,230,11,43008
A 31,35
M v,136,v,240,a,164
T v,232,8,35328
v_max3_f32 v26, v30, v31, v26
A 30,36
M v,132,v,244,a,160
T v,234,8,43520
A 31,37
M v,140,v,244,a,164
T v,236,11,35328
W 7
v_max3_f32 v26, v30, v31, v26
M v,144,v,248,a,160
T v,238,11,43520
A 30,38
M v,152,v,248,a,164
T v,240,8,35840
A 31,39
v_max3_f32 v26, v30, v31, v26
M v,148,v,252,a,160
T v,242,8,44032
A 30,40
M v,156,v,252,a,164
T v,244,11,35840
W 7
M v,160,v,224,a,160
T v,246,11,44032
A 31,41
M v,168,v,224,a,164
T v,248,8,36352
v_max3_f32 v27, v30, v31, v27
M v,164,v,228,a,160
T v,250,8,44544
A 30,42
M v,172,v,228,a,164
T v,252,11,36352
W 7
M v,176,v,232,a,160
T v,254,11,44544
A 31,43
M v,184,v,232,a,164
s_barrier
R v,224,2,49152
v_max3_f32 v27, v30, v31, v27
M v,180,v,236,a,160
R v,226,5,49152
A 30,44
M v,188,v,236,a,164
R v,228,2,57344
W 7
M v,192,v,240,a,160
R v,230,5,57344
A 31,45
M v,200,v,240,a,164
R v,232,2,49664
v_max3_f32 v27, v30, v31, v27
M v,196,v,244,a,160
R v,234,5,49664
A 30,46
M v,204,v,244,a,164
R v,236,2,57856
W 7
M v,208,v,248,a,160
R v,238,5,57856
A 31,47
M v,216,v,248,a,164
R v,240,2,50176
v_max3_f32 v27, v30, v31, v27
M v,212,v,252,a,160
R v,242,5,50176
M v,220,v,252,a,164
R v,244,2,58368
W 7
v_mfma_f32_16x16x32_bf16 a[48:51], v[224:227], v[32:35], 0
R v,246,5,58368
v_mfma_f32_16x16x32_bf16 a[56:59], v[224:227], v[36:39], 0
R v,248,2,50688
v_mfma_f32_16x16x32_bf16 a[52:55], v[228:231], v[32:35], 0
R v,250,5,50688
v_mfma_f32_16x16x32_bf16 a[60:63], v[228:231], v[36:39], 0
R v,252,2,58880
W 7
M a,48,v,232,v,40
R v,254,5,58880
M a,56,v,232,v,44
R v,224,2,51200
M a,52,v,236,v,40
R v,226,5,51200
M a,60,v,236,v,44
R v,228,2,59392
W 7
M a,48,v,240,v,48
R v,230,5,59392
M a,56,v,240,v,52
R v,232,2,51712
M a,52,v,244,v,48
R v,234,5,51712
M a,60,v,244,v,52
R v,236,2,59904
W 7
M a,48,v,248,v,56
R v,238,5,59904
M a,56,v,248,v,60
R v,240,2,52224
M a,52,v,252,v,56
R v,242,5,52224
M a,60,v,252,v,60
R v,244,2,60416
W 7
M a,48,v,224,v,64
R v,246,5,60416
M a,56,v,224,v,68
R v,248,2,52736
M a,52,v,228,v,64
R v,250,5,52736
M a,60,v,228,v,68
R v,252,2,60928
W 7
M a,48,v,232,v,72
R v,254,5,60928
M a,56,v,232,v,76
s_barrier
T v,224,9,0
M a,52,v,236,v,72
T v,226,9,8192
M a,60,v,236,v,76
T v,228,12,0
W 7
M a,48,v,240,v,80
T v,230,12,8192
M a,56,v,240,v,84
T v,232,9,512
M a,52,v,244,v,80
T v,234,9,8704
M a,60,v,244,v,84
T v,236,12,512
W 7
M a,48,v,248,v,88
T v,238,12,8704
M a,56,v,248,v,92
T v,240,9,1024
M a,52,v,252,v,88
T v,242,9,9216
M a,60,v,252,v,92
T v,244,12,1024
W 7
M v,96,v,224,a,168
T v,246,12,9216
M v,104,v,224,a,172
T v,248,9,1536
M v,100,v,228,a,168
T v,250,9,9728
M v,108,v,228,a,172
T v,252,12,1536
W 7
M v,112,v,232,a,168
T v,254,12,9728
M v,120,v,232,a,172
T v,224,9,2048
A 30,48
A 31,49
M v,116,v,236,a,168
T v,226,9,10240
v_max3_f32 v26, v30, v31, v26
M v,124,v,236,a,172
T v,228,12,2048
W 7
A 30,50
M v,128,v,240,a,168
T v,230,12,10240
A 31,51
M v,136,v,240,a,172
T v,232,9,2560
v_max3_f32 v26, v30, v31, v26
A 30,52
M v,132,v,244,a,168
T v,234,9,10752
A 31,53
M v,140,v,244,a,172
T v,236,12,2560
W 7
v_max3_f32 v26, v30, v31, v26
M v,144,v,248,a,168
T v,238,12,10752
A 30,54
M v,152,v,248,a,172
T v,240,9,3072
A 31,55
v_max3_f32 v26, v30, v31, v26
M v,148,v,252,a,168
T v,242,9,11264
A 30,56
M v,156,v,252,a,172
T v,244,12,3072
W 7
M v,160,v,224,a,168
T v,246,12,11264
A 31,57
M v,168,v,224,a,172
T v,248,9,3584
v_max3_f32 v27, v30, v31, v27
M v,164,v,228,a,168
T v,250,9,11776
A 30,58
M v,172,v,228,a,172
T v,252,12,3584
W 7
M v,176,v,232,a,168
T v,254,12,11776
A 31,59
M v,184,v,232,a,172
s_barrier
R v,224,3,16384
v_max3_f32 v27, v30, v31, v27
M v,180,v,236,a,168
R v,226,6,16384
A 30,60
M v,188,v,236,a,172
R v,228,3,24576
W 7
M v,192,v,240,a,168
R v,230,6,24576
A 31,61
M v,200,v,240,a,172
R v,232,3,16896
v_max3_f32 v27, v30, v31, v27
M v,196,v,244,a,168
R v,234,6,16896
A 30,62
M v,204,v,244,a,172
R v,236,3,25088
W 7
M v,208,v,248,a,168
R v,238,6,25088
A 31,63
M v,216,v,248,a,172
R v,240,3,17408
v_max3_f32 v27, v30, v31, v27
M v,212,v,252,a,168
R v,242,6,17408
M v,220,v,252,a,172
R v,244,3,25600
W 7
v_mfma_f32_16x16x32_bf16 a[64:67], v[224:227], v[32:35], 0
R v,246,6,25600
v_mfma_f32_16x16x32_bf16 a[72:75], v[224:227], v[36:39], 0
R v,248,3,17920
v_mfma_f32_16x16x32_bf16 a[68:71], v[228:231], v[32:35], 0
R v,250,6,17920
v_mfma_f32_16x16x32_bf16 a[76:79], v[228:231], v[36:39], 0
R v,252,3,26112
W 7
M a,64,v,232,v,40
R v,254,6,26112
M a,72,v,232,v,44
R v,224,3,18432
M a,68,v,236,v,40
R v,226,6,18432
M a,76,v,236,v,44
R v,228,3,26624
W 7
M a,64,v,240,v,48
R v,230,6,26624
M a,72,v,240,v,52
R v,232,3,18944
M a,68,v,244,v,48
R v,234,6,18944
M a,76,v,244,v,52
R v,236,3,27136
W 7
M a,64,v,248,v,56
R v,238,6,27136
M a,72,v,248,v,60
R v,240,3,19456
M a,68,v,252,v,56
R v,242,6,19456
M a,76,v,252,v,60
R v,244,3,27648
W 7
M a,64,v,224,v,64
R v,246,6,27648
M a,72,v,224,v,68
R v,248,3,19968
M a,68,v,228,v,64
R v,250,6,19968
M a,76,v,228,v,68
R v,252,3,28160
W 7
M a,64,v,232,v,72
R v,254,6,28160
M a,72,v,232,v,76
s_barrier
T v,224,7,0
M a,68,v,236,v,72
T v,226,7,8192
M a,76,v,236,v,76
T v,228,10,0
W 7
M a,64,v,240,v,80
T v,230,10,8192
M a,72,v,240,v,84
T v,232,7,512
M a,68,v,244,v,80
T v,234,7,8704
M a,76,v,244,v,84
T v,236,10,512
W 7
M a,64,v,248,v,88
T v,238,10,8704
M a,72,v,248,v,92
T v,240,7,1024
M a,68,v,252,v,88
T v,242,7,9216
M a,76,v,252,v,92
T v,244,10,1024
W 7
M v,96,v,224,a,176
T v,246,10,9216
M v,104,v,224,a,180
T v,248,7,1536
M v,100,v,228,a,176
T v,250,7,9728
M v,108,v,228,a,180
T v,252,10,1536
W 7
M v,112,v,232,a,176
T v,254,10,9728
M v,120,v,232,a,180
T v,224,7,2048
A 30,64
A 31,65
M v,116,v,236,a,176
T v,226,7,10240
v_max3_f32 v26, v30, v31, v26
M v,124,v,236,a,180
T v,228,10,2048
W 7
A 30,66
M v,128,v,240,a,176
T v,230,10,10240
A 31,67
M v,136,v,240,a,180
T v,232,7,2560
v_max3_f32 v26, v30, v31, v26
A 30,68
M v,132,v,244,a,176
T v,234,7,10752
A 31,69
M v,140,v,244,a,180
T v,236,10,2560
W 7
v_max3_f32 v26, v30, v31, v26
M v,144,v,248,a,176
T v,238,10,10752
A 30,70
M v,152,v,248,a,180
T v,240,7,3072
A 31,71
v_max3_f32 v26, v30, v31, v26
M v,148,v,252,a,176
T v,242,7,11264
A 30,72
M v,156,v,252,a,180
T v,244,10,3072
W 7
M v,160,v,224,a,176
T v,246,10,11264
A 31,73
M v,168,v,224,a,180
T v,248,7,3584
v_max3_f32 v27, v30, v31, v27
M v,164,v,228,a,176
T v,250,7,11776
A 30,74
M v,172,v,228,a,180
T v,252,10,3584
W 7
M v,176,v,232,a,176
T v,254,10,11776
A 31,75
M v,184,v,232,a,180
s_barrier
R v,224,1,16384
v_max3_f32 v27, v30, v31, v27
M v,180,v,236,a,176
R v,226,4,16384
A 30,76
M v,188,v,236,a,180
R v,228,1,24576
W 7
M v,192,v,240,a,176
R v,230,4,24576
)x"
R"x(A 31,77
M v,200,v,240,a,180
R v,232,1,16896
v_max3_f32 v27, v30, v31, v27
M v,196,v,244,a,176
R v,234,4,16896
A 30,78
M v,204,v,244,a,180
R v,236,1,25088
W 7
M v,208,v,248,a,176
R v,238,4,25088
A 31,79
M v,216,v,248,a,180
R v,240,1,17408
v_max3_f32 v27, v30, v31, v27
M v,212,v,252,a,176
R v,242,4,17408
M v,220,v,252,a,180
R v,244,1,25600
W 7
v_mfma_f32_16x16x32_bf16 a[80:83], v[224:227], v[32:35], 0
R v,246,4,25600
v_mfma_f32_16x16x32_bf16 a[88:91], v[224:227], v[36:39], 0
R v,248,1,17920
v_mfma_f32_16x16x32_bf16 a[84:87], v[228:231], v[32:35], 0
R v,250,4,17920
v_mfma_f32_16x16x32_bf16 a[92:95], v[228:231], v[36:39], 0
R v,252,1,26112
W 7
M a,80,v,232,v,40
R v,254,4,26112
M a,88,v,232,v,44
R v,224,1,18432
M a,84,v,236,v,40
R v,226,4,18432
M a,92,v,236,v,44
R v,228,1,26624
W 7
M a,80,v,240,v,48
R v,230,4,26624
M a,88,v,240,v,52
R v,232,1,18944
M a,84,v,244,v,48
R v,234,4,18944
M a,92,v,244,v,52
R v,236,1,27136
W 7
M a,80,v,248,v,56
R v,238,4,27136
M a,88,v,248,v,60
R v,240,1,19456
M a,84,v,252,v,56
R v,242,4,19456
M a,92,v,252,v,60
R v,244,1,27648
W 7
M a,80,v,224,v,64
R v,246,4,27648
M a,88,v,224,v,68
R v,248,1,19968
M a,84,v,228,v,64
R v,250,4,19968
M a,92,v,228,v,68
R v,252,1,28160
W 7
M a,80,v,232,v,72
R v,254,4,28160
M a,88,v,232,v,76
s_barrier
.Lc0_40_%=:
T v,224,7,32768
M a,84,v,236,v,72
T v,226,7,40960
M a,92,v,236,v,76
T v,228,10,32768
W 7
M a,80,v,240,v,80
T v,230,10,40960
M a,88,v,240,v,84
T v,232,7,33280
M a,84,v,244,v,80
T v,234,7,41472
M a,92,v,244,v,84
T v,236,10,33280
W 7
M a,80,v,248,v,88
T v,238,10,41472
M a,88,v,248,v,92
T v,240,7,33792
M a,84,v,252,v,88
T v,242,7,41984
M a,92,v,252,v,92
T v,244,10,33792
W 7
M v,96,v,224,a,184
T v,246,10,41984
M v,104,v,224,a,188
T v,248,7,34304
M v,100,v,228,a,184
T v,250,7,42496
M v,108,v,228,a,188
T v,252,10,34304
W 7
M v,112,v,232,a,184
T v,254,10,42496
M v,120,v,232,a,188
T v,224,7,34816
A 30,80
A 31,81
M v,116,v,236,a,184
T v,226,7,43008
v_max3_f32 v26, v30, v31, v26
M v,124,v,236,a,188
T v,228,10,34816
W 7
A 30,82
M v,128,v,240,a,184
T v,230,10,43008
A 31,83
M v,136,v,240,a,188
T v,232,7,35328
v_max3_f32 v26, v30, v31, v26
A 30,84
M v,132,v,244,a,184
T v,234,7,43520
A 31,85
M v,140,v,244,a,188
T v,236,10,35328
W 7
v_max3_f32 v26, v30, v31, v26
M v,144,v,248,a,184
T v,238,10,43520
A 30,86
M v,152,v,248,a,188
T v,240,7,35840
A 31,87
v_max3_f32 v26, v30, v31, v26
M v,148,v,252,a,184
T v,242,7,44032
A 30,88
M v,156,v,252,a,188
T v,244,10,35840
W 7
M v,160,v,224,a,184
T v,246,10,44032
A 31,89
M v,168,v,224,a,188
T v,248,7,36352
v_max3_f32 v27, v30, v31, v27
M v,164,v,228,a,184
T v,250,7,44544
A 30,90
M v,172,v,228,a,188
T v,252,10,36352
W 7
M v,176,v,232,a,184
T v,254,10,44544
A 31,91
M v,184,v,232,a,188
s_barrier
R v,224,1,49152
v_max3_f32 v27, v30, v31, v27
M v,180,v,236,a,184
R v,226,4,49152
A 30,92
M v,188,v,236,a,188
R v,228,1,57344
W 7
M v,192,v,240,a,184
R v,230,4,57344
A 31,93
M v,200,v,240,a,188
R v,232,1,49664
v_max3_f32 v27, v30, v31, v27
M v,196,v,244,a,184
R v,234,4,49664
A 30,94
M v,204,v,244,a,188
R v,236,1,57856
W 7
M v,208,v,248,a,184
R v,238,4,57856
A 31,95
M v,216,v,248,a,188
R v,240,1,50176
v_max3_f32 v27, v30, v31, v27
M v,212,v,252,a,184
R v,242,4,50176
M v,220,v,252,a,188
R v,244,1,58368
W 7
v_mfma_f32_16x16x32_bf16 a[96:99], v[224:227], v[32:35], 0
R v,246,4,58368
v_mfma_f32_16x16x32_bf16 a[104:107], v[224:227], v[36:39], 0
R v,248,1,50688
v_mfma_f32_16x16x32_bf16 a[100:103], v[228:231], v[32:35], 0
R v,250,4,50688
v_mfma_f32_16x16x32_bf16 a[108:111], v[228:231], v[36:39], 0
R v,252,1,58880
W 7
M a,96,v,232,v,40
R v,254,4,58880
M a,104,v,232,v,44
R v,224,1,51200
M a,100,v,236,v,40
R v,226,4,51200
M a,108,v,236,v,44
R v,228,1,59392
W 7
M a,96,v,240,v,48
R v,230,4,59392
M a,104,v,240,v,52
R v,232,1,51712
M a,100,v,244,v,48
R v,234,4,51712
M a,108,v,244,v,52
R v,236,1,59904
W 7
M a,96,v,248,v,56
R v,238,4,59904
M a,104,v,248,v,60
R v,240,1,52224
M a,100,v,252,v,56
R v,242,4,52224
M a,108,v,252,v,60
R v,244,1,60416
W 7
M a,96,v,224,v,64
R v,246,4,60416
M a,104,v,224,v,68
R v,248,1,52736
M a,100,v,228,v,64
R v,250,4,52736
M a,108,v,228,v,68
R v,252,1,60928
W 7
M a,96,v,232,v,72
R v,254,4,60928
M a,104,v,232,v,76
s_barrier
T v,224,8,0
M a,100,v,236,v,72
T v,226,8,8192
M a,108,v,236,v,76
T v,228,11,0
W 7
M a,96,v,240,v,80
T v,230,11,8192
M a,104,v,240,v,84
T v,232,8,512
M a,100,v,244,v,80
T v,234,8,8704
M a,108,v,244,v,84
T v,236,11,512
W 7
M a,96,v,248,v,88
T v,238,11,8704
M a,104,v,248,v,92
T v,240,8,1024
M a,100,v,252,v,88
T v,242,8,9216
M a,108,v,252,v,92
T v,244,11,1024
W 7
M v,96,v,224,a,192
T v,246,11,9216
M v,104,v,224,a,196
T v,248,8,1536
M v,100,v,228,a,192
T v,250,8,9728
M v,108,v,228,a,196
T v,252,11,1536
W 7
M v,112,v,232,a,192
T v,254,11,9728
M v,120,v,232,a,196
T v,224,8,2048
A 30,96
A 31,97
M v,116,v,236,a,192
T v,226,8,10240
v_max3_f32 v26, v30, v31, v26
M v,124,v,236,a,196
T v,228,11,2048
W 7
A 30,98
M v,128,v,240,a,192
T v,230,11,10240
A 31,99
M v,136,v,240,a,196
T v,232,8,2560
v_max3_f32 v26, v30, v31, v26
A 30,100
M v,132,v,244,a,192
T v,234,8,10752
A 31,101
M v,140,v,244,a,196
T v,236,11,2560
W 7
v_max3_f32 v26, v30, v31, v26
M v,144,v,248,a,192
T v,238,11,10752
A 30,102
M v,152,v,248,a,196
T v,240,8,3072
A 31,103
v_max3_f32 v26, v30, v31, v26
M v,148,v,252,a,192
T v,242,8,11264
A 30,104
M v,156,v,252,a,196
T v,244,11,3072
W 7
M v,160,v,224,a,192
T v,246,11,11264
A 31,105
M v,168,v,224,a,196
T v,248,8,3584
v_max3_f32 v27, v30, v31, v27
M v,164,v,228,a,192
T v,250,8,11776
A 30,106
M v,172,v,228,a,196
T v,252,11,3584
W 7
M v,176,v,232,a,192
T v,254,11,11776
A 31,107
M v,184,v,232,a,196
s_barrier
R v,224,2,16384
v_max3_f32 v27, v30, v31, v27
M v,180,v,236,a,192
R v,226,5,16384
A 30,108
M v,188,v,236,a,196
R v,228,2,24576
W 7
M v,192,v,240,a,192
R v,230,5,24576
A 31,109
M v,200,v,240,a,196
R v,232,2,16896
v_max3_f32 v27, v30, v31, v27
M v,196,v,244,a,192
R v,234,5,16896
A 30,110
M v,204,v,244,a,196
R v,236,2,25088
W 7
M v,208,v,248,a,192
R v,238,5,25088
A 31,111
M v,216,v,248,a,196
R v,240,2,17408
v_max3_f32 v27, v30, v31, v27
M v,212,v,252,a,192
R v,242,5,17408
M v,220,v,252,a,196
R v,244,2,25600
W 7
v_mfma_f32_16x16x32_bf16 a[112:115], v[224:227], v[32:35], 0
R v,246,5,25600
v_mfma_f32_16x16x32_bf16 a[120:123], v[224:227], v[36:39], 0
R v,248,2,17920
v_mfma_f32_16x16x32_bf16 a[116:119], v[228:231], v[32:35], 0
R v,250,5,17920
v_mfma_f32_16x16x32_bf16 a[124:127], v[228:231], v[36:39], 0
R v,252,2,26112
W 7
M a,112,v,232,v,40
R v,254,5,26112
M a,120,v,232,v,44
R v,224,2,18432
M a,116,v,236,v,40
R v,226,5,18432
M a,124,v,236,v,44
R v,228,2,26624
W 7
M a,112,v,240,v,48
R v,230,5,26624
M a,120,v,240,v,52
R v,232,2,18944
M a,116,v,244,v,48
R v,234,5,18944
M a,124,v,244,v,52
R v,236,2,27136
W 7
M a,112,v,248,v,56
R v,238,5,27136
M a,120,v,248,v,60
R v,240,2,19456
M a,116,v,252,v,56
R v,242,5,19456
M a,124,v,252,v,60
R v,244,2,27648
W 7
M a,112,v,224,v,64
R v,246,5,27648
)x"
R"x(M a,120,v,224,v,68
R v,248,2,19968
M a,116,v,228,v,64
R v,250,5,19968
M a,124,v,228,v,68
R v,252,2,28160
W 7
M a,112,v,232,v,72
R v,254,5,28160
M a,120,v,232,v,76
s_barrier
T v,224,8,32768
M a,116,v,236,v,72
T v,226,8,40960
M a,124,v,236,v,76
T v,228,11,32768
W 7
M a,112,v,240,v,80
T v,230,11,40960
M a,120,v,240,v,84
T v,232,8,33280
M a,116,v,244,v,80
T v,234,8,41472
M a,124,v,244,v,84
T v,236,11,33280
W 7
M a,112,v,248,v,88
T v,238,11,41472
M a,120,v,248,v,92
T v,240,8,33792
M a,116,v,252,v,88
T v,242,8,41984
M a,124,v,252,v,92
T v,244,11,33792
W 7
M v,96,v,224,a,200
T v,246,11,41984
M v,104,v,224,a,204
T v,248,8,34304
M v,100,v,228,a,200
T v,250,8,42496
M v,108,v,228,a,204
T v,252,11,34304
W 7
M v,112,v,232,a,200
T v,254,11,42496
M v,120,v,232,a,204
T v,224,8,34816
A 30,112
A 31,113
M v,116,v,236,a,200
T v,226,8,43008
v_max3_f32 v26, v30, v31, v26
M v,124,v,236,a,204
T v,228,11,34816
W 7
A 30,114
M v,128,v,240,a,200
T v,230,11,43008
A 31,115
M v,136,v,240,a,204
T v,232,8,35328
v_max3_f32 v26, v30, v31, v26
A 30,116
M v,132,v,244,a,200
T v,234,8,43520
A 31,117
M v,140,v,244,a,204
T v,236,11,35328
W 7
v_max3_f32 v26, v30, v31, v26
M v,144,v,248,a,200
T v,238,11,43520
A 30,118
M v,152,v,248,a,204
T v,240,8,35840
A 31,119
v_max3_f32 v26, v30, v31, v26
M v,148,v,252,a,200
T v,242,8,44032
A 30,120
M v,156,v,252,a,204
T v,244,11,35840
W 7
M v,160,v,224,a,200
T v,246,11,44032
A 31,121
M v,168,v,224,a,204
T v,248,8,36352
v_max3_f32 v27, v30, v31, v27
M v,164,v,228,a,200
T v,250,8,44544
A 30,122
M v,172,v,228,a,204
T v,252,11,36352
W 7
M v,176,v,232,a,200
T v,254,11,44544
A 31,123
M v,184,v,232,a,204
s_barrier
R v,224,2,49152
v_max3_f32 v27, v30, v31, v27
M v,180,v,236,a,200
R v,226,5,49152
A 30,124
M v,188,v,236,a,204
R v,228,2,57344
W 7
M v,192,v,240,a,200
R v,230,5,57344
A 31,125
M v,200,v,240,a,204
R v,232,2,49664
v_max3_f32 v27, v30, v31, v27
M v,196,v,244,a,200
R v,234,5,49664
A 30,126
M v,204,v,244,a,204
R v,236,2,57856
W 7
M v,208,v,248,a,200
R v,238,5,57856
A 31,127
M v,216,v,248,a,204
R v,240,2,50176
v_max3_f32 v27, v30, v31, v27
M v,212,v,252,a,200
R v,242,5,50176
M v,220,v,252,a,204
R v,244,2,58368
W 7
v_mfma_f32_16x16x32_bf16 a[128:131], v[224:227], v[32:35], 0
R v,246,5,58368
v_mfma_f32_16x16x32_bf16 a[136:139], v[224:227], v[36:39], 0
R v,248,2,50688
v_mfma_f32_16x16x32_bf16 a[132:135], v[228:231], v[32:35], 0
R v,250,5,50688
v_mfma_f32_16x16x32_bf16 a[140:143], v[228:231], v[36:39], 0
R v,252,2,58880
W 7
M a,128,v,232,v,40
R v,254,5,58880
M a,136,v,232,v,44
R v,224,2,51200
M a,132,v,236,v,40
R v,226,5,51200
M a,140,v,236,v,44
R v,228,2,59392
W 7
M a,128,v,240,v,48
R v,230,5,59392
M a,136,v,240,v,52
R v,232,2,51712
M a,132,v,244,v,48
R v,234,5,51712
M a,140,v,244,v,52
R v,236,2,59904
W 7
M a,128,v,248,v,56
R v,238,5,59904
M a,136,v,248,v,60
R v,240,2,52224
M a,132,v,252,v,56
R v,242,5,52224
M a,140,v,252,v,60
R v,244,2,60416
W 7
M a,128,v,224,v,64
R v,246,5,60416
M a,136,v,224,v,68
R v,248,2,52736
M a,132,v,228,v,64
R v,250,5,52736
M a,140,v,228,v,68
R v,252,2,60928
W 7
M a,128,v,232,v,72
R v,254,5,60928
M a,136,v,232,v,76
s_barrier
.Lc0_46_%=:
T v,224,9,0
M a,132,v,236,v,72
T v,226,9,8192
M a,140,v,236,v,76
T v,228,12,0
W 7
M a,128,v,240,v,80
T v,230,12,8192
M a,136,v,240,v,84
T v,232,9,512
M a,132,v,244,v,80
T v,234,9,8704
M a,140,v,244,v,84
T v,236,12,512
W 7
M a,128,v,248,v,88
T v,238,12,8704
M a,136,v,248,v,92
T v,240,9,1024
M a,132,v,252,v,88
T v,242,9,9216
M a,140,v,252,v,92
T v,244,12,1024
W 7
M v,96,v,224,a,208
T v,246,12,9216
M v,104,v,224,a,212
T v,248,9,1536
M v,100,v,228,a,208
T v,250,9,9728
M v,108,v,228,a,212
T v,252,12,1536
W 7
M v,112,v,232,a,208
T v,254,12,9728
M v,120,v,232,a,212
T v,224,9,2048
A 30,128
A 31,129
M v,116,v,236,a,208
T v,226,9,10240
v_max3_f32 v26, v30, v31, v26
M v,124,v,236,a,212
T v,228,12,2048
W 7
A 30,130
M v,128,v,240,a,208
T v,230,12,10240
A 31,131
M v,136,v,240,a,212
T v,232,9,2560
v_max3_f32 v26, v30, v31, v26
A 30,132
M v,132,v,244,a,208
T v,234,9,10752
A 31,133
M v,140,v,244,a,212
T v,236,12,2560
W 7
v_max3_f32 v26, v30, v31, v26
M v,144,v,248,a,208
T v,238,12,10752
A 30,134
M v,152,v,248,a,212
T v,240,9,3072
A 31,135
v_max3_f32 v26, v30, v31, v26
M v,148,v,252,a,208
T v,242,9,11264
A 30,136
M v,156,v,252,a,212
T v,244,12,3072
W 7
M v,160,v,224,a,208
T v,246,12,11264
A 31,137
M v,168,v,224,a,212
T v,248,9,3584
v_max3_f32 v27, v30, v31, v27
M v,164,v,228,a,208
T v,250,9,11776
A 30,138
M v,172,v,228,a,212
T v,252,12,3584
W 7
M v,176,v,232,a,208
T v,254,12,11776
A 31,139
M v,184,v,232,a,212
s_barrier
R v,224,3,16384
v_max3_f32 v27, v30, v31, v27
M v,180,v,236,a,208
R v,226,6,16384
A 30,140
M v,188,v,236,a,212
R v,228,3,24576
W 7
M v,192,v,240,a,208
R v,230,6,24576
A 31,141
M v,200,v,240,a,212
R v,232,3,16896
v_max3_f32 v27, v30, v31, v27
M v,196,v,244,a,208
R v,234,6,16896
A 30,142
M v,204,v,244,a,212
R v,236,3,25088
W 7
M v,208,v,248,a,208
R v,238,6,25088
A 31,143
M v,216,v,248,a,212
R v,240,3,17408
v_max3_f32 v27, v30, v31, v27
M v,212,v,252,a,208
R v,242,6,17408
M v,220,v,252,a,212
R v,244,3,25600
W 7
v_mfma_f32_16x16x32_bf16 a[144:147], v[224:227], v[32:35], 0
R v,246,6,25600
v_mfma_f32_16x16x32_bf16 a[152:155], v[224:227], v[36:39], 0
R v,248,3,17920
v_mfma_f32_16x16x32_bf16 a[148:151], v[228:231], v[32:35], 0
R v,250,6,17920
v_mfma_f32_16x16x32_bf16 a[156:159], v[228:231], v[36:39], 0
R v,252,3,26112
W 7
M a,144,v,232,v,40
R v,254,6,26112
M a,152,v,232,v,44
R v,224,3,18432
M a,148,v,236,v,40
R v,226,6,18432
M a,156,v,236,v,44
R v,228,3,26624
W 7
M a,144,v,240,v,48
R v,230,6,26624
M a,152,v,240,v,52
R v,232,3,18944
M a,148,v,244,v,48
R v,234,6,18944
M a,156,v,244,v,52
R v,236,3,27136
W 7
M a,144,v,248,v,56
R v,238,6,27136
M a,152,v,248,v,60
R v,240,3,19456
M a,148,v,252,v,56
R v,242,6,19456
M a,156,v,252,v,60
R v,244,3,27648
W 7
M a,144,v,224,v,64
R v,246,6,27648
M a,152,v,224,v,68
R v,248,3,19968
M a,148,v,228,v,64
R v,250,6,19968
M a,156,v,228,v,68
R v,252,3,28160
W 7
M a,144,v,232,v,72
R v,254,6,28160
M a,152,v,232,v,76
s_barrier
T v,224,7,0
M a,148,v,236,v,72
T v,226,7,8192
M a,156,v,236,v,76
T v,228,10,0
W 7
M a,144,v,240,v,80
T v,230,10,8192
M a,152,v,240,v,84
T v,232,7,512
M a,148,v,244,v,80
T v,234,7,8704
M a,156,v,244,v,84
T v,236,10,512
W 7
M a,144,v,248,v,88
T v,238,10,8704
M a,152,v,248,v,92
T v,240,7,1024
M a,148,v,252,v,88
T v,242,7,9216
M a,156,v,252,v,92
T v,244,10,1024
W 7
M v,96,v,224,a,216
T v,246,10,9216
M v,104,v,224,a,220
T v,248,7,1536
M v,100,v,228,a,216
T v,250,7,9728
M v,108,v,228,a,220
T v,252,10,1536
W 7
M v,112,v,232,a,216
T v,254,10,9728
M v,120,v,232,a,220
T v,224,7,2048
A 30,144
A 31,145
M v,116,v,236,a,216
T v,226,7,10240
v_max3_f32 v26, v30, v31, v26
M v,124,v,236,a,220
T v,228,10,2048
W 7
A 30,146
M v,128,v,240,a,216
T v,230,10,10240
)x"
R"x(A 31,147
M v,136,v,240,a,220
T v,232,7,2560
v_max3_f32 v26, v30, v31, v26
A 30,148
M v,132,v,244,a,216
T v,234,7,10752
A 31,149
M v,140,v,244,a,220
T v,236,10,2560
W 7
v_max3_f32 v26, v30, v31, v26
M v,144,v,248,a,216
T v,238,10,10752
A 30,150
M v,152,v,248,a,220
T v,240,7,3072
A 31,151
v_max3_f32 v26, v30, v31, v26
M v,148,v,252,a,216
T v,242,7,11264
A 30,152
M v,156,v,252,a,220
T v,244,10,3072
W 7
M v,160,v,224,a,216
T v,246,10,11264
A 31,153
M v,168,v,224,a,220
T v,248,7,3584
v_max3_f32 v27, v30, v31, v27
M v,164,v,228,a,216
T v,250,7,11776
A 30,154
M v,172,v,228,a,220
T v,252,10,3584
W 7
M v,176,v,232,a,216
T v,254,10,11776
A 31,155
M v,184,v,232,a,220
s_barrier
R v,224,1,16384
v_max3_f32 v27, v30, v31, v27
M v,180,v,236,a,216
R v,226,4,16384
A 30,156
M v,188,v,236,a,220
R v,228,1,24576
W 7
M v,192,v,240,a,216
R v,230,4,24576
A 31,157
M v,200,v,240,a,220
R v,232,1,16896
v_max3_f32 v27, v30, v31, v27
M v,196,v,244,a,216
R v,234,4,16896
A 30,158
M v,204,v,244,a,220
R v,236,1,25088
W 7
M v,208,v,248,a,216
R v,238,4,25088
A 31,159
M v,216,v,248,a,220
R v,240,1,17408
v_max3_f32 v27, v30, v31, v27
M v,212,v,252,a,216
R v,242,4,17408
M v,220,v,252,a,220
R v,244,1,25600
W 7
v_mfma_f32_16x16x32_bf16 a[160:163], v[224:227], v[32:35], 0
R v,246,4,25600
v_mfma_f32_16x16x32_bf16 a[168:171], v[224:227], v[36:39], 0
R v,248,1,17920
v_mfma_f32_16x16x32_bf16 a[164:167], v[228:231], v[32:35], 0
R v,250,4,17920
v_mfma_f32_16x16x32_bf16 a[172:175], v[228:231], v[36:39], 0
R v,252,1,26112
W 7
M a,160,v,232,v,40
R v,254,4,26112
M a,168,v,232,v,44
R v,224,1,18432
M a,164,v,236,v,40
R v,226,4,18432
M a,172,v,236,v,44
R v,228,1,26624
W 7
M a,160,v,240,v,48
R v,230,4,26624
M a,168,v,240,v,52
R v,232,1,18944
M a,164,v,244,v,48
R v,234,4,18944
M a,172,v,244,v,52
R v,236,1,27136
W 7
M a,160,v,248,v,56
R v,238,4,27136
M a,168,v,248,v,60
R v,240,1,19456
M a,164,v,252,v,56
R v,242,4,19456
M a,172,v,252,v,60
R v,244,1,27648
W 7
M a,160,v,224,v,64
R v,246,4,27648
M a,168,v,224,v,68
R v,248,1,19968
M a,164,v,228,v,64
R v,250,4,19968
M a,172,v,228,v,68
R v,252,1,28160
W 7
M a,160,v,232,v,72
R v,254,4,28160
M a,168,v,232,v,76
s_barrier
T v,224,7,32768
M a,164,v,236,v,72
T v,226,7,40960
M a,172,v,236,v,76
T v,228,10,32768
W 7
M a,160,v,240,v,80
T v,230,10,40960
M a,168,v,240,v,84
T v,232,7,33280
M a,164,v,244,v,80
T v,234,7,41472
M a,172,v,244,v,84
T v,236,10,33280
W 7
M a,160,v,248,v,88
T v,238,10,41472
M a,168,v,248,v,92
T v,240,7,33792
M a,164,v,252,v,88
T v,242,7,41984
M a,172,v,252,v,92
T v,244,10,33792
W 7
M v,96,v,224,a,224
T v,246,10,41984
M v,104,v,224,a,228
T v,248,7,34304
M v,100,v,228,a,224
T v,250,7,42496
M v,108,v,228,a,228
T v,252,10,34304
W 7
M v,112,v,232,a,224
T v,254,10,42496
M v,120,v,232,a,228
T v,224,7,34816
A 30,160
A 31,161
M v,116,v,236,a,224
T v,226,7,43008
v_max3_f32 v26, v30, v31, v26
M v,124,v,236,a,228
T v,228,10,34816
W 7
A 30,162
M v,128,v,240,a,224
T v,230,10,43008
A 31,163
M v,136,v,240,a,228
T v,232,7,35328
v_max3_f32 v26, v30, v31, v26
A 30,164
M v,132,v,244,a,224
T v,234,7,43520
A 31,165
M v,140,v,244,a,228
T v,236,10,35328
W 7
v_max3_f32 v26, v30, v31, v26
M v,144,v,248,a,224
T v,238,10,43520
A 30,166
M v,152,v,248,a,228
T v,240,7,35840
A 31,167
v_max3_f32 v26, v30, v31, v26
M v,148,v,252,a,224
T v,242,7,44032
A 30,168
M v,156,v,252,a,228
T v,244,10,35840
W 7
M v,160,v,224,a,224
T v,246,10,44032
A 31,169
M v,168,v,224,a,228
T v,248,7,36352
v_max3_f32 v27, v30, v31, v27
M v,164,v,228,a,224
T v,250,7,44544
A 30,170
M v,172,v,228,a,228
T v,252,10,36352
W 7
M v,176,v,232,a,224
T v,254,10,44544
A 31,171
M v,184,v,232,a,228
s_barrier
R v,224,1,49152
v_max3_f32 v27, v30, v31, v27
M v,180,v,236,a,224
R v,226,4,49152
A 30,172
M v,188,v,236,a,228
R v,228,1,57344
W 7
M v,192,v,240,a,224
R v,230,4,57344
A 31,173
M v,200,v,240,a,228
R v,232,1,49664
v_max3_f32 v27, v30, v31, v27
M v,196,v,244,a,224
R v,234,4,49664
A 30,174
M v,204,v,244,a,228
R v,236,1,57856
W 7
M v,208,v,248,a,224
R v,238,4,57856
A 31,175
M v,216,v,248,a,228
R v,240,1,50176
v_max3_f32 v27, v30, v31, v27
M v,212,v,252,a,224
R v,242,4,50176
M v,220,v,252,a,228
R v,244,1,58368
W 7
v_mfma_f32_16x16x32_bf16 a[176:179], v[224:227], v[32:35], 0
R v,246,4,58368
v_mfma_f32_16x16x32_bf16 a[184:187], v[224:227], v[36:39], 0
R v,248,1,50688
v_mfma_f32_16x16x32_bf16 a[180:183], v[228:231], v[32:35], 0
R v,250,4,50688
v_mfma_f32_16x16x32_bf16 a[188:191], v[228:231], v[36:39], 0
R v,252,1,58880
W 7
M a,176,v,232,v,40
R v,254,4,58880
M a,184,v,232,v,44
R v,224,1,51200
M a,180,v,236,v,40
R v,226,4,51200
M a,188,v,236,v,44
R v,228,1,59392
W 7
M a,176,v,240,v,48
R v,230,4,59392
M a,184,v,240,v,52
R v,232,1,51712
M a,180,v,244,v,48
R v,234,4,51712
M a,188,v,244,v,52
R v,236,1,59904
W 7
M a,176,v,248,v,56
R v,238,4,59904
M a,184,v,248,v,60
R v,240,1,52224
M a,180,v,252,v,56
R v,242,4,52224
M a,188,v,252,v,60
R v,244,1,60416
W 7
M a,176,v,224,v,64
R v,246,4,60416
M a,184,v,224,v,68
R v,248,1,52736
M a,180,v,228,v,64
R v,250,4,52736
M a,188,v,228,v,68
R v,252,1,60928
W 7
M a,176,v,232,v,72
R v,254,4,60928
M a,184,v,232,v,76
s_barrier
.Lc0_52_%=:
T v,224,8,0
M a,180,v,236,v,72
T v,226,8,8192
M a,188,v,236,v,76
T v,228,11,0
W 7
M a,176,v,240,v,80
T v,230,11,8192
M a,184,v,240,v,84
T v,232,8,512
M a,180,v,244,v,80
T v,234,8,8704
M a,188,v,244,v,84
T v,236,11,512
W 7
M a,176,v,248,v,88
T v,238,11,8704
M a,184,v,248,v,92
T v,240,8,1024
M a,180,v,252,v,88
T v,242,8,9216
M a,188,v,252,v,92
T v,244,11,1024
W 7
M v,96,v,224,a,232
T v,246,11,9216
M v,104,v,224,a,236
T v,248,8,1536
M v,100,v,228,a,232
T v,250,8,9728
M v,108,v,228,a,236
T v,252,11,1536
W 7
M v,112,v,232,a,232
T v,254,11,9728
M v,120,v,232,a,236
T v,224,8,2048
A 30,176
A 31,177
M v,116,v,236,a,232
T v,226,8,10240
v_max3_f32 v26, v30, v31, v26
M v,124,v,236,a,236
T v,228,11,2048
W 7
A 30,178
M v,128,v,240,a,232
T v,230,11,10240
A 31,179
M v,136,v,240,a,236
T v,232,8,2560
v_max3_f32 v26, v30, v31, v26
A 30,180
M v,132,v,244,a,232
T v,234,8,10752
A 31,181
M v,140,v,244,a,236
T v,236,11,2560
W 7
v_max3_f32 v26, v30, v31, v26
M v,144,v,248,a,232
T v,238,11,10752
A 30,182
M v,152,v,248,a,236
T v,240,8,3072
A 31,183
v_max3_f32 v26, v30, v31, v26
M v,148,v,252,a,232
T v,242,8,11264
A 30,184
M v,156,v,252,a,236
T v,244,11,3072
W 7
M v,160,v,224,a,232
T v,246,11,11264
A 31,185
M v,168,v,224,a,236
T v,248,8,3584
v_max3_f32 v27, v30, v31, v27
M v,164,v,228,a,232
T v,250,8,11776
A 30,186
M v,172,v,228,a,236
T v,252,11,3584
W 7
M v,176,v,232,a,232
T v,254,11,11776
A 31,187
M v,184,v,232,a,236
s_barrier
R v,224,2,16384
v_max3_f32 v27, v30, v31, v27
M v,180,v,236,a,232
R v,226,5,16384
A 30,188
M v,188,v,236,a,236
R v,228,2,24576
W 7
M v,192,v,240,a,232
R v,230,5,24576
A 31,189
M v,200,v,240,a,236
R v,232,2,16896
v_max3_f32 v27, v30, v31, v27
M v,196,v,244,a,232
R v,234,5,16896
A 30,190
)x"
R"x(M v,204,v,244,a,236
R v,236,2,25088
W 7
M v,208,v,248,a,232
R v,238,5,25088
A 31,191
M v,216,v,248,a,236
R v,240,2,17408
v_max3_f32 v27, v30, v31, v27
M v,212,v,252,a,232
R v,242,5,17408
M v,220,v,252,a,236
R v,244,2,25600
W 7
v_mfma_f32_16x16x32_bf16 a[192:195], v[224:227], v[32:35], 0
R v,246,5,25600
v_mfma_f32_16x16x32_bf16 a[200:203], v[224:227], v[36:39], 0
R v,248,2,17920
v_mfma_f32_16x16x32_bf16 a[196:199], v[228:231], v[32:35], 0
R v,250,5,17920
v_mfma_f32_16x16x32_bf16 a[204:207], v[228:231], v[36:39], 0
R v,252,2,26112
W 7
M a,192,v,232,v,40
R v,254,5,26112
M a,200,v,232,v,44
R v,224,2,18432
M a,196,v,236,v,40
R v,226,5,18432
M a,204,v,236,v,44
R v,228,2,26624
W 7
M a,192,v,240,v,48
R v,230,5,26624
M a,200,v,240,v,52
R v,232,2,18944
M a,196,v,244,v,48
R v,234,5,18944
M a,204,v,244,v,52
R v,236,2,27136
W 7
M a,192,v,248,v,56
R v,238,5,27136
M a,200,v,248,v,60
R v,240,2,19456
M a,196,v,252,v,56
R v,242,5,19456
M a,204,v,252,v,60
R v,244,2,27648
W 7
M a,192,v,224,v,64
R v,246,5,27648
M a,200,v,224,v,68
R v,248,2,19968
M a,196,v,228,v,64
R v,250,5,19968
M a,204,v,228,v,68
R v,252,2,28160
W 7
M a,192,v,232,v,72
R v,254,5,28160
M a,200,v,232,v,76
s_barrier
T v,224,8,32768
M a,196,v,236,v,72
T v,226,8,40960
M a,204,v,236,v,76
T v,228,11,32768
W 7
M a,192,v,240,v,80
T v,230,11,40960
M a,200,v,240,v,84
T v,232,8,33280
M a,196,v,244,v,80
T v,234,8,41472
M a,204,v,244,v,84
T v,236,11,33280
W 7
M a,192,v,248,v,88
T v,238,11,41472
M a,200,v,248,v,92
T v,240,8,33792
M a,196,v,252,v,88
T v,242,8,41984
M a,204,v,252,v,92
T v,244,11,33792
W 7
M v,96,v,224,a,240
T v,246,11,41984
M v,104,v,224,a,244
T v,248,8,34304
M v,100,v,228,a,240
T v,250,8,42496
M v,108,v,228,a,244
T v,252,11,34304
W 7
M v,112,v,232,a,240
T v,254,11,42496
M v,120,v,232,a,244
T v,224,8,34816
A 30,192
A 31,193
M v,116,v,236,a,240
T v,226,8,43008
v_max3_f32 v26, v30, v31, v26
M v,124,v,236,a,244
T v,228,11,34816
W 7
A 30,194
M v,128,v,240,a,240
T v,230,11,43008
A 31,195
M v,136,v,240,a,244
T v,232,8,35328
v_max3_f32 v26, v30, v31, v26
A 30,196
M v,132,v,244,a,240
T v,234,8,43520
A 31,197
M v,140,v,244,a,244
T v,236,11,35328
W 7
v_max3_f32 v26, v30, v31, v26
M v,144,v,248,a,240
T v,238,11,43520
A 30,198
M v,152,v,248,a,244
T v,240,8,35840
A 31,199
v_max3_f32 v26, v30, v31, v26
M v,148,v,252,a,240
T v,242,8,44032
A 30,200
M v,156,v,252,a,244
T v,244,11,35840
W 7
M v,160,v,224,a,240
T v,246,11,44032
A 31,201
M v,168,v,224,a,244
T v,248,8,36352
v_max3_f32 v27, v30, v31, v27
M v,164,v,228,a,240
T v,250,8,44544
A 30,202
M v,172,v,228,a,244
T v,252,11,36352
W 7
M v,176,v,232,a,240
T v,254,11,44544
A 31,203
M v,184,v,232,a,244
s_barrier
R v,224,2,49152
v_max3_f32 v27, v30, v31, v27
M v,180,v,236,a,240
R v,226,5,49152
A 30,204
M v,188,v,236,a,244
R v,228,2,57344
W 7
M v,192,v,240,a,240
R v,230,5,57344
A 31,205
M v,200,v,240,a,244
R v,232,2,49664
v_max3_f32 v27, v30, v31, v27
M v,196,v,244,a,240
R v,234,5,49664
A 30,206
M v,204,v,244,a,244
R v,236,2,57856
W 7
M v,208,v,248,a,240
R v,238,5,57856
A 31,207
M v,216,v,248,a,244
R v,240,2,50176
v_max3_f32 v27, v30, v31, v27
M v,212,v,252,a,240
R v,242,5,50176
M v,220,v,252,a,244
R v,244,2,58368
W 7
v_mfma_f32_16x16x32_bf16 a[208:211], v[224:227], v[32:35], 0
R v,246,5,58368
v_mfma_f32_16x16x32_bf16 a[216:219], v[224:227], v[36:39], 0
R v,248,2,50688
v_mfma_f32_16x16x32_bf16 a[212:215], v[228:231], v[32:35], 0
R v,250,5,50688
v_mfma_f32_16x16x32_bf16 a[220:223], v[228:231], v[36:39], 0
R v,252,2,58880
W 7
M a,208,v,232,v,40
R v,254,5,58880
M a,216,v,232,v,44
R v,224,2,51200
M a,212,v,236,v,40
R v,226,5,51200
M a,220,v,236,v,44
R v,228,2,59392
W 7
M a,208,v,240,v,48
R v,230,5,59392
M a,216,v,240,v,52
R v,232,2,51712
M a,212,v,244,v,48
R v,234,5,51712
M a,220,v,244,v,52
R v,236,2,59904
W 7
M a,208,v,248,v,56
R v,238,5,59904
M a,216,v,248,v,60
R v,240,2,52224
M a,212,v,252,v,56
R v,242,5,52224
M a,220,v,252,v,60
R v,244,2,60416
W 7
M a,208,v,224,v,64
R v,246,5,60416
M a,216,v,224,v,68
R v,248,2,52736
M a,212,v,228,v,64
R v,250,5,52736
M a,220,v,228,v,68
R v,252,2,60928
W 7
M a,208,v,232,v,72
R v,254,5,60928
M a,216,v,232,v,76
s_barrier
T v,224,9,0
M a,212,v,236,v,72
T v,226,9,8192
M a,220,v,236,v,76
T v,228,12,0
W 7
M a,208,v,240,v,80
T v,230,12,8192
M a,216,v,240,v,84
T v,232,9,512
M a,212,v,244,v,80
T v,234,9,8704
M a,220,v,244,v,84
T v,236,12,512
W 7
M a,208,v,248,v,88
T v,238,12,8704
M a,216,v,248,v,92
T v,240,9,1024
M a,212,v,252,v,88
T v,242,9,9216
M a,220,v,252,v,92
T v,244,12,1024
W 7
M v,96,v,224,a,248
T v,246,12,9216
M v,104,v,224,a,252
T v,248,9,1536
M v,100,v,228,a,248
T v,250,9,9728
M v,108,v,228,a,252
T v,252,12,1536
W 7
M v,112,v,232,a,248
T v,254,12,9728
M v,120,v,232,a,252
T v,224,9,2048
A 30,208
A 31,209
M v,116,v,236,a,248
T v,226,9,10240
v_max3_f32 v26, v30, v31, v26
M v,124,v,236,a,252
T v,228,12,2048
W 7
A 30,210
M v,128,v,240,a,248
T v,230,12,10240
A 31,211
M v,136,v,240,a,252
T v,232,9,2560
v_max3_f32 v26, v30, v31, v26
A 30,212
M v,132,v,244,a,248
T v,234,9,10752
A 31,213
M v,140,v,244,a,252
T v,236,12,2560
W 7
v_max3_f32 v26, v30, v31, v26
M v,144,v,248,a,248
T v,238,12,10752
A 30,214
M v,152,v,248,a,252
T v,240,9,3072
A 31,215
v_max3_f32 v26, v30, v31, v26
M v,148,v,252,a,248
T v,242,9,11264
A 30,216
M v,156,v,252,a,252
T v,244,12,3072
W 7
M v,160,v,224,a,248
T v,246,12,11264
A 31,217
M v,168,v,224,a,252
T v,248,9,3584
v_max3_f32 v27, v30, v31, v27
M v,164,v,228,a,248
T v,250,9,11776
A 30,218
M v,172,v,228,a,252
T v,252,12,3584
W 7
M v,176,v,232,a,248
T v,254,12,11776
A 31,219
M v,184,v,232,a,252
s_barrier
R v,224,3,16384
v_max3_f32 v27, v30, v31, v27
M v,180,v,236,a,248
R v,226,6,16384
A 30,220
M v,188,v,236,a,252
R v,228,3,24576
W 7
M v,192,v,240,a,248
R v,230,6,24576
A 31,221
M v,200,v,240,a,252
R v,232,3,16896
v_max3_f32 v27, v30, v31, v27
M v,196,v,244,a,248
R v,234,6,16896
A 30,222
M v,204,v,244,a,252
R v,236,3,25088
W 7
M v,208,v,248,a,248
R v,238,6,25088
A 31,223
M v,216,v,248,a,252
R v,240,3,17408
v_max3_f32 v27, v30, v31, v27
M v,212,v,252,a,248
R v,242,6,17408
M v,220,v,252,a,252
R v,244,3,25600
W 7
v_mfma_f32_16x16x32_bf16 a[224:227], v[224:227], v[32:35], 0
R v,246,6,25600
v_mfma_f32_16x16x32_bf16 a[232:235], v[224:227], v[36:39], 0
R v,248,3,17920
v_mfma_f32_16x16x32_bf16 a[228:231], v[228:231], v[32:35], 0
R v,250,6,17920
v_mfma_f32_16x16x32_bf16 a[236:239], v[228:231], v[36:39], 0
R v,252,3,26112
W 7
M a,224,v,232,v,40
R v,254,6,26112
M a,232,v,232,v,44
R v,224,3,18432
M a,228,v,236,v,40
R v,226,6,18432
M a,236,v,236,v,44
R v,228,3,26624
W 7
M a,224,v,240,v,48
R v,230,6,26624
M a,232,v,240,v,52
R v,232,3,18944
M a,228,v,244,v,48
R v,234,6,18944
M a,236,v,244,v,52
R v,236,3,27136
W 7
M a,224,v,248,v,56
R v,238,6,27136
M a,232,v,248,v,60
R v,240,3,19456
M a,228,v,252,v,56
R v,242,6,19456
M a,236,v,252,v,60
R v,244,3,27648
W 7
M a,224,v,224,v,64
R v,246,6,27648
M a,232,v,224,v,68
R v,248,3,19968
M a,228,v,228,v,64
R v,250,6,19968
M a,236,v,228,v,68
R v,252,3,28160
W 7
M a,224,v,232,v,72
)x"
R"x(R v,254,6,28160
M a,232,v,232,v,76
s_barrier
.Lc0_58_%=:
R v,224,1,0
M a,228,v,236,v,72
R v,226,4,0
M a,236,v,236,v,76
R v,228,1,8192
W 7
M a,224,v,240,v,80
R v,230,4,8192
M a,232,v,240,v,84
R v,232,1,512
M a,228,v,244,v,80
R v,234,4,512
M a,236,v,244,v,84
R v,236,1,8704
W 7
M a,224,v,248,v,88
R v,238,4,8704
M a,232,v,248,v,92
R v,240,1,1024
M a,228,v,252,v,88
R v,242,4,1024
M a,236,v,252,v,92
R v,244,1,9216
W 7
v_mfma_f32_16x16x32_bf16 a[240:243], v[224:227], v[32:35], 0
R v,246,4,9216
v_mfma_f32_16x16x32_bf16 a[248:251], v[224:227], v[36:39], 0
R v,248,1,1536
v_mfma_f32_16x16x32_bf16 a[244:247], v[228:231], v[32:35], 0
R v,250,4,1536
v_mfma_f32_16x16x32_bf16 a[252:255], v[228:231], v[36:39], 0
R v,252,1,9728
W 7
M a,240,v,232,v,40
R v,254,4,9728
M a,248,v,232,v,44
R v,224,1,2048
A 30,224
A 31,225
M a,244,v,236,v,40
R v,226,4,2048
v_max3_f32 v26, v30, v31, v26
M a,252,v,236,v,44
R v,228,1,10240
W 7
A 30,226
M a,240,v,240,v,48
R v,230,4,10240
A 31,227
M a,248,v,240,v,52
R v,232,1,2560
v_max3_f32 v26, v30, v31, v26
A 30,228
M a,244,v,244,v,48
R v,234,4,2560
A 31,229
M a,252,v,244,v,52
R v,236,1,10752
W 7
v_max3_f32 v26, v30, v31, v26
M a,240,v,248,v,56
R v,238,4,10752
A 30,230
M a,248,v,248,v,60
R v,240,1,3072
A 31,231
v_max3_f32 v26, v30, v31, v26
M a,244,v,252,v,56
R v,242,4,3072
A 30,232
M a,252,v,252,v,60
R v,244,1,11264
W 7
M a,240,v,224,v,64
R v,246,4,11264
A 31,233
M a,248,v,224,v,68
R v,248,1,3584
v_max3_f32 v27, v30, v31, v27
M a,244,v,228,v,64
R v,250,4,3584
A 30,234
M a,252,v,228,v,68
R v,252,1,11776
W 7
M a,240,v,232,v,72
R v,254,4,11776
A 31,235
M a,248,v,232,v,76
v_max3_f32 v27, v30, v31, v27
A 30,236
M a,244,v,236,v,72
A 31,237
v_max3_f32 v27, v30, v31, v27
M a,252,v,236,v,76
W 6
A 30,238
M a,240,v,240,v,80
A 31,239
v_max3_f32 v27, v30, v31, v27
M a,248,v,240,v,84
W 4
M a,244,v,244,v,80
M a,252,v,244,v,84
W 2
M a,240,v,248,v,88
M a,248,v,248,v,92
W 0
M a,244,v,252,v,88
M a,252,v,252,v,92
s_barrier
s_nop 7
v_cvt_pk_bf16_f32 v32, v96, v97
v_cvt_pk_bf16_f32 v33, v98, v99
v_cvt_pk_bf16_f32 v34, v100, v101
v_cvt_pk_bf16_f32 v35, v102, v103
v_cvt_pk_bf16_f32 v36, v104, v105
v_cvt_pk_bf16_f32 v37, v106, v107
v_cvt_pk_bf16_f32 v38, v108, v109
v_cvt_pk_bf16_f32 v39, v110, v111
v_cvt_pk_bf16_f32 v40, v112, v113
v_cvt_pk_bf16_f32 v41, v114, v115
v_cvt_pk_bf16_f32 v42, v116, v117
v_cvt_pk_bf16_f32 v43, v118, v119
v_cvt_pk_bf16_f32 v44, v120, v121
v_cvt_pk_bf16_f32 v45, v122, v123
v_cvt_pk_bf16_f32 v46, v124, v125
v_cvt_pk_bf16_f32 v47, v126, v127
v_cvt_pk_bf16_f32 v48, v128, v129
v_cvt_pk_bf16_f32 v49, v130, v131
v_cvt_pk_bf16_f32 v50, v132, v133
v_cvt_pk_bf16_f32 v51, v134, v135
v_cvt_pk_bf16_f32 v52, v136, v137
v_cvt_pk_bf16_f32 v53, v138, v139
v_cvt_pk_bf16_f32 v54, v140, v141
v_cvt_pk_bf16_f32 v55, v142, v143
v_cvt_pk_bf16_f32 v56, v144, v145
v_cvt_pk_bf16_f32 v57, v146, v147
v_cvt_pk_bf16_f32 v58, v148, v149
v_cvt_pk_bf16_f32 v59, v150, v151
v_cvt_pk_bf16_f32 v60, v152, v153
v_cvt_pk_bf16_f32 v61, v154, v155
v_cvt_pk_bf16_f32 v62, v156, v157
v_cvt_pk_bf16_f32 v63, v158, v159
v_cvt_pk_bf16_f32 v64, v160, v161
v_cvt_pk_bf16_f32 v65, v162, v163
v_cvt_pk_bf16_f32 v66, v164, v165
v_cvt_pk_bf16_f32 v67, v166, v167
v_cvt_pk_bf16_f32 v68, v168, v169
v_cvt_pk_bf16_f32 v69, v170, v171
v_cvt_pk_bf16_f32 v70, v172, v173
v_cvt_pk_bf16_f32 v71, v174, v175
v_cvt_pk_bf16_f32 v72, v176, v177
v_cvt_pk_bf16_f32 v73, v178, v179
v_cvt_pk_bf16_f32 v74, v180, v181
v_cvt_pk_bf16_f32 v75, v182, v183
v_cvt_pk_bf16_f32 v76, v184, v185
v_cvt_pk_bf16_f32 v77, v186, v187
v_cvt_pk_bf16_f32 v78, v188, v189
v_cvt_pk_bf16_f32 v79, v190, v191
v_cvt_pk_bf16_f32 v80, v192, v193
v_cvt_pk_bf16_f32 v81, v194, v195
v_cvt_pk_bf16_f32 v82, v196, v197
v_cvt_pk_bf16_f32 v83, v198, v199
v_cvt_pk_bf16_f32 v84, v200, v201
v_cvt_pk_bf16_f32 v85, v202, v203
v_cvt_pk_bf16_f32 v86, v204, v205
v_cvt_pk_bf16_f32 v87, v206, v207
v_cvt_pk_bf16_f32 v88, v208, v209
v_cvt_pk_bf16_f32 v89, v210, v211
v_cvt_pk_bf16_f32 v90, v212, v213
v_cvt_pk_bf16_f32 v91, v214, v215
v_cvt_pk_bf16_f32 v92, v216, v217
v_cvt_pk_bf16_f32 v93, v218, v219
v_cvt_pk_bf16_f32 v94, v220, v221
v_cvt_pk_bf16_f32 v95, v222, v223
v_lshrrev_b32 v30, 5, v0
v_mul_u32_u24 v30, 0xc00, v30
v_sub_u32 v13, v7, v30
v_sub_u32 v16, v10, v30
v_add_u32 v14, 0x10000, v13
v_add_u32 v15, 0x20000, v13
v_add_u32 v17, 0x10000, v16
v_add_u32 v18, 0x20000, v16
A 30,240
A 31,241
v_max3_f32 v26, v30, v31, v26
A 30,242
A 31,243
v_max3_f32 v26, v30, v31, v26
A 30,244
A 31,245
v_max3_f32 v26, v30, v31, v26
A 30,246
A 31,247
v_max3_f32 v26, v30, v31, v26
A 30,248
A 31,249
v_max3_f32 v27, v30, v31, v27
A 30,250
A 31,251
v_max3_f32 v27, v30, v31, v27
A 30,252
A 31,253
v_max3_f32 v27, v30, v31, v27
A 30,254
A 31,255
v_max3_f32 v27, v30, v31, v27
v_mov_b32 v244, 0x3f803f80
v_mov_b32 v245, 0x3f803f80
v_mov_b32 v246, 0x3f803f80
v_mov_b32 v247, 0x3f803f80
v_mov_b32 v30, v26
s_nop 1
v_permlane32_swap_b32 v30, v26
s_nop 1
v_max_f32 v26, v30, v26
s_nop 0
v_mov_b32 v30, v26
s_nop 1
v_permlane16_swap_b32 v30, v26
s_nop 1
v_max_f32 v26, v30, v26
v_mov_b32 v30, v27
s_nop 1
v_permlane32_swap_b32 v30, v27
s_nop 1
v_max_f32 v27, v30, v27
s_nop 0
v_mov_b32 v30, v27
s_nop 1
v_permlane16_swap_b32 v30, v27
s_nop 1
v_max_f32 v27, v30, v27
v_mul_f32 v30, s62, v26
v_sub_f32 v30, 0, v30
v_mov_b32 v31, v30
v_mul_f32 v28, s62, v27
v_sub_f32 v28, 0, v28
v_mov_b32 v29, v28
A 224,0
A 225,1
A 226,2
A 227,3
A 228,4
A 229,5
A 230,6
A 231,7
v_pk_fma_f32 v[224:225], v[224:225], s[62:63], v[30:31]
v_pk_fma_f32 v[226:227], v[226:227], s[62:63], v[30:31]
v_pk_fma_f32 v[228:229], v[228:229], s[62:63], v[30:31]
v_pk_fma_f32 v[230:231], v[230:231], s[62:63], v[30:31]
v_exp_f32 v224, v224
v_exp_f32 v225, v225
v_exp_f32 v226, v226
v_exp_f32 v227, v227
v_exp_f32 v228, v228
v_exp_f32 v229, v229
v_exp_f32 v230, v230
v_exp_f32 v231, v231
A 232,8
A 233,9
A 234,10
A 235,11
A 236,12
A 237,13
A 238,14
A 239,15
v_pk_fma_f32 v[232:233], v[232:233], s[62:63], v[28:29]
v_pk_fma_f32 v[234:235], v[234:235], s[62:63], v[28:29]
v_pk_fma_f32 v[236:237], v[236:237], s[62:63], v[28:29]
v_pk_fma_f32 v[238:239], v[238:239], s[62:63], v[28:29]
v_cvt_pk_bf16_f32 v96, v224, v225
v_cvt_pk_bf16_f32 v97, v226, v227
v_cvt_pk_bf16_f32 v98, v228, v229
v_cvt_pk_bf16_f32 v99, v230, v231
v_exp_f32 v232, v232
v_exp_f32 v233, v233
v_exp_f32 v234, v234
v_exp_f32 v235, v235
v_mfma_f32_16x16x32_bf16 v[240:243], v[244:247], v[96:99], 0
v_exp_f32 v236, v236
v_exp_f32 v237, v237
v_exp_f32 v238, v238
v_exp_f32 v239, v239
A 224,16
A 225,17
A 226,18
A 227,19
A 228,20
A 229,21
A 230,22
A 231,23
v_pk_fma_f32 v[224:225], v[224:225], s[62:63], v[30:31]
v_pk_fma_f32 v[226:227], v[226:227], s[62:63], v[30:31]
v_pk_fma_f32 v[228:229], v[228:229], s[62:63], v[30:31]
v_pk_fma_f32 v[230:231], v[230:231], s[62:63], v[30:31]
v_cvt_pk_bf16_f32 v100, v232, v233
v_cvt_pk_bf16_f32 v101, v234, v235
v_cvt_pk_bf16_f32 v102, v236, v237
v_cvt_pk_bf16_f32 v103, v238, v239
v_exp_f32 v224, v224
v_exp_f32 v225, v225
v_exp_f32 v226, v226
v_exp_f32 v227, v227
v_mfma_f32_16x16x32_bf16 v[248:251], v[244:247], v[100:103], 0
v_exp_f32 v228, v228
v_exp_f32 v229, v229
v_exp_f32 v230, v230
v_exp_f32 v231, v231
A 232,24
A 233,25
A 234,26
A 235,27
A 236,28
A 237,29
A 238,30
A 239,31
v_pk_fma_f32 v[232:233], v[232:233], s[62:63], v[28:29]
v_pk_fma_f32 v[234:235], v[234:235], s[62:63], v[28:29]
v_pk_fma_f32 v[236:237], v[236:237], s[62:63], v[28:29]
v_pk_fma_f32 v[238:239], v[238:239], s[62:63], v[28:29]
v_cvt_pk_bf16_f32 v104, v224, v225
v_cvt_pk_bf16_f32 v105, v226, v227
v_cvt_pk_bf16_f32 v106, v228, v229
v_cvt_pk_bf16_f32 v107, v230, v231
v_exp_f32 v232, v232
v_exp_f32 v233, v233
v_exp_f32 v234, v234
v_exp_f32 v235, v235
M v,240,v,244,v,104
v_exp_f32 v236, v236
v_exp_f32 v237, v237
v_exp_f32 v238, v238
v_exp_f32 v239, v239
A 224,32
A 225,33
A 226,34
A 227,35
A 228,36
A 229,37
A 230,38
A 231,39
v_pk_fma_f32 v[224:225], v[224:225], s[62:63], v[30:31]
v_pk_fma_f32 v[226:227], v[226:227], s[62:63], v[30:31]
v_pk_fma_f32 v[228:229], v[228:229], s[62:63], v[30:31]
v_pk_fma_f32 v[230:231], v[230:231], s[62:63], v[30:31]
v_cvt_pk_bf16_f32 v108, v232, v233
v_cvt_pk_bf16_f32 v109, v234, v235
v_cvt_pk_bf16_f32 v110, v236, v237
v_cvt_pk_bf16_f32 v111, v238, v239
v_exp_f32 v224, v224
v_exp_f32 v225, v225
v_exp_f32 v226, v226
v_exp_f32 v227, v227
M v,248,v,244,v,108
v_exp_f32 v228, v228
v_exp_f32 v229, v229
v_exp_f32 v230, v230
v_exp_f32 v231, v231
A 232,40
A 233,41
A 234,42
A 235,43
A 236,44
A 237,45
A 238,46
A 239,47
v_pk_fma_f32 v[232:233], v[232:233], s[62:63], v[28:29]
v_pk_fma_f32 v[234:235], v[234:235], s[62:63], v[28:29]
v_pk_fma_f32 v[236:237], v[236:237], s[62:63], v[28:29]
v_pk_fma_f32 v[238:239], v[238:239], s[62:63], v[28:29]
v_cvt_pk_bf16_f32 v112, v224, v225
v_cvt_pk_bf16_f32 v113, v226, v227
v_cvt_pk_bf16_f32 v114, v228, v229
v_cvt_pk_bf16_f32 v115, v230, v231
v_exp_f32 v232, v232
v_exp_f32 v233, v233
v_exp_f32 v234, v234
v_exp_f32 v235, v235
M v,240,v,244,v,112
v_exp_f32 v236, v236
v_exp_f32 v237, v237
v_exp_f32 v238, v238
v_exp_f32 v239, v239
A 224,48
A 225,49
A 226,50
A 227,51
A 228,52
A 229,53
A 230,54
A 231,55
v_pk_fma_f32 v[224:225], v[224:225], s[62:63], v[30:31]
v_pk_fma_f32 v[226:227], v[226:227], s[62:63], v[30:31]
v_pk_fma_f32 v[228:229], v[228:229], s[62:63], v[30:31]
)x"
R"x(v_pk_fma_f32 v[230:231], v[230:231], s[62:63], v[30:31]
v_cvt_pk_bf16_f32 v116, v232, v233
v_cvt_pk_bf16_f32 v117, v234, v235
v_cvt_pk_bf16_f32 v118, v236, v237
v_cvt_pk_bf16_f32 v119, v238, v239
v_exp_f32 v224, v224
v_exp_f32 v225, v225
v_exp_f32 v226, v226
v_exp_f32 v227, v227
M v,248,v,244,v,116
v_exp_f32 v228, v228
v_exp_f32 v229, v229
v_exp_f32 v230, v230
v_exp_f32 v231, v231
A 232,56
A 233,57
A 234,58
A 235,59
A 236,60
A 237,61
A 238,62
A 239,63
v_pk_fma_f32 v[232:233], v[232:233], s[62:63], v[28:29]
v_pk_fma_f32 v[234:235], v[234:235], s[62:63], v[28:29]
v_pk_fma_f32 v[236:237], v[236:237], s[62:63], v[28:29]
v_pk_fma_f32 v[238:239], v[238:239], s[62:63], v[28:29]
v_cvt_pk_bf16_f32 v120, v224, v225
v_cvt_pk_bf16_f32 v121, v226, v227
v_cvt_pk_bf16_f32 v122, v228, v229
v_cvt_pk_bf16_f32 v123, v230, v231
v_exp_f32 v232, v232
v_exp_f32 v233, v233
v_exp_f32 v234, v234
v_exp_f32 v235, v235
M v,240,v,244,v,120
v_exp_f32 v236, v236
v_exp_f32 v237, v237
v_exp_f32 v238, v238
v_exp_f32 v239, v239
A 224,64
A 225,65
A 226,66
A 227,67
A 228,68
A 229,69
A 230,70
A 231,71
v_pk_fma_f32 v[224:225], v[224:225], s[62:63], v[30:31]
v_pk_fma_f32 v[226:227], v[226:227], s[62:63], v[30:31]
v_pk_fma_f32 v[228:229], v[228:229], s[62:63], v[30:31]
v_pk_fma_f32 v[230:231], v[230:231], s[62:63], v[30:31]
v_cvt_pk_bf16_f32 v124, v232, v233
v_cvt_pk_bf16_f32 v125, v234, v235
v_cvt_pk_bf16_f32 v126, v236, v237
v_cvt_pk_bf16_f32 v127, v238, v239
v_exp_f32 v224, v224
v_exp_f32 v225, v225
v_exp_f32 v226, v226
v_exp_f32 v227, v227
M v,248,v,244,v,124
v_exp_f32 v228, v228
v_exp_f32 v229, v229
v_exp_f32 v230, v230
v_exp_f32 v231, v231
A 232,72
A 233,73
A 234,74
A 235,75
A 236,76
A 237,77
A 238,78
A 239,79
v_pk_fma_f32 v[232:233], v[232:233], s[62:63], v[28:29]
v_pk_fma_f32 v[234:235], v[234:235], s[62:63], v[28:29]
v_pk_fma_f32 v[236:237], v[236:237], s[62:63], v[28:29]
v_pk_fma_f32 v[238:239], v[238:239], s[62:63], v[28:29]
v_cvt_pk_bf16_f32 v128, v224, v225
v_cvt_pk_bf16_f32 v129, v226, v227
v_cvt_pk_bf16_f32 v130, v228, v229
v_cvt_pk_bf16_f32 v131, v230, v231
v_exp_f32 v232, v232
v_exp_f32 v233, v233
v_exp_f32 v234, v234
v_exp_f32 v235, v235
M v,240,v,244,v,128
v_exp_f32 v236, v236
v_exp_f32 v237, v237
v_exp_f32 v238, v238
v_exp_f32 v239, v239
A 224,80
A 225,81
A 226,82
A 227,83
A 228,84
A 229,85
A 230,86
A 231,87
v_pk_fma_f32 v[224:225], v[224:225], s[62:63], v[30:31]
v_pk_fma_f32 v[226:227], v[226:227], s[62:63], v[30:31]
v_pk_fma_f32 v[228:229], v[228:229], s[62:63], v[30:31]
v_pk_fma_f32 v[230:231], v[230:231], s[62:63], v[30:31]
v_cvt_pk_bf16_f32 v132, v232, v233
v_cvt_pk_bf16_f32 v133, v234, v235
v_cvt_pk_bf16_f32 v134, v236, v237
v_cvt_pk_bf16_f32 v135, v238, v239
v_exp_f32 v224, v224
v_exp_f32 v225, v225
v_exp_f32 v226, v226
v_exp_f32 v227, v227
M v,248,v,244,v,132
v_exp_f32 v228, v228
v_exp_f32 v229, v229
v_exp_f32 v230, v230
v_exp_f32 v231, v231
A 232,88
A 233,89
A 234,90
A 235,91
A 236,92
A 237,93
A 238,94
A 239,95
v_pk_fma_f32 v[232:233], v[232:233], s[62:63], v[28:29]
v_pk_fma_f32 v[234:235], v[234:235], s[62:63], v[28:29]
v_pk_fma_f32 v[236:237], v[236:237], s[62:63], v[28:29]
v_pk_fma_f32 v[238:239], v[238:239], s[62:63], v[28:29]
v_cvt_pk_bf16_f32 v136, v224, v225
v_cvt_pk_bf16_f32 v137, v226, v227
v_cvt_pk_bf16_f32 v138, v228, v229
v_cvt_pk_bf16_f32 v139, v230, v231
v_exp_f32 v232, v232
v_exp_f32 v233, v233
v_exp_f32 v234, v234
v_exp_f32 v235, v235
M v,240,v,244,v,136
v_exp_f32 v236, v236
v_exp_f32 v237, v237
v_exp_f32 v238, v238
v_exp_f32 v239, v239
A 224,96
A 225,97
A 226,98
A 227,99
A 228,100
A 229,101
A 230,102
A 231,103
v_pk_fma_f32 v[224:225], v[224:225], s[62:63], v[30:31]
v_pk_fma_f32 v[226:227], v[226:227], s[62:63], v[30:31]
v_pk_fma_f32 v[228:229], v[228:229], s[62:63], v[30:31]
v_pk_fma_f32 v[230:231], v[230:231], s[62:63], v[30:31]
v_cvt_pk_bf16_f32 v140, v232, v233
v_cvt_pk_bf16_f32 v141, v234, v235
v_cvt_pk_bf16_f32 v142, v236, v237
v_cvt_pk_bf16_f32 v143, v238, v239
v_exp_f32 v224, v224
v_exp_f32 v225, v225
v_exp_f32 v226, v226
v_exp_f32 v227, v227
M v,248,v,244,v,140
v_exp_f32 v228, v228
v_exp_f32 v229, v229
v_exp_f32 v230, v230
v_exp_f32 v231, v231
A 232,104
A 233,105
A 234,106
A 235,107
A 236,108
A 237,109
A 238,110
A 239,111
v_pk_fma_f32 v[232:233], v[232:233], s[62:63], v[28:29]
v_pk_fma_f32 v[234:235], v[234:235], s[62:63], v[28:29]
v_pk_fma_f32 v[236:237], v[236:237], s[62:63], v[28:29]
v_pk_fma_f32 v[238:239], v[238:239], s[62:63], v[28:29]
v_cvt_pk_bf16_f32 v144, v224, v225
v_cvt_pk_bf16_f32 v145, v226, v227
v_cvt_pk_bf16_f32 v146, v228, v229
v_cvt_pk_bf16_f32 v147, v230, v231
v_exp_f32 v232, v232
v_exp_f32 v233, v233
v_exp_f32 v234, v234
v_exp_f32 v235, v235
M v,240,v,244,v,144
v_exp_f32 v236, v236
v_exp_f32 v237, v237
v_exp_f32 v238, v238
v_exp_f32 v239, v239
A 224,112
A 225,113
A 226,114
A 227,115
A 228,116
A 229,117
A 230,118
A 231,119
v_pk_fma_f32 v[224:225], v[224:225], s[62:63], v[30:31]
v_pk_fma_f32 v[226:227], v[226:227], s[62:63], v[30:31]
v_pk_fma_f32 v[228:229], v[228:229], s[62:63], v[30:31]
v_pk_fma_f32 v[230:231], v[230:231], s[62:63], v[30:31]
v_cvt_pk_bf16_f32 v148, v232, v233
v_cvt_pk_bf16_f32 v149, v234, v235
v_cvt_pk_bf16_f32 v150, v236, v237
v_cvt_pk_bf16_f32 v151, v238, v239
v_exp_f32 v224, v224
v_exp_f32 v225, v225
v_exp_f32 v226, v226
v_exp_f32 v227, v227
M v,248,v,244,v,148
v_exp_f32 v228, v228
v_exp_f32 v229, v229
v_exp_f32 v230, v230
v_exp_f32 v231, v231
A 232,120
A 233,121
A 234,122
A 235,123
A 236,124
A 237,125
A 238,126
A 239,127
v_pk_fma_f32 v[232:233], v[232:233], s[62:63], v[28:29]
v_pk_fma_f32 v[234:235], v[234:235], s[62:63], v[28:29]
v_pk_fma_f32 v[236:237], v[236:237], s[62:63], v[28:29]
v_pk_fma_f32 v[238:239], v[238:239], s[62:63], v[28:29]
v_cvt_pk_bf16_f32 v152, v224, v225
v_cvt_pk_bf16_f32 v153, v226, v227
v_cvt_pk_bf16_f32 v154, v228, v229
v_cvt_pk_bf16_f32 v155, v230, v231
v_exp_f32 v232, v232
v_exp_f32 v233, v233
v_exp_f32 v234, v234
v_exp_f32 v235, v235
M v,240,v,244,v,152
v_exp_f32 v236, v236
v_exp_f32 v237, v237
v_exp_f32 v238, v238
v_exp_f32 v239, v239
A 224,128
A 225,129
A 226,130
A 227,131
A 228,132
A 229,133
A 230,134
A 231,135
v_pk_fma_f32 v[224:225], v[224:225], s[62:63], v[30:31]
v_pk_fma_f32 v[226:227], v[226:227], s[62:63], v[30:31]
v_pk_fma_f32 v[228:229], v[228:229], s[62:63], v[30:31]
v_pk_fma_f32 v[230:231], v[230:231], s[62:63], v[30:31]
v_cvt_pk_bf16_f32 v156, v232, v233
v_cvt_pk_bf16_f32 v157, v234, v235
v_cvt_pk_bf16_f32 v158, v236, v237
v_cvt_pk_bf16_f32 v159, v238, v239
v_exp_f32 v224, v224
v_exp_f32 v225, v225
v_exp_f32 v226, v226
v_exp_f32 v227, v227
M v,248,v,244,v,156
v_exp_f32 v228, v228
v_exp_f32 v229, v229
v_exp_f32 v230, v230
v_exp_f32 v231, v231
A 232,136
A 233,137
A 234,138
A 235,139
A 236,140
A 237,141
A 238,142
A 239,143
v_pk_fma_f32 v[232:233], v[232:233], s[62:63], v[28:29]
v_pk_fma_f32 v[234:235], v[234:235], s[62:63], v[28:29]
v_pk_fma_f32 v[236:237], v[236:237], s[62:63], v[28:29]
v_pk_fma_f32 v[238:239], v[238:239], s[62:63], v[28:29]
v_cvt_pk_bf16_f32 v160, v224, v225
v_cvt_pk_bf16_f32 v161, v226, v227
v_cvt_pk_bf16_f32 v162, v228, v229
v_cvt_pk_bf16_f32 v163, v230, v231
v_exp_f32 v232, v232
v_exp_f32 v233, v233
v_exp_f32 v234, v234
v_exp_f32 v235, v235
M v,240,v,244,v,160
v_exp_f32 v236, v236
v_exp_f32 v237, v237
v_exp_f32 v238, v238
v_exp_f32 v239, v239
A 224,144
A 225,145
A 226,146
A 227,147
A 228,148
A 229,149
A 230,150
A 231,151
v_pk_fma_f32 v[224:225], v[224:225], s[62:63], v[30:31]
v_pk_fma_f32 v[226:227], v[226:227], s[62:63], v[30:31]
v_pk_fma_f32 v[228:229], v[228:229], s[62:63], v[30:31]
v_pk_fma_f32 v[230:231], v[230:231], s[62:63], v[30:31]
v_cvt_pk_bf16_f32 v164, v232, v233
v_cvt_pk_bf16_f32 v165, v234, v235
v_cvt_pk_bf16_f32 v166, v236, v237
v_cvt_pk_bf16_f32 v167, v238, v239
v_exp_f32 v224, v224
v_exp_f32 v225, v225
v_exp_f32 v226, v226
v_exp_f32 v227, v227
M v,248,v,244,v,164
v_exp_f32 v228, v228
v_exp_f32 v229, v229
v_exp_f32 v230, v230
v_exp_f32 v231, v231
A 232,152
A 233,153
A 234,154
A 235,155
A 236,156
A 237,157
A 238,158
A 239,159
v_pk_fma_f32 v[232:233], v[232:233], s[62:63], v[28:29]
v_pk_fma_f32 v[234:235], v[234:235], s[62:63], v[28:29]
v_pk_fma_f32 v[236:237], v[236:237], s[62:63], v[28:29]
v_pk_fma_f32 v[238:239], v[238:239], s[62:63], v[28:29]
v_cvt_pk_bf16_f32 v168, v224, v225
v_cvt_pk_bf16_f32 v169, v226, v227
v_cvt_pk_bf16_f32 v170, v228, v229
v_cvt_pk_bf16_f32 v171, v230, v231
v_exp_f32 v232, v232
v_exp_f32 v233, v233
v_exp_f32 v234, v234
v_exp_f32 v235, v235
M v,240,v,244,v,168
v_exp_f32 v236, v236
v_exp_f32 v237, v237
v_exp_f32 v238, v238
v_exp_f32 v239, v239
A 224,160
A 225,161
A 226,162
A 227,163
A 228,164
A 229,165
A 230,166
A 231,167
v_pk_fma_f32 v[224:225], v[224:225], s[62:63], v[30:31]
v_pk_fma_f32 v[226:227], v[226:227], s[62:63], v[30:31]
v_pk_fma_f32 v[228:229], v[228:229], s[62:63], v[30:31]
v_pk_fma_f32 v[230:231], v[230:231], s[62:63], v[30:31]
v_cvt_pk_bf16_f32 v172, v232, v233
v_cvt_pk_bf16_f32 v173, v234, v235
v_cvt_pk_bf16_f32 v174, v236, v237
v_cvt_pk_bf16_f32 v175, v238, v239
v_exp_f32 v224, v224
v_exp_f32 v225, v225
v_exp_f32 v226, v226
v_exp_f32 v227, v227
M v,248,v,244,v,172
v_exp_f32 v228, v228
v_exp_f32 v229, v229
v_exp_f32 v230, v230
v_exp_f32 v231, v231
A 232,168
A 233,169
A 234,170
A 235,171
A 236,172
A 237,173
A 238,174
A 239,175
v_pk_fma_f32 v[232:233], v[232:233], s[62:63], v[28:29]
v_pk_fma_f32 v[234:235], v[234:235], s[62:63], v[28:29]
v_pk_fma_f32 v[236:237], v[236:237], s[62:63], v[28:29]
v_pk_fma_f32 v[238:239], v[238:239], s[62:63], v[28:29]
v_cvt_pk_bf16_f32 v176, v224, v225
v_cvt_pk_bf16_f32 v177, v226, v227
v_cvt_pk_bf16_f32 v178, v228, v229
v_cvt_pk_bf16_f32 v179, v230, v231
v_exp_f32 v232, v232
v_exp_f32 v233, v233
v_exp_f32 v234, v234
v_exp_f32 v235, v235
M v,240,v,244,v,176
v_exp_f32 v236, v236
v_exp_f32 v237, v237
v_exp_f32 v238, v238
v_exp_f32 v239, v239
A 224,176
A 225,177
A 226,178
A 227,179
A 228,180
A 229,181
A 230,182
A 231,183
v_pk_fma_f32 v[224:225], v[224:225], s[62:63], v[30:31]
v_pk_fma_f32 v[226:227], v[226:227], s[62:63], v[30:31]
v_pk_fma_f32 v[228:229], v[228:229], s[62:63], v[30:31]
)x"
R"x(v_pk_fma_f32 v[230:231], v[230:231], s[62:63], v[30:31]
v_cvt_pk_bf16_f32 v180, v232, v233
v_cvt_pk_bf16_f32 v181, v234, v235
v_cvt_pk_bf16_f32 v182, v236, v237
v_cvt_pk_bf16_f32 v183, v238, v239
v_exp_f32 v224, v224
v_exp_f32 v225, v225
v_exp_f32 v226, v226
v_exp_f32 v227, v227
M v,248,v,244,v,180
v_exp_f32 v228, v228
v_exp_f32 v229, v229
v_exp_f32 v230, v230
v_exp_f32 v231, v231
A 232,184
A 233,185
A 234,186
A 235,187
A 236,188
A 237,189
A 238,190
A 239,191
v_pk_fma_f32 v[232:233], v[232:233], s[62:63], v[28:29]
v_pk_fma_f32 v[234:235], v[234:235], s[62:63], v[28:29]
v_pk_fma_f32 v[236:237], v[236:237], s[62:63], v[28:29]
v_pk_fma_f32 v[238:239], v[238:239], s[62:63], v[28:29]
v_cvt_pk_bf16_f32 v184, v224, v225
v_cvt_pk_bf16_f32 v185, v226, v227
v_cvt_pk_bf16_f32 v186, v228, v229
v_cvt_pk_bf16_f32 v187, v230, v231
v_exp_f32 v232, v232
v_exp_f32 v233, v233
v_exp_f32 v234, v234
v_exp_f32 v235, v235
M v,240,v,244,v,184
v_exp_f32 v236, v236
v_exp_f32 v237, v237
v_exp_f32 v238, v238
v_exp_f32 v239, v239
A 224,192
A 225,193
A 226,194
A 227,195
A 228,196
A 229,197
A 230,198
A 231,199
v_pk_fma_f32 v[224:225], v[224:225], s[62:63], v[30:31]
v_pk_fma_f32 v[226:227], v[226:227], s[62:63], v[30:31]
v_pk_fma_f32 v[228:229], v[228:229], s[62:63], v[30:31]
v_pk_fma_f32 v[230:231], v[230:231], s[62:63], v[30:31]
v_cvt_pk_bf16_f32 v188, v232, v233
v_cvt_pk_bf16_f32 v189, v234, v235
v_cvt_pk_bf16_f32 v190, v236, v237
v_cvt_pk_bf16_f32 v191, v238, v239
v_exp_f32 v224, v224
v_exp_f32 v225, v225
v_exp_f32 v226, v226
v_exp_f32 v227, v227
M v,248,v,244,v,188
v_exp_f32 v228, v228
v_exp_f32 v229, v229
v_exp_f32 v230, v230
v_exp_f32 v231, v231
A 232,200
A 233,201
A 234,202
A 235,203
A 236,204
A 237,205
A 238,206
A 239,207
v_pk_fma_f32 v[232:233], v[232:233], s[62:63], v[28:29]
v_pk_fma_f32 v[234:235], v[234:235], s[62:63], v[28:29]
v_pk_fma_f32 v[236:237], v[236:237], s[62:63], v[28:29]
v_pk_fma_f32 v[238:239], v[238:239], s[62:63], v[28:29]
v_cvt_pk_bf16_f32 v192, v224, v225
v_cvt_pk_bf16_f32 v193, v226, v227
v_cvt_pk_bf16_f32 v194, v228, v229
v_cvt_pk_bf16_f32 v195, v230, v231
v_exp_f32 v232, v232
v_exp_f32 v233, v233
v_exp_f32 v234, v234
v_exp_f32 v235, v235
M v,240,v,244,v,192
v_exp_f32 v236, v236
v_exp_f32 v237, v237
v_exp_f32 v238, v238
v_exp_f32 v239, v239
A 224,208
A 225,209
A 226,210
A 227,211
A 228,212
A 229,213
A 230,214
A 231,215
v_pk_fma_f32 v[224:225], v[224:225], s[62:63], v[30:31]
v_pk_fma_f32 v[226:227], v[226:227], s[62:63], v[30:31]
v_pk_fma_f32 v[228:229], v[228:229], s[62:63], v[30:31]
v_pk_fma_f32 v[230:231], v[230:231], s[62:63], v[30:31]
v_cvt_pk_bf16_f32 v196, v232, v233
v_cvt_pk_bf16_f32 v197, v234, v235
v_cvt_pk_bf16_f32 v198, v236, v237
v_cvt_pk_bf16_f32 v199, v238, v239
v_exp_f32 v224, v224
v_exp_f32 v225, v225
v_exp_f32 v226, v226
v_exp_f32 v227, v227
M v,248,v,244,v,196
v_exp_f32 v228, v228
v_exp_f32 v229, v229
v_exp_f32 v230, v230
v_exp_f32 v231, v231
A 232,216
A 233,217
A 234,218
A 235,219
A 236,220
A 237,221
A 238,222
A 239,223
v_pk_fma_f32 v[232:233], v[232:233], s[62:63], v[28:29]
v_pk_fma_f32 v[234:235], v[234:235], s[62:63], v[28:29]
v_pk_fma_f32 v[236:237], v[236:237], s[62:63], v[28:29]
v_pk_fma_f32 v[238:239], v[238:239], s[62:63], v[28:29]
v_cvt_pk_bf16_f32 v200, v224, v225
v_cvt_pk_bf16_f32 v201, v226, v227
v_cvt_pk_bf16_f32 v202, v228, v229
v_cvt_pk_bf16_f32 v203, v230, v231
v_exp_f32 v232, v232
v_exp_f32 v233, v233
v_exp_f32 v234, v234
v_exp_f32 v235, v235
M v,240,v,244,v,200
v_exp_f32 v236, v236
v_exp_f32 v237, v237
v_exp_f32 v238, v238
v_exp_f32 v239, v239
A 224,224
A 225,225
A 226,226
A 227,227
A 228,228
A 229,229
A 230,230
A 231,231
v_pk_fma_f32 v[224:225], v[224:225], s[62:63], v[30:31]
v_pk_fma_f32 v[226:227], v[226:227], s[62:63], v[30:31]
v_pk_fma_f32 v[228:229], v[228:229], s[62:63], v[30:31]
v_pk_fma_f32 v[230:231], v[230:231], s[62:63], v[30:31]
v_cvt_pk_bf16_f32 v204, v232, v233
v_cvt_pk_bf16_f32 v205, v234, v235
v_cvt_pk_bf16_f32 v206, v236, v237
v_cvt_pk_bf16_f32 v207, v238, v239
v_exp_f32 v224, v224
v_exp_f32 v225, v225
v_exp_f32 v226, v226
v_exp_f32 v227, v227
M v,248,v,244,v,204
v_exp_f32 v228, v228
v_exp_f32 v229, v229
v_exp_f32 v230, v230
v_exp_f32 v231, v231
A 232,232
A 233,233
A 234,234
A 235,235
A 236,236
A 237,237
A 238,238
A 239,239
v_pk_fma_f32 v[232:233], v[232:233], s[62:63], v[28:29]
v_pk_fma_f32 v[234:235], v[234:235], s[62:63], v[28:29]
v_pk_fma_f32 v[236:237], v[236:237], s[62:63], v[28:29]
v_pk_fma_f32 v[238:239], v[238:239], s[62:63], v[28:29]
v_cvt_pk_bf16_f32 v208, v224, v225
v_cvt_pk_bf16_f32 v209, v226, v227
v_cvt_pk_bf16_f32 v210, v228, v229
v_cvt_pk_bf16_f32 v211, v230, v231
v_exp_f32 v232, v232
v_exp_f32 v233, v233
v_exp_f32 v234, v234
v_exp_f32 v235, v235
M v,240,v,244,v,208
v_exp_f32 v236, v236
v_exp_f32 v237, v237
v_exp_f32 v238, v238
v_exp_f32 v239, v239
A 224,240
A 225,241
A 226,242
A 227,243
A 228,244
A 229,245
A 230,246
A 231,247
v_pk_fma_f32 v[224:225], v[224:225], s[62:63], v[30:31]
v_pk_fma_f32 v[226:227], v[226:227], s[62:63], v[30:31]
v_pk_fma_f32 v[228:229], v[228:229], s[62:63], v[30:31]
v_pk_fma_f32 v[230:231], v[230:231], s[62:63], v[30:31]
v_cvt_pk_bf16_f32 v212, v232, v233
v_cvt_pk_bf16_f32 v213, v234, v235
v_cvt_pk_bf16_f32 v214, v236, v237
v_cvt_pk_bf16_f32 v215, v238, v239
v_exp_f32 v224, v224
v_exp_f32 v225, v225
v_exp_f32 v226, v226
v_exp_f32 v227, v227
M v,248,v,244,v,212
v_exp_f32 v228, v228
v_exp_f32 v229, v229
v_exp_f32 v230, v230
v_exp_f32 v231, v231
A 232,248
A 233,249
A 234,250
A 235,251
A 236,252
A 237,253
A 238,254
A 239,255
v_pk_fma_f32 v[232:233], v[232:233], s[62:63], v[28:29]
v_pk_fma_f32 v[234:235], v[234:235], s[62:63], v[28:29]
v_pk_fma_f32 v[236:237], v[236:237], s[62:63], v[28:29]
v_pk_fma_f32 v[238:239], v[238:239], s[62:63], v[28:29]
v_cvt_pk_bf16_f32 v216, v224, v225
v_cvt_pk_bf16_f32 v217, v226, v227
v_cvt_pk_bf16_f32 v218, v228, v229
v_cvt_pk_bf16_f32 v219, v230, v231
v_exp_f32 v232, v232
v_exp_f32 v233, v233
v_exp_f32 v234, v234
v_exp_f32 v235, v235
M v,240,v,244,v,216
v_exp_f32 v236, v236
v_exp_f32 v237, v237
v_exp_f32 v238, v238
v_exp_f32 v239, v239
s_nop 0
v_cvt_pk_bf16_f32 v220, v232, v233
v_cvt_pk_bf16_f32 v221, v234, v235
v_cvt_pk_bf16_f32 v222, v236, v237
v_cvt_pk_bf16_f32 v223, v238, v239
s_nop 1
M v,248,v,244,v,220
s_nop 7
v_rcp_f32 v28, v240
v_rcp_f32 v29, v248
s_nop 1
s_barrier
T a,224,13,16384
T a,226,13,18432
T a,228,16,16384
T a,230,16,18432
T a,232,13,16896
T a,234,13,18944
T a,236,16,16896
T a,238,16,18944
T a,240,13,20480
T a,242,13,22528
T a,244,16,20480
W 7
v_mfma_f32_16x16x32_bf16 v[224:227], a[224:227], v[96:99], 0
T a,246,16,22528
v_mfma_f32_16x16x32_bf16 v[232:235], a[224:227], v[100:103], 0
T a,248,13,20992
v_mfma_f32_16x16x32_bf16 v[228:231], a[228:231], v[96:99], 0
T a,250,13,23040
v_mfma_f32_16x16x32_bf16 v[236:239], a[228:231], v[100:103], 0
T a,252,16,20992
W 7
v_mfma_f32_16x16x32_bf16 v[240:243], a[232:235], v[96:99], 0
T a,254,16,23040
v_mfma_f32_16x16x32_bf16 v[248:251], a[232:235], v[100:103], 0
T a,224,13,24576
v_mfma_f32_16x16x32_bf16 v[244:247], a[236:239], v[96:99], 0
T a,226,13,26624
v_mfma_f32_16x16x32_bf16 v[252:255], a[236:239], v[100:103], 0
T a,228,16,24576
W 7
M v,224,a,240,v,104
T a,230,16,26624
M v,232,a,240,v,108
T a,232,13,25088
M v,228,a,244,v,104
T a,234,13,27136
M v,236,a,244,v,108
T a,236,16,25088
W 7
M v,240,a,248,v,104
T a,238,16,27136
M v,248,a,248,v,108
T a,240,13,28672
M v,244,a,252,v,104
T a,242,13,30720
M v,252,a,252,v,108
T a,244,16,28672
W 7
M v,224,a,224,v,112
T a,246,16,30720
M v,232,a,224,v,116
T a,248,13,29184
M v,228,a,228,v,112
T a,250,13,31232
M v,236,a,228,v,116
T a,252,16,29184
W 7
M v,240,a,232,v,112
T a,254,16,31232
M v,248,a,232,v,116
s_barrier
T a,224,13,32768
M v,244,a,236,v,112
T a,226,13,34816
M v,252,a,236,v,116
T a,228,16,32768
W 7
M v,224,a,240,v,120
T a,230,16,34816
M v,232,a,240,v,124
T a,232,13,33280
M v,228,a,244,v,120
T a,234,13,35328
M v,236,a,244,v,124
T a,236,16,33280
W 7
M v,240,a,248,v,120
T a,238,16,35328
M v,248,a,248,v,124
T a,240,13,36864
M v,244,a,252,v,120
T a,242,13,38912
M v,252,a,252,v,124
T a,244,16,36864
W 7
M v,224,a,224,v,128
T a,246,16,38912
M v,232,a,224,v,132
T a,248,13,37376
M v,228,a,228,v,128
T a,250,13,39424
M v,236,a,228,v,132
T a,252,16,37376
W 7
M v,240,a,232,v,128
T a,254,16,39424
M v,248,a,232,v,132
T a,224,13,40960
M v,244,a,236,v,128
T a,226,13,43008
M v,252,a,236,v,132
T a,228,16,40960
W 7
M v,224,a,240,v,136
T a,230,16,43008
M v,232,a,240,v,140
T a,232,13,41472
M v,228,a,244,v,136
T a,234,13,43520
M v,236,a,244,v,140
T a,236,16,41472
W 7
M v,240,a,248,v,136
T a,238,16,43520
M v,248,a,248,v,140
T a,240,13,45056
M v,244,a,252,v,136
T a,242,13,47104
M v,252,a,252,v,140
T a,244,16,45056
W 7
M v,224,a,224,v,144
T a,246,16,47104
M v,232,a,224,v,148
T a,248,13,45568
M v,228,a,228,v,144
T a,250,13,47616
M v,236,a,228,v,148
T a,252,16,45568
W 7
M v,240,a,232,v,144
T a,254,16,47616
M v,248,a,232,v,148
s_barrier
T a,224,13,49152
M v,244,a,236,v,144
T a,226,13,51200
M v,252,a,236,v,148
T a,228,16,49152
W 7
M v,224,a,240,v,152
T a,230,16,51200
M v,232,a,240,v,156
T a,232,13,49664
M v,228,a,244,v,152
T a,234,13,51712
M v,236,a,244,v,156
T a,236,16,49664
W 7
)x"
R"x(M v,240,a,248,v,152
T a,238,16,51712
M v,248,a,248,v,156
T a,240,13,53248
M v,244,a,252,v,152
T a,242,13,55296
M v,252,a,252,v,156
T a,244,16,53248
W 7
M v,224,a,224,v,160
T a,246,16,55296
M v,232,a,224,v,164
T a,248,13,53760
M v,228,a,228,v,160
T a,250,13,55808
M v,236,a,228,v,164
T a,252,16,53760
W 7
M v,240,a,232,v,160
T a,254,16,55808
M v,248,a,232,v,164
T a,224,13,57344
M v,244,a,236,v,160
T a,226,13,59392
M v,252,a,236,v,164
T a,228,16,57344
W 7
M v,224,a,240,v,168
T a,230,16,59392
M v,232,a,240,v,172
T a,232,13,57856
M v,228,a,244,v,168
T a,234,13,59904
M v,236,a,244,v,172
T a,236,16,57856
W 7
M v,240,a,248,v,168
T a,238,16,59904
M v,248,a,248,v,172
T a,240,13,61440
M v,244,a,252,v,168
T a,242,13,63488
M v,252,a,252,v,172
T a,244,16,61440
W 7
M v,224,a,224,v,176
T a,246,16,63488
M v,232,a,224,v,180
T a,248,13,61952
M v,228,a,228,v,176
T a,250,13,64000
M v,236,a,228,v,180
T a,252,16,61952
W 7
M v,240,a,232,v,176
T a,254,16,64000
M v,248,a,232,v,180
s_barrier
T a,224,14,0
M v,244,a,236,v,176
T a,226,14,2048
M v,252,a,236,v,180
T a,228,17,0
W 7
M v,224,a,240,v,184
T a,230,17,2048
M v,232,a,240,v,188
T a,232,14,512
M v,228,a,244,v,184
T a,234,14,2560
M v,236,a,244,v,188
T a,236,17,512
W 7
M v,240,a,248,v,184
T a,238,17,2560
M v,248,a,248,v,188
T a,240,14,4096
M v,244,a,252,v,184
T a,242,14,6144
M v,252,a,252,v,188
T a,244,17,4096
W 7
M v,224,a,224,v,192
T a,246,17,6144
M v,232,a,224,v,196
T a,248,14,4608
M v,228,a,228,v,192
T a,250,14,6656
M v,236,a,228,v,196
T a,252,17,4608
W 7
M v,240,a,232,v,192
T a,254,17,6656
M v,248,a,232,v,196
T a,224,14,8192
M v,244,a,236,v,192
T a,226,14,10240
M v,252,a,236,v,196
T a,228,17,8192
W 7
M v,224,a,240,v,200
T a,230,17,10240
M v,232,a,240,v,204
T a,232,14,8704
M v,228,a,244,v,200
T a,234,14,10752
M v,236,a,244,v,204
T a,236,17,8704
W 7
M v,240,a,248,v,200
T a,238,17,10752
M v,248,a,248,v,204
T a,240,14,12288
M v,244,a,252,v,200
T a,242,14,14336
M v,252,a,252,v,204
T a,244,17,12288
W 7
M v,224,a,224,v,208
T a,246,17,14336
M v,232,a,224,v,212
T a,248,14,12800
M v,228,a,228,v,208
T a,250,14,14848
M v,236,a,228,v,212
T a,252,17,12800
W 7
M v,240,a,232,v,208
T a,254,17,14848
M v,248,a,232,v,212
s_barrier
T a,224,8,16384
M v,244,a,236,v,208
T a,226,8,24576
M v,252,a,236,v,212
T a,228,11,16384
W 7
M v,224,a,240,v,216
T a,230,11,24576
M v,232,a,240,v,220
T a,232,8,16896
M v,228,a,244,v,216
T a,234,8,25088
M v,236,a,244,v,220
T a,236,11,16896
W 7
M v,240,a,248,v,216
T a,238,11,25088
M v,248,a,248,v,220
T a,240,8,17408
M v,244,a,252,v,216
T a,242,8,25600
M v,252,a,252,v,220
s_nop 7
v_mul_f32 v224, v28, v224
v_mul_f32 v225, v28, v225
v_mul_f32 v226, v28, v226
v_mul_f32 v227, v28, v227
v_mul_f32 v228, v28, v228
v_mul_f32 v229, v28, v229
v_mul_f32 v230, v28, v230
v_mul_f32 v231, v28, v231
v_mul_f32 v232, v29, v232
v_mul_f32 v233, v29, v233
v_mul_f32 v234, v29, v234
v_mul_f32 v235, v29, v235
v_mul_f32 v236, v29, v236
v_mul_f32 v237, v29, v237
v_mul_f32 v238, v29, v238
v_mul_f32 v239, v29, v239
v_cvt_pk_bf16_f32 v224, v224, v225
v_cvt_pk_bf16_f32 v225, v226, v227
v_cvt_pk_bf16_f32 v226, v228, v229
v_cvt_pk_bf16_f32 v227, v230, v231
v_cvt_pk_bf16_f32 v228, v232, v233
v_cvt_pk_bf16_f32 v229, v234, v235
v_cvt_pk_bf16_f32 v230, v236, v237
v_cvt_pk_bf16_f32 v231, v238, v239
v_mul_f32 v240, v28, v240
v_mul_f32 v241, v28, v241
v_mul_f32 v242, v28, v242
v_mul_f32 v243, v28, v243
v_mul_f32 v244, v28, v244
v_mul_f32 v245, v28, v245
v_mul_f32 v246, v28, v246
v_mul_f32 v247, v28, v247
v_mul_f32 v248, v29, v248
v_mul_f32 v249, v29, v249
v_mul_f32 v250, v29, v250
v_mul_f32 v251, v29, v251
v_mul_f32 v252, v29, v252
v_mul_f32 v253, v29, v253
v_mul_f32 v254, v29, v254
v_mul_f32 v255, v29, v255
v_cvt_pk_bf16_f32 v240, v240, v241
v_cvt_pk_bf16_f32 v241, v242, v243
v_cvt_pk_bf16_f32 v242, v244, v245
v_cvt_pk_bf16_f32 v243, v246, v247
v_cvt_pk_bf16_f32 v244, v248, v249
v_cvt_pk_bf16_f32 v245, v250, v251
v_cvt_pk_bf16_f32 v246, v252, v253
v_cvt_pk_bf16_f32 v247, v254, v255
s_nop 1
T a,244,11,17408
W 7
v_mfma_f32_16x16x32_bf16 a[0:3], a[224:227], v[224:227], 0
T a,246,11,25600
v_mfma_f32_16x16x32_bf16 a[8:11], a[224:227], v[228:231], 0
T a,248,8,17920
v_mfma_f32_16x16x32_bf16 a[4:7], a[228:231], v[224:227], 0
T a,250,8,26112
v_mfma_f32_16x16x32_bf16 a[12:15], a[228:231], v[228:231], 0
T a,252,11,17920
W 7
v_mfma_f32_16x16x32_bf16 a[16:19], a[232:235], v[224:227], 0
T a,254,11,26112
v_mfma_f32_16x16x32_bf16 a[24:27], a[232:235], v[228:231], 0
T a,224,8,18432
v_mfma_f32_16x16x32_bf16 a[20:23], a[236:239], v[224:227], 0
T a,226,8,26624
v_mfma_f32_16x16x32_bf16 a[28:31], a[236:239], v[228:231], 0
T a,228,11,18432
W 7
v_mfma_f32_16x16x32_bf16 a[32:35], a[240:243], v[224:227], 0
T a,230,11,26624
v_mfma_f32_16x16x32_bf16 a[40:43], a[240:243], v[228:231], 0
T a,232,8,18944
v_mfma_f32_16x16x32_bf16 a[36:39], a[244:247], v[224:227], 0
T a,234,8,27136
v_mfma_f32_16x16x32_bf16 a[44:47], a[244:247], v[228:231], 0
T a,236,11,18944
W 7
v_mfma_f32_16x16x32_bf16 a[48:51], a[248:251], v[224:227], 0
T a,238,11,27136
v_mfma_f32_16x16x32_bf16 a[56:59], a[248:251], v[228:231], 0
T a,240,8,19456
v_mfma_f32_16x16x32_bf16 a[52:55], a[252:255], v[224:227], 0
T a,242,8,27648
v_mfma_f32_16x16x32_bf16 a[60:63], a[252:255], v[228:231], 0
T a,244,11,19456
W 7
v_mfma_f32_16x16x32_bf16 a[64:67], a[224:227], v[224:227], 0
T a,246,11,27648
v_mfma_f32_16x16x32_bf16 a[72:75], a[224:227], v[228:231], 0
T a,248,8,19968
v_mfma_f32_16x16x32_bf16 a[68:71], a[228:231], v[224:227], 0
T a,250,8,28160
v_mfma_f32_16x16x32_bf16 a[76:79], a[228:231], v[228:231], 0
T a,252,11,19968
W 7
v_mfma_f32_16x16x32_bf16 a[80:83], a[232:235], v[224:227], 0
T a,254,11,28160
v_mfma_f32_16x16x32_bf16 a[88:91], a[232:235], v[228:231], 0
s_barrier
.Lc0_64_%=:
T a,224,8,32768
v_mfma_f32_16x16x32_bf16 a[84:87], a[236:239], v[224:227], 0
T a,226,8,40960
v_mfma_f32_16x16x32_bf16 a[92:95], a[236:239], v[228:231], 0
T a,228,11,32768
W 7
v_mfma_f32_16x16x32_bf16 a[96:99], a[240:243], v[224:227], 0
T a,230,11,40960
v_mfma_f32_16x16x32_bf16 a[104:107], a[240:243], v[228:231], 0
T a,232,8,33280
v_mfma_f32_16x16x32_bf16 a[100:103], a[244:247], v[224:227], 0
T a,234,8,41472
v_mfma_f32_16x16x32_bf16 a[108:111], a[244:247], v[228:231], 0
T a,236,11,33280
W 7
v_mfma_f32_16x16x32_bf16 a[112:115], a[248:251], v[224:227], 0
T a,238,11,41472
v_mfma_f32_16x16x32_bf16 a[120:123], a[248:251], v[228:231], 0
T a,240,8,33792
v_mfma_f32_16x16x32_bf16 a[116:119], a[252:255], v[224:227], 0
T a,242,8,41984
v_mfma_f32_16x16x32_bf16 a[124:127], a[252:255], v[228:231], 0
T a,244,11,33792
W 7
v_mfma_f32_16x16x32_bf16 a[128:131], a[224:227], v[224:227], 0
T a,246,11,41984
v_mfma_f32_16x16x32_bf16 a[136:139], a[224:227], v[228:231], 0
T a,248,8,34304
v_mfma_f32_16x16x32_bf16 a[132:135], a[228:231], v[224:227], 0
T a,250,8,42496
v_mfma_f32_16x16x32_bf16 a[140:143], a[228:231], v[228:231], 0
T a,252,11,34304
W 7
v_mfma_f32_16x16x32_bf16 a[144:147], a[232:235], v[224:227], 0
T a,254,11,42496
v_mfma_f32_16x16x32_bf16 a[152:155], a[232:235], v[228:231], 0
T a,224,8,34816
v_mfma_f32_16x16x32_bf16 a[148:151], a[236:239], v[224:227], 0
T a,226,8,43008
v_mfma_f32_16x16x32_bf16 a[156:159], a[236:239], v[228:231], 0
T a,228,11,34816
W 7
v_mfma_f32_16x16x32_bf16 a[160:163], a[240:243], v[224:227], 0
T a,230,11,43008
v_mfma_f32_16x16x32_bf16 a[168:171], a[240:243], v[228:231], 0
T a,232,8,35328
v_mfma_f32_16x16x32_bf16 a[164:167], a[244:247], v[224:227], 0
T a,234,8,43520
v_mfma_f32_16x16x32_bf16 a[172:175], a[244:247], v[228:231], 0
T a,236,11,35328
W 7
v_mfma_f32_16x16x32_bf16 a[176:179], a[248:251], v[224:227], 0
T a,238,11,43520
v_mfma_f32_16x16x32_bf16 a[184:187], a[248:251], v[228:231], 0
s_barrier
T a,240,8,49152
v_mfma_f32_16x16x32_bf16 a[180:183], a[252:255], v[224:227], 0
T a,242,8,57344
v_mfma_f32_16x16x32_bf16 a[188:191], a[252:255], v[228:231], 0
T a,244,11,49152
W 7
v_mfma_f32_16x16x32_bf16 a[192:195], a[224:227], v[224:227], 0
T a,246,11,57344
v_mfma_f32_16x16x32_bf16 a[200:203], a[224:227], v[228:231], 0
T a,248,8,49664
v_mfma_f32_16x16x32_bf16 a[196:199], a[228:231], v[224:227], 0
T a,250,8,57856
v_mfma_f32_16x16x32_bf16 a[204:207], a[228:231], v[228:231], 0
T a,252,11,49664
W 7
v_mfma_f32_16x16x32_bf16 a[208:211], a[232:235], v[224:227], 0
T a,254,11,57856
v_mfma_f32_16x16x32_bf16 a[216:219], a[232:235], v[228:231], 0
T a,224,8,50176
v_mfma_f32_16x16x32_bf16 a[212:215], a[236:239], v[224:227], 0
T a,226,8,58368
v_mfma_f32_16x16x32_bf16 a[220:223], a[236:239], v[228:231], 0
T a,228,11,50176
W 7
M a,0,a,240,v,240
T a,230,11,58368
M a,8,a,240,v,244
T a,232,8,50688
M a,4,a,244,v,240
T a,234,8,58880
M a,12,a,244,v,244
T a,236,11,50688
W 7
M a,16,a,248,v,240
T a,238,11,58880
M a,24,a,248,v,244
T a,240,8,51200
M a,20,a,252,v,240
T a,242,8,59392
M a,28,a,252,v,244
T a,244,11,51200
W 7
M a,32,a,224,v,240
T a,246,11,59392
M a,40,a,224,v,244
T a,248,8,51712
M a,36,a,228,v,240
T a,250,8,59904
M a,44,a,228,v,244
T a,252,11,51712
W 7
M a,48,a,232,v,240
T a,254,11,59904
M a,56,a,232,v,244
T a,224,8,52224
M a,52,a,236,v,240
T a,226,8,60416
M a,60,a,236,v,244
T a,228,11,52224
W 7
M a,64,a,240,v,240
T a,230,11,60416
M a,72,a,240,v,244
T a,232,8,52736
M a,68,a,244,v,240
T a,234,8,60928
M a,76,a,244,v,244
T a,236,11,52736
W 7
M a,80,a,248,v,240
T a,238,11,60928
M a,88,a,248,v,244
s_barrier
T a,240,9,0
M a,84,a,252,v,240
T a,242,9,8192
M a,92,a,252,v,244
T a,244,12,0
W 7
M a,96,a,224,v,240
T a,246,12,8192
M a,104,a,224,v,244
T a,248,9,512
M a,100,a,228,v,240
T a,250,9,8704
M a,108,a,228,v,244
T a,252,12,512
W 7
M a,112,a,232,v,240
T a,254,12,8704
)x"
R"x(M a,120,a,232,v,244
T a,224,9,1024
M a,116,a,236,v,240
T a,226,9,9216
M a,124,a,236,v,244
T a,228,12,1024
W 7
M a,128,a,240,v,240
T a,230,12,9216
M a,136,a,240,v,244
T a,232,9,1536
M a,132,a,244,v,240
T a,234,9,9728
M a,140,a,244,v,244
T a,236,12,1536
W 7
M a,144,a,248,v,240
T a,238,12,9728
M a,152,a,248,v,244
T a,240,9,2048
M a,148,a,252,v,240
T a,242,9,10240
M a,156,a,252,v,244
T a,244,12,2048
W 7
M a,160,a,224,v,240
T a,246,12,10240
M a,168,a,224,v,244
T a,248,9,2560
M a,164,a,228,v,240
T a,250,9,10752
M a,172,a,228,v,244
T a,252,12,2560
W 7
M a,176,a,232,v,240
T a,254,12,10752
M a,184,a,232,v,244
s_barrier
T a,224,15,16384
M a,180,a,236,v,240
T a,226,15,18432
M a,188,a,236,v,244
T a,228,18,16384
W 7
M a,192,a,240,v,240
T a,230,18,18432
M a,200,a,240,v,244
T a,232,15,16896
M a,196,a,244,v,240
T a,234,15,18944
M a,204,a,244,v,244
T a,236,18,16896
W 7
M a,208,a,248,v,240
T a,238,18,18944
M a,216,a,248,v,244
T a,240,15,20480
M a,212,a,252,v,240
T a,242,15,22528
M a,220,a,252,v,244
T a,244,18,20480
W 7
v_mfma_f32_16x16x32_bf16 v[224:227], a[224:227], v[96:99], 0
T a,246,18,22528
v_mfma_f32_16x16x32_bf16 v[232:235], a[224:227], v[100:103], 0
T a,248,15,20992
v_mfma_f32_16x16x32_bf16 v[228:231], a[228:231], v[96:99], 0
T a,250,15,23040
v_mfma_f32_16x16x32_bf16 v[236:239], a[228:231], v[100:103], 0
T a,252,18,20992
W 7
v_mfma_f32_16x16x32_bf16 v[240:243], a[232:235], v[96:99], 0
T a,254,18,23040
v_mfma_f32_16x16x32_bf16 v[248:251], a[232:235], v[100:103], 0
T a,224,15,24576
v_mfma_f32_16x16x32_bf16 v[244:247], a[236:239], v[96:99], 0
T a,226,15,26624
v_mfma_f32_16x16x32_bf16 v[252:255], a[236:239], v[100:103], 0
T a,228,18,24576
W 7
M v,224,a,240,v,104
T a,230,18,26624
M v,232,a,240,v,108
T a,232,15,25088
M v,228,a,244,v,104
T a,234,15,27136
M v,236,a,244,v,108
T a,236,18,25088
W 7
M v,240,a,248,v,104
T a,238,18,27136
M v,248,a,248,v,108
T a,240,15,28672
M v,244,a,252,v,104
T a,242,15,30720
M v,252,a,252,v,108
T a,244,18,28672
W 7
M v,224,a,224,v,112
T a,246,18,30720
M v,232,a,224,v,116
T a,248,15,29184
M v,228,a,228,v,112
T a,250,15,31232
M v,236,a,228,v,116
T a,252,18,29184
W 7
M v,240,a,232,v,112
T a,254,18,31232
M v,248,a,232,v,116
s_barrier
T a,224,13,0
M v,244,a,236,v,112
T a,226,13,2048
M v,252,a,236,v,116
T a,228,16,0
W 7
M v,224,a,240,v,120
T a,230,16,2048
M v,232,a,240,v,124
T a,232,13,512
M v,228,a,244,v,120
T a,234,13,2560
M v,236,a,244,v,124
T a,236,16,512
W 7
M v,240,a,248,v,120
T a,238,16,2560
M v,248,a,248,v,124
T a,240,13,4096
M v,244,a,252,v,120
T a,242,13,6144
M v,252,a,252,v,124
T a,244,16,4096
W 7
M v,224,a,224,v,128
T a,246,16,6144
M v,232,a,224,v,132
T a,248,13,4608
M v,228,a,228,v,128
T a,250,13,6656
M v,236,a,228,v,132
T a,252,16,4608
W 7
M v,240,a,232,v,128
T a,254,16,6656
M v,248,a,232,v,132
T a,224,13,8192
M v,244,a,236,v,128
T a,226,13,10240
M v,252,a,236,v,132
T a,228,16,8192
W 7
M v,224,a,240,v,136
T a,230,16,10240
M v,232,a,240,v,140
T a,232,13,8704
M v,228,a,244,v,136
T a,234,13,10752
M v,236,a,244,v,140
T a,236,16,8704
W 7
M v,240,a,248,v,136
T a,238,16,10752
M v,248,a,248,v,140
T a,240,13,12288
M v,244,a,252,v,136
T a,242,13,14336
M v,252,a,252,v,140
T a,244,16,12288
W 7
M v,224,a,224,v,144
T a,246,16,14336
M v,232,a,224,v,148
T a,248,13,12800
M v,228,a,228,v,144
T a,250,13,14848
M v,236,a,228,v,148
T a,252,16,12800
W 7
M v,240,a,232,v,144
T a,254,16,14848
M v,248,a,232,v,148
s_barrier
T a,224,13,16384
M v,244,a,236,v,144
T a,226,13,18432
M v,252,a,236,v,148
T a,228,16,16384
W 7
M v,224,a,240,v,152
T a,230,16,18432
M v,232,a,240,v,156
T a,232,13,16896
M v,228,a,244,v,152
T a,234,13,18944
M v,236,a,244,v,156
T a,236,16,16896
W 7
M v,240,a,248,v,152
T a,238,16,18944
M v,248,a,248,v,156
T a,240,13,20480
M v,244,a,252,v,152
T a,242,13,22528
M v,252,a,252,v,156
T a,244,16,20480
W 7
M v,224,a,224,v,160
T a,246,16,22528
M v,232,a,224,v,164
T a,248,13,20992
M v,228,a,228,v,160
T a,250,13,23040
M v,236,a,228,v,164
T a,252,16,20992
W 7
M v,240,a,232,v,160
T a,254,16,23040
M v,248,a,232,v,164
T a,224,13,24576
M v,244,a,236,v,160
T a,226,13,26624
M v,252,a,236,v,164
T a,228,16,24576
W 7
M v,224,a,240,v,168
T a,230,16,26624
M v,232,a,240,v,172
T a,232,13,25088
M v,228,a,244,v,168
T a,234,13,27136
M v,236,a,244,v,172
T a,236,16,25088
W 7
M v,240,a,248,v,168
T a,238,16,27136
M v,248,a,248,v,172
T a,240,13,28672
M v,244,a,252,v,168
T a,242,13,30720
M v,252,a,252,v,172
T a,244,16,28672
W 7
M v,224,a,224,v,176
T a,246,16,30720
M v,232,a,224,v,180
T a,248,13,29184
M v,228,a,228,v,176
T a,250,13,31232
M v,236,a,228,v,180
T a,252,16,29184
W 7
M v,240,a,232,v,176
T a,254,16,31232
M v,248,a,232,v,180
s_barrier
.Lc0_70_%=:
T a,224,13,32768
M v,244,a,236,v,176
T a,226,13,34816
M v,252,a,236,v,180
T a,228,16,32768
W 7
M v,224,a,240,v,184
T a,230,16,34816
M v,232,a,240,v,188
T a,232,13,33280
M v,228,a,244,v,184
T a,234,13,35328
M v,236,a,244,v,188
T a,236,16,33280
W 7
M v,240,a,248,v,184
T a,238,16,35328
M v,248,a,248,v,188
T a,240,13,36864
M v,244,a,252,v,184
T a,242,13,38912
M v,252,a,252,v,188
T a,244,16,36864
W 7
M v,224,a,224,v,192
T a,246,16,38912
M v,232,a,224,v,196
T a,248,13,37376
M v,228,a,228,v,192
T a,250,13,39424
M v,236,a,228,v,196
T a,252,16,37376
W 7
M v,240,a,232,v,192
T a,254,16,39424
M v,248,a,232,v,196
T a,224,13,40960
M v,244,a,236,v,192
T a,226,13,43008
M v,252,a,236,v,196
T a,228,16,40960
W 7
M v,224,a,240,v,200
T a,230,16,43008
M v,232,a,240,v,204
T a,232,13,41472
M v,228,a,244,v,200
T a,234,13,43520
M v,236,a,244,v,204
T a,236,16,41472
W 7
M v,240,a,248,v,200
T a,238,16,43520
M v,248,a,248,v,204
T a,240,13,45056
M v,244,a,252,v,200
T a,242,13,47104
M v,252,a,252,v,204
T a,244,16,45056
W 7
M v,224,a,224,v,208
T a,246,16,47104
M v,232,a,224,v,212
T a,248,13,45568
M v,228,a,228,v,208
T a,250,13,47616
M v,236,a,228,v,212
T a,252,16,45568
W 7
M v,240,a,232,v,208
T a,254,16,47616
M v,248,a,232,v,212
s_barrier
T a,224,7,49152
M v,244,a,236,v,208
T a,226,7,57344
M v,252,a,236,v,212
T a,228,10,49152
W 7
M v,224,a,240,v,216
T a,230,10,57344
M v,232,a,240,v,220
T a,232,7,49664
M v,228,a,244,v,216
T a,234,7,57856
M v,236,a,244,v,220
T a,236,10,49664
W 7
M v,240,a,248,v,216
T a,238,10,57856
M v,248,a,248,v,220
T a,240,7,50176
M v,244,a,252,v,216
T a,242,7,58368
M v,252,a,252,v,220
s_nop 7
v_mul_f32 v224, v28, v224
v_mul_f32 v225, v28, v225
v_mul_f32 v226, v28, v226
v_mul_f32 v227, v28, v227
v_mul_f32 v228, v28, v228
v_mul_f32 v229, v28, v229
v_mul_f32 v230, v28, v230
v_mul_f32 v231, v28, v231
v_mul_f32 v232, v29, v232
v_mul_f32 v233, v29, v233
v_mul_f32 v234, v29, v234
v_mul_f32 v235, v29, v235
v_mul_f32 v236, v29, v236
v_mul_f32 v237, v29, v237
v_mul_f32 v238, v29, v238
v_mul_f32 v239, v29, v239
v_cvt_pk_bf16_f32 v224, v224, v225
v_cvt_pk_bf16_f32 v225, v226, v227
v_cvt_pk_bf16_f32 v226, v228, v229
v_cvt_pk_bf16_f32 v227, v230, v231
v_cvt_pk_bf16_f32 v228, v232, v233
v_cvt_pk_bf16_f32 v229, v234, v235
v_cvt_pk_bf16_f32 v230, v236, v237
v_cvt_pk_bf16_f32 v231, v238, v239
v_mul_f32 v240, v28, v240
v_mul_f32 v241, v28, v241
v_mul_f32 v242, v28, v242
v_mul_f32 v243, v28, v243
v_mul_f32 v244, v28, v244
v_mul_f32 v245, v28, v245
v_mul_f32 v246, v28, v246
v_mul_f32 v247, v28, v247
v_mul_f32 v248, v29, v248
v_mul_f32 v249, v29, v249
v_mul_f32 v250, v29, v250
v_mul_f32 v251, v29, v251
v_mul_f32 v252, v29, v252
v_mul_f32 v253, v29, v253
v_mul_f32 v254, v29, v254
v_mul_f32 v255, v29, v255
v_cvt_pk_bf16_f32 v240, v240, v241
v_cvt_pk_bf16_f32 v241, v242, v243
v_cvt_pk_bf16_f32 v242, v244, v245
v_cvt_pk_bf16_f32 v243, v246, v247
v_cvt_pk_bf16_f32 v244, v248, v249
v_cvt_pk_bf16_f32 v245, v250, v251
)x"
R"x(v_cvt_pk_bf16_f32 v246, v252, v253
v_cvt_pk_bf16_f32 v247, v254, v255
s_nop 1
T a,244,10,50176
W 7
M a,0,a,224,v,224
T a,246,10,58368
M a,8,a,224,v,228
T a,248,7,50688
M a,4,a,228,v,224
T a,250,7,58880
M a,12,a,228,v,228
T a,252,10,50688
W 7
M a,16,a,232,v,224
T a,254,10,58880
M a,24,a,232,v,228
T a,224,7,51200
M a,20,a,236,v,224
T a,226,7,59392
M a,28,a,236,v,228
T a,228,10,51200
W 7
M a,32,a,240,v,224
T a,230,10,59392
M a,40,a,240,v,228
T a,232,7,51712
M a,36,a,244,v,224
T a,234,7,59904
M a,44,a,244,v,228
T a,236,10,51712
W 7
M a,48,a,248,v,224
T a,238,10,59904
M a,56,a,248,v,228
T a,240,7,52224
M a,52,a,252,v,224
T a,242,7,60416
M a,60,a,252,v,228
T a,244,10,52224
W 7
M a,64,a,224,v,224
T a,246,10,60416
M a,72,a,224,v,228
T a,248,7,52736
M a,68,a,228,v,224
T a,250,7,60928
M a,76,a,228,v,228
T a,252,10,52736
W 7
M a,80,a,232,v,224
T a,254,10,60928
M a,88,a,232,v,228
s_barrier
T a,224,8,0
M a,84,a,236,v,224
T a,226,8,8192
M a,92,a,236,v,228
T a,228,11,0
W 7
M a,96,a,240,v,224
T a,230,11,8192
M a,104,a,240,v,228
T a,232,8,512
M a,100,a,244,v,224
T a,234,8,8704
M a,108,a,244,v,228
T a,236,11,512
W 7
M a,112,a,248,v,224
T a,238,11,8704
M a,120,a,248,v,228
T a,240,8,1024
M a,116,a,252,v,224
T a,242,8,9216
M a,124,a,252,v,228
T a,244,11,1024
W 7
M a,128,a,224,v,224
T a,246,11,9216
M a,136,a,224,v,228
T a,248,8,1536
M a,132,a,228,v,224
T a,250,8,9728
M a,140,a,228,v,228
T a,252,11,1536
W 7
M a,144,a,232,v,224
T a,254,11,9728
M a,152,a,232,v,228
T a,224,8,2048
M a,148,a,236,v,224
T a,226,8,10240
M a,156,a,236,v,228
T a,228,11,2048
W 7
M a,160,a,240,v,224
T a,230,11,10240
M a,168,a,240,v,228
T a,232,8,2560
M a,164,a,244,v,224
T a,234,8,10752
M a,172,a,244,v,228
T a,236,11,2560
W 7
M a,176,a,248,v,224
T a,238,11,10752
M a,184,a,248,v,228
s_barrier
T a,240,8,16384
M a,180,a,252,v,224
T a,242,8,24576
M a,188,a,252,v,228
T a,244,11,16384
W 7
M a,192,a,224,v,224
T a,246,11,24576
M a,200,a,224,v,228
T a,248,8,16896
M a,196,a,228,v,224
T a,250,8,25088
M a,204,a,228,v,228
T a,252,11,16896
W 7
M a,208,a,232,v,224
T a,254,11,25088
M a,216,a,232,v,228
T a,224,8,17408
M a,212,a,236,v,224
T a,226,8,25600
M a,220,a,236,v,228
T a,228,11,17408
W 7
M a,0,a,240,v,240
T a,230,11,25600
M a,8,a,240,v,244
T a,232,8,17920
M a,4,a,244,v,240
T a,234,8,26112
M a,12,a,244,v,244
T a,236,11,17920
W 7
M a,16,a,248,v,240
T a,238,11,26112
M a,24,a,248,v,244
T a,240,8,18432
M a,20,a,252,v,240
T a,242,8,26624
M a,28,a,252,v,244
T a,244,11,18432
W 7
M a,32,a,224,v,240
T a,246,11,26624
M a,40,a,224,v,244
T a,248,8,18944
M a,36,a,228,v,240
T a,250,8,27136
M a,44,a,228,v,244
T a,252,11,18944
W 7
M a,48,a,232,v,240
T a,254,11,27136
M a,56,a,232,v,244
T a,224,8,19456
M a,52,a,236,v,240
T a,226,8,27648
M a,60,a,236,v,244
T a,228,11,19456
W 7
M a,64,a,240,v,240
T a,230,11,27648
M a,72,a,240,v,244
T a,232,8,19968
M a,68,a,244,v,240
T a,234,8,28160
M a,76,a,244,v,244
T a,236,11,19968
W 7
M a,80,a,248,v,240
T a,238,11,28160
M a,88,a,248,v,244
s_barrier
T a,240,8,32768
M a,84,a,252,v,240
T a,242,8,40960
M a,92,a,252,v,244
T a,244,11,32768
W 7
M a,96,a,224,v,240
T a,246,11,40960
M a,104,a,224,v,244
T a,248,8,33280
M a,100,a,228,v,240
T a,250,8,41472
M a,108,a,228,v,244
T a,252,11,33280
W 7
M a,112,a,232,v,240
T a,254,11,41472
M a,120,a,232,v,244
T a,224,8,33792
M a,116,a,236,v,240
T a,226,8,41984
M a,124,a,236,v,244
T a,228,11,33792
W 7
M a,128,a,240,v,240
T a,230,11,41984
M a,136,a,240,v,244
T a,232,8,34304
M a,132,a,244,v,240
T a,234,8,42496
M a,140,a,244,v,244
T a,236,11,34304
W 7
M a,144,a,248,v,240
T a,238,11,42496
M a,152,a,248,v,244
T a,240,8,34816
M a,148,a,252,v,240
T a,242,8,43008
M a,156,a,252,v,244
T a,244,11,34816
W 7
M a,160,a,224,v,240
T a,246,11,43008
M a,168,a,224,v,244
T a,248,8,35328
M a,164,a,228,v,240
T a,250,8,43520
M a,172,a,228,v,244
T a,252,11,35328
W 7
M a,176,a,232,v,240
T a,254,11,43520
M a,184,a,232,v,244
s_barrier
T a,224,14,49152
M a,180,a,236,v,240
T a,226,14,51200
M a,188,a,236,v,244
T a,228,17,49152
W 7
M a,192,a,240,v,240
T a,230,17,51200
M a,200,a,240,v,244
T a,232,14,49664
M a,196,a,244,v,240
T a,234,14,51712
M a,204,a,244,v,244
T a,236,17,49664
W 7
M a,208,a,248,v,240
T a,238,17,51712
M a,216,a,248,v,244
T a,240,14,53248
M a,212,a,252,v,240
T a,242,14,55296
M a,220,a,252,v,244
T a,244,17,53248
W 7
v_mfma_f32_16x16x32_bf16 v[224:227], a[224:227], v[96:99], 0
T a,246,17,55296
v_mfma_f32_16x16x32_bf16 v[232:235], a[224:227], v[100:103], 0
T a,248,14,53760
v_mfma_f32_16x16x32_bf16 v[228:231], a[228:231], v[96:99], 0
T a,250,14,55808
v_mfma_f32_16x16x32_bf16 v[236:239], a[228:231], v[100:103], 0
T a,252,17,53760
W 7
v_mfma_f32_16x16x32_bf16 v[240:243], a[232:235], v[96:99], 0
T a,254,17,55808
v_mfma_f32_16x16x32_bf16 v[248:251], a[232:235], v[100:103], 0
T a,224,14,57344
v_mfma_f32_16x16x32_bf16 v[244:247], a[236:239], v[96:99], 0
T a,226,14,59392
v_mfma_f32_16x16x32_bf16 v[252:255], a[236:239], v[100:103], 0
T a,228,17,57344
W 7
M v,224,a,240,v,104
T a,230,17,59392
M v,232,a,240,v,108
T a,232,14,57856
M v,228,a,244,v,104
T a,234,14,59904
M v,236,a,244,v,108
T a,236,17,57856
W 7
M v,240,a,248,v,104
T a,238,17,59904
M v,248,a,248,v,108
T a,240,14,61440
M v,244,a,252,v,104
T a,242,14,63488
M v,252,a,252,v,108
T a,244,17,61440
W 7
M v,224,a,224,v,112
T a,246,17,63488
M v,232,a,224,v,116
T a,248,14,61952
M v,228,a,228,v,112
T a,250,14,64000
M v,236,a,228,v,116
T a,252,17,61952
W 7
M v,240,a,232,v,112
T a,254,17,64000
M v,248,a,232,v,116
s_barrier
.Lc0_76_%=:
T a,224,15,0
M v,244,a,236,v,112
T a,226,15,2048
M v,252,a,236,v,116
T a,228,18,0
W 7
M v,224,a,240,v,120
T a,230,18,2048
M v,232,a,240,v,124
T a,232,15,512
M v,228,a,244,v,120
T a,234,15,2560
M v,236,a,244,v,124
T a,236,18,512
W 7
M v,240,a,248,v,120
T a,238,18,2560
M v,248,a,248,v,124
T a,240,15,4096
M v,244,a,252,v,120
T a,242,15,6144
M v,252,a,252,v,124
T a,244,18,4096
W 7
M v,224,a,224,v,128
T a,246,18,6144
M v,232,a,224,v,132
T a,248,15,4608
M v,228,a,228,v,128
T a,250,15,6656
M v,236,a,228,v,132
T a,252,18,4608
W 7
M v,240,a,232,v,128
T a,254,18,6656
M v,248,a,232,v,132
T a,224,15,8192
M v,244,a,236,v,128
T a,226,15,10240
M v,252,a,236,v,132
T a,228,18,8192
W 7
M v,224,a,240,v,136
T a,230,18,10240
M v,232,a,240,v,140
T a,232,15,8704
M v,228,a,244,v,136
T a,234,15,10752
M v,236,a,244,v,140
T a,236,18,8704
W 7
M v,240,a,248,v,136
T a,238,18,10752
M v,248,a,248,v,140
T a,240,15,12288
M v,244,a,252,v,136
T a,242,15,14336
M v,252,a,252,v,140
T a,244,18,12288
W 7
M v,224,a,224,v,144
T a,246,18,14336
M v,232,a,224,v,148
T a,248,15,12800
M v,228,a,228,v,144
T a,250,15,14848
M v,236,a,228,v,148
T a,252,18,12800
W 7
M v,240,a,232,v,144
T a,254,18,14848
M v,248,a,232,v,148
s_barrier
T a,224,15,16384
M v,244,a,236,v,144
T a,226,15,18432
M v,252,a,236,v,148
T a,228,18,16384
W 7
M v,224,a,240,v,152
T a,230,18,18432
M v,232,a,240,v,156
T a,232,15,16896
M v,228,a,244,v,152
T a,234,15,18944
M v,236,a,244,v,156
T a,236,18,16896
W 7
M v,240,a,248,v,152
)x"
R"x(T a,238,18,18944
M v,248,a,248,v,156
T a,240,15,20480
M v,244,a,252,v,152
T a,242,15,22528
M v,252,a,252,v,156
T a,244,18,20480
W 7
M v,224,a,224,v,160
T a,246,18,22528
M v,232,a,224,v,164
T a,248,15,20992
M v,228,a,228,v,160
T a,250,15,23040
M v,236,a,228,v,164
T a,252,18,20992
W 7
M v,240,a,232,v,160
T a,254,18,23040
M v,248,a,232,v,164
T a,224,15,24576
M v,244,a,236,v,160
T a,226,15,26624
M v,252,a,236,v,164
T a,228,18,24576
W 7
M v,224,a,240,v,168
T a,230,18,26624
M v,232,a,240,v,172
T a,232,15,25088
M v,228,a,244,v,168
T a,234,15,27136
M v,236,a,244,v,172
T a,236,18,25088
W 7
M v,240,a,248,v,168
T a,238,18,27136
M v,248,a,248,v,172
T a,240,15,28672
M v,244,a,252,v,168
T a,242,15,30720
M v,252,a,252,v,172
T a,244,18,28672
W 7
M v,224,a,224,v,176
T a,246,18,30720
M v,232,a,224,v,180
T a,248,15,29184
M v,228,a,228,v,176
T a,250,15,31232
M v,236,a,228,v,180
T a,252,18,29184
W 7
M v,240,a,232,v,176
T a,254,18,31232
M v,248,a,232,v,180
s_barrier
T a,224,13,0
M v,244,a,236,v,176
T a,226,13,2048
M v,252,a,236,v,180
T a,228,16,0
W 7
M v,224,a,240,v,184
T a,230,16,2048
M v,232,a,240,v,188
T a,232,13,512
M v,228,a,244,v,184
T a,234,13,2560
M v,236,a,244,v,188
T a,236,16,512
W 7
M v,240,a,248,v,184
T a,238,16,2560
M v,248,a,248,v,188
T a,240,13,4096
M v,244,a,252,v,184
T a,242,13,6144
M v,252,a,252,v,188
T a,244,16,4096
W 7
M v,224,a,224,v,192
T a,246,16,6144
M v,232,a,224,v,196
T a,248,13,4608
M v,228,a,228,v,192
T a,250,13,6656
M v,236,a,228,v,196
T a,252,16,4608
W 7
M v,240,a,232,v,192
T a,254,16,6656
M v,248,a,232,v,196
T a,224,13,8192
M v,244,a,236,v,192
T a,226,13,10240
M v,252,a,236,v,196
T a,228,16,8192
W 7
M v,224,a,240,v,200
T a,230,16,10240
M v,232,a,240,v,204
T a,232,13,8704
M v,228,a,244,v,200
T a,234,13,10752
M v,236,a,244,v,204
T a,236,16,8704
W 7
M v,240,a,248,v,200
T a,238,16,10752
M v,248,a,248,v,204
T a,240,13,12288
M v,244,a,252,v,200
T a,242,13,14336
M v,252,a,252,v,204
T a,244,16,12288
W 7
M v,224,a,224,v,208
T a,246,16,14336
M v,232,a,224,v,212
T a,248,13,12800
M v,228,a,228,v,208
T a,250,13,14848
M v,236,a,228,v,212
T a,252,16,12800
W 7
M v,240,a,232,v,208
T a,254,16,14848
M v,248,a,232,v,212
s_barrier
T a,224,7,16384
M v,244,a,236,v,208
T a,226,7,24576
M v,252,a,236,v,212
T a,228,10,16384
W 7
M v,224,a,240,v,216
T a,230,10,24576
M v,232,a,240,v,220
T a,232,7,16896
M v,228,a,244,v,216
T a,234,7,25088
M v,236,a,244,v,220
T a,236,10,16896
W 7
M v,240,a,248,v,216
T a,238,10,25088
M v,248,a,248,v,220
T a,240,7,17408
M v,244,a,252,v,216
T a,242,7,25600
M v,252,a,252,v,220
s_nop 7
v_mul_f32 v224, v28, v224
v_mul_f32 v225, v28, v225
v_mul_f32 v226, v28, v226
v_mul_f32 v227, v28, v227
v_mul_f32 v228, v28, v228
v_mul_f32 v229, v28, v229
v_mul_f32 v230, v28, v230
v_mul_f32 v231, v28, v231
v_mul_f32 v232, v29, v232
v_mul_f32 v233, v29, v233
v_mul_f32 v234, v29, v234
v_mul_f32 v235, v29, v235
v_mul_f32 v236, v29, v236
v_mul_f32 v237, v29, v237
v_mul_f32 v238, v29, v238
v_mul_f32 v239, v29, v239
v_cvt_pk_bf16_f32 v224, v224, v225
v_cvt_pk_bf16_f32 v225, v226, v227
v_cvt_pk_bf16_f32 v226, v228, v229
v_cvt_pk_bf16_f32 v227, v230, v231
v_cvt_pk_bf16_f32 v228, v232, v233
v_cvt_pk_bf16_f32 v229, v234, v235
v_cvt_pk_bf16_f32 v230, v236, v237
v_cvt_pk_bf16_f32 v231, v238, v239
v_mul_f32 v240, v28, v240
v_mul_f32 v241, v28, v241
v_mul_f32 v242, v28, v242
v_mul_f32 v243, v28, v243
v_mul_f32 v244, v28, v244
v_mul_f32 v245, v28, v245
v_mul_f32 v246, v28, v246
v_mul_f32 v247, v28, v247
v_mul_f32 v248, v29, v248
v_mul_f32 v249, v29, v249
v_mul_f32 v250, v29, v250
v_mul_f32 v251, v29, v251
v_mul_f32 v252, v29, v252
v_mul_f32 v253, v29, v253
v_mul_f32 v254, v29, v254
v_mul_f32 v255, v29, v255
v_cvt_pk_bf16_f32 v240, v240, v241
v_cvt_pk_bf16_f32 v241, v242, v243
v_cvt_pk_bf16_f32 v242, v244, v245
v_cvt_pk_bf16_f32 v243, v246, v247
v_cvt_pk_bf16_f32 v244, v248, v249
v_cvt_pk_bf16_f32 v245, v250, v251
v_cvt_pk_bf16_f32 v246, v252, v253
v_cvt_pk_bf16_f32 v247, v254, v255
s_nop 1
T a,244,10,17408
W 7
M a,0,a,224,v,224
T a,246,10,25600
M a,8,a,224,v,228
T a,248,7,17920
M a,4,a,228,v,224
T a,250,7,26112
M a,12,a,228,v,228
T a,252,10,17920
W 7
M a,16,a,232,v,224
T a,254,10,26112
M a,24,a,232,v,228
T a,224,7,18432
M a,20,a,236,v,224
T a,226,7,26624
M a,28,a,236,v,228
T a,228,10,18432
W 7
M a,32,a,240,v,224
T a,230,10,26624
M a,40,a,240,v,228
T a,232,7,18944
M a,36,a,244,v,224
T a,234,7,27136
M a,44,a,244,v,228
T a,236,10,18944
W 7
M a,48,a,248,v,224
T a,238,10,27136
M a,56,a,248,v,228
T a,240,7,19456
M a,52,a,252,v,224
T a,242,7,27648
M a,60,a,252,v,228
T a,244,10,19456
W 7
M a,64,a,224,v,224
T a,246,10,27648
M a,72,a,224,v,228
T a,248,7,19968
M a,68,a,228,v,224
T a,250,7,28160
M a,76,a,228,v,228
T a,252,10,19968
W 7
M a,80,a,232,v,224
T a,254,10,28160
M a,88,a,232,v,228
s_barrier
T a,224,7,32768
M a,84,a,236,v,224
T a,226,7,40960
M a,92,a,236,v,228
T a,228,10,32768
W 7
M a,96,a,240,v,224
T a,230,10,40960
M a,104,a,240,v,228
T a,232,7,33280
M a,100,a,244,v,224
T a,234,7,41472
M a,108,a,244,v,228
T a,236,10,33280
W 7
M a,112,a,248,v,224
T a,238,10,41472
M a,120,a,248,v,228
T a,240,7,33792
M a,116,a,252,v,224
T a,242,7,41984
M a,124,a,252,v,228
T a,244,10,33792
W 7
M a,128,a,224,v,224
T a,246,10,41984
M a,136,a,224,v,228
T a,248,7,34304
M a,132,a,228,v,224
T a,250,7,42496
M a,140,a,228,v,228
T a,252,10,34304
W 7
M a,144,a,232,v,224
T a,254,10,42496
M a,152,a,232,v,228
T a,224,7,34816
M a,148,a,236,v,224
T a,226,7,43008
M a,156,a,236,v,228
T a,228,10,34816
W 7
M a,160,a,240,v,224
T a,230,10,43008
M a,168,a,240,v,228
T a,232,7,35328
M a,164,a,244,v,224
T a,234,7,43520
M a,172,a,244,v,228
T a,236,10,35328
W 7
M a,176,a,248,v,224
T a,238,10,43520
M a,184,a,248,v,228
s_barrier
T a,240,7,49152
M a,180,a,252,v,224
T a,242,7,57344
M a,188,a,252,v,228
T a,244,10,49152
W 7
M a,192,a,224,v,224
T a,246,10,57344
M a,200,a,224,v,228
T a,248,7,49664
M a,196,a,228,v,224
T a,250,7,57856
M a,204,a,228,v,228
T a,252,10,49664
W 7
M a,208,a,232,v,224
T a,254,10,57856
M a,216,a,232,v,228
T a,224,7,50176
M a,212,a,236,v,224
T a,226,7,58368
M a,220,a,236,v,228
T a,228,10,50176
W 7
M a,0,a,240,v,240
T a,230,10,58368
M a,8,a,240,v,244
T a,232,7,50688
M a,4,a,244,v,240
T a,234,7,58880
M a,12,a,244,v,244
T a,236,10,50688
W 7
M a,16,a,248,v,240
T a,238,10,58880
M a,24,a,248,v,244
T a,240,7,51200
M a,20,a,252,v,240
T a,242,7,59392
M a,28,a,252,v,244
T a,244,10,51200
W 7
M a,32,a,224,v,240
T a,246,10,59392
M a,40,a,224,v,244
T a,248,7,51712
M a,36,a,228,v,240
T a,250,7,59904
M a,44,a,228,v,244
T a,252,10,51712
W 7
M a,48,a,232,v,240
T a,254,10,59904
M a,56,a,232,v,244
T a,224,7,52224
M a,52,a,236,v,240
T a,226,7,60416
M a,60,a,236,v,244
T a,228,10,52224
W 7
M a,64,a,240,v,240
T a,230,10,60416
M a,72,a,240,v,244
T a,232,7,52736
M a,68,a,244,v,240
T a,234,7,60928
M a,76,a,244,v,244
T a,236,10,52736
W 7
M a,80,a,248,v,240
T a,238,10,60928
M a,88,a,248,v,244
s_barrier
.Lc0_82_%=:
T a,240,8,0
M a,84,a,252,v,240
T a,242,8,8192
M a,92,a,252,v,244
T a,244,11,0
W 7
M a,96,a,224,v,240
T a,246,11,8192
M a,104,a,224,v,244
T a,248,8,512
M a,100,a,228,v,240
T a,250,8,8704
M a,108,a,228,v,244
T a,252,11,512
W 7
M a,112,a,232,v,240
T a,254,11,8704
M a,120,a,232,v,244
)x"
R"x(T a,224,8,1024
M a,116,a,236,v,240
T a,226,8,9216
M a,124,a,236,v,244
T a,228,11,1024
W 7
M a,128,a,240,v,240
T a,230,11,9216
M a,136,a,240,v,244
T a,232,8,1536
M a,132,a,244,v,240
T a,234,8,9728
M a,140,a,244,v,244
T a,236,11,1536
W 7
M a,144,a,248,v,240
T a,238,11,9728
M a,152,a,248,v,244
T a,240,8,2048
M a,148,a,252,v,240
T a,242,8,10240
M a,156,a,252,v,244
T a,244,11,2048
W 7
M a,160,a,224,v,240
T a,246,11,10240
M a,168,a,224,v,244
T a,248,8,2560
M a,164,a,228,v,240
T a,250,8,10752
M a,172,a,228,v,244
T a,252,11,2560
W 7
M a,176,a,232,v,240
T a,254,11,10752
M a,184,a,232,v,244
s_barrier
T a,224,14,16384
M a,180,a,236,v,240
T a,226,14,18432
M a,188,a,236,v,244
T a,228,17,16384
W 7
M a,192,a,240,v,240
T a,230,17,18432
M a,200,a,240,v,244
T a,232,14,16896
M a,196,a,244,v,240
T a,234,14,18944
M a,204,a,244,v,244
T a,236,17,16896
W 7
M a,208,a,248,v,240
T a,238,17,18944
M a,216,a,248,v,244
T a,240,14,20480
M a,212,a,252,v,240
T a,242,14,22528
M a,220,a,252,v,244
T a,244,17,20480
W 7
v_mfma_f32_16x16x32_bf16 v[224:227], a[224:227], v[96:99], 0
T a,246,17,22528
v_mfma_f32_16x16x32_bf16 v[232:235], a[224:227], v[100:103], 0
T a,248,14,20992
v_mfma_f32_16x16x32_bf16 v[228:231], a[228:231], v[96:99], 0
T a,250,14,23040
v_mfma_f32_16x16x32_bf16 v[236:239], a[228:231], v[100:103], 0
T a,252,17,20992
W 7
v_mfma_f32_16x16x32_bf16 v[240:243], a[232:235], v[96:99], 0
T a,254,17,23040
v_mfma_f32_16x16x32_bf16 v[248:251], a[232:235], v[100:103], 0
T a,224,14,24576
v_mfma_f32_16x16x32_bf16 v[244:247], a[236:239], v[96:99], 0
T a,226,14,26624
v_mfma_f32_16x16x32_bf16 v[252:255], a[236:239], v[100:103], 0
T a,228,17,24576
W 7
M v,224,a,240,v,104
T a,230,17,26624
M v,232,a,240,v,108
T a,232,14,25088
M v,228,a,244,v,104
T a,234,14,27136
M v,236,a,244,v,108
T a,236,17,25088
W 7
M v,240,a,248,v,104
T a,238,17,27136
M v,248,a,248,v,108
T a,240,14,28672
M v,244,a,252,v,104
T a,242,14,30720
M v,252,a,252,v,108
T a,244,17,28672
W 7
M v,224,a,224,v,112
T a,246,17,30720
M v,232,a,224,v,116
T a,248,14,29184
M v,228,a,228,v,112
T a,250,14,31232
M v,236,a,228,v,116
T a,252,17,29184
W 7
M v,240,a,232,v,112
T a,254,17,31232
M v,248,a,232,v,116
s_barrier
T a,224,14,32768
M v,244,a,236,v,112
T a,226,14,34816
M v,252,a,236,v,116
T a,228,17,32768
W 7
M v,224,a,240,v,120
T a,230,17,34816
M v,232,a,240,v,124
T a,232,14,33280
M v,228,a,244,v,120
T a,234,14,35328
M v,236,a,244,v,124
T a,236,17,33280
W 7
M v,240,a,248,v,120
T a,238,17,35328
M v,248,a,248,v,124
T a,240,14,36864
M v,244,a,252,v,120
T a,242,14,38912
M v,252,a,252,v,124
T a,244,17,36864
W 7
M v,224,a,224,v,128
T a,246,17,38912
M v,232,a,224,v,132
T a,248,14,37376
M v,228,a,228,v,128
T a,250,14,39424
M v,236,a,228,v,132
T a,252,17,37376
W 7
M v,240,a,232,v,128
T a,254,17,39424
M v,248,a,232,v,132
T a,224,14,40960
M v,244,a,236,v,128
T a,226,14,43008
M v,252,a,236,v,132
T a,228,17,40960
W 7
M v,224,a,240,v,136
T a,230,17,43008
M v,232,a,240,v,140
T a,232,14,41472
M v,228,a,244,v,136
T a,234,14,43520
M v,236,a,244,v,140
T a,236,17,41472
W 7
M v,240,a,248,v,136
T a,238,17,43520
M v,248,a,248,v,140
T a,240,14,45056
M v,244,a,252,v,136
T a,242,14,47104
M v,252,a,252,v,140
T a,244,17,45056
W 7
M v,224,a,224,v,144
T a,246,17,47104
M v,232,a,224,v,148
T a,248,14,45568
M v,228,a,228,v,144
T a,250,14,47616
M v,236,a,228,v,148
T a,252,17,45568
W 7
M v,240,a,232,v,144
T a,254,17,47616
M v,248,a,232,v,148
s_barrier
T a,224,14,49152
M v,244,a,236,v,144
T a,226,14,51200
M v,252,a,236,v,148
T a,228,17,49152
W 7
M v,224,a,240,v,152
T a,230,17,51200
M v,232,a,240,v,156
T a,232,14,49664
M v,228,a,244,v,152
T a,234,14,51712
M v,236,a,244,v,156
T a,236,17,49664
W 7
M v,240,a,248,v,152
T a,238,17,51712
M v,248,a,248,v,156
T a,240,14,53248
M v,244,a,252,v,152
T a,242,14,55296
M v,252,a,252,v,156
T a,244,17,53248
W 7
M v,224,a,224,v,160
T a,246,17,55296
M v,232,a,224,v,164
T a,248,14,53760
M v,228,a,228,v,160
T a,250,14,55808
M v,236,a,228,v,164
T a,252,17,53760
W 7
M v,240,a,232,v,160
T a,254,17,55808
M v,248,a,232,v,164
T a,224,14,57344
M v,244,a,236,v,160
T a,226,14,59392
M v,252,a,236,v,164
T a,228,17,57344
W 7
M v,224,a,240,v,168
T a,230,17,59392
M v,232,a,240,v,172
T a,232,14,57856
M v,228,a,244,v,168
T a,234,14,59904
M v,236,a,244,v,172
T a,236,17,57856
W 7
M v,240,a,248,v,168
T a,238,17,59904
M v,248,a,248,v,172
T a,240,14,61440
M v,244,a,252,v,168
T a,242,14,63488
M v,252,a,252,v,172
T a,244,17,61440
W 7
M v,224,a,224,v,176
T a,246,17,63488
M v,232,a,224,v,180
T a,248,14,61952
M v,228,a,228,v,176
T a,250,14,64000
M v,236,a,228,v,180
T a,252,17,61952
W 7
M v,240,a,232,v,176
T a,254,17,64000
M v,248,a,232,v,180
s_barrier
T a,224,15,0
M v,244,a,236,v,176
T a,226,15,2048
M v,252,a,236,v,180
T a,228,18,0
W 7
M v,224,a,240,v,184
T a,230,18,2048
M v,232,a,240,v,188
T a,232,15,512
M v,228,a,244,v,184
T a,234,15,2560
M v,236,a,244,v,188
T a,236,18,512
W 7
M v,240,a,248,v,184
T a,238,18,2560
M v,248,a,248,v,188
T a,240,15,4096
M v,244,a,252,v,184
T a,242,15,6144
M v,252,a,252,v,188
T a,244,18,4096
W 7
M v,224,a,224,v,192
T a,246,18,6144
M v,232,a,224,v,196
T a,248,15,4608
M v,228,a,228,v,192
T a,250,15,6656
M v,236,a,228,v,196
T a,252,18,4608
W 7
M v,240,a,232,v,192
T a,254,18,6656
M v,248,a,232,v,196
T a,224,15,8192
M v,244,a,236,v,192
T a,226,15,10240
M v,252,a,236,v,196
T a,228,18,8192
W 7
M v,224,a,240,v,200
T a,230,18,10240
M v,232,a,240,v,204
T a,232,15,8704
M v,228,a,244,v,200
T a,234,15,10752
M v,236,a,244,v,204
T a,236,18,8704
W 7
M v,240,a,248,v,200
T a,238,18,10752
M v,248,a,248,v,204
T a,240,15,12288
M v,244,a,252,v,200
T a,242,15,14336
M v,252,a,252,v,204
T a,244,18,12288
W 7
M v,224,a,224,v,208
T a,246,18,14336
M v,232,a,224,v,212
T a,248,15,12800
M v,228,a,228,v,208
T a,250,15,14848
M v,236,a,228,v,212
T a,252,18,12800
W 7
M v,240,a,232,v,208
T a,254,18,14848
M v,248,a,232,v,212
s_barrier
T a,224,9,16384
M v,244,a,236,v,208
T a,226,9,24576
M v,252,a,236,v,212
T a,228,12,16384
W 7
M v,224,a,240,v,216
T a,230,12,24576
M v,232,a,240,v,220
T a,232,9,16896
M v,228,a,244,v,216
T a,234,9,25088
M v,236,a,244,v,220
T a,236,12,16896
W 7
M v,240,a,248,v,216
T a,238,12,25088
M v,248,a,248,v,220
T a,240,9,17408
M v,244,a,252,v,216
T a,242,9,25600
M v,252,a,252,v,220
s_nop 7
v_mul_f32 v224, v28, v224
v_mul_f32 v225, v28, v225
v_mul_f32 v226, v28, v226
v_mul_f32 v227, v28, v227
v_mul_f32 v228, v28, v228
v_mul_f32 v229, v28, v229
v_mul_f32 v230, v28, v230
v_mul_f32 v231, v28, v231
v_mul_f32 v232, v29, v232
v_mul_f32 v233, v29, v233
v_mul_f32 v234, v29, v234
v_mul_f32 v235, v29, v235
v_mul_f32 v236, v29, v236
v_mul_f32 v237, v29, v237
v_mul_f32 v238, v29, v238
v_mul_f32 v239, v29, v239
v_cvt_pk_bf16_f32 v224, v224, v225
v_cvt_pk_bf16_f32 v225, v226, v227
v_cvt_pk_bf16_f32 v226, v228, v229
v_cvt_pk_bf16_f32 v227, v230, v231
v_cvt_pk_bf16_f32 v228, v232, v233
v_cvt_pk_bf16_f32 v229, v234, v235
v_cvt_pk_bf16_f32 v230, v236, v237
v_cvt_pk_bf16_f32 v231, v238, v239
v_mul_f32 v240, v28, v240
v_mul_f32 v241, v28, v241
v_mul_f32 v242, v28, v242
v_mul_f32 v243, v28, v243
v_mul_f32 v244, v28, v244
v_mul_f32 v245, v28, v245
v_mul_f32 v246, v28, v246
v_mul_f32 v247, v28, v247
v_mul_f32 v248, v29, v248
v_mul_f32 v249, v29, v249
v_mul_f32 v250, v29, v250
v_mul_f32 v251, v29, v251
v_mul_f32 v252, v29, v252
v_mul_f32 v253, v29, v253
v_mul_f32 v254, v29, v254
v_mul_f32 v255, v29, v255
v_cvt_pk_bf16_f32 v240, v240, v241
v_cvt_pk_bf16_f32 v241, v242, v243
v_cvt_pk_bf16_f32 v242, v244, v245
v_cvt_pk_bf16_f32 v243, v246, v247
v_cvt_pk_bf16_f32 v244, v248, v249
v_cvt_pk_bf16_f32 v245, v250, v251
v_cvt_pk_bf16_f32 v246, v252, v253
v_cvt_pk_bf16_f32 v247, v254, v255
)x"
R"x(s_nop 1
T a,244,12,17408
W 7
M a,0,a,224,v,224
T a,246,12,25600
M a,8,a,224,v,228
T a,248,9,17920
M a,4,a,228,v,224
T a,250,9,26112
M a,12,a,228,v,228
T a,252,12,17920
W 7
M a,16,a,232,v,224
T a,254,12,26112
M a,24,a,232,v,228
T a,224,9,18432
M a,20,a,236,v,224
T a,226,9,26624
M a,28,a,236,v,228
T a,228,12,18432
W 7
M a,32,a,240,v,224
T a,230,12,26624
M a,40,a,240,v,228
T a,232,9,18944
M a,36,a,244,v,224
T a,234,9,27136
M a,44,a,244,v,228
T a,236,12,18944
W 7
M a,48,a,248,v,224
T a,238,12,27136
M a,56,a,248,v,228
T a,240,9,19456
M a,52,a,252,v,224
T a,242,9,27648
M a,60,a,252,v,228
T a,244,12,19456
W 7
M a,64,a,224,v,224
T a,246,12,27648
M a,72,a,224,v,228
T a,248,9,19968
M a,68,a,228,v,224
T a,250,9,28160
M a,76,a,228,v,228
T a,252,12,19968
W 7
M a,80,a,232,v,224
T a,254,12,28160
M a,88,a,232,v,228
s_barrier
.Lc0_88_%=:
T a,224,7,0
M a,84,a,236,v,224
T a,226,7,8192
M a,92,a,236,v,228
T a,228,10,0
W 7
M a,96,a,240,v,224
T a,230,10,8192
M a,104,a,240,v,228
T a,232,7,512
M a,100,a,244,v,224
T a,234,7,8704
M a,108,a,244,v,228
T a,236,10,512
W 7
M a,112,a,248,v,224
T a,238,10,8704
M a,120,a,248,v,228
T a,240,7,1024
M a,116,a,252,v,224
T a,242,7,9216
M a,124,a,252,v,228
T a,244,10,1024
W 7
M a,128,a,224,v,224
T a,246,10,9216
M a,136,a,224,v,228
T a,248,7,1536
M a,132,a,228,v,224
T a,250,7,9728
M a,140,a,228,v,228
T a,252,10,1536
W 7
M a,144,a,232,v,224
T a,254,10,9728
M a,152,a,232,v,228
T a,224,7,2048
M a,148,a,236,v,224
T a,226,7,10240
M a,156,a,236,v,228
T a,228,10,2048
W 7
M a,160,a,240,v,224
T a,230,10,10240
M a,168,a,240,v,228
T a,232,7,2560
M a,164,a,244,v,224
T a,234,7,10752
M a,172,a,244,v,228
T a,236,10,2560
W 7
M a,176,a,248,v,224
T a,238,10,10752
M a,184,a,248,v,228
s_barrier
T a,240,7,16384
M a,180,a,252,v,224
T a,242,7,24576
M a,188,a,252,v,228
T a,244,10,16384
W 7
M a,192,a,224,v,224
T a,246,10,24576
M a,200,a,224,v,228
T a,248,7,16896
M a,196,a,228,v,224
T a,250,7,25088
M a,204,a,228,v,228
T a,252,10,16896
W 7
M a,208,a,232,v,224
T a,254,10,25088
M a,216,a,232,v,228
T a,224,7,17408
M a,212,a,236,v,224
T a,226,7,25600
M a,220,a,236,v,228
T a,228,10,17408
W 7
M a,0,a,240,v,240
T a,230,10,25600
M a,8,a,240,v,244
T a,232,7,17920
M a,4,a,244,v,240
T a,234,7,26112
M a,12,a,244,v,244
T a,236,10,17920
W 7
M a,16,a,248,v,240
T a,238,10,26112
M a,24,a,248,v,244
T a,240,7,18432
M a,20,a,252,v,240
T a,242,7,26624
M a,28,a,252,v,244
T a,244,10,18432
W 7
M a,32,a,224,v,240
T a,246,10,26624
M a,40,a,224,v,244
T a,248,7,18944
M a,36,a,228,v,240
T a,250,7,27136
M a,44,a,228,v,244
T a,252,10,18944
W 7
M a,48,a,232,v,240
T a,254,10,27136
M a,56,a,232,v,244
T a,224,7,19456
M a,52,a,236,v,240
T a,226,7,27648
M a,60,a,236,v,244
T a,228,10,19456
W 7
M a,64,a,240,v,240
T a,230,10,27648
M a,72,a,240,v,244
T a,232,7,19968
M a,68,a,244,v,240
T a,234,7,28160
M a,76,a,244,v,244
T a,236,10,19968
W 7
M a,80,a,248,v,240
T a,238,10,28160
M a,88,a,248,v,244
s_barrier
T a,240,7,32768
M a,84,a,252,v,240
T a,242,7,40960
M a,92,a,252,v,244
T a,244,10,32768
W 7
M a,96,a,224,v,240
T a,246,10,40960
M a,104,a,224,v,244
T a,248,7,33280
M a,100,a,228,v,240
T a,250,7,41472
M a,108,a,228,v,244
T a,252,10,33280
W 7
M a,112,a,232,v,240
T a,254,10,41472
M a,120,a,232,v,244
T a,224,7,33792
M a,116,a,236,v,240
T a,226,7,41984
M a,124,a,236,v,244
T a,228,10,33792
W 7
M a,128,a,240,v,240
T a,230,10,41984
M a,136,a,240,v,244
T a,232,7,34304
M a,132,a,244,v,240
T a,234,7,42496
M a,140,a,244,v,244
T a,236,10,34304
W 7
M a,144,a,248,v,240
T a,238,10,42496
M a,152,a,248,v,244
T a,240,7,34816
M a,148,a,252,v,240
T a,242,7,43008
M a,156,a,252,v,244
T a,244,10,34816
W 7
M a,160,a,224,v,240
T a,246,10,43008
M a,168,a,224,v,244
T a,248,7,35328
M a,164,a,228,v,240
T a,250,7,43520
M a,172,a,228,v,244
T a,252,10,35328
W 7
M a,176,a,232,v,240
T a,254,10,43520
M a,184,a,232,v,244
M a,180,a,236,v,240
M a,188,a,236,v,244
W 6
M a,192,a,240,v,240
M a,200,a,240,v,244
W 4
M a,196,a,244,v,240
M a,204,a,244,v,244
W 2
M a,208,a,248,v,240
M a,216,a,248,v,244
W 0
M a,212,a,252,v,240
M a,220,a,252,v,244
s_barrier
s_nop 7
A 224,0
A 225,1
v_cvt_pk_bf16_f32 v96, v224, v225
A 226,2
A 227,3
v_cvt_pk_bf16_f32 v97, v226, v227
A 228,4
A 229,5
v_cvt_pk_bf16_f32 v98, v228, v229
A 230,6
A 231,7
v_cvt_pk_bf16_f32 v99, v230, v231
A 232,8
A 233,9
v_cvt_pk_bf16_f32 v100, v232, v233
A 234,10
A 235,11
v_cvt_pk_bf16_f32 v101, v234, v235
A 236,12
A 237,13
v_cvt_pk_bf16_f32 v102, v236, v237
A 238,14
A 239,15
v_cvt_pk_bf16_f32 v103, v238, v239
A 224,16
A 225,17
v_cvt_pk_bf16_f32 v104, v224, v225
A 226,18
A 227,19
v_cvt_pk_bf16_f32 v105, v226, v227
A 228,20
A 229,21
v_cvt_pk_bf16_f32 v106, v228, v229
A 230,22
A 231,23
v_cvt_pk_bf16_f32 v107, v230, v231
A 232,24
A 233,25
v_cvt_pk_bf16_f32 v108, v232, v233
A 234,26
A 235,27
v_cvt_pk_bf16_f32 v109, v234, v235
A 236,28
A 237,29
v_cvt_pk_bf16_f32 v110, v236, v237
A 238,30
A 239,31
v_cvt_pk_bf16_f32 v111, v238, v239
A 224,32
A 225,33
v_cvt_pk_bf16_f32 v112, v224, v225
A 226,34
A 227,35
v_cvt_pk_bf16_f32 v113, v226, v227
A 228,36
A 229,37
v_cvt_pk_bf16_f32 v114, v228, v229
A 230,38
A 231,39
v_cvt_pk_bf16_f32 v115, v230, v231
A 232,40
A 233,41
v_cvt_pk_bf16_f32 v116, v232, v233
A 234,42
A 235,43
v_cvt_pk_bf16_f32 v117, v234, v235
A 236,44
A 237,45
v_cvt_pk_bf16_f32 v118, v236, v237
A 238,46
A 239,47
v_cvt_pk_bf16_f32 v119, v238, v239
A 224,48
A 225,49
v_cvt_pk_bf16_f32 v120, v224, v225
A 226,50
A 227,51
v_cvt_pk_bf16_f32 v121, v226, v227
A 228,52
A 229,53
v_cvt_pk_bf16_f32 v122, v228, v229
A 230,54
A 231,55
v_cvt_pk_bf16_f32 v123, v230, v231
A 232,56
A 233,57
v_cvt_pk_bf16_f32 v124, v232, v233
A 234,58
A 235,59
v_cvt_pk_bf16_f32 v125, v234, v235
A 236,60
A 237,61
v_cvt_pk_bf16_f32 v126, v236, v237
A 238,62
A 239,63
v_cvt_pk_bf16_f32 v127, v238, v239
A 224,64
A 225,65
v_cvt_pk_bf16_f32 v128, v224, v225
A 226,66
A 227,67
v_cvt_pk_bf16_f32 v129, v226, v227
A 228,68
A 229,69
v_cvt_pk_bf16_f32 v130, v228, v229
A 230,70
A 231,71
v_cvt_pk_bf16_f32 v131, v230, v231
A 232,72
A 233,73
v_cvt_pk_bf16_f32 v132, v232, v233
A 234,74
A 235,75
v_cvt_pk_bf16_f32 v133, v234, v235
A 236,76
A 237,77
v_cvt_pk_bf16_f32 v134, v236, v237
A 238,78
A 239,79
v_cvt_pk_bf16_f32 v135, v238, v239
A 224,80
A 225,81
v_cvt_pk_bf16_f32 v136, v224, v225
A 226,82
A 227,83
v_cvt_pk_bf16_f32 v137, v226, v227
A 228,84
A 229,85
v_cvt_pk_bf16_f32 v138, v228, v229
A 230,86
A 231,87
v_cvt_pk_bf16_f32 v139, v230, v231
A 232,88
A 233,89
v_cvt_pk_bf16_f32 v140, v232, v233
A 234,90
A 235,91
v_cvt_pk_bf16_f32 v141, v234, v235
A 236,92
A 237,93
v_cvt_pk_bf16_f32 v142, v236, v237
A 238,94
A 239,95
v_cvt_pk_bf16_f32 v143, v238, v239
A 224,96
A 225,97
v_cvt_pk_bf16_f32 v144, v224, v225
A 226,98
A 227,99
)x"
R"x(v_cvt_pk_bf16_f32 v145, v226, v227
A 228,100
A 229,101
v_cvt_pk_bf16_f32 v146, v228, v229
A 230,102
A 231,103
v_cvt_pk_bf16_f32 v147, v230, v231
A 232,104
A 233,105
v_cvt_pk_bf16_f32 v148, v232, v233
A 234,106
A 235,107
v_cvt_pk_bf16_f32 v149, v234, v235
A 236,108
A 237,109
v_cvt_pk_bf16_f32 v150, v236, v237
A 238,110
A 239,111
v_cvt_pk_bf16_f32 v151, v238, v239
A 224,112
A 225,113
v_cvt_pk_bf16_f32 v152, v224, v225
A 226,114
A 227,115
v_cvt_pk_bf16_f32 v153, v226, v227
A 228,116
A 229,117
v_cvt_pk_bf16_f32 v154, v228, v229
A 230,118
A 231,119
v_cvt_pk_bf16_f32 v155, v230, v231
A 232,120
A 233,121
v_cvt_pk_bf16_f32 v156, v232, v233
A 234,122
A 235,123
v_cvt_pk_bf16_f32 v157, v234, v235
A 236,124
A 237,125
v_cvt_pk_bf16_f32 v158, v236, v237
A 238,126
A 239,127
v_cvt_pk_bf16_f32 v159, v238, v239
A 224,128
A 225,129
v_cvt_pk_bf16_f32 v160, v224, v225
A 226,130
A 227,131
v_cvt_pk_bf16_f32 v161, v226, v227
A 228,132
A 229,133
v_cvt_pk_bf16_f32 v162, v228, v229
A 230,134
A 231,135
v_cvt_pk_bf16_f32 v163, v230, v231
A 232,136
A 233,137
v_cvt_pk_bf16_f32 v164, v232, v233
A 234,138
A 235,139
v_cvt_pk_bf16_f32 v165, v234, v235
A 236,140
A 237,141
v_cvt_pk_bf16_f32 v166, v236, v237
A 238,142
A 239,143
v_cvt_pk_bf16_f32 v167, v238, v239
A 224,144
A 225,145
v_cvt_pk_bf16_f32 v168, v224, v225
A 226,146
A 227,147
v_cvt_pk_bf16_f32 v169, v226, v227
A 228,148
A 229,149
v_cvt_pk_bf16_f32 v170, v228, v229
A 230,150
A 231,151
v_cvt_pk_bf16_f32 v171, v230, v231
A 232,152
A 233,153
v_cvt_pk_bf16_f32 v172, v232, v233
A 234,154
A 235,155
v_cvt_pk_bf16_f32 v173, v234, v235
A 236,156
A 237,157
v_cvt_pk_bf16_f32 v174, v236, v237
A 238,158
A 239,159
v_cvt_pk_bf16_f32 v175, v238, v239
A 224,160
A 225,161
v_cvt_pk_bf16_f32 v176, v224, v225
A 226,162
A 227,163
v_cvt_pk_bf16_f32 v177, v226, v227
A 228,164
A 229,165
v_cvt_pk_bf16_f32 v178, v228, v229
A 230,166
A 231,167
v_cvt_pk_bf16_f32 v179, v230, v231
A 232,168
A 233,169
v_cvt_pk_bf16_f32 v180, v232, v233
A 234,170
A 235,171
v_cvt_pk_bf16_f32 v181, v234, v235
A 236,172
A 237,173
v_cvt_pk_bf16_f32 v182, v236, v237
A 238,174
A 239,175
v_cvt_pk_bf16_f32 v183, v238, v239
A 224,176
A 225,177
v_cvt_pk_bf16_f32 v184, v224, v225
A 226,178
A 227,179
v_cvt_pk_bf16_f32 v185, v226, v227
A 228,180
A 229,181
v_cvt_pk_bf16_f32 v186, v228, v229
A 230,182
A 231,183
v_cvt_pk_bf16_f32 v187, v230, v231
A 232,184
A 233,185
v_cvt_pk_bf16_f32 v188, v232, v233
A 234,186
A 235,187
v_cvt_pk_bf16_f32 v189, v234, v235
A 236,188
A 237,189
v_cvt_pk_bf16_f32 v190, v236, v237
A 238,190
A 239,191
v_cvt_pk_bf16_f32 v191, v238, v239
A 224,192
A 225,193
v_cvt_pk_bf16_f32 v192, v224, v225
A 226,194
A 227,195
v_cvt_pk_bf16_f32 v193, v226, v227
A 228,196
A 229,197
v_cvt_pk_bf16_f32 v194, v228, v229
A 230,198
A 231,199
v_cvt_pk_bf16_f32 v195, v230, v231
A 232,200
A 233,201
v_cvt_pk_bf16_f32 v196, v232, v233
A 234,202
A 235,203
v_cvt_pk_bf16_f32 v197, v234, v235
A 236,204
A 237,205
v_cvt_pk_bf16_f32 v198, v236, v237
A 238,206
A 239,207
v_cvt_pk_bf16_f32 v199, v238, v239
A 224,208
A 225,209
v_cvt_pk_bf16_f32 v200, v224, v225
A 226,210
A 227,211
v_cvt_pk_bf16_f32 v201, v226, v227
A 228,212
A 229,213
v_cvt_pk_bf16_f32 v202, v228, v229
A 230,214
A 231,215
v_cvt_pk_bf16_f32 v203, v230, v231
A 232,216
A 233,217
v_cvt_pk_bf16_f32 v204, v232, v233
A 234,218
A 235,219
v_cvt_pk_bf16_f32 v205, v234, v235
A 236,220
A 237,221
v_cvt_pk_bf16_f32 v206, v236, v237
A 238,222
A 239,223
v_cvt_pk_bf16_f32 v207, v238, v239
s_barrier
R v,224,1,49152
R v,226,4,49152
R v,228,1,57344
R v,230,4,57344
R v,232,1,49664
R v,234,4,49664
R v,236,1,57856
R v,238,4,57856
R v,240,1,50176
R v,242,4,50176
R v,244,1,58368
W 7
v_mfma_f32_16x16x32_bf16 a[0:3], v[224:227], v[32:35], 0
R v,246,4,58368
v_mfma_f32_16x16x32_bf16 a[8:11], v[224:227], v[36:39], 0
R v,248,1,50688
v_mfma_f32_16x16x32_bf16 a[4:7], v[228:231], v[32:35], 0
R v,250,4,50688
v_mfma_f32_16x16x32_bf16 a[12:15], v[228:231], v[36:39], 0
R v,252,1,58880
W 7
M a,0,v,232,v,40
R v,254,4,58880
M a,8,v,232,v,44
R v,224,1,51200
M a,4,v,236,v,40
R v,226,4,51200
M a,12,v,236,v,44
R v,228,1,59392
W 7
M a,0,v,240,v,48
R v,230,4,59392
M a,8,v,240,v,52
R v,232,1,51712
M a,4,v,244,v,48
R v,234,4,51712
M a,12,v,244,v,52
R v,236,1,59904
W 7
M a,0,v,248,v,56
R v,238,4,59904
M a,8,v,248,v,60
R v,240,1,52224
M a,4,v,252,v,56
R v,242,4,52224
M a,12,v,252,v,60
R v,244,1,60416
W 7
M a,0,v,224,v,64
R v,246,4,60416
M a,8,v,224,v,68
R v,248,1,52736
M a,4,v,228,v,64
R v,250,4,52736
M a,12,v,228,v,68
R v,252,1,60928
W 7
M a,0,v,232,v,72
R v,254,4,60928
M a,8,v,232,v,76
s_barrier
R v,224,2,0
M a,4,v,236,v,72
R v,226,5,0
M a,12,v,236,v,76
R v,228,2,8192
W 7
M a,0,v,240,v,80
R v,230,5,8192
M a,8,v,240,v,84
R v,232,2,512
M a,4,v,244,v,80
R v,234,5,512
M a,12,v,244,v,84
R v,236,2,8704
W 7
M a,0,v,248,v,88
R v,238,5,8704
M a,8,v,248,v,92
R v,240,2,1024
M a,4,v,252,v,88
R v,242,5,1024
M a,12,v,252,v,92
R v,244,2,9216
W 7
v_mfma_f32_16x16x32_bf16 a[16:19], v[224:227], v[32:35], 0
R v,246,5,9216
v_mfma_f32_16x16x32_bf16 a[24:27], v[224:227], v[36:39], 0
R v,248,2,1536
v_mfma_f32_16x16x32_bf16 a[20:23], v[228:231], v[32:35], 0
R v,250,5,1536
v_mfma_f32_16x16x32_bf16 a[28:31], v[228:231], v[36:39], 0
R v,252,2,9728
W 7
M a,16,v,232,v,40
R v,254,5,9728
M a,24,v,232,v,44
R v,224,2,2048
A 30,0
A 31,1
M a,20,v,236,v,40
R v,226,5,2048
v_max_f32 v26, v30, v31
M a,28,v,236,v,44
R v,228,2,10240
W 7
A 30,2
M a,16,v,240,v,48
R v,230,5,10240
A 31,3
M a,24,v,240,v,52
R v,232,2,2560
v_max3_f32 v26, v30, v31, v26
A 30,4
M a,20,v,244,v,48
R v,234,5,2560
A 31,5
M a,28,v,244,v,52
R v,236,2,10752
W 7
v_max3_f32 v26, v30, v31, v26
M a,16,v,248,v,56
R v,238,5,10752
A 30,6
M a,24,v,248,v,60
R v,240,2,3072
A 31,7
v_max3_f32 v26, v30, v31, v26
M a,20,v,252,v,56
R v,242,5,3072
A 30,8
M a,28,v,252,v,60
R v,244,2,11264
W 7
M a,16,v,224,v,64
R v,246,5,11264
A 31,9
M a,24,v,224,v,68
R v,248,2,3584
v_max_f32 v27, v30, v31
M a,20,v,228,v,64
R v,250,5,3584
A 30,10
M a,28,v,228,v,68
R v,252,2,11776
W 7
M a,16,v,232,v,72
R v,254,5,11776
A 31,11
M a,24,v,232,v,76
s_barrier
R v,224,2,16384
v_max3_f32 v27, v30, v31, v27
M a,20,v,236,v,72
R v,226,5,16384
A 30,12
M a,28,v,236,v,76
R v,228,2,24576
W 7
M a,16,v,240,v,80
R v,230,5,24576
A 31,13
M a,24,v,240,v,84
R v,232,2,16896
v_max3_f32 v27, v30, v31, v27
M a,20,v,244,v,80
R v,234,5,16896
A 30,14
M a,28,v,244,v,84
R v,236,2,25088
W 7
M a,16,v,248,v,88
R v,238,5,25088
A 31,15
M a,24,v,248,v,92
R v,240,2,17408
v_max3_f32 v27, v30, v31, v27
M a,20,v,252,v,88
R v,242,5,17408
M a,28,v,252,v,92
R v,244,2,25600
W 7
v_mfma_f32_16x16x32_bf16 a[32:35], v[224:227], v[32:35], 0
R v,246,5,25600
v_mfma_f32_16x16x32_bf16 a[40:43], v[224:227], v[36:39], 0
R v,248,2,17920
v_mfma_f32_16x16x32_bf16 a[36:39], v[228:231], v[32:35], 0
R v,250,5,17920
v_mfma_f32_16x16x32_bf16 a[44:47], v[228:231], v[36:39], 0
R v,252,2,26112
W 7
M a,32,v,232,v,40
R v,254,5,26112
M a,40,v,232,v,44
R v,224,2,18432
A 30,16
A 31,17
M a,36,v,236,v,40
R v,226,5,18432
v_max3_f32 v26, v30, v31, v26
M a,44,v,236,v,44
R v,228,2,26624
W 7
A 30,18
M a,32,v,240,v,48
R v,230,5,26624
A 31,19
M a,40,v,240,v,52
R v,232,2,18944
v_max3_f32 v26, v30, v31, v26
A 30,20
M a,36,v,244,v,48
)x"
R"x(R v,234,5,18944
A 31,21
M a,44,v,244,v,52
R v,236,2,27136
W 7
v_max3_f32 v26, v30, v31, v26
M a,32,v,248,v,56
R v,238,5,27136
A 30,22
M a,40,v,248,v,60
R v,240,2,19456
A 31,23
v_max3_f32 v26, v30, v31, v26
M a,36,v,252,v,56
R v,242,5,19456
A 30,24
M a,44,v,252,v,60
R v,244,2,27648
W 7
M a,32,v,224,v,64
R v,246,5,27648
A 31,25
M a,40,v,224,v,68
R v,248,2,19968
v_max3_f32 v27, v30, v31, v27
M a,36,v,228,v,64
R v,250,5,19968
A 30,26
M a,44,v,228,v,68
R v,252,2,28160
W 7
M a,32,v,232,v,72
R v,254,5,28160
A 31,27
M a,40,v,232,v,76
s_barrier
.Lc0_94_%=:
R v,224,2,32768
v_max3_f32 v27, v30, v31, v27
M a,36,v,236,v,72
R v,226,5,32768
A 30,28
M a,44,v,236,v,76
R v,228,2,40960
W 7
M a,32,v,240,v,80
R v,230,5,40960
A 31,29
M a,40,v,240,v,84
R v,232,2,33280
v_max3_f32 v27, v30, v31, v27
M a,36,v,244,v,80
R v,234,5,33280
A 30,30
M a,44,v,244,v,84
R v,236,2,41472
W 7
M a,32,v,248,v,88
R v,238,5,41472
A 31,31
M a,40,v,248,v,92
R v,240,2,33792
v_max3_f32 v27, v30, v31, v27
M a,36,v,252,v,88
R v,242,5,33792
M a,44,v,252,v,92
R v,244,2,41984
W 7
v_mfma_f32_16x16x32_bf16 a[48:51], v[224:227], v[32:35], 0
R v,246,5,41984
v_mfma_f32_16x16x32_bf16 a[56:59], v[224:227], v[36:39], 0
R v,248,2,34304
v_mfma_f32_16x16x32_bf16 a[52:55], v[228:231], v[32:35], 0
R v,250,5,34304
v_mfma_f32_16x16x32_bf16 a[60:63], v[228:231], v[36:39], 0
R v,252,2,42496
W 7
M a,48,v,232,v,40
R v,254,5,42496
M a,56,v,232,v,44
R v,224,2,34816
A 30,32
A 31,33
M a,52,v,236,v,40
R v,226,5,34816
v_max3_f32 v26, v30, v31, v26
M a,60,v,236,v,44
R v,228,2,43008
W 7
A 30,34
M a,48,v,240,v,48
R v,230,5,43008
A 31,35
M a,56,v,240,v,52
R v,232,2,35328
v_max3_f32 v26, v30, v31, v26
A 30,36
M a,52,v,244,v,48
R v,234,5,35328
A 31,37
M a,60,v,244,v,52
R v,236,2,43520
W 7
v_max3_f32 v26, v30, v31, v26
M a,48,v,248,v,56
R v,238,5,43520
A 30,38
M a,56,v,248,v,60
R v,240,2,35840
A 31,39
v_max3_f32 v26, v30, v31, v26
M a,52,v,252,v,56
R v,242,5,35840
A 30,40
M a,60,v,252,v,60
R v,244,2,44032
W 7
M a,48,v,224,v,64
R v,246,5,44032
A 31,41
M a,56,v,224,v,68
R v,248,2,36352
v_max3_f32 v27, v30, v31, v27
M a,52,v,228,v,64
R v,250,5,36352
A 30,42
M a,60,v,228,v,68
R v,252,2,44544
W 7
M a,48,v,232,v,72
R v,254,5,44544
A 31,43
M a,56,v,232,v,76
s_barrier
R v,224,2,49152
v_max3_f32 v27, v30, v31, v27
M a,52,v,236,v,72
R v,226,5,49152
A 30,44
M a,60,v,236,v,76
R v,228,2,57344
W 7
M a,48,v,240,v,80
R v,230,5,57344
A 31,45
M a,56,v,240,v,84
R v,232,2,49664
v_max3_f32 v27, v30, v31, v27
M a,52,v,244,v,80
R v,234,5,49664
A 30,46
M a,60,v,244,v,84
R v,236,2,57856
W 7
M a,48,v,248,v,88
R v,238,5,57856
A 31,47
M a,56,v,248,v,92
R v,240,2,50176
v_max3_f32 v27, v30, v31, v27
M a,52,v,252,v,88
R v,242,5,50176
M a,60,v,252,v,92
R v,244,2,58368
W 7
v_mfma_f32_16x16x32_bf16 a[64:67], v[224:227], v[32:35], 0
R v,246,5,58368
v_mfma_f32_16x16x32_bf16 a[72:75], v[224:227], v[36:39], 0
R v,248,2,50688
v_mfma_f32_16x16x32_bf16 a[68:71], v[228:231], v[32:35], 0
R v,250,5,50688
v_mfma_f32_16x16x32_bf16 a[76:79], v[228:231], v[36:39], 0
R v,252,2,58880
W 7
M a,64,v,232,v,40
R v,254,5,58880
M a,72,v,232,v,44
R v,224,2,51200
A 30,48
A 31,49
M a,68,v,236,v,40
R v,226,5,51200
v_max3_f32 v26, v30, v31, v26
M a,76,v,236,v,44
R v,228,2,59392
W 7
A 30,50
M a,64,v,240,v,48
R v,230,5,59392
A 31,51
M a,72,v,240,v,52
R v,232,2,51712
v_max3_f32 v26, v30, v31, v26
A 30,52
M a,68,v,244,v,48
R v,234,5,51712
A 31,53
M a,76,v,244,v,52
R v,236,2,59904
W 7
v_max3_f32 v26, v30, v31, v26
M a,64,v,248,v,56
R v,238,5,59904
A 30,54
M a,72,v,248,v,60
R v,240,2,52224
A 31,55
v_max3_f32 v26, v30, v31, v26
M a,68,v,252,v,56
R v,242,5,52224
A 30,56
M a,76,v,252,v,60
R v,244,2,60416
W 7
M a,64,v,224,v,64
R v,246,5,60416
A 31,57
M a,72,v,224,v,68
R v,248,2,52736
v_max3_f32 v27, v30, v31, v27
M a,68,v,228,v,64
R v,250,5,52736
A 30,58
M a,76,v,228,v,68
R v,252,2,60928
W 7
M a,64,v,232,v,72
R v,254,5,60928
A 31,59
M a,72,v,232,v,76
s_barrier
R v,224,3,0
v_max3_f32 v27, v30, v31, v27
M a,68,v,236,v,72
R v,226,6,0
A 30,60
M a,76,v,236,v,76
R v,228,3,8192
W 7
M a,64,v,240,v,80
R v,230,6,8192
A 31,61
M a,72,v,240,v,84
R v,232,3,512
v_max3_f32 v27, v30, v31, v27
M a,68,v,244,v,80
R v,234,6,512
A 30,62
M a,76,v,244,v,84
R v,236,3,8704
W 7
M a,64,v,248,v,88
R v,238,6,8704
A 31,63
M a,72,v,248,v,92
R v,240,3,1024
v_max3_f32 v27, v30, v31, v27
M a,68,v,252,v,88
R v,242,6,1024
M a,76,v,252,v,92
R v,244,3,9216
W 7
v_mfma_f32_16x16x32_bf16 a[80:83], v[224:227], v[32:35], 0
R v,246,6,9216
v_mfma_f32_16x16x32_bf16 a[88:91], v[224:227], v[36:39], 0
R v,248,3,1536
v_mfma_f32_16x16x32_bf16 a[84:87], v[228:231], v[32:35], 0
R v,250,6,1536
v_mfma_f32_16x16x32_bf16 a[92:95], v[228:231], v[36:39], 0
R v,252,3,9728
W 7
M a,80,v,232,v,40
R v,254,6,9728
M a,88,v,232,v,44
R v,224,3,2048
A 30,64
A 31,65
M a,84,v,236,v,40
R v,226,6,2048
v_max3_f32 v26, v30, v31, v26
M a,92,v,236,v,44
R v,228,3,10240
W 7
A 30,66
M a,80,v,240,v,48
R v,230,6,10240
A 31,67
M a,88,v,240,v,52
R v,232,3,2560
v_max3_f32 v26, v30, v31, v26
A 30,68
M a,84,v,244,v,48
R v,234,6,2560
A 31,69
M a,92,v,244,v,52
R v,236,3,10752
W 7
v_max3_f32 v26, v30, v31, v26
M a,80,v,248,v,56
R v,238,6,10752
A 30,70
M a,88,v,248,v,60
R v,240,3,3072
A 31,71
v_max3_f32 v26, v30, v31, v26
M a,84,v,252,v,56
R v,242,6,3072
A 30,72
M a,92,v,252,v,60
R v,244,3,11264
W 7
M a,80,v,224,v,64
R v,246,6,11264
A 31,73
M a,88,v,224,v,68
R v,248,3,3584
v_max3_f32 v27, v30, v31, v27
M a,84,v,228,v,64
R v,250,6,3584
A 30,74
M a,92,v,228,v,68
R v,252,3,11776
W 7
M a,80,v,232,v,72
R v,254,6,11776
A 31,75
M a,88,v,232,v,76
s_barrier
R v,224,3,16384
v_max3_f32 v27, v30, v31, v27
M a,84,v,236,v,72
R v,226,6,16384
A 30,76
M a,92,v,236,v,76
R v,228,3,24576
W 7
M a,80,v,240,v,80
R v,230,6,24576
A 31,77
M a,88,v,240,v,84
R v,232,3,16896
v_max3_f32 v27, v30, v31, v27
M a,84,v,244,v,80
R v,234,6,16896
A 30,78
M a,92,v,244,v,84
R v,236,3,25088
W 7
M a,80,v,248,v,88
R v,238,6,25088
A 31,79
M a,88,v,248,v,92
R v,240,3,17408
v_max3_f32 v27, v30, v31, v27
M a,84,v,252,v,88
R v,242,6,17408
M a,92,v,252,v,92
R v,244,3,25600
W 7
v_mfma_f32_16x16x32_bf16 a[96:99], v[224:227], v[32:35], 0
R v,246,6,25600
v_mfma_f32_16x16x32_bf16 a[104:107], v[224:227], v[36:39], 0
R v,248,3,17920
v_mfma_f32_16x16x32_bf16 a[100:103], v[228:231], v[32:35], 0
R v,250,6,17920
v_mfma_f32_16x16x32_bf16 a[108:111], v[228:231], v[36:39], 0
R v,252,3,26112
W 7
M a,96,v,232,v,40
R v,254,6,26112
M a,104,v,232,v,44
R v,224,3,18432
A 30,80
A 31,81
M a,100,v,236,v,40
R v,226,6,18432
v_max3_f32 v26, v30, v31, v26
M a,108,v,236,v,44
R v,228,3,26624
W 7
A 30,82
M a,96,v,240,v,48
R v,230,6,26624
A 31,83
M a,104,v,240,v,52
R v,232,3,18944
v_max3_f32 v26, v30, v31, v26
A 30,84
M a,100,v,244,v,48
R v,234,6,18944
A 31,85
M a,108,v,244,v,52
R v,236,3,27136
W 7
v_max3_f32 v26, v30, v31, v26
M a,96,v,248,v,56
R v,238,6,27136
A 30,86
M a,104,v,248,v,60
R v,240,3,19456
)x"
R"x(A 31,87
v_max3_f32 v26, v30, v31, v26
M a,100,v,252,v,56
R v,242,6,19456
A 30,88
M a,108,v,252,v,60
R v,244,3,27648
W 7
M a,96,v,224,v,64
R v,246,6,27648
A 31,89
M a,104,v,224,v,68
R v,248,3,19968
v_max3_f32 v27, v30, v31, v27
M a,100,v,228,v,64
R v,250,6,19968
A 30,90
M a,108,v,228,v,68
R v,252,3,28160
W 7
M a,96,v,232,v,72
R v,254,6,28160
A 31,91
M a,104,v,232,v,76
s_barrier
R v,224,1,0
v_max3_f32 v27, v30, v31, v27
M a,100,v,236,v,72
R v,226,4,0
A 30,92
M a,108,v,236,v,76
R v,228,1,8192
W 7
M a,96,v,240,v,80
R v,230,4,8192
A 31,93
M a,104,v,240,v,84
R v,232,1,512
v_max3_f32 v27, v30, v31, v27
M a,100,v,244,v,80
R v,234,4,512
A 30,94
M a,108,v,244,v,84
R v,236,1,8704
W 7
M a,96,v,248,v,88
R v,238,4,8704
A 31,95
M a,104,v,248,v,92
R v,240,1,1024
v_max3_f32 v27, v30, v31, v27
M a,100,v,252,v,88
R v,242,4,1024
M a,108,v,252,v,92
R v,244,1,9216
W 7
v_mfma_f32_16x16x32_bf16 a[112:115], v[224:227], v[32:35], 0
R v,246,4,9216
v_mfma_f32_16x16x32_bf16 a[120:123], v[224:227], v[36:39], 0
R v,248,1,1536
v_mfma_f32_16x16x32_bf16 a[116:119], v[228:231], v[32:35], 0
R v,250,4,1536
v_mfma_f32_16x16x32_bf16 a[124:127], v[228:231], v[36:39], 0
R v,252,1,9728
W 7
M a,112,v,232,v,40
R v,254,4,9728
M a,120,v,232,v,44
R v,224,1,2048
A 30,96
A 31,97
M a,116,v,236,v,40
R v,226,4,2048
v_max3_f32 v26, v30, v31, v26
M a,124,v,236,v,44
R v,228,1,10240
W 7
A 30,98
M a,112,v,240,v,48
R v,230,4,10240
A 31,99
M a,120,v,240,v,52
R v,232,1,2560
v_max3_f32 v26, v30, v31, v26
A 30,100
M a,116,v,244,v,48
R v,234,4,2560
A 31,101
M a,124,v,244,v,52
R v,236,1,10752
W 7
v_max3_f32 v26, v30, v31, v26
M a,112,v,248,v,56
R v,238,4,10752
A 30,102
M a,120,v,248,v,60
R v,240,1,3072
A 31,103
v_max3_f32 v26, v30, v31, v26
M a,116,v,252,v,56
R v,242,4,3072
A 30,104
M a,124,v,252,v,60
R v,244,1,11264
W 7
M a,112,v,224,v,64
R v,246,4,11264
A 31,105
M a,120,v,224,v,68
R v,248,1,3584
v_max3_f32 v27, v30, v31, v27
M a,116,v,228,v,64
R v,250,4,3584
A 30,106
M a,124,v,228,v,68
R v,252,1,11776
W 7
M a,112,v,232,v,72
R v,254,4,11776
A 31,107
M a,120,v,232,v,76
s_barrier
R v,224,1,16384
v_max3_f32 v27, v30, v31, v27
M a,116,v,236,v,72
R v,226,4,16384
A 30,108
M a,124,v,236,v,76
R v,228,1,24576
W 7
M a,112,v,240,v,80
R v,230,4,24576
A 31,109
M a,120,v,240,v,84
R v,232,1,16896
v_max3_f32 v27, v30, v31, v27
M a,116,v,244,v,80
R v,234,4,16896
A 30,110
M a,124,v,244,v,84
R v,236,1,25088
W 7
M a,112,v,248,v,88
R v,238,4,25088
A 31,111
M a,120,v,248,v,92
R v,240,1,17408
v_max3_f32 v27, v30, v31, v27
M a,116,v,252,v,88
R v,242,4,17408
M a,124,v,252,v,92
R v,244,1,25600
W 7
v_mfma_f32_16x16x32_bf16 a[128:131], v[224:227], v[32:35], 0
R v,246,4,25600
v_mfma_f32_16x16x32_bf16 a[136:139], v[224:227], v[36:39], 0
R v,248,1,17920
v_mfma_f32_16x16x32_bf16 a[132:135], v[228:231], v[32:35], 0
R v,250,4,17920
v_mfma_f32_16x16x32_bf16 a[140:143], v[228:231], v[36:39], 0
R v,252,1,26112
W 7
M a,128,v,232,v,40
R v,254,4,26112
M a,136,v,232,v,44
R v,224,1,18432
A 30,112
A 31,113
M a,132,v,236,v,40
R v,226,4,18432
v_max3_f32 v26, v30, v31, v26
M a,140,v,236,v,44
R v,228,1,26624
W 7
A 30,114
M a,128,v,240,v,48
R v,230,4,26624
A 31,115
M a,136,v,240,v,52
R v,232,1,18944
v_max3_f32 v26, v30, v31, v26
A 30,116
M a,132,v,244,v,48
R v,234,4,18944
A 31,117
M a,140,v,244,v,52
R v,236,1,27136
W 7
v_max3_f32 v26, v30, v31, v26
M a,128,v,248,v,56
R v,238,4,27136
A 30,118
M a,136,v,248,v,60
R v,240,1,19456
A 31,119
v_max3_f32 v26, v30, v31, v26
M a,132,v,252,v,56
R v,242,4,19456
A 30,120
M a,140,v,252,v,60
R v,244,1,27648
W 7
M a,128,v,224,v,64
R v,246,4,27648
A 31,121
M a,136,v,224,v,68
R v,248,1,19968
v_max3_f32 v27, v30, v31, v27
M a,132,v,228,v,64
R v,250,4,19968
A 30,122
M a,140,v,228,v,68
R v,252,1,28160
W 7
M a,128,v,232,v,72
R v,254,4,28160
A 31,123
M a,136,v,232,v,76
s_barrier
.Lc0_100_%=:
R v,224,1,32768
v_max3_f32 v27, v30, v31, v27
M a,132,v,236,v,72
R v,226,4,32768
A 30,124
M a,140,v,236,v,76
R v,228,1,40960
W 7
M a,128,v,240,v,80
R v,230,4,40960
A 31,125
M a,136,v,240,v,84
R v,232,1,33280
v_max3_f32 v27, v30, v31, v27
M a,132,v,244,v,80
R v,234,4,33280
A 30,126
M a,140,v,244,v,84
R v,236,1,41472
W 7
M a,128,v,248,v,88
R v,238,4,41472
A 31,127
M a,136,v,248,v,92
R v,240,1,33792
v_max3_f32 v27, v30, v31, v27
M a,132,v,252,v,88
R v,242,4,33792
M a,140,v,252,v,92
R v,244,1,41984
W 7
v_mfma_f32_16x16x32_bf16 a[144:147], v[224:227], v[32:35], 0
R v,246,4,41984
v_mfma_f32_16x16x32_bf16 a[152:155], v[224:227], v[36:39], 0
R v,248,1,34304
v_mfma_f32_16x16x32_bf16 a[148:151], v[228:231], v[32:35], 0
R v,250,4,34304
v_mfma_f32_16x16x32_bf16 a[156:159], v[228:231], v[36:39], 0
R v,252,1,42496
W 7
M a,144,v,232,v,40
R v,254,4,42496
M a,152,v,232,v,44
R v,224,1,34816
A 30,128
A 31,129
M a,148,v,236,v,40
R v,226,4,34816
v_max3_f32 v26, v30, v31, v26
M a,156,v,236,v,44
R v,228,1,43008
W 7
A 30,130
M a,144,v,240,v,48
R v,230,4,43008
A 31,131
M a,152,v,240,v,52
R v,232,1,35328
v_max3_f32 v26, v30, v31, v26
A 30,132
M a,148,v,244,v,48
R v,234,4,35328
A 31,133
M a,156,v,244,v,52
R v,236,1,43520
W 7
v_max3_f32 v26, v30, v31, v26
M a,144,v,248,v,56
R v,238,4,43520
A 30,134
M a,152,v,248,v,60
R v,240,1,35840
A 31,135
v_max3_f32 v26, v30, v31, v26
M a,148,v,252,v,56
R v,242,4,35840
A 30,136
M a,156,v,252,v,60
R v,244,1,44032
W 7
M a,144,v,224,v,64
R v,246,4,44032
A 31,137
M a,152,v,224,v,68
R v,248,1,36352
v_max3_f32 v27, v30, v31, v27
M a,148,v,228,v,64
R v,250,4,36352
A 30,138
M a,156,v,228,v,68
R v,252,1,44544
W 7
M a,144,v,232,v,72
R v,254,4,44544
A 31,139
M a,152,v,232,v,76
s_barrier
R v,224,1,49152
v_max3_f32 v27, v30, v31, v27
M a,148,v,236,v,72
R v,226,4,49152
A 30,140
M a,156,v,236,v,76
R v,228,1,57344
W 7
M a,144,v,240,v,80
R v,230,4,57344
A 31,141
M a,152,v,240,v,84
R v,232,1,49664
v_max3_f32 v27, v30, v31, v27
M a,148,v,244,v,80
R v,234,4,49664
A 30,142
M a,156,v,244,v,84
R v,236,1,57856
W 7
M a,144,v,248,v,88
R v,238,4,57856
A 31,143
M a,152,v,248,v,92
R v,240,1,50176
v_max3_f32 v27, v30, v31, v27
M a,148,v,252,v,88
R v,242,4,50176
M a,156,v,252,v,92
R v,244,1,58368
W 7
v_mfma_f32_16x16x32_bf16 a[160:163], v[224:227], v[32:35], 0
R v,246,4,58368
v_mfma_f32_16x16x32_bf16 a[168:171], v[224:227], v[36:39], 0
R v,248,1,50688
v_mfma_f32_16x16x32_bf16 a[164:167], v[228:231], v[32:35], 0
R v,250,4,50688
v_mfma_f32_16x16x32_bf16 a[172:175], v[228:231], v[36:39], 0
R v,252,1,58880
W 7
M a,160,v,232,v,40
R v,254,4,58880
M a,168,v,232,v,44
R v,224,1,51200
A 30,144
A 31,145
M a,164,v,236,v,40
R v,226,4,51200
v_max3_f32 v26, v30, v31, v26
M a,172,v,236,v,44
R v,228,1,59392
W 7
A 30,146
M a,160,v,240,v,48
R v,230,4,59392
A 31,147
M a,168,v,240,v,52
R v,232,1,51712
v_max3_f32 v26, v30, v31, v26
A 30,148
M a,164,v,244,v,48
R v,234,4,51712
A 31,149
M a,172,v,244,v,52
R v,236,1,59904
W 7
v_max3_f32 v26, v30, v31, v26
M a,160,v,248,v,56
R v,238,4,59904
A 30,150
M a,168,v,248,v,60
R v,240,1,52224
A 31,151
v_max3_f32 v26, v30, v31, v26
M a,164,v,252,v,56
R v,242,4,52224
A 30,152
M a,172,v,252,v,60
R v,244,1,60416
W 7
M a,160,v,224,v,64
R v,246,4,60416
A 31,153
)x"
R"x(M a,168,v,224,v,68
R v,248,1,52736
v_max3_f32 v27, v30, v31, v27
M a,164,v,228,v,64
R v,250,4,52736
A 30,154
M a,172,v,228,v,68
R v,252,1,60928
W 7
M a,160,v,232,v,72
R v,254,4,60928
A 31,155
M a,168,v,232,v,76
s_barrier
R v,224,2,0
v_max3_f32 v27, v30, v31, v27
M a,164,v,236,v,72
R v,226,5,0
A 30,156
M a,172,v,236,v,76
R v,228,2,8192
W 7
M a,160,v,240,v,80
R v,230,5,8192
A 31,157
M a,168,v,240,v,84
R v,232,2,512
v_max3_f32 v27, v30, v31, v27
M a,164,v,244,v,80
R v,234,5,512
A 30,158
M a,172,v,244,v,84
R v,236,2,8704
W 7
M a,160,v,248,v,88
R v,238,5,8704
A 31,159
M a,168,v,248,v,92
R v,240,2,1024
v_max3_f32 v27, v30, v31, v27
M a,164,v,252,v,88
R v,242,5,1024
M a,172,v,252,v,92
R v,244,2,9216
W 7
v_mfma_f32_16x16x32_bf16 a[176:179], v[224:227], v[32:35], 0
R v,246,5,9216
v_mfma_f32_16x16x32_bf16 a[184:187], v[224:227], v[36:39], 0
R v,248,2,1536
v_mfma_f32_16x16x32_bf16 a[180:183], v[228:231], v[32:35], 0
R v,250,5,1536
v_mfma_f32_16x16x32_bf16 a[188:191], v[228:231], v[36:39], 0
R v,252,2,9728
W 7
M a,176,v,232,v,40
R v,254,5,9728
M a,184,v,232,v,44
R v,224,2,2048
A 30,160
A 31,161
M a,180,v,236,v,40
R v,226,5,2048
v_max3_f32 v26, v30, v31, v26
M a,188,v,236,v,44
R v,228,2,10240
W 7
A 30,162
M a,176,v,240,v,48
R v,230,5,10240
A 31,163
M a,184,v,240,v,52
R v,232,2,2560
v_max3_f32 v26, v30, v31, v26
A 30,164
M a,180,v,244,v,48
R v,234,5,2560
A 31,165
M a,188,v,244,v,52
R v,236,2,10752
W 7
v_max3_f32 v26, v30, v31, v26
M a,176,v,248,v,56
R v,238,5,10752
A 30,166
M a,184,v,248,v,60
R v,240,2,3072
A 31,167
v_max3_f32 v26, v30, v31, v26
M a,180,v,252,v,56
R v,242,5,3072
A 30,168
M a,188,v,252,v,60
R v,244,2,11264
W 7
M a,176,v,224,v,64
R v,246,5,11264
A 31,169
M a,184,v,224,v,68
R v,248,2,3584
v_max3_f32 v27, v30, v31, v27
M a,180,v,228,v,64
R v,250,5,3584
A 30,170
M a,188,v,228,v,68
R v,252,2,11776
W 7
M a,176,v,232,v,72
R v,254,5,11776
A 31,171
M a,184,v,232,v,76
s_barrier
R v,224,2,16384
v_max3_f32 v27, v30, v31, v27
M a,180,v,236,v,72
R v,226,5,16384
A 30,172
M a,188,v,236,v,76
R v,228,2,24576
W 7
M a,176,v,240,v,80
R v,230,5,24576
A 31,173
M a,184,v,240,v,84
R v,232,2,16896
v_max3_f32 v27, v30, v31, v27
M a,180,v,244,v,80
R v,234,5,16896
A 30,174
M a,188,v,244,v,84
R v,236,2,25088
W 7
M a,176,v,248,v,88
R v,238,5,25088
A 31,175
M a,184,v,248,v,92
R v,240,2,17408
v_max3_f32 v27, v30, v31, v27
M a,180,v,252,v,88
R v,242,5,17408
M a,188,v,252,v,92
R v,244,2,25600
W 7
v_mfma_f32_16x16x32_bf16 a[192:195], v[224:227], v[32:35], 0
R v,246,5,25600
v_mfma_f32_16x16x32_bf16 a[200:203], v[224:227], v[36:39], 0
R v,248,2,17920
v_mfma_f32_16x16x32_bf16 a[196:199], v[228:231], v[32:35], 0
R v,250,5,17920
v_mfma_f32_16x16x32_bf16 a[204:207], v[228:231], v[36:39], 0
R v,252,2,26112
W 7
M a,192,v,232,v,40
R v,254,5,26112
M a,200,v,232,v,44
R v,224,2,18432
A 30,176
A 31,177
M a,196,v,236,v,40
R v,226,5,18432
v_max3_f32 v26, v30, v31, v26
M a,204,v,236,v,44
R v,228,2,26624
W 7
A 30,178
M a,192,v,240,v,48
R v,230,5,26624
A 31,179
M a,200,v,240,v,52
R v,232,2,18944
v_max3_f32 v26, v30, v31, v26
A 30,180
M a,196,v,244,v,48
R v,234,5,18944
A 31,181
M a,204,v,244,v,52
R v,236,2,27136
W 7
v_max3_f32 v26, v30, v31, v26
M a,192,v,248,v,56
R v,238,5,27136
A 30,182
M a,200,v,248,v,60
R v,240,2,19456
A 31,183
v_max3_f32 v26, v30, v31, v26
M a,196,v,252,v,56
R v,242,5,19456
A 30,184
M a,204,v,252,v,60
R v,244,2,27648
W 7
M a,192,v,224,v,64
R v,246,5,27648
A 31,185
M a,200,v,224,v,68
R v,248,2,19968
v_max3_f32 v27, v30, v31, v27
M a,196,v,228,v,64
R v,250,5,19968
A 30,186
M a,204,v,228,v,68
R v,252,2,28160
W 7
M a,192,v,232,v,72
R v,254,5,28160
A 31,187
M a,200,v,232,v,76
s_barrier
R v,224,2,32768
v_max3_f32 v27, v30, v31, v27
M a,196,v,236,v,72
R v,226,5,32768
A 30,188
M a,204,v,236,v,76
R v,228,2,40960
W 7
M a,192,v,240,v,80
R v,230,5,40960
A 31,189
M a,200,v,240,v,84
R v,232,2,33280
v_max3_f32 v27, v30, v31, v27
M a,196,v,244,v,80
R v,234,5,33280
A 30,190
M a,204,v,244,v,84
R v,236,2,41472
W 7
M a,192,v,248,v,88
R v,238,5,41472
A 31,191
M a,200,v,248,v,92
R v,240,2,33792
v_max3_f32 v27, v30, v31, v27
M a,196,v,252,v,88
R v,242,5,33792
M a,204,v,252,v,92
R v,244,2,41984
W 7
v_mfma_f32_16x16x32_bf16 a[208:211], v[224:227], v[32:35], 0
R v,246,5,41984
v_mfma_f32_16x16x32_bf16 a[216:219], v[224:227], v[36:39], 0
R v,248,2,34304
v_mfma_f32_16x16x32_bf16 a[212:215], v[228:231], v[32:35], 0
R v,250,5,34304
v_mfma_f32_16x16x32_bf16 a[220:223], v[228:231], v[36:39], 0
R v,252,2,42496
W 7
M a,208,v,232,v,40
R v,254,5,42496
M a,216,v,232,v,44
R v,224,2,34816
A 30,192
A 31,193
M a,212,v,236,v,40
R v,226,5,34816
v_max3_f32 v26, v30, v31, v26
M a,220,v,236,v,44
R v,228,2,43008
W 7
A 30,194
M a,208,v,240,v,48
R v,230,5,43008
A 31,195
M a,216,v,240,v,52
R v,232,2,35328
v_max3_f32 v26, v30, v31, v26
A 30,196
M a,212,v,244,v,48
R v,234,5,35328
A 31,197
M a,220,v,244,v,52
R v,236,2,43520
W 7
v_max3_f32 v26, v30, v31, v26
M a,208,v,248,v,56
R v,238,5,43520
A 30,198
M a,216,v,248,v,60
R v,240,2,35840
A 31,199
v_max3_f32 v26, v30, v31, v26
M a,212,v,252,v,56
R v,242,5,35840
A 30,200
M a,220,v,252,v,60
R v,244,2,44032
W 7
M a,208,v,224,v,64
R v,246,5,44032
A 31,201
M a,216,v,224,v,68
R v,248,2,36352
v_max3_f32 v27, v30, v31, v27
M a,212,v,228,v,64
R v,250,5,36352
A 30,202
M a,220,v,228,v,68
R v,252,2,44544
W 7
M a,208,v,232,v,72
R v,254,5,44544
A 31,203
M a,216,v,232,v,76
s_barrier
R v,224,2,49152
v_max3_f32 v27, v30, v31, v27
M a,212,v,236,v,72
R v,226,5,49152
A 30,204
M a,220,v,236,v,76
R v,228,2,57344
W 7
M a,208,v,240,v,80
R v,230,5,57344
A 31,205
M a,216,v,240,v,84
R v,232,2,49664
v_max3_f32 v27, v30, v31, v27
M a,212,v,244,v,80
R v,234,5,49664
A 30,206
M a,220,v,244,v,84
R v,236,2,57856
W 7
M a,208,v,248,v,88
R v,238,5,57856
A 31,207
M a,216,v,248,v,92
R v,240,2,50176
v_max3_f32 v27, v30, v31, v27
M a,212,v,252,v,88
R v,242,5,50176
M a,220,v,252,v,92
R v,244,2,58368
W 7
v_mfma_f32_16x16x32_bf16 a[224:227], v[224:227], v[32:35], 0
R v,246,5,58368
v_mfma_f32_16x16x32_bf16 a[232:235], v[224:227], v[36:39], 0
R v,248,2,50688
v_mfma_f32_16x16x32_bf16 a[228:231], v[228:231], v[32:35], 0
R v,250,5,50688
v_mfma_f32_16x16x32_bf16 a[236:239], v[228:231], v[36:39], 0
R v,252,2,58880
W 7
M a,224,v,232,v,40
R v,254,5,58880
M a,232,v,232,v,44
R v,224,2,51200
A 30,208
A 31,209
M a,228,v,236,v,40
R v,226,5,51200
v_max3_f32 v26, v30, v31, v26
M a,236,v,236,v,44
R v,228,2,59392
W 7
A 30,210
M a,224,v,240,v,48
R v,230,5,59392
A 31,211
M a,232,v,240,v,52
R v,232,2,51712
v_max3_f32 v26, v30, v31, v26
A 30,212
M a,228,v,244,v,48
R v,234,5,51712
A 31,213
M a,236,v,244,v,52
R v,236,2,59904
W 7
v_max3_f32 v26, v30, v31, v26
M a,224,v,248,v,56
R v,238,5,59904
A 30,214
M a,232,v,248,v,60
R v,240,2,52224
A 31,215
v_max3_f32 v26, v30, v31, v26
M a,228,v,252,v,56
R v,242,5,52224
A 30,216
M a,236,v,252,v,60
R v,244,2,60416
W 7
M a,224,v,224,v,64
R v,246,5,60416
A 31,217
M a,232,v,224,v,68
R v,248,2,52736
v_max3_f32 v27, v30, v31, v27
M a,228,v,228,v,64
R v,250,5,52736
A 30,218
M a,236,v,228,v,68
R v,252,2,60928
W 7
M a,224,v,232,v,72
R v,254,5,60928
A 31,219
)x"
R"x(M a,232,v,232,v,76
s_barrier
.Lc0_106_%=:
R v,224,3,0
v_max3_f32 v27, v30, v31, v27
M a,228,v,236,v,72
R v,226,6,0
A 30,220
M a,236,v,236,v,76
R v,228,3,8192
W 7
M a,224,v,240,v,80
R v,230,6,8192
A 31,221
M a,232,v,240,v,84
R v,232,3,512
v_max3_f32 v27, v30, v31, v27
M a,228,v,244,v,80
R v,234,6,512
A 30,222
M a,236,v,244,v,84
R v,236,3,8704
W 7
M a,224,v,248,v,88
R v,238,6,8704
A 31,223
M a,232,v,248,v,92
R v,240,3,1024
v_max3_f32 v27, v30, v31, v27
M a,228,v,252,v,88
R v,242,6,1024
M a,236,v,252,v,92
R v,244,3,9216
W 7
v_mfma_f32_16x16x32_bf16 a[240:243], v[224:227], v[32:35], 0
R v,246,6,9216
v_mfma_f32_16x16x32_bf16 a[248:251], v[224:227], v[36:39], 0
R v,248,3,1536
v_mfma_f32_16x16x32_bf16 a[244:247], v[228:231], v[32:35], 0
R v,250,6,1536
v_mfma_f32_16x16x32_bf16 a[252:255], v[228:231], v[36:39], 0
R v,252,3,9728
W 7
M a,240,v,232,v,40
R v,254,6,9728
M a,248,v,232,v,44
R v,224,3,2048
A 30,224
A 31,225
M a,244,v,236,v,40
R v,226,6,2048
v_max3_f32 v26, v30, v31, v26
M a,252,v,236,v,44
R v,228,3,10240
W 7
A 30,226
M a,240,v,240,v,48
R v,230,6,10240
A 31,227
M a,248,v,240,v,52
R v,232,3,2560
v_max3_f32 v26, v30, v31, v26
A 30,228
M a,244,v,244,v,48
R v,234,6,2560
A 31,229
M a,252,v,244,v,52
R v,236,3,10752
W 7
v_max3_f32 v26, v30, v31, v26
M a,240,v,248,v,56
R v,238,6,10752
A 30,230
M a,248,v,248,v,60
R v,240,3,3072
A 31,231
v_max3_f32 v26, v30, v31, v26
M a,244,v,252,v,56
R v,242,6,3072
A 30,232
M a,252,v,252,v,60
R v,244,3,11264
W 7
M a,240,v,224,v,64
R v,246,6,11264
A 31,233
M a,248,v,224,v,68
R v,248,3,3584
v_max3_f32 v27, v30, v31, v27
M a,244,v,228,v,64
R v,250,6,3584
A 30,234
M a,252,v,228,v,68
R v,252,3,11776
W 7
M a,240,v,232,v,72
R v,254,6,11776
A 31,235
M a,248,v,232,v,76
v_max3_f32 v27, v30, v31, v27
A 30,236
M a,244,v,236,v,72
A 31,237
v_max3_f32 v27, v30, v31, v27
M a,252,v,236,v,76
W 6
A 30,238
M a,240,v,240,v,80
A 31,239
v_max3_f32 v27, v30, v31, v27
M a,248,v,240,v,84
W 4
M a,244,v,244,v,80
M a,252,v,244,v,84
W 2
M a,240,v,248,v,88
M a,248,v,248,v,92
W 0
M a,244,v,252,v,88
M a,252,v,252,v,92
s_barrier
s_nop 7
v_and_b32 v30, 1, v0
v_lshlrev_b32 v30, 4, v30
v_mov_b32 v31, 0x3f80
v_lshlrev_b32 v31, v30, v31
v_bfe_u32 v30, v0, 2, 2
v_lshrrev_b32 v248, 4, v0
v_xor_b32 v30, v30, v248
v_bfe_u32 v248, v0, 1, 1
v_lshl_add_u32 v30, v30, 1, v248
v_mov_b32 v248, 0
v_mov_b32 v249, 0
v_mov_b32 v252, 0
v_mov_b32 v253, 0
v_cmp_eq_u32 vcc, 0, v30
s_nop 1
v_cndmask_b32 v250, v248, v31, vcc
v_cmp_eq_u32 vcc, 1, v30
s_nop 1
v_cndmask_b32 v251, v248, v31, vcc
A 30,240
A 31,241
v_max3_f32 v26, v30, v31, v26
A 30,242
A 31,243
v_max3_f32 v26, v30, v31, v26
A 30,244
A 31,245
v_max3_f32 v26, v30, v31, v26
A 30,246
A 31,247
v_max3_f32 v26, v30, v31, v26
A 30,248
A 31,249
v_max3_f32 v27, v30, v31, v27
A 30,250
A 31,251
v_max3_f32 v27, v30, v31, v27
A 30,252
A 31,253
v_max3_f32 v27, v30, v31, v27
A 30,254
A 31,255
v_max3_f32 v27, v30, v31, v27
v_mov_b32 v244, 0x3f803f80
v_mov_b32 v245, 0x3f803f80
v_mov_b32 v246, 0x3f803f80
v_mov_b32 v247, 0x3f803f80
v_mov_b32 v30, v26
s_nop 1
v_permlane32_swap_b32 v30, v26
s_nop 1
v_max_f32 v26, v30, v26
s_nop 0
v_mov_b32 v30, v26
s_nop 1
v_permlane16_swap_b32 v30, v26
s_nop 1
v_max_f32 v26, v30, v26
v_mov_b32 v30, v27
s_nop 1
v_permlane32_swap_b32 v30, v27
s_nop 1
v_max_f32 v27, v30, v27
s_nop 0
v_mov_b32 v30, v27
s_nop 1
v_permlane16_swap_b32 v30, v27
s_nop 1
v_max_f32 v27, v30, v27
v_mul_f32 v30, s62, v26
v_sub_f32 v30, 0, v30
v_mov_b32 v31, v30
v_mul_f32 v28, s62, v27
v_sub_f32 v28, 0, v28
v_mov_b32 v29, v28
A 224,0
A 225,1
A 226,2
A 227,3
A 228,4
A 229,5
A 230,6
A 231,7
v_mfma_f32_16x16x32_bf16 a[0:3], v[250:253], v[96:99], 0
v_pk_fma_f32 v[224:225], v[224:225], s[62:63], v[30:31]
v_pk_fma_f32 v[226:227], v[226:227], s[62:63], v[30:31]
v_mfma_f32_16x16x32_bf16 a[4:7], v[248:251], v[96:99], 0
v_pk_fma_f32 v[228:229], v[228:229], s[62:63], v[30:31]
v_pk_fma_f32 v[230:231], v[230:231], s[62:63], v[30:31]
v_exp_f32 v224, v224
v_exp_f32 v225, v225
v_exp_f32 v226, v226
v_exp_f32 v227, v227
v_exp_f32 v228, v228
v_exp_f32 v229, v229
v_exp_f32 v230, v230
v_exp_f32 v231, v231
A 232,8
A 233,9
A 234,10
A 235,11
A 236,12
A 237,13
A 238,14
A 239,15
v_mfma_f32_16x16x32_bf16 a[8:11], v[250:253], v[100:103], 0
v_pk_fma_f32 v[232:233], v[232:233], s[62:63], v[28:29]
v_pk_fma_f32 v[234:235], v[234:235], s[62:63], v[28:29]
v_mfma_f32_16x16x32_bf16 a[12:15], v[248:251], v[100:103], 0
v_pk_fma_f32 v[236:237], v[236:237], s[62:63], v[28:29]
v_pk_fma_f32 v[238:239], v[238:239], s[62:63], v[28:29]
v_cvt_pk_bf16_f32 v32, v224, v225
v_cvt_pk_bf16_f32 v33, v226, v227
v_cvt_pk_bf16_f32 v34, v228, v229
v_cvt_pk_bf16_f32 v35, v230, v231
v_exp_f32 v232, v232
v_exp_f32 v233, v233
v_exp_f32 v234, v234
v_exp_f32 v235, v235
v_mfma_f32_16x16x32_bf16 v[240:243], v[244:247], v[32:35], 0
v_exp_f32 v236, v236
v_exp_f32 v237, v237
v_exp_f32 v238, v238
v_exp_f32 v239, v239
A 224,16
A 225,17
A 226,18
A 227,19
A 228,20
A 229,21
A 230,22
A 231,23
v_mfma_f32_16x16x32_bf16 a[16:19], v[250:253], v[104:107], 0
v_pk_fma_f32 v[224:225], v[224:225], s[62:63], v[30:31]
v_pk_fma_f32 v[226:227], v[226:227], s[62:63], v[30:31]
v_mfma_f32_16x16x32_bf16 a[20:23], v[248:251], v[104:107], 0
v_pk_fma_f32 v[228:229], v[228:229], s[62:63], v[30:31]
v_pk_fma_f32 v[230:231], v[230:231], s[62:63], v[30:31]
v_cvt_pk_bf16_f32 v36, v232, v233
v_cvt_pk_bf16_f32 v37, v234, v235
v_cvt_pk_bf16_f32 v38, v236, v237
v_cvt_pk_bf16_f32 v39, v238, v239
v_exp_f32 v224, v224
v_exp_f32 v225, v225
v_exp_f32 v226, v226
v_exp_f32 v227, v227
v_mfma_f32_16x16x32_bf16 v[208:211], v[244:247], v[36:39], 0
v_exp_f32 v228, v228
v_exp_f32 v229, v229
v_exp_f32 v230, v230
v_exp_f32 v231, v231
A 232,24
A 233,25
A 234,26
A 235,27
A 236,28
A 237,29
A 238,30
A 239,31
v_mfma_f32_16x16x32_bf16 a[24:27], v[250:253], v[108:111], 0
v_pk_fma_f32 v[232:233], v[232:233], s[62:63], v[28:29]
v_pk_fma_f32 v[234:235], v[234:235], s[62:63], v[28:29]
v_mfma_f32_16x16x32_bf16 a[28:31], v[248:251], v[108:111], 0
v_pk_fma_f32 v[236:237], v[236:237], s[62:63], v[28:29]
v_pk_fma_f32 v[238:239], v[238:239], s[62:63], v[28:29]
v_cvt_pk_bf16_f32 v40, v224, v225
v_cvt_pk_bf16_f32 v41, v226, v227
v_cvt_pk_bf16_f32 v42, v228, v229
v_cvt_pk_bf16_f32 v43, v230, v231
v_exp_f32 v232, v232
v_exp_f32 v233, v233
v_exp_f32 v234, v234
v_exp_f32 v235, v235
M v,240,v,244,v,40
v_exp_f32 v236, v236
v_exp_f32 v237, v237
v_exp_f32 v238, v238
v_exp_f32 v239, v239
A 224,32
A 225,33
A 226,34
A 227,35
A 228,36
A 229,37
A 230,38
A 231,39
v_mfma_f32_16x16x32_bf16 a[32:35], v[250:253], v[112:115], 0
v_pk_fma_f32 v[224:225], v[224:225], s[62:63], v[30:31]
v_pk_fma_f32 v[226:227], v[226:227], s[62:63], v[30:31]
v_mfma_f32_16x16x32_bf16 a[36:39], v[248:251], v[112:115], 0
v_pk_fma_f32 v[228:229], v[228:229], s[62:63], v[30:31]
v_pk_fma_f32 v[230:231], v[230:231], s[62:63], v[30:31]
v_cvt_pk_bf16_f32 v44, v232, v233
v_cvt_pk_bf16_f32 v45, v234, v235
v_cvt_pk_bf16_f32 v46, v236, v237
v_cvt_pk_bf16_f32 v47, v238, v239
v_exp_f32 v224, v224
v_exp_f32 v225, v225
v_exp_f32 v226, v226
v_exp_f32 v227, v227
M v,208,v,244,v,44
v_exp_f32 v228, v228
v_exp_f32 v229, v229
v_exp_f32 v230, v230
v_exp_f32 v231, v231
A 232,40
A 233,41
A 234,42
A 235,43
A 236,44
A 237,45
A 238,46
A 239,47
v_mfma_f32_16x16x32_bf16 a[40:43], v[250:253], v[116:119], 0
v_pk_fma_f32 v[232:233], v[232:233], s[62:63], v[28:29]
v_pk_fma_f32 v[234:235], v[234:235], s[62:63], v[28:29]
v_mfma_f32_16x16x32_bf16 a[44:47], v[248:251], v[116:119], 0
v_pk_fma_f32 v[236:237], v[236:237], s[62:63], v[28:29]
v_pk_fma_f32 v[238:239], v[238:239], s[62:63], v[28:29]
v_cvt_pk_bf16_f32 v48, v224, v225
v_cvt_pk_bf16_f32 v49, v226, v227
v_cvt_pk_bf16_f32 v50, v228, v229
v_cvt_pk_bf16_f32 v51, v230, v231
v_exp_f32 v232, v232
v_exp_f32 v233, v233
v_exp_f32 v234, v234
v_exp_f32 v235, v235
M v,240,v,244,v,48
v_exp_f32 v236, v236
v_exp_f32 v237, v237
v_exp_f32 v238, v238
v_exp_f32 v239, v239
A 224,48
A 225,49
A 226,50
A 227,51
A 228,52
A 229,53
A 230,54
A 231,55
v_mfma_f32_16x16x32_bf16 a[48:51], v[250:253], v[120:123], 0
v_pk_fma_f32 v[224:225], v[224:225], s[62:63], v[30:31]
v_pk_fma_f32 v[226:227], v[226:227], s[62:63], v[30:31]
v_mfma_f32_16x16x32_bf16 a[52:55], v[248:251], v[120:123], 0
v_pk_fma_f32 v[228:229], v[228:229], s[62:63], v[30:31]
v_pk_fma_f32 v[230:231], v[230:231], s[62:63], v[30:31]
v_cvt_pk_bf16_f32 v52, v232, v233
v_cvt_pk_bf16_f32 v53, v234, v235
v_cvt_pk_bf16_f32 v54, v236, v237
v_cvt_pk_bf16_f32 v55, v238, v239
v_exp_f32 v224, v224
v_exp_f32 v225, v225
v_exp_f32 v226, v226
v_exp_f32 v227, v227
M v,208,v,244,v,52
v_exp_f32 v228, v228
v_exp_f32 v229, v229
v_exp_f32 v230, v230
v_exp_f32 v231, v231
A 232,56
A 233,57
A 234,58
A 235,59
A 236,60
A 237,61
A 238,62
A 239,63
v_mfma_f32_16x16x32_bf16 a[56:59], v[250:253], v[124:127], 0
v_pk_fma_f32 v[232:233], v[232:233], s[62:63], v[28:29]
v_pk_fma_f32 v[234:235], v[234:235], s[62:63], v[28:29]
v_mfma_f32_16x16x32_bf16 a[60:63], v[248:251], v[124:127], 0
v_pk_fma_f32 v[236:237], v[236:237], s[62:63], v[28:29]
v_pk_fma_f32 v[238:239], v[238:239], s[62:63], v[28:29]
v_cvt_pk_bf16_f32 v56, v224, v225
v_cvt_pk_bf16_f32 v57, v226, v227
v_cvt_pk_bf16_f32 v58, v228, v229
v_cvt_pk_bf16_f32 v59, v230, v231
v_exp_f32 v232, v232
)x"
R"x(v_exp_f32 v233, v233
v_exp_f32 v234, v234
v_exp_f32 v235, v235
M v,240,v,244,v,56
v_exp_f32 v236, v236
v_exp_f32 v237, v237
v_exp_f32 v238, v238
v_exp_f32 v239, v239
A 224,64
A 225,65
A 226,66
A 227,67
A 228,68
A 229,69
A 230,70
A 231,71
v_mfma_f32_16x16x32_bf16 a[64:67], v[250:253], v[128:131], 0
v_pk_fma_f32 v[224:225], v[224:225], s[62:63], v[30:31]
v_pk_fma_f32 v[226:227], v[226:227], s[62:63], v[30:31]
v_mfma_f32_16x16x32_bf16 a[68:71], v[248:251], v[128:131], 0
v_pk_fma_f32 v[228:229], v[228:229], s[62:63], v[30:31]
v_pk_fma_f32 v[230:231], v[230:231], s[62:63], v[30:31]
v_cvt_pk_bf16_f32 v60, v232, v233
v_cvt_pk_bf16_f32 v61, v234, v235
v_cvt_pk_bf16_f32 v62, v236, v237
v_cvt_pk_bf16_f32 v63, v238, v239
v_exp_f32 v224, v224
v_exp_f32 v225, v225
v_exp_f32 v226, v226
v_exp_f32 v227, v227
M v,208,v,244,v,60
v_exp_f32 v228, v228
v_exp_f32 v229, v229
v_exp_f32 v230, v230
v_exp_f32 v231, v231
A 232,72
A 233,73
A 234,74
A 235,75
A 236,76
A 237,77
A 238,78
A 239,79
v_mfma_f32_16x16x32_bf16 a[72:75], v[250:253], v[132:135], 0
v_pk_fma_f32 v[232:233], v[232:233], s[62:63], v[28:29]
v_pk_fma_f32 v[234:235], v[234:235], s[62:63], v[28:29]
v_mfma_f32_16x16x32_bf16 a[76:79], v[248:251], v[132:135], 0
v_pk_fma_f32 v[236:237], v[236:237], s[62:63], v[28:29]
v_pk_fma_f32 v[238:239], v[238:239], s[62:63], v[28:29]
v_cvt_pk_bf16_f32 v64, v224, v225
v_cvt_pk_bf16_f32 v65, v226, v227
v_cvt_pk_bf16_f32 v66, v228, v229
v_cvt_pk_bf16_f32 v67, v230, v231
v_exp_f32 v232, v232
v_exp_f32 v233, v233
v_exp_f32 v234, v234
v_exp_f32 v235, v235
M v,240,v,244,v,64
v_exp_f32 v236, v236
v_exp_f32 v237, v237
v_exp_f32 v238, v238
v_exp_f32 v239, v239
A 224,80
A 225,81
A 226,82
A 227,83
A 228,84
A 229,85
A 230,86
A 231,87
v_mfma_f32_16x16x32_bf16 a[80:83], v[250:253], v[136:139], 0
v_pk_fma_f32 v[224:225], v[224:225], s[62:63], v[30:31]
v_pk_fma_f32 v[226:227], v[226:227], s[62:63], v[30:31]
v_mfma_f32_16x16x32_bf16 a[84:87], v[248:251], v[136:139], 0
v_pk_fma_f32 v[228:229], v[228:229], s[62:63], v[30:31]
v_pk_fma_f32 v[230:231], v[230:231], s[62:63], v[30:31]
v_cvt_pk_bf16_f32 v68, v232, v233
v_cvt_pk_bf16_f32 v69, v234, v235
v_cvt_pk_bf16_f32 v70, v236, v237
v_cvt_pk_bf16_f32 v71, v238, v239
v_exp_f32 v224, v224
v_exp_f32 v225, v225
v_exp_f32 v226, v226
v_exp_f32 v227, v227
M v,208,v,244,v,68
v_exp_f32 v228, v228
v_exp_f32 v229, v229
v_exp_f32 v230, v230
v_exp_f32 v231, v231
A 232,88
A 233,89
A 234,90
A 235,91
A 236,92
A 237,93
A 238,94
A 239,95
v_mfma_f32_16x16x32_bf16 a[88:91], v[250:253], v[140:143], 0
v_pk_fma_f32 v[232:233], v[232:233], s[62:63], v[28:29]
v_pk_fma_f32 v[234:235], v[234:235], s[62:63], v[28:29]
v_mfma_f32_16x16x32_bf16 a[92:95], v[248:251], v[140:143], 0
v_pk_fma_f32 v[236:237], v[236:237], s[62:63], v[28:29]
v_pk_fma_f32 v[238:239], v[238:239], s[62:63], v[28:29]
v_cvt_pk_bf16_f32 v72, v224, v225
v_cvt_pk_bf16_f32 v73, v226, v227
v_cvt_pk_bf16_f32 v74, v228, v229
v_cvt_pk_bf16_f32 v75, v230, v231
v_exp_f32 v232, v232
v_exp_f32 v233, v233
v_exp_f32 v234, v234
v_exp_f32 v235, v235
M v,240,v,244,v,72
v_exp_f32 v236, v236
v_exp_f32 v237, v237
v_exp_f32 v238, v238
v_exp_f32 v239, v239
A 224,96
A 225,97
A 226,98
A 227,99
A 228,100
A 229,101
A 230,102
A 231,103
v_mfma_f32_16x16x32_bf16 a[96:99], v[250:253], v[144:147], 0
v_pk_fma_f32 v[224:225], v[224:225], s[62:63], v[30:31]
v_pk_fma_f32 v[226:227], v[226:227], s[62:63], v[30:31]
v_mfma_f32_16x16x32_bf16 a[100:103], v[248:251], v[144:147], 0
v_pk_fma_f32 v[228:229], v[228:229], s[62:63], v[30:31]
v_pk_fma_f32 v[230:231], v[230:231], s[62:63], v[30:31]
v_cvt_pk_bf16_f32 v76, v232, v233
v_cvt_pk_bf16_f32 v77, v234, v235
v_cvt_pk_bf16_f32 v78, v236, v237
v_cvt_pk_bf16_f32 v79, v238, v239
v_exp_f32 v224, v224
v_exp_f32 v225, v225
v_exp_f32 v226, v226
v_exp_f32 v227, v227
M v,208,v,244,v,76
v_exp_f32 v228, v228
v_exp_f32 v229, v229
v_exp_f32 v230, v230
v_exp_f32 v231, v231
A 232,104
A 233,105
A 234,106
A 235,107
A 236,108
A 237,109
A 238,110
A 239,111
v_mfma_f32_16x16x32_bf16 a[104:107], v[250:253], v[148:151], 0
v_pk_fma_f32 v[232:233], v[232:233], s[62:63], v[28:29]
v_pk_fma_f32 v[234:235], v[234:235], s[62:63], v[28:29]
v_mfma_f32_16x16x32_bf16 a[108:111], v[248:251], v[148:151], 0
v_pk_fma_f32 v[236:237], v[236:237], s[62:63], v[28:29]
v_pk_fma_f32 v[238:239], v[238:239], s[62:63], v[28:29]
v_cvt_pk_bf16_f32 v80, v224, v225
v_cvt_pk_bf16_f32 v81, v226, v227
v_cvt_pk_bf16_f32 v82, v228, v229
v_cvt_pk_bf16_f32 v83, v230, v231
v_exp_f32 v232, v232
v_exp_f32 v233, v233
v_exp_f32 v234, v234
v_exp_f32 v235, v235
M v,240,v,244,v,80
v_exp_f32 v236, v236
v_exp_f32 v237, v237
v_exp_f32 v238, v238
v_exp_f32 v239, v239
A 224,112
A 225,113
A 226,114
A 227,115
A 228,116
A 229,117
A 230,118
A 231,119
v_mfma_f32_16x16x32_bf16 a[112:115], v[250:253], v[152:155], 0
v_pk_fma_f32 v[224:225], v[224:225], s[62:63], v[30:31]
v_pk_fma_f32 v[226:227], v[226:227], s[62:63], v[30:31]
v_mfma_f32_16x16x32_bf16 a[116:119], v[248:251], v[152:155], 0
v_pk_fma_f32 v[228:229], v[228:229], s[62:63], v[30:31]
v_pk_fma_f32 v[230:231], v[230:231], s[62:63], v[30:31]
v_cvt_pk_bf16_f32 v84, v232, v233
v_cvt_pk_bf16_f32 v85, v234, v235
v_cvt_pk_bf16_f32 v86, v236, v237
v_cvt_pk_bf16_f32 v87, v238, v239
v_exp_f32 v224, v224
v_exp_f32 v225, v225
v_exp_f32 v226, v226
v_exp_f32 v227, v227
M v,208,v,244,v,84
v_exp_f32 v228, v228
v_exp_f32 v229, v229
v_exp_f32 v230, v230
v_exp_f32 v231, v231
A 232,120
A 233,121
A 234,122
A 235,123
A 236,124
A 237,125
A 238,126
A 239,127
v_mfma_f32_16x16x32_bf16 a[120:123], v[250:253], v[156:159], 0
v_pk_fma_f32 v[232:233], v[232:233], s[62:63], v[28:29]
v_pk_fma_f32 v[234:235], v[234:235], s[62:63], v[28:29]
v_mfma_f32_16x16x32_bf16 a[124:127], v[248:251], v[156:159], 0
v_pk_fma_f32 v[236:237], v[236:237], s[62:63], v[28:29]
v_pk_fma_f32 v[238:239], v[238:239], s[62:63], v[28:29]
v_cvt_pk_bf16_f32 v88, v224, v225
v_cvt_pk_bf16_f32 v89, v226, v227
v_cvt_pk_bf16_f32 v90, v228, v229
v_cvt_pk_bf16_f32 v91, v230, v231
v_exp_f32 v232, v232
v_exp_f32 v233, v233
v_exp_f32 v234, v234
v_exp_f32 v235, v235
M v,240,v,244,v,88
v_exp_f32 v236, v236
v_exp_f32 v237, v237
v_exp_f32 v238, v238
v_exp_f32 v239, v239
A 224,128
A 225,129
A 226,130
A 227,131
A 228,132
A 229,133
A 230,134
A 231,135
v_mfma_f32_16x16x32_bf16 a[128:131], v[250:253], v[160:163], 0
v_pk_fma_f32 v[224:225], v[224:225], s[62:63], v[30:31]
v_pk_fma_f32 v[226:227], v[226:227], s[62:63], v[30:31]
v_mfma_f32_16x16x32_bf16 a[132:135], v[248:251], v[160:163], 0
v_pk_fma_f32 v[228:229], v[228:229], s[62:63], v[30:31]
v_pk_fma_f32 v[230:231], v[230:231], s[62:63], v[30:31]
v_cvt_pk_bf16_f32 v92, v232, v233
v_cvt_pk_bf16_f32 v93, v234, v235
v_cvt_pk_bf16_f32 v94, v236, v237
v_cvt_pk_bf16_f32 v95, v238, v239
v_exp_f32 v224, v224
v_exp_f32 v225, v225
v_exp_f32 v226, v226
v_exp_f32 v227, v227
M v,208,v,244,v,92
v_exp_f32 v228, v228
v_exp_f32 v229, v229
v_exp_f32 v230, v230
v_exp_f32 v231, v231
A 232,136
A 233,137
A 234,138
A 235,139
A 236,140
A 237,141
A 238,142
A 239,143
v_mfma_f32_16x16x32_bf16 a[136:139], v[250:253], v[164:167], 0
v_pk_fma_f32 v[232:233], v[232:233], s[62:63], v[28:29]
v_pk_fma_f32 v[234:235], v[234:235], s[62:63], v[28:29]
v_mfma_f32_16x16x32_bf16 a[140:143], v[248:251], v[164:167], 0
v_pk_fma_f32 v[236:237], v[236:237], s[62:63], v[28:29]
v_pk_fma_f32 v[238:239], v[238:239], s[62:63], v[28:29]
v_cvt_pk_bf16_f32 v96, v224, v225
v_cvt_pk_bf16_f32 v97, v226, v227
v_cvt_pk_bf16_f32 v98, v228, v229
v_cvt_pk_bf16_f32 v99, v230, v231
v_exp_f32 v232, v232
v_exp_f32 v233, v233
v_exp_f32 v234, v234
v_exp_f32 v235, v235
M v,240,v,244,v,96
v_exp_f32 v236, v236
v_exp_f32 v237, v237
v_exp_f32 v238, v238
v_exp_f32 v239, v239
A 224,144
A 225,145
A 226,146
A 227,147
A 228,148
A 229,149
A 230,150
A 231,151
v_mfma_f32_16x16x32_bf16 a[144:147], v[250:253], v[168:171], 0
v_pk_fma_f32 v[224:225], v[224:225], s[62:63], v[30:31]
v_pk_fma_f32 v[226:227], v[226:227], s[62:63], v[30:31]
v_mfma_f32_16x16x32_bf16 a[148:151], v[248:251], v[168:171], 0
v_pk_fma_f32 v[228:229], v[228:229], s[62:63], v[30:31]
v_pk_fma_f32 v[230:231], v[230:231], s[62:63], v[30:31]
v_cvt_pk_bf16_f32 v100, v232, v233
v_cvt_pk_bf16_f32 v101, v234, v235
v_cvt_pk_bf16_f32 v102, v236, v237
v_cvt_pk_bf16_f32 v103, v238, v239
v_exp_f32 v224, v224
v_exp_f32 v225, v225
v_exp_f32 v226, v226
v_exp_f32 v227, v227
M v,208,v,244,v,100
v_exp_f32 v228, v228
v_exp_f32 v229, v229
v_exp_f32 v230, v230
v_exp_f32 v231, v231
A 232,152
A 233,153
A 234,154
A 235,155
A 236,156
A 237,157
A 238,158
A 239,159
v_mfma_f32_16x16x32_bf16 a[152:155], v[250:253], v[172:175], 0
v_pk_fma_f32 v[232:233], v[232:233], s[62:63], v[28:29]
v_pk_fma_f32 v[234:235], v[234:235], s[62:63], v[28:29]
v_mfma_f32_16x16x32_bf16 a[156:159], v[248:251], v[172:175], 0
v_pk_fma_f32 v[236:237], v[236:237], s[62:63], v[28:29]
v_pk_fma_f32 v[238:239], v[238:239], s[62:63], v[28:29]
v_cvt_pk_bf16_f32 v104, v224, v225
v_cvt_pk_bf16_f32 v105, v226, v227
v_cvt_pk_bf16_f32 v106, v228, v229
v_cvt_pk_bf16_f32 v107, v230, v231
v_exp_f32 v232, v232
v_exp_f32 v233, v233
v_exp_f32 v234, v234
v_exp_f32 v235, v235
M v,240,v,244,v,104
v_exp_f32 v236, v236
v_exp_f32 v237, v237
v_exp_f32 v238, v238
v_exp_f32 v239, v239
A 224,160
A 225,161
A 226,162
A 227,163
A 228,164
A 229,165
A 230,166
A 231,167
v_mfma_f32_16x16x32_bf16 a[160:163], v[250:253], v[176:179], 0
v_pk_fma_f32 v[224:225], v[224:225], s[62:63], v[30:31]
v_pk_fma_f32 v[226:227], v[226:227], s[62:63], v[30:31]
v_mfma_f32_16x16x32_bf16 a[164:167], v[248:251], v[176:179], 0
v_pk_fma_f32 v[228:229], v[228:229], s[62:63], v[30:31]
v_pk_fma_f32 v[230:231], v[230:231], s[62:63], v[30:31]
v_cvt_pk_bf16_f32 v108, v232, v233
v_cvt_pk_bf16_f32 v109, v234, v235
v_cvt_pk_bf16_f32 v110, v236, v237
v_cvt_pk_bf16_f32 v111, v238, v239
v_exp_f32 v224, v224
v_exp_f32 v225, v225
v_exp_f32 v226, v226
v_exp_f32 v227, v227
M v,208,v,244,v,108
v_exp_f32 v228, v228
v_exp_f32 v229, v229
v_exp_f32 v230, v230
v_exp_f32 v231, v231
A 232,168
A 233,169
A 234,170
A 235,171
A 236,172
A 237,173
A 238,174
A 239,175
v_mfma_f32_16x16x32_bf16 a[168:171], v[250:253], v[180:183], 0
v_pk_fma_f32 v[232:233], v[232:233], s[62:63], v[28:29]
v_pk_fma_f32 v[234:235], v[234:235], s[62:63], v[28:29]
v_mfma_f32_16x16x32_bf16 a[172:175], v[248:251], v[180:183], 0
v_pk_fma_f32 v[236:237], v[236:237], s[62:63], v[28:29]
v_pk_fma_f32 v[238:239], v[238:239], s[62:63], v[28:29]
v_cvt_pk_bf16_f32 v112, v224, v225
v_cvt_pk_bf16_f32 v113, v226, v227
v_cvt_pk_bf16_f32 v114, v228, v229
v_cvt_pk_bf16_f32 v115, v230, v231
v_exp_f32 v232, v232
v_exp_f32 v233, v233
v_exp_f32 v234, v234
v_exp_f32 v235, v235
M v,240,v,244,v,112
v_exp_f32 v236, v236
v_exp_f32 v237, v237
v_exp_f32 v238, v238
v_exp_f32 v239, v239
A 224,176
A 225,177
A 226,178
A 227,179
A 228,180
A 229,181
A 230,182
A 231,183
v_mfma_f32_16x16x32_bf16 a[176:179], v[250:253], v[184:187], 0
v_pk_fma_f32 v[224:225], v[224:225], s[62:63], v[30:31]
v_pk_fma_f32 v[226:227], v[226:227], s[62:63], v[30:31]
v_mfma_f32_16x16x32_bf16 a[180:183], v[248:251], v[184:187], 0
v_pk_fma_f32 v[228:229], v[228:229], s[62:63], v[30:31]
v_pk_fma_f32 v[230:231], v[230:231], s[62:63], v[30:31]
)x"
R"x(v_cvt_pk_bf16_f32 v116, v232, v233
v_cvt_pk_bf16_f32 v117, v234, v235
v_cvt_pk_bf16_f32 v118, v236, v237
v_cvt_pk_bf16_f32 v119, v238, v239
v_exp_f32 v224, v224
v_exp_f32 v225, v225
v_exp_f32 v226, v226
v_exp_f32 v227, v227
M v,208,v,244,v,116
v_exp_f32 v228, v228
v_exp_f32 v229, v229
v_exp_f32 v230, v230
v_exp_f32 v231, v231
A 232,184
A 233,185
A 234,186
A 235,187
A 236,188
A 237,189
A 238,190
A 239,191
v_mfma_f32_16x16x32_bf16 a[184:187], v[250:253], v[188:191], 0
v_pk_fma_f32 v[232:233], v[232:233], s[62:63], v[28:29]
v_pk_fma_f32 v[234:235], v[234:235], s[62:63], v[28:29]
v_mfma_f32_16x16x32_bf16 a[188:191], v[248:251], v[188:191], 0
v_pk_fma_f32 v[236:237], v[236:237], s[62:63], v[28:29]
v_pk_fma_f32 v[238:239], v[238:239], s[62:63], v[28:29]
v_cvt_pk_bf16_f32 v120, v224, v225
v_cvt_pk_bf16_f32 v121, v226, v227
v_cvt_pk_bf16_f32 v122, v228, v229
v_cvt_pk_bf16_f32 v123, v230, v231
v_exp_f32 v232, v232
v_exp_f32 v233, v233
v_exp_f32 v234, v234
v_exp_f32 v235, v235
M v,240,v,244,v,120
v_exp_f32 v236, v236
v_exp_f32 v237, v237
v_exp_f32 v238, v238
v_exp_f32 v239, v239
A 224,192
A 225,193
A 226,194
A 227,195
A 228,196
A 229,197
A 230,198
A 231,199
v_mfma_f32_16x16x32_bf16 a[192:195], v[250:253], v[192:195], 0
v_pk_fma_f32 v[224:225], v[224:225], s[62:63], v[30:31]
v_pk_fma_f32 v[226:227], v[226:227], s[62:63], v[30:31]
v_mfma_f32_16x16x32_bf16 a[196:199], v[248:251], v[192:195], 0
v_pk_fma_f32 v[228:229], v[228:229], s[62:63], v[30:31]
v_pk_fma_f32 v[230:231], v[230:231], s[62:63], v[30:31]
v_cvt_pk_bf16_f32 v124, v232, v233
v_cvt_pk_bf16_f32 v125, v234, v235
v_cvt_pk_bf16_f32 v126, v236, v237
v_cvt_pk_bf16_f32 v127, v238, v239
v_exp_f32 v224, v224
v_exp_f32 v225, v225
v_exp_f32 v226, v226
v_exp_f32 v227, v227
M v,208,v,244,v,124
v_exp_f32 v228, v228
v_exp_f32 v229, v229
v_exp_f32 v230, v230
v_exp_f32 v231, v231
A 232,200
A 233,201
A 234,202
A 235,203
A 236,204
A 237,205
A 238,206
A 239,207
v_mfma_f32_16x16x32_bf16 a[200:203], v[250:253], v[196:199], 0
v_pk_fma_f32 v[232:233], v[232:233], s[62:63], v[28:29]
v_pk_fma_f32 v[234:235], v[234:235], s[62:63], v[28:29]
v_mfma_f32_16x16x32_bf16 a[204:207], v[248:251], v[196:199], 0
v_pk_fma_f32 v[236:237], v[236:237], s[62:63], v[28:29]
v_pk_fma_f32 v[238:239], v[238:239], s[62:63], v[28:29]
v_cvt_pk_bf16_f32 v128, v224, v225
v_cvt_pk_bf16_f32 v129, v226, v227
v_cvt_pk_bf16_f32 v130, v228, v229
v_cvt_pk_bf16_f32 v131, v230, v231
v_exp_f32 v232, v232
v_exp_f32 v233, v233
v_exp_f32 v234, v234
v_exp_f32 v235, v235
M v,240,v,244,v,128
v_exp_f32 v236, v236
v_exp_f32 v237, v237
v_exp_f32 v238, v238
v_exp_f32 v239, v239
A 224,208
A 225,209
A 226,210
A 227,211
A 228,212
A 229,213
A 230,214
A 231,215
v_mfma_f32_16x16x32_bf16 a[208:211], v[250:253], v[200:203], 0
v_pk_fma_f32 v[224:225], v[224:225], s[62:63], v[30:31]
v_pk_fma_f32 v[226:227], v[226:227], s[62:63], v[30:31]
v_mfma_f32_16x16x32_bf16 a[212:215], v[248:251], v[200:203], 0
v_pk_fma_f32 v[228:229], v[228:229], s[62:63], v[30:31]
v_pk_fma_f32 v[230:231], v[230:231], s[62:63], v[30:31]
v_cvt_pk_bf16_f32 v132, v232, v233
v_cvt_pk_bf16_f32 v133, v234, v235
v_cvt_pk_bf16_f32 v134, v236, v237
v_cvt_pk_bf16_f32 v135, v238, v239
v_exp_f32 v224, v224
v_exp_f32 v225, v225
v_exp_f32 v226, v226
v_exp_f32 v227, v227
M v,208,v,244,v,132
v_exp_f32 v228, v228
v_exp_f32 v229, v229
v_exp_f32 v230, v230
v_exp_f32 v231, v231
A 232,216
A 233,217
A 234,218
A 235,219
A 236,220
A 237,221
A 238,222
A 239,223
v_mfma_f32_16x16x32_bf16 a[216:219], v[250:253], v[204:207], 0
v_pk_fma_f32 v[232:233], v[232:233], s[62:63], v[28:29]
v_pk_fma_f32 v[234:235], v[234:235], s[62:63], v[28:29]
v_mfma_f32_16x16x32_bf16 a[220:223], v[248:251], v[204:207], 0
v_pk_fma_f32 v[236:237], v[236:237], s[62:63], v[28:29]
v_pk_fma_f32 v[238:239], v[238:239], s[62:63], v[28:29]
v_cvt_pk_bf16_f32 v136, v224, v225
v_cvt_pk_bf16_f32 v137, v226, v227
v_cvt_pk_bf16_f32 v138, v228, v229
v_cvt_pk_bf16_f32 v139, v230, v231
v_exp_f32 v232, v232
v_exp_f32 v233, v233
v_exp_f32 v234, v234
v_exp_f32 v235, v235
M v,240,v,244,v,136
v_exp_f32 v236, v236
v_exp_f32 v237, v237
v_exp_f32 v238, v238
v_exp_f32 v239, v239
A 224,224
A 225,225
A 226,226
A 227,227
A 228,228
A 229,229
A 230,230
A 231,231
v_pk_fma_f32 v[224:225], v[224:225], s[62:63], v[30:31]
v_pk_fma_f32 v[226:227], v[226:227], s[62:63], v[30:31]
v_pk_fma_f32 v[228:229], v[228:229], s[62:63], v[30:31]
v_pk_fma_f32 v[230:231], v[230:231], s[62:63], v[30:31]
v_cvt_pk_bf16_f32 v140, v232, v233
v_cvt_pk_bf16_f32 v141, v234, v235
v_cvt_pk_bf16_f32 v142, v236, v237
v_cvt_pk_bf16_f32 v143, v238, v239
v_exp_f32 v224, v224
v_exp_f32 v225, v225
v_exp_f32 v226, v226
v_exp_f32 v227, v227
M v,208,v,244,v,140
v_exp_f32 v228, v228
v_exp_f32 v229, v229
v_exp_f32 v230, v230
v_exp_f32 v231, v231
A 232,232
A 233,233
A 234,234
A 235,235
A 236,236
A 237,237
A 238,238
A 239,239
v_pk_fma_f32 v[232:233], v[232:233], s[62:63], v[28:29]
v_pk_fma_f32 v[234:235], v[234:235], s[62:63], v[28:29]
v_pk_fma_f32 v[236:237], v[236:237], s[62:63], v[28:29]
v_pk_fma_f32 v[238:239], v[238:239], s[62:63], v[28:29]
v_cvt_pk_bf16_f32 v144, v224, v225
v_cvt_pk_bf16_f32 v145, v226, v227
v_cvt_pk_bf16_f32 v146, v228, v229
v_cvt_pk_bf16_f32 v147, v230, v231
v_exp_f32 v232, v232
v_exp_f32 v233, v233
v_exp_f32 v234, v234
v_exp_f32 v235, v235
M v,240,v,244,v,144
v_exp_f32 v236, v236
v_exp_f32 v237, v237
v_exp_f32 v238, v238
v_exp_f32 v239, v239
A 224,240
A 225,241
A 226,242
A 227,243
A 228,244
A 229,245
A 230,246
A 231,247
v_pk_fma_f32 v[224:225], v[224:225], s[62:63], v[30:31]
v_pk_fma_f32 v[226:227], v[226:227], s[62:63], v[30:31]
v_pk_fma_f32 v[228:229], v[228:229], s[62:63], v[30:31]
v_pk_fma_f32 v[230:231], v[230:231], s[62:63], v[30:31]
v_cvt_pk_bf16_f32 v148, v232, v233
v_cvt_pk_bf16_f32 v149, v234, v235
v_cvt_pk_bf16_f32 v150, v236, v237
v_cvt_pk_bf16_f32 v151, v238, v239
v_exp_f32 v224, v224
v_exp_f32 v225, v225
v_exp_f32 v226, v226
v_exp_f32 v227, v227
M v,208,v,244,v,148
v_exp_f32 v228, v228
v_exp_f32 v229, v229
v_exp_f32 v230, v230
v_exp_f32 v231, v231
A 232,248
A 233,249
A 234,250
A 235,251
A 236,252
A 237,253
A 238,254
A 239,255
v_pk_fma_f32 v[232:233], v[232:233], s[62:63], v[28:29]
v_pk_fma_f32 v[234:235], v[234:235], s[62:63], v[28:29]
v_pk_fma_f32 v[236:237], v[236:237], s[62:63], v[28:29]
v_pk_fma_f32 v[238:239], v[238:239], s[62:63], v[28:29]
v_cvt_pk_bf16_f32 v152, v224, v225
v_cvt_pk_bf16_f32 v153, v226, v227
v_cvt_pk_bf16_f32 v154, v228, v229
v_cvt_pk_bf16_f32 v155, v230, v231
v_exp_f32 v232, v232
v_exp_f32 v233, v233
v_exp_f32 v234, v234
v_exp_f32 v235, v235
M v,240,v,244,v,152
v_exp_f32 v236, v236
v_exp_f32 v237, v237
v_exp_f32 v238, v238
v_exp_f32 v239, v239
s_nop 0
v_cvt_pk_bf16_f32 v156, v232, v233
v_cvt_pk_bf16_f32 v157, v234, v235
v_cvt_pk_bf16_f32 v158, v236, v237
v_cvt_pk_bf16_f32 v159, v238, v239
s_nop 1
M v,208,v,244,v,156
s_nop 7
v_rcp_f32 v28, v240
v_rcp_f32 v29, v208
s_nop 1
s_barrier
T a,224,15,16384
T a,226,15,18432
T a,228,18,16384
T a,230,18,18432
T a,232,15,16896
T a,234,15,18944
T a,236,18,16896
T a,238,18,18944
T a,240,15,20480
T a,242,15,22528
T a,244,18,20480
W 7
v_mfma_f32_16x16x32_bf16 v[224:227], a[224:227], v[32:35], 0
T a,246,18,22528
v_mfma_f32_16x16x32_bf16 v[232:235], a[224:227], v[36:39], 0
T a,248,15,20992
v_mfma_f32_16x16x32_bf16 v[228:231], a[228:231], v[32:35], 0
T a,250,15,23040
v_mfma_f32_16x16x32_bf16 v[236:239], a[228:231], v[36:39], 0
T a,252,18,20992
W 7
v_mfma_f32_16x16x32_bf16 v[240:243], a[232:235], v[32:35], 0
T a,254,18,23040
v_mfma_f32_16x16x32_bf16 v[248:251], a[232:235], v[36:39], 0
T a,224,15,24576
v_mfma_f32_16x16x32_bf16 v[244:247], a[236:239], v[32:35], 0
T a,226,15,26624
v_mfma_f32_16x16x32_bf16 v[252:255], a[236:239], v[36:39], 0
T a,228,18,24576
W 7
M v,224,a,240,v,40
T a,230,18,26624
M v,232,a,240,v,44
T a,232,15,25088
M v,228,a,244,v,40
T a,234,15,27136
M v,236,a,244,v,44
T a,236,18,25088
W 7
M v,240,a,248,v,40
T a,238,18,27136
M v,248,a,248,v,44
T a,240,15,28672
M v,244,a,252,v,40
T a,242,15,30720
M v,252,a,252,v,44
T a,244,18,28672
W 7
M v,224,a,224,v,48
T a,246,18,30720
M v,232,a,224,v,52
T a,248,15,29184
M v,228,a,228,v,48
T a,250,15,31232
M v,236,a,228,v,52
T a,252,18,29184
W 7
M v,240,a,232,v,48
T a,254,18,31232
M v,248,a,232,v,52
s_barrier
T a,224,13,0
M v,244,a,236,v,48
T a,226,13,2048
M v,252,a,236,v,52
T a,228,16,0
W 7
M v,224,a,240,v,56
T a,230,16,2048
M v,232,a,240,v,60
T a,232,13,512
M v,228,a,244,v,56
T a,234,13,2560
M v,236,a,244,v,60
T a,236,16,512
W 7
M v,240,a,248,v,56
T a,238,16,2560
M v,248,a,248,v,60
T a,240,13,4096
M v,244,a,252,v,56
T a,242,13,6144
M v,252,a,252,v,60
T a,244,16,4096
W 7
M v,224,a,224,v,64
T a,246,16,6144
M v,232,a,224,v,68
T a,248,13,4608
M v,228,a,228,v,64
T a,250,13,6656
M v,236,a,228,v,68
T a,252,16,4608
W 7
M v,240,a,232,v,64
T a,254,16,6656
M v,248,a,232,v,68
T a,224,13,8192
M v,244,a,236,v,64
T a,226,13,10240
M v,252,a,236,v,68
T a,228,16,8192
W 7
M v,224,a,240,v,72
T a,230,16,10240
M v,232,a,240,v,76
T a,232,13,8704
M v,228,a,244,v,72
T a,234,13,10752
M v,236,a,244,v,76
T a,236,16,8704
W 7
M v,240,a,248,v,72
T a,238,16,10752
M v,248,a,248,v,76
T a,240,13,12288
M v,244,a,252,v,72
T a,242,13,14336
M v,252,a,252,v,76
T a,244,16,12288
W 7
M v,224,a,224,v,80
T a,246,16,14336
M v,232,a,224,v,84
T a,248,13,12800
M v,228,a,228,v,80
T a,250,13,14848
M v,236,a,228,v,84
T a,252,16,12800
W 7
M v,240,a,232,v,80
T a,254,16,14848
M v,248,a,232,v,84
s_barrier
T a,224,13,16384
M v,244,a,236,v,80
T a,226,13,18432
M v,252,a,236,v,84
T a,228,16,16384
W 7
)x"
R"x(M v,224,a,240,v,88
T a,230,16,18432
M v,232,a,240,v,92
T a,232,13,16896
M v,228,a,244,v,88
T a,234,13,18944
M v,236,a,244,v,92
T a,236,16,16896
W 7
M v,240,a,248,v,88
T a,238,16,18944
M v,248,a,248,v,92
T a,240,13,20480
M v,244,a,252,v,88
T a,242,13,22528
M v,252,a,252,v,92
T a,244,16,20480
W 7
M v,224,a,224,v,96
T a,246,16,22528
M v,232,a,224,v,100
T a,248,13,20992
M v,228,a,228,v,96
T a,250,13,23040
M v,236,a,228,v,100
T a,252,16,20992
W 7
M v,240,a,232,v,96
T a,254,16,23040
M v,248,a,232,v,100
T a,224,13,24576
M v,244,a,236,v,96
T a,226,13,26624
M v,252,a,236,v,100
T a,228,16,24576
W 7
M v,224,a,240,v,104
T a,230,16,26624
M v,232,a,240,v,108
T a,232,13,25088
M v,228,a,244,v,104
T a,234,13,27136
M v,236,a,244,v,108
T a,236,16,25088
W 7
M v,240,a,248,v,104
T a,238,16,27136
M v,248,a,248,v,108
T a,240,13,28672
M v,244,a,252,v,104
T a,242,13,30720
M v,252,a,252,v,108
T a,244,16,28672
W 7
M v,224,a,224,v,112
T a,246,16,30720
M v,232,a,224,v,116
T a,248,13,29184
M v,228,a,228,v,112
T a,250,13,31232
M v,236,a,228,v,116
T a,252,16,29184
W 7
M v,240,a,232,v,112
T a,254,16,31232
M v,248,a,232,v,116
s_barrier
T a,224,13,32768
M v,244,a,236,v,112
T a,226,13,34816
M v,252,a,236,v,116
T a,228,16,32768
W 7
M v,224,a,240,v,120
T a,230,16,34816
M v,232,a,240,v,124
T a,232,13,33280
M v,228,a,244,v,120
T a,234,13,35328
M v,236,a,244,v,124
T a,236,16,33280
W 7
M v,240,a,248,v,120
T a,238,16,35328
M v,248,a,248,v,124
T a,240,13,36864
M v,244,a,252,v,120
T a,242,13,38912
M v,252,a,252,v,124
T a,244,16,36864
W 7
M v,224,a,224,v,128
T a,246,16,38912
M v,232,a,224,v,132
T a,248,13,37376
M v,228,a,228,v,128
T a,250,13,39424
M v,236,a,228,v,132
T a,252,16,37376
W 7
M v,240,a,232,v,128
T a,254,16,39424
M v,248,a,232,v,132
T a,224,13,40960
M v,244,a,236,v,128
T a,226,13,43008
M v,252,a,236,v,132
T a,228,16,40960
W 7
M v,224,a,240,v,136
T a,230,16,43008
M v,232,a,240,v,140
T a,232,13,41472
M v,228,a,244,v,136
T a,234,13,43520
M v,236,a,244,v,140
T a,236,16,41472
W 7
M v,240,a,248,v,136
T a,238,16,43520
M v,248,a,248,v,140
T a,240,13,45056
M v,244,a,252,v,136
T a,242,13,47104
M v,252,a,252,v,140
T a,244,16,45056
W 7
M v,224,a,224,v,144
T a,246,16,47104
M v,232,a,224,v,148
T a,248,13,45568
M v,228,a,228,v,144
T a,250,13,47616
M v,236,a,228,v,148
T a,252,16,45568
W 7
M v,240,a,232,v,144
T a,254,16,47616
M v,248,a,232,v,148
s_barrier
T a,224,7,49152
M v,244,a,236,v,144
T a,226,7,57344
M v,252,a,236,v,148
T a,228,10,49152
W 7
M v,224,a,240,v,152
T a,230,10,57344
M v,232,a,240,v,156
T a,232,7,49664
M v,228,a,244,v,152
T a,234,7,57856
M v,236,a,244,v,156
T a,236,10,49664
W 7
M v,240,a,248,v,152
T a,238,10,57856
M v,248,a,248,v,156
T a,240,7,50176
M v,244,a,252,v,152
T a,242,7,58368
M v,252,a,252,v,156
s_nop 7
v_mul_f32 v224, v28, v224
v_mul_f32 v225, v28, v225
v_mul_f32 v226, v28, v226
v_mul_f32 v227, v28, v227
v_mul_f32 v228, v28, v228
v_mul_f32 v229, v28, v229
v_mul_f32 v230, v28, v230
v_mul_f32 v231, v28, v231
v_mul_f32 v232, v29, v232
v_mul_f32 v233, v29, v233
v_mul_f32 v234, v29, v234
v_mul_f32 v235, v29, v235
v_mul_f32 v236, v29, v236
v_mul_f32 v237, v29, v237
v_mul_f32 v238, v29, v238
v_mul_f32 v239, v29, v239
v_cvt_pk_bf16_f32 v224, v224, v225
v_cvt_pk_bf16_f32 v225, v226, v227
v_cvt_pk_bf16_f32 v226, v228, v229
v_cvt_pk_bf16_f32 v227, v230, v231
v_cvt_pk_bf16_f32 v228, v232, v233
v_cvt_pk_bf16_f32 v229, v234, v235
v_cvt_pk_bf16_f32 v230, v236, v237
v_cvt_pk_bf16_f32 v231, v238, v239
v_mul_f32 v240, v28, v240
v_mul_f32 v241, v28, v241
v_mul_f32 v242, v28, v242
v_mul_f32 v243, v28, v243
v_mul_f32 v244, v28, v244
v_mul_f32 v245, v28, v245
v_mul_f32 v246, v28, v246
v_mul_f32 v247, v28, v247
v_mul_f32 v248, v29, v248
v_mul_f32 v249, v29, v249
v_mul_f32 v250, v29, v250
v_mul_f32 v251, v29, v251
v_mul_f32 v252, v29, v252
v_mul_f32 v253, v29, v253
v_mul_f32 v254, v29, v254
v_mul_f32 v255, v29, v255
v_cvt_pk_bf16_f32 v240, v240, v241
v_cvt_pk_bf16_f32 v241, v242, v243
v_cvt_pk_bf16_f32 v242, v244, v245
v_cvt_pk_bf16_f32 v243, v246, v247
v_cvt_pk_bf16_f32 v244, v248, v249
v_cvt_pk_bf16_f32 v245, v250, v251
v_cvt_pk_bf16_f32 v246, v252, v253
v_cvt_pk_bf16_f32 v247, v254, v255
s_nop 1
T a,244,10,50176
W 7
M a,0,a,224,v,224
T a,246,10,58368
M a,8,a,224,v,228
T a,248,7,50688
M a,4,a,228,v,224
T a,250,7,58880
M a,12,a,228,v,228
T a,252,10,50688
W 7
M a,16,a,232,v,224
T a,254,10,58880
M a,24,a,232,v,228
T a,224,7,51200
M a,20,a,236,v,224
T a,226,7,59392
M a,28,a,236,v,228
T a,228,10,51200
W 7
M a,32,a,240,v,224
T a,230,10,59392
M a,40,a,240,v,228
T a,232,7,51712
M a,36,a,244,v,224
T a,234,7,59904
M a,44,a,244,v,228
T a,236,10,51712
W 7
M a,48,a,248,v,224
T a,238,10,59904
M a,56,a,248,v,228
T a,240,7,52224
M a,52,a,252,v,224
T a,242,7,60416
M a,60,a,252,v,228
T a,244,10,52224
W 7
M a,64,a,224,v,224
T a,246,10,60416
M a,72,a,224,v,228
T a,248,7,52736
M a,68,a,228,v,224
T a,250,7,60928
M a,76,a,228,v,228
T a,252,10,52736
W 7
M a,80,a,232,v,224
T a,254,10,60928
M a,88,a,232,v,228
s_barrier
.Lc0_112_%=:
T a,224,8,0
M a,84,a,236,v,224
T a,226,8,8192
M a,92,a,236,v,228
T a,228,11,0
W 7
M a,96,a,240,v,224
T a,230,11,8192
M a,104,a,240,v,228
T a,232,8,512
M a,100,a,244,v,224
T a,234,8,8704
M a,108,a,244,v,228
T a,236,11,512
W 7
M a,112,a,248,v,224
T a,238,11,8704
M a,120,a,248,v,228
T a,240,8,1024
M a,116,a,252,v,224
T a,242,8,9216
M a,124,a,252,v,228
T a,244,11,1024
W 7
M a,128,a,224,v,224
T a,246,11,9216
M a,136,a,224,v,228
T a,248,8,1536
M a,132,a,228,v,224
T a,250,8,9728
M a,140,a,228,v,228
T a,252,11,1536
W 7
M a,144,a,232,v,224
T a,254,11,9728
M a,152,a,232,v,228
T a,224,8,2048
M a,148,a,236,v,224
T a,226,8,10240
M a,156,a,236,v,228
T a,228,11,2048
W 7
M a,160,a,240,v,224
T a,230,11,10240
M a,168,a,240,v,228
T a,232,8,2560
M a,164,a,244,v,224
T a,234,8,10752
M a,172,a,244,v,228
T a,236,11,2560
W 7
M a,176,a,248,v,224
T a,238,11,10752
M a,184,a,248,v,228
s_barrier
T a,240,8,16384
M a,180,a,252,v,224
T a,242,8,24576
M a,188,a,252,v,228
T a,244,11,16384
W 7
M a,192,a,224,v,224
T a,246,11,24576
M a,200,a,224,v,228
T a,248,8,16896
M a,196,a,228,v,224
T a,250,8,25088
M a,204,a,228,v,228
T a,252,11,16896
W 7
M a,208,a,232,v,224
T a,254,11,25088
M a,216,a,232,v,228
T a,224,8,17408
M a,212,a,236,v,224
T a,226,8,25600
M a,220,a,236,v,228
T a,228,11,17408
W 7
M a,0,a,240,v,240
T a,230,11,25600
M a,8,a,240,v,244
T a,232,8,17920
M a,4,a,244,v,240
T a,234,8,26112
M a,12,a,244,v,244
T a,236,11,17920
W 7
M a,16,a,248,v,240
T a,238,11,26112
M a,24,a,248,v,244
T a,240,8,18432
M a,20,a,252,v,240
T a,242,8,26624
M a,28,a,252,v,244
T a,244,11,18432
W 7
M a,32,a,224,v,240
T a,246,11,26624
M a,40,a,224,v,244
T a,248,8,18944
M a,36,a,228,v,240
T a,250,8,27136
M a,44,a,228,v,244
T a,252,11,18944
W 7
M a,48,a,232,v,240
T a,254,11,27136
M a,56,a,232,v,244
T a,224,8,19456
M a,52,a,236,v,240
T a,226,8,27648
M a,60,a,236,v,244
T a,228,11,19456
W 7
M a,64,a,240,v,240
T a,230,11,27648
M a,72,a,240,v,244
T a,232,8,19968
M a,68,a,244,v,240
T a,234,8,28160
M a,76,a,244,v,244
T a,236,11,19968
W 7
M a,80,a,248,v,240
T a,238,11,28160
M a,88,a,248,v,244
s_barrier
T a,240,8,32768
M a,84,a,252,v,240
T a,242,8,40960
M a,92,a,252,v,244
T a,244,11,32768
W 7
M a,96,a,224,v,240
T a,246,11,40960
)x"
R"x(M a,104,a,224,v,244
T a,248,8,33280
M a,100,a,228,v,240
T a,250,8,41472
M a,108,a,228,v,244
T a,252,11,33280
W 7
M a,112,a,232,v,240
T a,254,11,41472
M a,120,a,232,v,244
T a,224,8,33792
M a,116,a,236,v,240
T a,226,8,41984
M a,124,a,236,v,244
T a,228,11,33792
W 7
M a,128,a,240,v,240
T a,230,11,41984
M a,136,a,240,v,244
T a,232,8,34304
M a,132,a,244,v,240
T a,234,8,42496
M a,140,a,244,v,244
T a,236,11,34304
W 7
M a,144,a,248,v,240
T a,238,11,42496
M a,152,a,248,v,244
T a,240,8,34816
M a,148,a,252,v,240
T a,242,8,43008
M a,156,a,252,v,244
T a,244,11,34816
W 7
M a,160,a,224,v,240
T a,246,11,43008
M a,168,a,224,v,244
T a,248,8,35328
M a,164,a,228,v,240
T a,250,8,43520
M a,172,a,228,v,244
T a,252,11,35328
W 7
M a,176,a,232,v,240
T a,254,11,43520
M a,184,a,232,v,244
s_barrier
T a,224,14,49152
M a,180,a,236,v,240
T a,226,14,51200
M a,188,a,236,v,244
T a,228,17,49152
W 7
M a,192,a,240,v,240
T a,230,17,51200
M a,200,a,240,v,244
T a,232,14,49664
M a,196,a,244,v,240
T a,234,14,51712
M a,204,a,244,v,244
T a,236,17,49664
W 7
M a,208,a,248,v,240
T a,238,17,51712
M a,216,a,248,v,244
T a,240,14,53248
M a,212,a,252,v,240
T a,242,14,55296
M a,220,a,252,v,244
T a,244,17,53248
W 7
v_mfma_f32_16x16x32_bf16 v[224:227], a[224:227], v[32:35], 0
T a,246,17,55296
v_mfma_f32_16x16x32_bf16 v[232:235], a[224:227], v[36:39], 0
T a,248,14,53760
v_mfma_f32_16x16x32_bf16 v[228:231], a[228:231], v[32:35], 0
T a,250,14,55808
v_mfma_f32_16x16x32_bf16 v[236:239], a[228:231], v[36:39], 0
T a,252,17,53760
W 7
v_mfma_f32_16x16x32_bf16 v[240:243], a[232:235], v[32:35], 0
T a,254,17,55808
v_mfma_f32_16x16x32_bf16 v[248:251], a[232:235], v[36:39], 0
T a,224,14,57344
v_mfma_f32_16x16x32_bf16 v[244:247], a[236:239], v[32:35], 0
T a,226,14,59392
v_mfma_f32_16x16x32_bf16 v[252:255], a[236:239], v[36:39], 0
T a,228,17,57344
W 7
M v,224,a,240,v,40
T a,230,17,59392
M v,232,a,240,v,44
T a,232,14,57856
M v,228,a,244,v,40
T a,234,14,59904
M v,236,a,244,v,44
T a,236,17,57856
W 7
M v,240,a,248,v,40
T a,238,17,59904
M v,248,a,248,v,44
T a,240,14,61440
M v,244,a,252,v,40
T a,242,14,63488
M v,252,a,252,v,44
T a,244,17,61440
W 7
M v,224,a,224,v,48
T a,246,17,63488
M v,232,a,224,v,52
T a,248,14,61952
M v,228,a,228,v,48
T a,250,14,64000
M v,236,a,228,v,52
T a,252,17,61952
W 7
M v,240,a,232,v,48
T a,254,17,64000
M v,248,a,232,v,52
s_barrier
T a,224,15,0
M v,244,a,236,v,48
T a,226,15,2048
M v,252,a,236,v,52
T a,228,18,0
W 7
M v,224,a,240,v,56
T a,230,18,2048
M v,232,a,240,v,60
T a,232,15,512
M v,228,a,244,v,56
T a,234,15,2560
M v,236,a,244,v,60
T a,236,18,512
W 7
M v,240,a,248,v,56
T a,238,18,2560
M v,248,a,248,v,60
T a,240,15,4096
M v,244,a,252,v,56
T a,242,15,6144
M v,252,a,252,v,60
T a,244,18,4096
W 7
M v,224,a,224,v,64
T a,246,18,6144
M v,232,a,224,v,68
T a,248,15,4608
M v,228,a,228,v,64
T a,250,15,6656
M v,236,a,228,v,68
T a,252,18,4608
W 7
M v,240,a,232,v,64
T a,254,18,6656
M v,248,a,232,v,68
T a,224,15,8192
M v,244,a,236,v,64
T a,226,15,10240
M v,252,a,236,v,68
T a,228,18,8192
W 7
M v,224,a,240,v,72
T a,230,18,10240
M v,232,a,240,v,76
T a,232,15,8704
M v,228,a,244,v,72
T a,234,15,10752
M v,236,a,244,v,76
T a,236,18,8704
W 7
M v,240,a,248,v,72
T a,238,18,10752
M v,248,a,248,v,76
T a,240,15,12288
M v,244,a,252,v,72
T a,242,15,14336
M v,252,a,252,v,76
T a,244,18,12288
W 7
M v,224,a,224,v,80
T a,246,18,14336
M v,232,a,224,v,84
T a,248,15,12800
M v,228,a,228,v,80
T a,250,15,14848
M v,236,a,228,v,84
T a,252,18,12800
W 7
M v,240,a,232,v,80
T a,254,18,14848
M v,248,a,232,v,84
s_barrier
T a,224,15,16384
M v,244,a,236,v,80
T a,226,15,18432
M v,252,a,236,v,84
T a,228,18,16384
W 7
M v,224,a,240,v,88
T a,230,18,18432
M v,232,a,240,v,92
T a,232,15,16896
M v,228,a,244,v,88
T a,234,15,18944
M v,236,a,244,v,92
T a,236,18,16896
W 7
M v,240,a,248,v,88
T a,238,18,18944
M v,248,a,248,v,92
T a,240,15,20480
M v,244,a,252,v,88
T a,242,15,22528
M v,252,a,252,v,92
T a,244,18,20480
W 7
M v,224,a,224,v,96
T a,246,18,22528
M v,232,a,224,v,100
T a,248,15,20992
M v,228,a,228,v,96
T a,250,15,23040
M v,236,a,228,v,100
T a,252,18,20992
W 7
M v,240,a,232,v,96
T a,254,18,23040
M v,248,a,232,v,100
T a,224,15,24576
M v,244,a,236,v,96
T a,226,15,26624
M v,252,a,236,v,100
T a,228,18,24576
W 7
M v,224,a,240,v,104
T a,230,18,26624
M v,232,a,240,v,108
T a,232,15,25088
M v,228,a,244,v,104
T a,234,15,27136
M v,236,a,244,v,108
T a,236,18,25088
W 7
M v,240,a,248,v,104
T a,238,18,27136
M v,248,a,248,v,108
T a,240,15,28672
M v,244,a,252,v,104
T a,242,15,30720
M v,252,a,252,v,108
T a,244,18,28672
W 7
M v,224,a,224,v,112
T a,246,18,30720
M v,232,a,224,v,116
T a,248,15,29184
M v,228,a,228,v,112
T a,250,15,31232
M v,236,a,228,v,116
T a,252,18,29184
W 7
M v,240,a,232,v,112
T a,254,18,31232
M v,248,a,232,v,116
s_barrier
.Lc0_118_%=:
T a,224,13,0
M v,244,a,236,v,112
T a,226,13,2048
M v,252,a,236,v,116
T a,228,16,0
W 7
M v,224,a,240,v,120
T a,230,16,2048
M v,232,a,240,v,124
T a,232,13,512
M v,228,a,244,v,120
T a,234,13,2560
M v,236,a,244,v,124
T a,236,16,512
W 7
M v,240,a,248,v,120
T a,238,16,2560
M v,248,a,248,v,124
T a,240,13,4096
M v,244,a,252,v,120
T a,242,13,6144
M v,252,a,252,v,124
T a,244,16,4096
W 7
M v,224,a,224,v,128
T a,246,16,6144
M v,232,a,224,v,132
T a,248,13,4608
M v,228,a,228,v,128
T a,250,13,6656
M v,236,a,228,v,132
T a,252,16,4608
W 7
M v,240,a,232,v,128
T a,254,16,6656
M v,248,a,232,v,132
T a,224,13,8192
M v,244,a,236,v,128
T a,226,13,10240
M v,252,a,236,v,132
T a,228,16,8192
W 7
M v,224,a,240,v,136
T a,230,16,10240
M v,232,a,240,v,140
T a,232,13,8704
M v,228,a,244,v,136
T a,234,13,10752
M v,236,a,244,v,140
T a,236,16,8704
W 7
M v,240,a,248,v,136
T a,238,16,10752
M v,248,a,248,v,140
T a,240,13,12288
M v,244,a,252,v,136
T a,242,13,14336
M v,252,a,252,v,140
T a,244,16,12288
W 7
M v,224,a,224,v,144
T a,246,16,14336
M v,232,a,224,v,148
T a,248,13,12800
M v,228,a,228,v,144
T a,250,13,14848
M v,236,a,228,v,148
T a,252,16,12800
W 7
M v,240,a,232,v,144
T a,254,16,14848
M v,248,a,232,v,148
s_barrier
T a,224,7,16384
M v,244,a,236,v,144
T a,226,7,24576
M v,252,a,236,v,148
T a,228,10,16384
W 7
M v,224,a,240,v,152
T a,230,10,24576
M v,232,a,240,v,156
T a,232,7,16896
M v,228,a,244,v,152
T a,234,7,25088
M v,236,a,244,v,156
T a,236,10,16896
W 7
M v,240,a,248,v,152
T a,238,10,25088
M v,248,a,248,v,156
T a,240,7,17408
M v,244,a,252,v,152
T a,242,7,25600
M v,252,a,252,v,156
s_nop 7
v_mul_f32 v224, v28, v224
v_mul_f32 v225, v28, v225
v_mul_f32 v226, v28, v226
v_mul_f32 v227, v28, v227
v_mul_f32 v228, v28, v228
v_mul_f32 v229, v28, v229
v_mul_f32 v230, v28, v230
v_mul_f32 v231, v28, v231
v_mul_f32 v232, v29, v232
v_mul_f32 v233, v29, v233
v_mul_f32 v234, v29, v234
v_mul_f32 v235, v29, v235
v_mul_f32 v236, v29, v236
v_mul_f32 v237, v29, v237
v_mul_f32 v238, v29, v238
v_mul_f32 v239, v29, v239
v_cvt_pk_bf16_f32 v224, v224, v225
v_cvt_pk_bf16_f32 v225, v226, v227
v_cvt_pk_bf16_f32 v226, v228, v229
v_cvt_pk_bf16_f32 v227, v230, v231
v_cvt_pk_bf16_f32 v228, v232, v233
v_cvt_pk_bf16_f32 v229, v234, v235
v_cvt_pk_bf16_f32 v230, v236, v237
v_cvt_pk_bf16_f32 v231, v238, v239
v_mul_f32 v240, v28, v240
v_mul_f32 v241, v28, v241
v_mul_f32 v242, v28, v242
v_mul_f32 v243, v28, v243
v_mul_f32 v244, v28, v244
v_mul_f32 v245, v28, v245
v_mul_f32 v246, v28, v246
v_mul_f32 v247, v28, v247
v_mul_f32 v248, v29, v248
v_mul_f32 v249, v29, v249
v_mul_f32 v250, v29, v250
v_mul_f32 v251, v29, v251
v_mul_f32 v252, v29, v252
)x"
R"x(v_mul_f32 v253, v29, v253
v_mul_f32 v254, v29, v254
v_mul_f32 v255, v29, v255
v_cvt_pk_bf16_f32 v240, v240, v241
v_cvt_pk_bf16_f32 v241, v242, v243
v_cvt_pk_bf16_f32 v242, v244, v245
v_cvt_pk_bf16_f32 v243, v246, v247
v_cvt_pk_bf16_f32 v244, v248, v249
v_cvt_pk_bf16_f32 v245, v250, v251
v_cvt_pk_bf16_f32 v246, v252, v253
v_cvt_pk_bf16_f32 v247, v254, v255
s_nop 1
T a,244,10,17408
W 7
M a,0,a,224,v,224
T a,246,10,25600
M a,8,a,224,v,228
T a,248,7,17920
M a,4,a,228,v,224
T a,250,7,26112
M a,12,a,228,v,228
T a,252,10,17920
W 7
M a,16,a,232,v,224
T a,254,10,26112
M a,24,a,232,v,228
T a,224,7,18432
M a,20,a,236,v,224
T a,226,7,26624
M a,28,a,236,v,228
T a,228,10,18432
W 7
M a,32,a,240,v,224
T a,230,10,26624
M a,40,a,240,v,228
T a,232,7,18944
M a,36,a,244,v,224
T a,234,7,27136
M a,44,a,244,v,228
T a,236,10,18944
W 7
M a,48,a,248,v,224
T a,238,10,27136
M a,56,a,248,v,228
T a,240,7,19456
M a,52,a,252,v,224
T a,242,7,27648
M a,60,a,252,v,228
T a,244,10,19456
W 7
M a,64,a,224,v,224
T a,246,10,27648
M a,72,a,224,v,228
T a,248,7,19968
M a,68,a,228,v,224
T a,250,7,28160
M a,76,a,228,v,228
T a,252,10,19968
W 7
M a,80,a,232,v,224
T a,254,10,28160
M a,88,a,232,v,228
s_barrier
T a,224,7,32768
M a,84,a,236,v,224
T a,226,7,40960
M a,92,a,236,v,228
T a,228,10,32768
W 7
M a,96,a,240,v,224
T a,230,10,40960
M a,104,a,240,v,228
T a,232,7,33280
M a,100,a,244,v,224
T a,234,7,41472
M a,108,a,244,v,228
T a,236,10,33280
W 7
M a,112,a,248,v,224
T a,238,10,41472
M a,120,a,248,v,228
T a,240,7,33792
M a,116,a,252,v,224
T a,242,7,41984
M a,124,a,252,v,228
T a,244,10,33792
W 7
M a,128,a,224,v,224
T a,246,10,41984
M a,136,a,224,v,228
T a,248,7,34304
M a,132,a,228,v,224
T a,250,7,42496
M a,140,a,228,v,228
T a,252,10,34304
W 7
M a,144,a,232,v,224
T a,254,10,42496
M a,152,a,232,v,228
T a,224,7,34816
M a,148,a,236,v,224
T a,226,7,43008
M a,156,a,236,v,228
T a,228,10,34816
W 7
M a,160,a,240,v,224
T a,230,10,43008
M a,168,a,240,v,228
T a,232,7,35328
M a,164,a,244,v,224
T a,234,7,43520
M a,172,a,244,v,228
T a,236,10,35328
W 7
M a,176,a,248,v,224
T a,238,10,43520
M a,184,a,248,v,228
s_barrier
T a,240,7,49152
M a,180,a,252,v,224
T a,242,7,57344
M a,188,a,252,v,228
T a,244,10,49152
W 7
M a,192,a,224,v,224
T a,246,10,57344
M a,200,a,224,v,228
T a,248,7,49664
M a,196,a,228,v,224
T a,250,7,57856
M a,204,a,228,v,228
T a,252,10,49664
W 7
M a,208,a,232,v,224
T a,254,10,57856
M a,216,a,232,v,228
T a,224,7,50176
M a,212,a,236,v,224
T a,226,7,58368
M a,220,a,236,v,228
T a,228,10,50176
W 7
M a,0,a,240,v,240
T a,230,10,58368
M a,8,a,240,v,244
T a,232,7,50688
M a,4,a,244,v,240
T a,234,7,58880
M a,12,a,244,v,244
T a,236,10,50688
W 7
M a,16,a,248,v,240
T a,238,10,58880
M a,24,a,248,v,244
T a,240,7,51200
M a,20,a,252,v,240
T a,242,7,59392
M a,28,a,252,v,244
T a,244,10,51200
W 7
M a,32,a,224,v,240
T a,246,10,59392
M a,40,a,224,v,244
T a,248,7,51712
M a,36,a,228,v,240
T a,250,7,59904
M a,44,a,228,v,244
T a,252,10,51712
W 7
M a,48,a,232,v,240
T a,254,10,59904
M a,56,a,232,v,244
T a,224,7,52224
M a,52,a,236,v,240
T a,226,7,60416
M a,60,a,236,v,244
T a,228,10,52224
W 7
M a,64,a,240,v,240
T a,230,10,60416
M a,72,a,240,v,244
T a,232,7,52736
M a,68,a,244,v,240
T a,234,7,60928
M a,76,a,244,v,244
T a,236,10,52736
W 7
M a,80,a,248,v,240
T a,238,10,60928
M a,88,a,248,v,244
s_barrier
T a,240,8,0
M a,84,a,252,v,240
T a,242,8,8192
M a,92,a,252,v,244
T a,244,11,0
W 7
M a,96,a,224,v,240
T a,246,11,8192
M a,104,a,224,v,244
T a,248,8,512
M a,100,a,228,v,240
T a,250,8,8704
M a,108,a,228,v,244
T a,252,11,512
W 7
M a,112,a,232,v,240
T a,254,11,8704
M a,120,a,232,v,244
T a,224,8,1024
M a,116,a,236,v,240
T a,226,8,9216
M a,124,a,236,v,244
T a,228,11,1024
W 7
M a,128,a,240,v,240
T a,230,11,9216
M a,136,a,240,v,244
T a,232,8,1536
M a,132,a,244,v,240
T a,234,8,9728
M a,140,a,244,v,244
T a,236,11,1536
W 7
M a,144,a,248,v,240
T a,238,11,9728
M a,152,a,248,v,244
T a,240,8,2048
M a,148,a,252,v,240
T a,242,8,10240
M a,156,a,252,v,244
T a,244,11,2048
W 7
M a,160,a,224,v,240
T a,246,11,10240
M a,168,a,224,v,244
T a,248,8,2560
M a,164,a,228,v,240
T a,250,8,10752
M a,172,a,228,v,244
T a,252,11,2560
W 7
M a,176,a,232,v,240
T a,254,11,10752
M a,184,a,232,v,244
s_barrier
T a,224,14,16384
M a,180,a,236,v,240
T a,226,14,18432
M a,188,a,236,v,244
T a,228,17,16384
W 7
M a,192,a,240,v,240
T a,230,17,18432
M a,200,a,240,v,244
T a,232,14,16896
M a,196,a,244,v,240
T a,234,14,18944
M a,204,a,244,v,244
T a,236,17,16896
W 7
M a,208,a,248,v,240
T a,238,17,18944
M a,216,a,248,v,244
T a,240,14,20480
M a,212,a,252,v,240
T a,242,14,22528
M a,220,a,252,v,244
T a,244,17,20480
W 7
v_mfma_f32_16x16x32_bf16 v[224:227], a[224:227], v[32:35], 0
T a,246,17,22528
v_mfma_f32_16x16x32_bf16 v[232:235], a[224:227], v[36:39], 0
T a,248,14,20992
v_mfma_f32_16x16x32_bf16 v[228:231], a[228:231], v[32:35], 0
T a,250,14,23040
v_mfma_f32_16x16x32_bf16 v[236:239], a[228:231], v[36:39], 0
T a,252,17,20992
W 7
v_mfma_f32_16x16x32_bf16 v[240:243], a[232:235], v[32:35], 0
T a,254,17,23040
v_mfma_f32_16x16x32_bf16 v[248:251], a[232:235], v[36:39], 0
T a,224,14,24576
v_mfma_f32_16x16x32_bf16 v[244:247], a[236:239], v[32:35], 0
T a,226,14,26624
v_mfma_f32_16x16x32_bf16 v[252:255], a[236:239], v[36:39], 0
T a,228,17,24576
W 7
M v,224,a,240,v,40
T a,230,17,26624
M v,232,a,240,v,44
T a,232,14,25088
M v,228,a,244,v,40
T a,234,14,27136
M v,236,a,244,v,44
T a,236,17,25088
W 7
M v,240,a,248,v,40
T a,238,17,27136
M v,248,a,248,v,44
T a,240,14,28672
M v,244,a,252,v,40
T a,242,14,30720
M v,252,a,252,v,44
T a,244,17,28672
W 7
M v,224,a,224,v,48
T a,246,17,30720
M v,232,a,224,v,52
T a,248,14,29184
M v,228,a,228,v,48
T a,250,14,31232
M v,236,a,228,v,52
T a,252,17,29184
W 7
M v,240,a,232,v,48
T a,254,17,31232
M v,248,a,232,v,52
s_barrier
.Lc0_124_%=:
T a,224,14,32768
M v,244,a,236,v,48
T a,226,14,34816
M v,252,a,236,v,52
T a,228,17,32768
W 7
M v,224,a,240,v,56
T a,230,17,34816
M v,232,a,240,v,60
T a,232,14,33280
M v,228,a,244,v,56
T a,234,14,35328
M v,236,a,244,v,60
T a,236,17,33280
W 7
M v,240,a,248,v,56
T a,238,17,35328
M v,248,a,248,v,60
T a,240,14,36864
M v,244,a,252,v,56
T a,242,14,38912
M v,252,a,252,v,60
T a,244,17,36864
W 7
M v,224,a,224,v,64
T a,246,17,38912
M v,232,a,224,v,68
T a,248,14,37376
M v,228,a,228,v,64
T a,250,14,39424
M v,236,a,228,v,68
T a,252,17,37376
W 7
M v,240,a,232,v,64
T a,254,17,39424
M v,248,a,232,v,68
T a,224,14,40960
M v,244,a,236,v,64
T a,226,14,43008
M v,252,a,236,v,68
T a,228,17,40960
W 7
M v,224,a,240,v,72
T a,230,17,43008
M v,232,a,240,v,76
T a,232,14,41472
M v,228,a,244,v,72
T a,234,14,43520
M v,236,a,244,v,76
T a,236,17,41472
W 7
M v,240,a,248,v,72
T a,238,17,43520
M v,248,a,248,v,76
T a,240,14,45056
M v,244,a,252,v,72
T a,242,14,47104
M v,252,a,252,v,76
T a,244,17,45056
W 7
M v,224,a,224,v,80
T a,246,17,47104
M v,232,a,224,v,84
T a,248,14,45568
M v,228,a,228,v,80
T a,250,14,47616
M v,236,a,228,v,84
T a,252,17,45568
W 7
M v,240,a,232,v,80
T a,254,17,47616
M v,248,a,232,v,84
s_barrier
T a,224,14,49152
M v,244,a,236,v,80
T a,226,14,51200
M v,252,a,236,v,84
T a,228,17,49152
W 7
M v,224,a,240,v,88
)x"
R"x(T a,230,17,51200
M v,232,a,240,v,92
T a,232,14,49664
M v,228,a,244,v,88
T a,234,14,51712
M v,236,a,244,v,92
T a,236,17,49664
W 7
M v,240,a,248,v,88
T a,238,17,51712
M v,248,a,248,v,92
T a,240,14,53248
M v,244,a,252,v,88
T a,242,14,55296
M v,252,a,252,v,92
T a,244,17,53248
W 7
M v,224,a,224,v,96
T a,246,17,55296
M v,232,a,224,v,100
T a,248,14,53760
M v,228,a,228,v,96
T a,250,14,55808
M v,236,a,228,v,100
T a,252,17,53760
W 7
M v,240,a,232,v,96
T a,254,17,55808
M v,248,a,232,v,100
T a,224,14,57344
M v,244,a,236,v,96
T a,226,14,59392
M v,252,a,236,v,100
T a,228,17,57344
W 7
M v,224,a,240,v,104
T a,230,17,59392
M v,232,a,240,v,108
T a,232,14,57856
M v,228,a,244,v,104
T a,234,14,59904
M v,236,a,244,v,108
T a,236,17,57856
W 7
M v,240,a,248,v,104
T a,238,17,59904
M v,248,a,248,v,108
T a,240,14,61440
M v,244,a,252,v,104
T a,242,14,63488
M v,252,a,252,v,108
T a,244,17,61440
W 7
M v,224,a,224,v,112
T a,246,17,63488
M v,232,a,224,v,116
T a,248,14,61952
M v,228,a,228,v,112
T a,250,14,64000
M v,236,a,228,v,116
T a,252,17,61952
W 7
M v,240,a,232,v,112
T a,254,17,64000
M v,248,a,232,v,116
s_barrier
T a,224,15,0
M v,244,a,236,v,112
T a,226,15,2048
M v,252,a,236,v,116
T a,228,18,0
W 7
M v,224,a,240,v,120
T a,230,18,2048
M v,232,a,240,v,124
T a,232,15,512
M v,228,a,244,v,120
T a,234,15,2560
M v,236,a,244,v,124
T a,236,18,512
W 7
M v,240,a,248,v,120
T a,238,18,2560
M v,248,a,248,v,124
T a,240,15,4096
M v,244,a,252,v,120
T a,242,15,6144
M v,252,a,252,v,124
T a,244,18,4096
W 7
M v,224,a,224,v,128
T a,246,18,6144
M v,232,a,224,v,132
T a,248,15,4608
M v,228,a,228,v,128
T a,250,15,6656
M v,236,a,228,v,132
T a,252,18,4608
W 7
M v,240,a,232,v,128
T a,254,18,6656
M v,248,a,232,v,132
T a,224,15,8192
M v,244,a,236,v,128
T a,226,15,10240
M v,252,a,236,v,132
T a,228,18,8192
W 7
M v,224,a,240,v,136
T a,230,18,10240
M v,232,a,240,v,140
T a,232,15,8704
M v,228,a,244,v,136
T a,234,15,10752
M v,236,a,244,v,140
T a,236,18,8704
W 7
M v,240,a,248,v,136
T a,238,18,10752
M v,248,a,248,v,140
T a,240,15,12288
M v,244,a,252,v,136
T a,242,15,14336
M v,252,a,252,v,140
T a,244,18,12288
W 7
M v,224,a,224,v,144
T a,246,18,14336
M v,232,a,224,v,148
T a,248,15,12800
M v,228,a,228,v,144
T a,250,15,14848
M v,236,a,228,v,148
T a,252,18,12800
W 7
M v,240,a,232,v,144
T a,254,18,14848
M v,248,a,232,v,148
s_barrier
T a,224,9,16384
M v,244,a,236,v,144
T a,226,9,24576
M v,252,a,236,v,148
T a,228,12,16384
W 7
M v,224,a,240,v,152
T a,230,12,24576
M v,232,a,240,v,156
T a,232,9,16896
M v,228,a,244,v,152
T a,234,9,25088
M v,236,a,244,v,156
T a,236,12,16896
W 7
M v,240,a,248,v,152
T a,238,12,25088
M v,248,a,248,v,156
T a,240,9,17408
M v,244,a,252,v,152
T a,242,9,25600
M v,252,a,252,v,156
s_nop 7
v_mul_f32 v224, v28, v224
v_mul_f32 v225, v28, v225
v_mul_f32 v226, v28, v226
v_mul_f32 v227, v28, v227
v_mul_f32 v228, v28, v228
v_mul_f32 v229, v28, v229
v_mul_f32 v230, v28, v230
v_mul_f32 v231, v28, v231
v_mul_f32 v232, v29, v232
v_mul_f32 v233, v29, v233
v_mul_f32 v234, v29, v234
v_mul_f32 v235, v29, v235
v_mul_f32 v236, v29, v236
v_mul_f32 v237, v29, v237
v_mul_f32 v238, v29, v238
v_mul_f32 v239, v29, v239
v_cvt_pk_bf16_f32 v224, v224, v225
v_cvt_pk_bf16_f32 v225, v226, v227
v_cvt_pk_bf16_f32 v226, v228, v229
v_cvt_pk_bf16_f32 v227, v230, v231
v_cvt_pk_bf16_f32 v228, v232, v233
v_cvt_pk_bf16_f32 v229, v234, v235
v_cvt_pk_bf16_f32 v230, v236, v237
v_cvt_pk_bf16_f32 v231, v238, v239
v_mul_f32 v240, v28, v240
v_mul_f32 v241, v28, v241
v_mul_f32 v242, v28, v242
v_mul_f32 v243, v28, v243
v_mul_f32 v244, v28, v244
v_mul_f32 v245, v28, v245
v_mul_f32 v246, v28, v246
v_mul_f32 v247, v28, v247
v_mul_f32 v248, v29, v248
v_mul_f32 v249, v29, v249
v_mul_f32 v250, v29, v250
v_mul_f32 v251, v29, v251
v_mul_f32 v252, v29, v252
v_mul_f32 v253, v29, v253
v_mul_f32 v254, v29, v254
v_mul_f32 v255, v29, v255
v_cvt_pk_bf16_f32 v240, v240, v241
v_cvt_pk_bf16_f32 v241, v242, v243
v_cvt_pk_bf16_f32 v242, v244, v245
v_cvt_pk_bf16_f32 v243, v246, v247
v_cvt_pk_bf16_f32 v244, v248, v249
v_cvt_pk_bf16_f32 v245, v250, v251
v_cvt_pk_bf16_f32 v246, v252, v253
v_cvt_pk_bf16_f32 v247, v254, v255
s_nop 1
T a,244,12,17408
W 7
M a,0,a,224,v,224
T a,246,12,25600
M a,8,a,224,v,228
T a,248,9,17920
M a,4,a,228,v,224
T a,250,9,26112
M a,12,a,228,v,228
T a,252,12,17920
W 7
M a,16,a,232,v,224
T a,254,12,26112
M a,24,a,232,v,228
T a,224,9,18432
M a,20,a,236,v,224
T a,226,9,26624
M a,28,a,236,v,228
T a,228,12,18432
W 7
M a,32,a,240,v,224
T a,230,12,26624
M a,40,a,240,v,228
T a,232,9,18944
M a,36,a,244,v,224
T a,234,9,27136
M a,44,a,244,v,228
T a,236,12,18944
W 7
M a,48,a,248,v,224
T a,238,12,27136
M a,56,a,248,v,228
T a,240,9,19456
M a,52,a,252,v,224
T a,242,9,27648
M a,60,a,252,v,228
T a,244,12,19456
W 7
M a,64,a,224,v,224
T a,246,12,27648
M a,72,a,224,v,228
T a,248,9,19968
M a,68,a,228,v,224
T a,250,9,28160
M a,76,a,228,v,228
T a,252,12,19968
W 7
M a,80,a,232,v,224
T a,254,12,28160
M a,88,a,232,v,228
s_barrier
T a,224,7,0
M a,84,a,236,v,224
T a,226,7,8192
M a,92,a,236,v,228
T a,228,10,0
W 7
M a,96,a,240,v,224
T a,230,10,8192
M a,104,a,240,v,228
T a,232,7,512
M a,100,a,244,v,224
T a,234,7,8704
M a,108,a,244,v,228
T a,236,10,512
W 7
M a,112,a,248,v,224
T a,238,10,8704
M a,120,a,248,v,228
T a,240,7,1024
M a,116,a,252,v,224
T a,242,7,9216
M a,124,a,252,v,228
T a,244,10,1024
W 7
M a,128,a,224,v,224
T a,246,10,9216
M a,136,a,224,v,228
T a,248,7,1536
M a,132,a,228,v,224
T a,250,7,9728
M a,140,a,228,v,228
T a,252,10,1536
W 7
M a,144,a,232,v,224
T a,254,10,9728
M a,152,a,232,v,228
T a,224,7,2048
M a,148,a,236,v,224
T a,226,7,10240
M a,156,a,236,v,228
T a,228,10,2048
W 7
M a,160,a,240,v,224
T a,230,10,10240
M a,168,a,240,v,228
T a,232,7,2560
M a,164,a,244,v,224
T a,234,7,10752
M a,172,a,244,v,228
T a,236,10,2560
W 7
M a,176,a,248,v,224
T a,238,10,10752
M a,184,a,248,v,228
s_barrier
T a,240,7,16384
M a,180,a,252,v,224
T a,242,7,24576
M a,188,a,252,v,228
T a,244,10,16384
W 7
M a,192,a,224,v,224
T a,246,10,24576
M a,200,a,224,v,228
T a,248,7,16896
M a,196,a,228,v,224
T a,250,7,25088
M a,204,a,228,v,228
T a,252,10,16896
W 7
M a,208,a,232,v,224
T a,254,10,25088
M a,216,a,232,v,228
T a,224,7,17408
M a,212,a,236,v,224
T a,226,7,25600
M a,220,a,236,v,228
T a,228,10,17408
W 7
M a,0,a,240,v,240
T a,230,10,25600
M a,8,a,240,v,244
T a,232,7,17920
M a,4,a,244,v,240
T a,234,7,26112
M a,12,a,244,v,244
T a,236,10,17920
W 7
M a,16,a,248,v,240
T a,238,10,26112
M a,24,a,248,v,244
T a,240,7,18432
M a,20,a,252,v,240
T a,242,7,26624
M a,28,a,252,v,244
T a,244,10,18432
W 7
M a,32,a,224,v,240
T a,246,10,26624
M a,40,a,224,v,244
T a,248,7,18944
M a,36,a,228,v,240
T a,250,7,27136
M a,44,a,228,v,244
T a,252,10,18944
W 7
M a,48,a,232,v,240
T a,254,10,27136
M a,56,a,232,v,244
T a,224,7,19456
M a,52,a,236,v,240
T a,226,7,27648
M a,60,a,236,v,244
T a,228,10,19456
W 7
M a,64,a,240,v,240
T a,230,10,27648
M a,72,a,240,v,244
T a,232,7,19968
M a,68,a,244,v,240
T a,234,7,28160
M a,76,a,244,v,244
T a,236,10,19968
W 7
M a,80,a,248,v,240
T a,238,10,28160
M a,88,a,248,v,244
s_barrier
.Lc0_130_%=:
T a,240,7,32768
M a,84,a,252,v,240
T a,242,7,40960
M a,92,a,252,v,244
T a,244,10,32768
W 7
M a,96,a,224,v,240
T a,246,10,40960
M a,104,a,224,v,244
)x"
R"x(T a,248,7,33280
M a,100,a,228,v,240
T a,250,7,41472
M a,108,a,228,v,244
T a,252,10,33280
W 7
M a,112,a,232,v,240
T a,254,10,41472
M a,120,a,232,v,244
T a,224,7,33792
M a,116,a,236,v,240
T a,226,7,41984
M a,124,a,236,v,244
T a,228,10,33792
W 7
M a,128,a,240,v,240
T a,230,10,41984
M a,136,a,240,v,244
T a,232,7,34304
M a,132,a,244,v,240
T a,234,7,42496
M a,140,a,244,v,244
T a,236,10,34304
W 7
M a,144,a,248,v,240
T a,238,10,42496
M a,152,a,248,v,244
T a,240,7,34816
M a,148,a,252,v,240
T a,242,7,43008
M a,156,a,252,v,244
T a,244,10,34816
W 7
M a,160,a,224,v,240
T a,246,10,43008
M a,168,a,224,v,244
T a,248,7,35328
M a,164,a,228,v,240
T a,250,7,43520
M a,172,a,228,v,244
T a,252,10,35328
W 7
M a,176,a,232,v,240
T a,254,10,43520
M a,184,a,232,v,244
s_barrier
T a,224,13,49152
M a,180,a,236,v,240
T a,226,13,51200
M a,188,a,236,v,244
T a,228,16,49152
W 7
M a,192,a,240,v,240
T a,230,16,51200
M a,200,a,240,v,244
T a,232,13,49664
M a,196,a,244,v,240
T a,234,13,51712
M a,204,a,244,v,244
T a,236,16,49664
W 7
M a,208,a,248,v,240
T a,238,16,51712
M a,216,a,248,v,244
T a,240,13,53248
M a,212,a,252,v,240
T a,242,13,55296
M a,220,a,252,v,244
T a,244,16,53248
W 7
v_mfma_f32_16x16x32_bf16 v[224:227], a[224:227], v[32:35], 0
T a,246,16,55296
v_mfma_f32_16x16x32_bf16 v[232:235], a[224:227], v[36:39], 0
T a,248,13,53760
v_mfma_f32_16x16x32_bf16 v[228:231], a[228:231], v[32:35], 0
T a,250,13,55808
v_mfma_f32_16x16x32_bf16 v[236:239], a[228:231], v[36:39], 0
T a,252,16,53760
W 7
v_mfma_f32_16x16x32_bf16 v[240:243], a[232:235], v[32:35], 0
T a,254,16,55808
v_mfma_f32_16x16x32_bf16 v[248:251], a[232:235], v[36:39], 0
T a,224,13,57344
v_mfma_f32_16x16x32_bf16 v[244:247], a[236:239], v[32:35], 0
T a,226,13,59392
v_mfma_f32_16x16x32_bf16 v[252:255], a[236:239], v[36:39], 0
T a,228,16,57344
W 7
M v,224,a,240,v,40
T a,230,16,59392
M v,232,a,240,v,44
T a,232,13,57856
M v,228,a,244,v,40
T a,234,13,59904
M v,236,a,244,v,44
T a,236,16,57856
W 7
M v,240,a,248,v,40
T a,238,16,59904
M v,248,a,248,v,44
T a,240,13,61440
M v,244,a,252,v,40
T a,242,13,63488
M v,252,a,252,v,44
T a,244,16,61440
W 7
M v,224,a,224,v,48
T a,246,16,63488
M v,232,a,224,v,52
T a,248,13,61952
M v,228,a,228,v,48
T a,250,13,64000
M v,236,a,228,v,52
T a,252,16,61952
W 7
M v,240,a,232,v,48
T a,254,16,64000
M v,248,a,232,v,52
s_barrier
T a,224,14,0
M v,244,a,236,v,48
T a,226,14,2048
M v,252,a,236,v,52
T a,228,17,0
W 7
M v,224,a,240,v,56
T a,230,17,2048
M v,232,a,240,v,60
T a,232,14,512
M v,228,a,244,v,56
T a,234,14,2560
M v,236,a,244,v,60
T a,236,17,512
W 7
M v,240,a,248,v,56
T a,238,17,2560
M v,248,a,248,v,60
T a,240,14,4096
M v,244,a,252,v,56
T a,242,14,6144
M v,252,a,252,v,60
T a,244,17,4096
W 7
M v,224,a,224,v,64
T a,246,17,6144
M v,232,a,224,v,68
T a,248,14,4608
M v,228,a,228,v,64
T a,250,14,6656
M v,236,a,228,v,68
T a,252,17,4608
W 7
M v,240,a,232,v,64
T a,254,17,6656
M v,248,a,232,v,68
T a,224,14,8192
M v,244,a,236,v,64
T a,226,14,10240
M v,252,a,236,v,68
T a,228,17,8192
W 7
M v,224,a,240,v,72
T a,230,17,10240
M v,232,a,240,v,76
T a,232,14,8704
M v,228,a,244,v,72
T a,234,14,10752
M v,236,a,244,v,76
T a,236,17,8704
W 7
M v,240,a,248,v,72
T a,238,17,10752
M v,248,a,248,v,76
T a,240,14,12288
M v,244,a,252,v,72
T a,242,14,14336
M v,252,a,252,v,76
T a,244,17,12288
W 7
M v,224,a,224,v,80
T a,246,17,14336
M v,232,a,224,v,84
T a,248,14,12800
M v,228,a,228,v,80
T a,250,14,14848
M v,236,a,228,v,84
T a,252,17,12800
W 7
M v,240,a,232,v,80
T a,254,17,14848
M v,248,a,232,v,84
s_barrier
T a,224,14,16384
M v,244,a,236,v,80
T a,226,14,18432
M v,252,a,236,v,84
T a,228,17,16384
W 7
M v,224,a,240,v,88
T a,230,17,18432
M v,232,a,240,v,92
T a,232,14,16896
M v,228,a,244,v,88
T a,234,14,18944
M v,236,a,244,v,92
T a,236,17,16896
W 7
M v,240,a,248,v,88
T a,238,17,18944
M v,248,a,248,v,92
T a,240,14,20480
M v,244,a,252,v,88
T a,242,14,22528
M v,252,a,252,v,92
T a,244,17,20480
W 7
M v,224,a,224,v,96
T a,246,17,22528
M v,232,a,224,v,100
T a,248,14,20992
M v,228,a,228,v,96
T a,250,14,23040
M v,236,a,228,v,100
T a,252,17,20992
W 7
M v,240,a,232,v,96
T a,254,17,23040
M v,248,a,232,v,100
T a,224,14,24576
M v,244,a,236,v,96
T a,226,14,26624
M v,252,a,236,v,100
T a,228,17,24576
W 7
M v,224,a,240,v,104
T a,230,17,26624
M v,232,a,240,v,108
T a,232,14,25088
M v,228,a,244,v,104
T a,234,14,27136
M v,236,a,244,v,108
T a,236,17,25088
W 7
M v,240,a,248,v,104
T a,238,17,27136
M v,248,a,248,v,108
T a,240,14,28672
M v,244,a,252,v,104
T a,242,14,30720
M v,252,a,252,v,108
T a,244,17,28672
W 7
M v,224,a,224,v,112
T a,246,17,30720
M v,232,a,224,v,116
T a,248,14,29184
M v,228,a,228,v,112
T a,250,14,31232
M v,236,a,228,v,116
T a,252,17,29184
W 7
M v,240,a,232,v,112
T a,254,17,31232
M v,248,a,232,v,116
s_barrier
T a,224,14,32768
M v,244,a,236,v,112
T a,226,14,34816
M v,252,a,236,v,116
T a,228,17,32768
W 7
M v,224,a,240,v,120
T a,230,17,34816
M v,232,a,240,v,124
T a,232,14,33280
M v,228,a,244,v,120
T a,234,14,35328
M v,236,a,244,v,124
T a,236,17,33280
W 7
M v,240,a,248,v,120
T a,238,17,35328
M v,248,a,248,v,124
T a,240,14,36864
M v,244,a,252,v,120
T a,242,14,38912
M v,252,a,252,v,124
T a,244,17,36864
W 7
M v,224,a,224,v,128
T a,246,17,38912
M v,232,a,224,v,132
T a,248,14,37376
M v,228,a,228,v,128
T a,250,14,39424
M v,236,a,228,v,132
T a,252,17,37376
W 7
M v,240,a,232,v,128
T a,254,17,39424
M v,248,a,232,v,132
T a,224,14,40960
M v,244,a,236,v,128
T a,226,14,43008
M v,252,a,236,v,132
T a,228,17,40960
W 7
M v,224,a,240,v,136
T a,230,17,43008
M v,232,a,240,v,140
T a,232,14,41472
M v,228,a,244,v,136
T a,234,14,43520
M v,236,a,244,v,140
T a,236,17,41472
W 7
M v,240,a,248,v,136
T a,238,17,43520
M v,248,a,248,v,140
T a,240,14,45056
M v,244,a,252,v,136
T a,242,14,47104
M v,252,a,252,v,140
T a,244,17,45056
W 7
M v,224,a,224,v,144
T a,246,17,47104
M v,232,a,224,v,148
T a,248,14,45568
M v,228,a,228,v,144
T a,250,14,47616
M v,236,a,228,v,148
T a,252,17,45568
W 7
M v,240,a,232,v,144
T a,254,17,47616
M v,248,a,232,v,148
s_barrier
T a,224,8,49152
M v,244,a,236,v,144
T a,226,8,57344
M v,252,a,236,v,148
T a,228,11,49152
W 7
M v,224,a,240,v,152
T a,230,11,57344
M v,232,a,240,v,156
T a,232,8,49664
M v,228,a,244,v,152
T a,234,8,57856
M v,236,a,244,v,156
T a,236,11,49664
W 7
M v,240,a,248,v,152
T a,238,11,57856
M v,248,a,248,v,156
T a,240,8,50176
M v,244,a,252,v,152
T a,242,8,58368
M v,252,a,252,v,156
s_nop 7
v_mul_f32 v224, v28, v224
v_mul_f32 v225, v28, v225
v_mul_f32 v226, v28, v226
v_mul_f32 v227, v28, v227
v_mul_f32 v228, v28, v228
v_mul_f32 v229, v28, v229
v_mul_f32 v230, v28, v230
v_mul_f32 v231, v28, v231
v_mul_f32 v232, v29, v232
v_mul_f32 v233, v29, v233
v_mul_f32 v234, v29, v234
v_mul_f32 v235, v29, v235
v_mul_f32 v236, v29, v236
v_mul_f32 v237, v29, v237
v_mul_f32 v238, v29, v238
v_mul_f32 v239, v29, v239
v_cvt_pk_bf16_f32 v224, v224, v225
v_cvt_pk_bf16_f32 v225, v226, v227
v_cvt_pk_bf16_f32 v226, v228, v229
v_cvt_pk_bf16_f32 v227, v230, v231
v_cvt_pk_bf16_f32 v228, v232, v233
v_cvt_pk_bf16_f32 v229, v234, v235
v_cvt_pk_bf16_f32 v230, v236, v237
v_cvt_pk_bf16_f32 v231, v238, v239
v_mul_f32 v240, v28, v240
v_mul_f32 v241, v28, v241
v_mul_f32 v242, v28, v242
v_mul_f32 v243, v28, v243
v_mul_f32 v244, v28, v244
v_mul_f32 v245, v28, v245
v_mul_f32 v246, v28, v246
v_mul_f32 v247, v28, v247
v_mul_f32 v248, v29, v248
v_mul_f32 v249, v29, v249
v_mul_f32 v250, v29, v250
v_mul_f32 v251, v29, v251
v_mul_f32 v252, v29, v252
v_mul_f32 v253, v29, v253
v_mul_f32 v254, v29, v254
)x"
R"x(v_mul_f32 v255, v29, v255
v_cvt_pk_bf16_f32 v240, v240, v241
v_cvt_pk_bf16_f32 v241, v242, v243
v_cvt_pk_bf16_f32 v242, v244, v245
v_cvt_pk_bf16_f32 v243, v246, v247
v_cvt_pk_bf16_f32 v244, v248, v249
v_cvt_pk_bf16_f32 v245, v250, v251
v_cvt_pk_bf16_f32 v246, v252, v253
v_cvt_pk_bf16_f32 v247, v254, v255
s_nop 1
T a,244,11,50176
W 7
M a,0,a,224,v,224
T a,246,11,58368
M a,8,a,224,v,228
T a,248,8,50688
M a,4,a,228,v,224
T a,250,8,58880
M a,12,a,228,v,228
T a,252,11,50688
W 7
M a,16,a,232,v,224
T a,254,11,58880
M a,24,a,232,v,228
T a,224,8,51200
M a,20,a,236,v,224
T a,226,8,59392
M a,28,a,236,v,228
T a,228,11,51200
W 7
M a,32,a,240,v,224
T a,230,11,59392
M a,40,a,240,v,228
T a,232,8,51712
M a,36,a,244,v,224
T a,234,8,59904
M a,44,a,244,v,228
T a,236,11,51712
W 7
M a,48,a,248,v,224
T a,238,11,59904
M a,56,a,248,v,228
T a,240,8,52224
M a,52,a,252,v,224
T a,242,8,60416
M a,60,a,252,v,228
T a,244,11,52224
W 7
M a,64,a,224,v,224
T a,246,11,60416
M a,72,a,224,v,228
T a,248,8,52736
M a,68,a,228,v,224
T a,250,8,60928
M a,76,a,228,v,228
T a,252,11,52736
W 7
M a,80,a,232,v,224
T a,254,11,60928
M a,88,a,232,v,228
s_barrier
T a,224,9,0
M a,84,a,236,v,224
T a,226,9,8192
M a,92,a,236,v,228
T a,228,12,0
W 7
M a,96,a,240,v,224
T a,230,12,8192
M a,104,a,240,v,228
T a,232,9,512
M a,100,a,244,v,224
T a,234,9,8704
M a,108,a,244,v,228
T a,236,12,512
W 7
M a,112,a,248,v,224
T a,238,12,8704
M a,120,a,248,v,228
T a,240,9,1024
v_lshlrev_b32 v25, 4, v0
M a,116,a,252,v,224
T a,242,9,9216
s_mul_i32 s27, s60, 7
M a,124,a,252,v,228
T a,244,12,1024
W 7
M a,0,a,224,v,240
T a,246,12,9216
v_add_u32 v25, s27, v25
M a,8,a,224,v,244
T a,248,9,1536
M a,4,a,228,v,240
T a,250,9,9728
M a,12,a,228,v,244
T a,252,12,1536
W 7
M a,16,a,232,v,240
T a,254,12,9728
M a,24,a,232,v,244
T a,224,9,2048
M a,20,a,236,v,240
T a,226,9,10240
M a,28,a,236,v,244
T a,228,12,2048
W 7
A 32,0
M a,32,a,240,v,240
T a,230,12,10240
A 33,1
M a,40,a,240,v,244
A 34,2
A 35,3
A 36,4
A 37,5
T a,232,9,2560
A 38,6
A 39,7
M a,36,a,244,v,240
T a,234,9,10752
v_cvt_pk_bf16_f32 v64, v32, v33
M a,44,a,244,v,244
v_cvt_pk_bf16_f32 v65, v34, v35
v_cvt_pk_bf16_f32 v66, v36, v37
v_cvt_pk_bf16_f32 v67, v38, v39
s_mov_b32 s27, 0
T a,236,12,2560
W 7
buffer_store_dwordx4 v[64:67], v25, s[56:59], s27 offen sc1
M a,48,a,248,v,240
T a,238,12,10752
A 40,8
M a,56,a,248,v,244
A 41,9
A 42,10
A 43,11
A 44,12
T a,240,9,3072
A 45,13
A 46,14
M a,52,a,252,v,240
T a,242,9,11264
A 47,15
M a,60,a,252,v,244
v_cvt_pk_bf16_f32 v68, v40, v41
v_cvt_pk_bf16_f32 v69, v42, v43
v_cvt_pk_bf16_f32 v70, v44, v45
v_cvt_pk_bf16_f32 v71, v46, v47
T a,244,12,3072
W 7
s_mov_b32 s27, 1024
M a,64,a,224,v,240
T a,246,12,11264
buffer_store_dwordx4 v[68:71], v25, s[56:59], s27 offen sc1
M a,72,a,224,v,244
A 48,16
A 49,17
A 50,18
A 51,19
T a,248,9,3584
A 52,20
A 53,21
M a,68,a,228,v,240
T a,250,9,11776
A 54,22
M a,76,a,228,v,244
A 55,23
v_cvt_pk_bf16_f32 v72, v48, v49
v_cvt_pk_bf16_f32 v73, v50, v51
v_cvt_pk_bf16_f32 v74, v52, v53
T a,252,12,3584
W 7
v_cvt_pk_bf16_f32 v75, v54, v55
M a,80,a,232,v,240
T a,254,12,11776
s_mov_b32 s27, 2048
M a,88,a,232,v,244
buffer_store_dwordx4 v[72:75], v25, s[56:59], s27 offen sc1
A 56,24
A 57,25
A 58,26
s_barrier
T a,224,9,16384
A 59,27
A 60,28
M a,84,a,236,v,240
T a,226,9,24576
A 61,29
M a,92,a,236,v,244
A 62,30
A 63,31
v_cvt_pk_bf16_f32 v76, v56, v57
v_cvt_pk_bf16_f32 v77, v58, v59
T a,228,12,16384
W 7
v_cvt_pk_bf16_f32 v78, v60, v61
M a,96,a,240,v,240
T a,230,12,24576
v_cvt_pk_bf16_f32 v79, v62, v63
M a,104,a,240,v,244
s_mov_b32 s27, 3072
buffer_store_dwordx4 v[76:79], v25, s[56:59], s27 offen sc1
A 32,32
A 33,33
T a,232,9,16896
A 34,34
A 35,35
M a,100,a,244,v,240
T a,234,9,25088
A 36,36
M a,108,a,244,v,244
A 37,37
A 38,38
A 39,39
v_cvt_pk_bf16_f32 v80, v32, v33
T a,236,12,16896
W 7
v_cvt_pk_bf16_f32 v81, v34, v35
M a,112,a,248,v,240
T a,238,12,25088
v_cvt_pk_bf16_f32 v82, v36, v37
M a,120,a,248,v,244
v_cvt_pk_bf16_f32 v83, v38, v39
s_mov_b32 s27, 4096
buffer_store_dwordx4 v[80:83], v25, s[56:59], s27 offen sc1
A 40,40
T a,240,9,17408
A 41,41
A 42,42
M a,116,a,252,v,240
T a,242,9,25600
A 43,43
M a,124,a,252,v,244
A 44,44
A 45,45
A 46,46
A 47,47
T a,244,12,17408
W 7
v_cvt_pk_bf16_f32 v84, v40, v41
M a,128,a,224,v,224
T a,246,12,25600
v_cvt_pk_bf16_f32 v85, v42, v43
M a,136,a,224,v,228
v_cvt_pk_bf16_f32 v86, v44, v45
v_cvt_pk_bf16_f32 v87, v46, v47
s_mov_b32 s27, 5120
buffer_store_dwordx4 v[84:87], v25, s[56:59], s27 offen sc1
T a,248,9,17920
A 48,48
A 49,49
M a,132,a,228,v,224
T a,250,9,26112
A 50,50
M a,140,a,228,v,228
A 51,51
A 52,52
A 53,53
A 54,54
T a,252,12,17920
W 7
A 55,55
M a,144,a,232,v,224
T a,254,12,26112
v_cvt_pk_bf16_f32 v88, v48, v49
M a,152,a,232,v,228
v_cvt_pk_bf16_f32 v89, v50, v51
v_cvt_pk_bf16_f32 v90, v52, v53
v_cvt_pk_bf16_f32 v91, v54, v55
s_mov_b32 s27, 6144
T a,224,9,18432
buffer_store_dwordx4 v[88:91], v25, s[56:59], s27 offen sc1
A 56,56
M a,148,a,236,v,224
T a,226,9,26624
A 57,57
M a,156,a,236,v,228
A 58,58
A 59,59
A 60,60
A 61,61
T a,228,12,18432
W 7
A 62,62
M a,160,a,240,v,224
T a,230,12,26624
A 63,63
M a,168,a,240,v,228
v_cvt_pk_bf16_f32 v92, v56, v57
v_cvt_pk_bf16_f32 v93, v58, v59
v_cvt_pk_bf16_f32 v94, v60, v61
v_cvt_pk_bf16_f32 v95, v62, v63
T a,232,9,18944
s_mov_b32 s27, 7168
buffer_store_dwordx4 v[92:95], v25, s[56:59], s27 offen sc1
M a,164,a,244,v,224
T a,234,9,27136
A 32,64
M a,172,a,244,v,228
A 33,65
A 34,66
A 35,67
A 36,68
T a,236,12,18944
W 7
A 37,69
M a,176,a,248,v,224
T a,238,12,27136
A 38,70
M a,184,a,248,v,228
A 39,71
v_cvt_pk_bf16_f32 v64, v32, v33
v_cvt_pk_bf16_f32 v65, v34, v35
v_cvt_pk_bf16_f32 v66, v36, v37
s_barrier
T a,240,7,0
v_cvt_pk_bf16_f32 v67, v38, v39
s_mov_b32 s27, 8192
M a,180,a,252,v,224
T a,242,7,8192
buffer_store_dwordx4 v[64:67], v25, s[56:59], s27 offen sc1
M a,188,a,252,v,228
A 40,72
A 41,73
A 42,74
A 43,75
T a,244,10,0
W 7
A 44,76
M a,192,a,224,v,224
T a,246,10,8192
A 45,77
M a,200,a,224,v,228
A 46,78
A 47,79
v_cvt_pk_bf16_f32 v68, v40, v41
v_cvt_pk_bf16_f32 v69, v42, v43
T a,248,7,512
v_cvt_pk_bf16_f32 v70, v44, v45
v_cvt_pk_bf16_f32 v71, v46, v47
M a,196,a,228,v,224
T a,250,7,8704
s_mov_b32 s27, 9216
M a,204,a,228,v,228
buffer_store_dwordx4 v[68:71], v25, s[56:59], s27 offen sc1
A 48,80
A 49,81
A 50,82
T a,252,10,512
W 7
A 51,83
M a,208,a,232,v,224
T a,254,10,8704
A 52,84
M a,216,a,232,v,228
A 53,85
A 54,86
A 55,87
v_cvt_pk_bf16_f32 v72, v48, v49
T a,224,7,1024
v_cvt_pk_bf16_f32 v73, v50, v51
v_cvt_pk_bf16_f32 v74, v52, v53
M a,212,a,236,v,224
T a,226,7,9216
v_cvt_pk_bf16_f32 v75, v54, v55
M a,220,a,236,v,228
s_mov_b32 s27, 10240
buffer_store_dwordx4 v[72:75], v25, s[56:59], s27 offen sc1
A 56,88
A 57,89
T a,228,10,1024
W 7
A 58,90
M a,128,a,240,v,240
T a,230,10,9216
A 59,91
M a,136,a,240,v,244
A 60,92
A 61,93
A 62,94
A 63,95
T a,232,7,1536
v_cvt_pk_bf16_f32 v76, v56, v57
v_cvt_pk_bf16_f32 v77, v58, v59
M a,132,a,244,v,240
T a,234,7,9728
v_cvt_pk_bf16_f32 v78, v60, v61
M a,140,a,244,v,244
v_cvt_pk_bf16_f32 v79, v62, v63
s_mov_b32 s27, 11264
buffer_store_dwordx4 v[76:79], v25, s[56:59], s27 offen sc1
A 32,96
T a,236,10,1536
W 7
A 33,97
M a,144,a,248,v,240
T a,238,10,9728
A 34,98
M a,152,a,248,v,244
A 35,99
)x"
R"x(A 36,100
A 37,101
A 38,102
T a,240,7,2048
A 39,103
v_cvt_pk_bf16_f32 v80, v32, v33
M a,148,a,252,v,240
T a,242,7,10240
v_cvt_pk_bf16_f32 v81, v34, v35
M a,156,a,252,v,244
v_cvt_pk_bf16_f32 v82, v36, v37
v_cvt_pk_bf16_f32 v83, v38, v39
s_mov_b32 s27, 12288
buffer_store_dwordx4 v[80:83], v25, s[56:59], s27 offen sc1
T a,244,10,2048
W 7
A 40,104
M a,160,a,224,v,240
T a,246,10,10240
A 41,105
M a,168,a,224,v,244
A 42,106
A 43,107
A 44,108
A 45,109
T a,248,7,2560
A 46,110
A 47,111
M a,164,a,228,v,240
T a,250,7,10752
v_cvt_pk_bf16_f32 v84, v40, v41
M a,172,a,228,v,244
v_cvt_pk_bf16_f32 v85, v42, v43
v_cvt_pk_bf16_f32 v86, v44, v45
v_cvt_pk_bf16_f32 v87, v46, v47
s_mov_b32 s27, 13312
T a,252,10,2560
W 7
buffer_store_dwordx4 v[84:87], v25, s[56:59], s27 offen sc1
M a,176,a,232,v,240
T a,254,10,10752
A 48,112
M a,184,a,232,v,244
A 49,113
A 50,114
A 51,115
A 52,116
A 53,117
A 54,118
A 55,119
M a,180,a,236,v,240
v_cvt_pk_bf16_f32 v88, v48, v49
v_cvt_pk_bf16_f32 v89, v50, v51
M a,188,a,236,v,244
v_cvt_pk_bf16_f32 v90, v52, v53
v_cvt_pk_bf16_f32 v91, v54, v55
s_mov_b32 s27, 14336
buffer_store_dwordx4 v[88:91], v25, s[56:59], s27 offen sc1
W 6
A 56,120
A 57,121
M a,192,a,240,v,240
A 58,122
A 59,123
M a,200,a,240,v,244
A 60,124
A 61,125
A 62,126
A 63,127
W 4
v_cvt_pk_bf16_f32 v92, v56, v57
v_cvt_pk_bf16_f32 v93, v58, v59
M a,196,a,244,v,240
v_cvt_pk_bf16_f32 v94, v60, v61
v_cvt_pk_bf16_f32 v95, v62, v63
M a,204,a,244,v,244
s_mov_b32 s27, 15360
buffer_store_dwordx4 v[92:95], v25, s[56:59], s27 offen sc1
A 32,128
A 33,129
W 2
A 34,130
A 35,131
M a,208,a,248,v,240
A 36,132
A 37,133
M a,216,a,248,v,244
A 38,134
A 39,135
v_cvt_pk_bf16_f32 v64, v32, v33
v_cvt_pk_bf16_f32 v65, v34, v35
W 0
v_cvt_pk_bf16_f32 v66, v36, v37
v_cvt_pk_bf16_f32 v67, v38, v39
M a,212,a,252,v,240
s_mov_b32 s27, 16384
buffer_store_dwordx4 v[64:67], v25, s[56:59], s27 offen sc1
M a,220,a,252,v,244
A 40,136
A 41,137
A 42,138
A 43,139
A 44,140
A 45,141
A 46,142
A 47,143
v_cvt_pk_bf16_f32 v68, v40, v41
v_cvt_pk_bf16_f32 v69, v42, v43
v_cvt_pk_bf16_f32 v70, v44, v45
v_cvt_pk_bf16_f32 v71, v46, v47
s_mov_b32 s27, 17408
buffer_store_dwordx4 v[68:71], v25, s[56:59], s27 offen sc1
A 48,144
A 49,145
A 50,146
A 51,147
A 52,148
A 53,149
A 54,150
A 55,151
v_cvt_pk_bf16_f32 v72, v48, v49
v_cvt_pk_bf16_f32 v73, v50, v51
v_cvt_pk_bf16_f32 v74, v52, v53
v_cvt_pk_bf16_f32 v75, v54, v55
s_mov_b32 s27, 18432
buffer_store_dwordx4 v[72:75], v25, s[56:59], s27 offen sc1
A 56,152
A 57,153
A 58,154
A 59,155
A 60,156
A 61,157
A 62,158
A 63,159
v_cvt_pk_bf16_f32 v76, v56, v57
v_cvt_pk_bf16_f32 v77, v58, v59
v_cvt_pk_bf16_f32 v78, v60, v61
v_cvt_pk_bf16_f32 v79, v62, v63
s_mov_b32 s27, 19456
buffer_store_dwordx4 v[76:79], v25, s[56:59], s27 offen sc1
A 32,160
A 33,161
A 34,162
A 35,163
A 36,164
A 37,165
A 38,166
A 39,167
v_cvt_pk_bf16_f32 v80, v32, v33
v_cvt_pk_bf16_f32 v81, v34, v35
v_cvt_pk_bf16_f32 v82, v36, v37
v_cvt_pk_bf16_f32 v83, v38, v39
s_mov_b32 s27, 20480
buffer_store_dwordx4 v[80:83], v25, s[56:59], s27 offen sc1
A 40,168
A 41,169
A 42,170
A 43,171
A 44,172
A 45,173
A 46,174
A 47,175
v_cvt_pk_bf16_f32 v84, v40, v41
v_cvt_pk_bf16_f32 v85, v42, v43
v_cvt_pk_bf16_f32 v86, v44, v45
v_cvt_pk_bf16_f32 v87, v46, v47
s_mov_b32 s27, 21504
buffer_store_dwordx4 v[84:87], v25, s[56:59], s27 offen sc1
A 48,176
A 49,177
A 50,178
A 51,179
A 52,180
A 53,181
A 54,182
A 55,183
v_cvt_pk_bf16_f32 v88, v48, v49
v_cvt_pk_bf16_f32 v89, v50, v51
v_cvt_pk_bf16_f32 v90, v52, v53
v_cvt_pk_bf16_f32 v91, v54, v55
s_mov_b32 s27, 22528
buffer_store_dwordx4 v[88:91], v25, s[56:59], s27 offen sc1
A 56,184
A 57,185
A 58,186
A 59,187
A 60,188
A 61,189
A 62,190
A 63,191
v_cvt_pk_bf16_f32 v92, v56, v57
v_cvt_pk_bf16_f32 v93, v58, v59
v_cvt_pk_bf16_f32 v94, v60, v61
v_cvt_pk_bf16_f32 v95, v62, v63
s_mov_b32 s27, 23552
buffer_store_dwordx4 v[92:95], v25, s[56:59], s27 offen sc1
s_nop 7
A 32,192
A 33,193
A 34,194
A 35,195
A 36,196
A 37,197
A 38,198
A 39,199
v_cvt_pk_bf16_f32 v64, v32, v33
v_cvt_pk_bf16_f32 v65, v34, v35
v_cvt_pk_bf16_f32 v66, v36, v37
v_cvt_pk_bf16_f32 v67, v38, v39
s_mov_b32 s27, 24576
buffer_store_dwordx4 v[64:67], v25, s[56:59], s27 offen sc1
A 40,200
A 41,201
A 42,202
A 43,203
A 44,204
A 45,205
A 46,206
A 47,207
v_cvt_pk_bf16_f32 v68, v40, v41
v_cvt_pk_bf16_f32 v69, v42, v43
v_cvt_pk_bf16_f32 v70, v44, v45
v_cvt_pk_bf16_f32 v71, v46, v47
s_mov_b32 s27, 25600
buffer_store_dwordx4 v[68:71], v25, s[56:59], s27 offen sc1
A 48,208
A 49,209
A 50,210
A 51,211
A 52,212
A 53,213
A 54,214
A 55,215
v_cvt_pk_bf16_f32 v72, v48, v49
v_cvt_pk_bf16_f32 v73, v50, v51
v_cvt_pk_bf16_f32 v74, v52, v53
v_cvt_pk_bf16_f32 v75, v54, v55
s_mov_b32 s27, 26624
buffer_store_dwordx4 v[72:75], v25, s[56:59], s27 offen sc1
A 56,216
A 57,217
A 58,218
A 59,219
A 60,220
A 61,221
A 62,222
A 63,223
v_cvt_pk_bf16_f32 v76, v56, v57
v_cvt_pk_bf16_f32 v77, v58, v59
v_cvt_pk_bf16_f32 v78, v60, v61
v_cvt_pk_bf16_f32 v79, v62, v63
s_mov_b32 s27, 27648
buffer_store_dwordx4 v[76:79], v25, s[56:59], s27 offen sc1
s_nop 0
.Lpf_end%=:
.purgem M
.purgem T
.purgem R
.purgem A
.purgem W
)x"
      :
      : "s"(pk), "s"(pv), "s"(px), "s"(pq), "s"(po), "s"(pp), "s"(w), "i"(0), "s"(ps), "s"(b), "s"(g >> 7),
        "s"(ws + (size_t)(br * 16 + b) * 8 * PART_BYTES + 87040  )
      : "v0","v1","v2","v3","v4","v5","v6","v7","v8","v9","v10","v11","v12","v13","v14","v15","v16","v17","v18","v19","v20","v21","v22","v23","v24","v25","v26","v27","v28","v29","v30","v31","v32","v33","v34","v35","v36","v37","v38","v39","v40","v41","v42","v43","v44","v45","v46","v47","v48","v49","v50","v51","v52","v53","v54","v55","v56","v57","v58","v59","v60","v61","v62","v63","v64","v65","v66","v67","v68","v69","v70","v71","v72","v73","v74","v75","v76","v77","v78","v79","v80","v81","v82","v83","v84","v85","v86","v87","v88","v89","v90","v91","v92","v93","v94","v95","v96","v97","v98","v99","v100","v101","v102","v103","v104","v105","v106","v107","v108","v109","v110","v111","v112","v113","v114","v115","v116","v117","v118","v119","v120","v121","v122","v123","v124","v125","v126","v127","v128","v129","v130","v131","v132","v133","v134","v135","v136","v137","v138","v139","v140","v141","v142","v143","v144","v145","v146","v147","v148","v149","v150","v151","v152","v153","v154","v155","v156","v157","v158","v159","v160","v161","v162","v163","v164","v165","v166","v167","v168","v169","v170","v171","v172","v173","v174","v175","v176","v177","v178","v179","v180","v181","v182","v183","v184","v185","v186","v187","v188","v189","v190","v191","v192","v193","v194","v195","v196","v197","v198","v199","v200","v201","v202","v203","v204","v205","v206","v207","v208","v209","v210","v211","v212","v213","v214","v215","v216","v217","v218","v219","v220","v221","v222","v223","v224","v225","v226","v227","v228","v229","v230","v231","v232","v233","v234","v235","v236","v237","v238","v239","v240","v241","v242","v243","v244","v245","v246","v247","v248","v249","v250","v251","v252","v253","v254","v255","a0","a1","a2","a3","a4","a5","a6","a7","a8","a9","a10","a11","a12","a13","a14","a15","a16","a17","a18","a19","a20","a21","a22","a23","a24","a25","a26","a27","a28","a29","a30","a31","a32","a33","a34","a35","a36","a37","a38","a39","a40","a41","a42","a43","a44","a45","a46","a47","a48","a49","a50","a51","a52","a53","a54","a55","a56","a57","a58","a59","a60","a61","a62","a63","a64","a65","a66","a67","a68","a69","a70","a71","a72","a73","a74","a75","a76","a77","a78","a79","a80","a81","a82","a83","a84","a85","a86","a87","a88","a89","a90","a91","a92","a93","a94","a95","a96","a97","a98","a99","a100","a101","a102","a103","a104","a105","a106","a107","a108","a109","a110","a111","a112","a113","a114","a115","a116","a117","a118","a119","a120","a121","a122","a123","a124","a125","a126","a127","a128","a129","a130","a131","a132","a133","a134","a135","a136","a137","a138","a139","a140","a141","a142","a143","a144","a145","a146","a147","a148","a149","a150","a151","a152","a153","a154","a155","a156","a157","a158","a159","a160","a161","a162","a163","a164","a165","a166","a167","a168","a169","a170","a171","a172","a173","a174","a175","a176","a177","a178","a179","a180","a181","a182","a183","a184","a185","a186","a187","a188","a189","a190","a191","a192","a193","a194","a195","a196","a197","a198","a199","a200","a201","a202","a203","a204","a205","a206","a207","a208","a209","a210","a211","a212","a213","a214","a215","a216","a217","a218","a219","a220","a221","a222","a223","a224","a225","a226","a227","a228","a229","a230","a231","a232","a233","a234","a235","a236","a237","a238","a239","a240","a241","a242","a243","a244","a245","a246","a247","a248","a249","a250","a251","a252","a253","a254","a255","s27","s28","s29","s30","s31","s33","s34","s35","s36","s37","s38","s39","s40","s41","s42","s43","s44","s45","s46","s47","s48","s49","s50","s51","s52","s53","s54","s55","s56","s57","s58","s59","s60","s61","s62","s63","s84","s90","s91","s92","s93","s94","s95","memory","scc","vcc");
#define TS(n)
  TS(0);
  unsigned lane;
  asm volatile("v_mbcnt_lo_u32_b32 %0, -1, 0\n v_mbcnt_hi_u32_b32 %0, -1, %0" : "=v"(lane));
  const unsigned bb = br * 16 + b;
  unsigned* sync = (unsigned*)(ws + (size_t)256 * PART_BYTES + 65536 + (size_t)bb * 512);
  const unsigned tag = __hip_atomic_load(sync, __ATOMIC_RELAXED, __HIP_MEMORY_SCOPE_AGENT) * 0x9E3779B1u + 0x7F4A7C15u;
  unsigned f = __hip_atomic_load(sync + 64 + (lane & 7), __ATOMIC_RELAXED, __HIP_MEMORY_SCOPE_AGENT);
  asm volatile("s_waitcnt vmcnt(0)\n s_barrier" ::"v"(tag), "v"(f) : "memory");
  TS(1);
  if (w == 0 && lane == 0) __hip_atomic_store(sync + 64 + hp, tag, __ATOMIC_RELAXED, __HIP_MEMORY_SCOPE_AGENT);
  const char* pg = ws + (size_t)bb * 8 * PART_BYTES;
  const unsigned wv = hp * 4 + w;
  unsigned item[4];
#pragma unroll
  for (int k = 0; k < 4; ++k) {
    const unsigned e = k < 3 ? (wv + 32 * k) * 64 + lane : 96 * 64 + wv * 4 + (lane & 3), j = e >> 6;
    item[k] = (j < 84 ? j : 2 * j - 84) * 64 + (e & 63);
  }
  const __amdgpu_buffer_rsrc_t rs = __builtin_amdgcn_make_buffer_rsrc((void*)pg, 0, 8 * PART_BYTES, 0x00020000);
  u32x4 v[4][8] = {};
  unsigned done = 0;
  for (unsigned it = 0; it < (1u << 21); ++it) {
    const unsigned todo = (((unsigned)__builtin_amdgcn_ballot_w64(f == tag) & 0xffu) | (1u << hp)) & ~done;
    asm volatile("" ::: "memory");
#pragma unroll
    for (int i = 0; i < 8; ++i)
      if (todo >> i & 1) {
#pragma unroll
        for (int k = 0; k < 4; ++k) v[k][i] = __builtin_amdgcn_raw_buffer_load_b128(rs, item[k] * 16, i * PART_BYTES, 16);
      }
    done |= todo;
    if (done == 0xffu) break;
    __builtin_amdgcn_s_sleep(2);
    f = __hip_atomic_load(sync + 64 + (lane & 7), __ATOMIC_RELAXED, __HIP_MEMORY_SCOPE_AGENT);
  }
  if (wv == 0 && lane == 0) __hip_atomic_store(sync, tag, __ATOMIC_RELAXED, __HIP_MEMORY_SCOPE_AGENT);
  TS(2);
#pragma unroll
  for (int k = 0; k < 4; ++k) {
    u32x4 o;
#pragma unroll
    for (int j = 0; j < 4; ++j) {
      float lo = 0.f, hi = 0.f;
#pragma unroll
      for (int i = 0; i < 8; ++i) {
        lo += __uint_as_float(v[k][i][j] << 16);
        hi += __uint_as_float(v[k][i][j] & 0xffff0000u);
      }
      o[j] = pack_bf16(lo, hi);
    }
    const unsigned l = item[k] & 63, wt = item[k] >> 6, sw = wt / 28, ts = wt % 28;
    const unsigned row = sw * 32 + (ts & 1) * 16 + (l & 15);
    char* q = out + ((size_t)(b * 112 + row) * 896 + br * 448 + (ts >> 1) * 32 + (l >> 4) * 4) * 2;
    if (k < 3 || lane < 4) {
      *(u32x2*)q = u32x2{o[0], o[1]};
      *(u32x2*)(q + 32) = u32x2{o[2], o[3]};
    }
  }
  asm volatile(".Lkern_end:");
}

extern "C" void kernel_launch(void* const* d_in, const int* in_sizes, int n_in, void* d_out, int out_size,
                              void* d_ws, size_t ws_size, hipStream_t stream) {
  fused_attn_kernel<<<256, 256, 0, stream>>>(
      (const char*)d_in[0], (const char*)d_in[1], (const char*)d_in[2], (const char*)d_in[3], (const char*)d_in[4],
      (const char*)d_in[5], (const char*)d_in[6], (const char*)d_in[7], (const char*)d_in[8], (const char*)d_in[9],
      (char*)d_ws, (char*)d_out);
}
```
